```python
import torch

def _softmax(x, axis=-1):
    return torch.softmax(x, dim=axis)
def _gelu(x):
    return torch.nn.functional.gelu(x, approximate="tanh")
def _silu(x):
    return torch.nn.functional.silu(x)
def _sigmoid(x):
    return torch.sigmoid(x)
def _layernorm(x, axis=-1, eps=1e-5):
    m = x.mean(dim=axis, keepdim=True)
    v = x.var(dim=axis, keepdim=True, unbiased=False)
    return (x - m) / torch.sqrt(v + eps)
def _scatter_add(tgt, idx, src, axis=0):
    return tgt.clone().index_add_(axis, idx, src)
def _segment_sum(src, seg, n):
    out = torch.zeros((n,) + src.shape[1:], dtype=src.dtype)
    return out.index_add_(0, seg, src)
def _pad(x, pw):
    flat = tuple(v for pair in reversed(pw) for v in pair)
    return torch.nn.functional.pad(x, flat)

kernel_name = 'concat_heads__ssm_conv_h3__compressed_kv_cla__prompt'

def setup_inputs():

    _w = (lambda a: (lambda t: t.to(torch.bfloat16) if t.is_floating_point() else t)(torch.as_tensor(a)))
    def _A_setup():
        B, S, D = 1, 2048, 448
        ks = 4
        return {
            "q": _w((lambda *s: torch.randn(*s, dtype=torch.bfloat16) * 0.05)(B, S, D)),
            "k": _w((lambda *s: torch.randn(*s, dtype=torch.bfloat16) * 0.1)(B, S, D)),
            "v": _w((lambda *s: torch.randn(*s, dtype=torch.bfloat16) * 0.1)(B, S, D)),
            "shift_k": _w((lambda *s: torch.randn(*s, dtype=torch.bfloat16) * 0.1)(D, ks) * 0.5),
            "ssm_k": _w((lambda *s: torch.randn(*s, dtype=torch.bfloat16) * 0.1)(D, S) * (1.0 / S ** 0.5)),
            "ssm_D": _w((lambda *s: torch.randn(*s, dtype=torch.bfloat16) * 0.1)(D)),
        }
    _A_in = _A_setup()

    def _B_setup():
        B, H, Sq, Sk, D, M = 1, 16, 2048, 2048, 256, 448
        Dc = 4 * D
        return {
            "x": _w((lambda *s: torch.randn(*s, dtype=torch.bfloat16) * 3.2)(B, Sq, M)),
            "c_kv": _w((lambda *s: torch.randn(*s, dtype=torch.bfloat16) * 0.05)(B, Sk, Dc)),
            "w_q": _w((lambda *s: torch.randn(*s, dtype=torch.bfloat16) * 3.2)(M, H, D) * (M ** -0.5)),
            "w_uk": _w((lambda *s: torch.randn(*s, dtype=torch.bfloat16) * 3.2)(Dc, H, D) * (Dc ** -0.5)),
            "w_uv": _w((lambda *s: torch.randn(*s, dtype=torch.bfloat16) * 0.05)(Dc, H, D) * (Dc ** -0.5)),
            "w_o": _w((lambda *s: torch.randn(*s, dtype=torch.bfloat16) * 0.05)(H, D, M) * ((H * D) ** -0.5)),
        }
    _B_in = _B_setup()

    return {
        "A_q": _A_in["q"],
        "A_k": _A_in["k"],
        "A_v": _A_in["v"],
        "A_shift_k": _A_in["shift_k"],
        "A_ssm_k": _A_in["ssm_k"],
        "A_ssm_D": _A_in["ssm_D"],
        "B_x": _B_in["x"],
        "B_c_kv": _B_in["c_kv"],
        "B_w_q": _B_in["w_q"],
        "B_w_uk": _B_in["w_uk"],
        "B_w_uv": _B_in["w_uv"],
        "B_w_o": _B_in["w_o"],
    }

def reference(A_q, A_k, A_v, A_shift_k, A_ssm_k, A_ssm_D, B_x, B_c_kv, B_w_q, B_w_uk, B_w_uv, B_w_o):
    def _A_ref(q, k, v, shift_k, ssm_k, ssm_D):
        B, S, D = q.shape
        ks = shift_k.shape[-1]
        k_pad = _pad(k, ((0, 0), (ks - 1, 0), (0, 0)))
        k_shift = torch.zeros_like(k)
        for t in range(ks):
            k_shift = k_shift + k_pad[:, t:t + S, :] * shift_k[:, ks - 1 - t][None, None, :]
        kv = k_shift * v
        L = 2 * S
        kv_pad = _pad(kv, ((0, 0), (0, S), (0, 0)))
        kf_pad = _pad(ssm_k, ((0, 0), (0, S)))
        kv_f = (lambda x, axes=(-2,-1): torch.fft.rfft2(x.float(), dim=axes))(kv_pad[:, None, :, :], axes=(1, 2))
        kf_f = (lambda x, axes=(-2,-1): torch.fft.rfft2(x.float(), dim=axes))(kf_pad[:, None, :], axes=(1, 2))
        kf_f = (lambda x, *d: x.permute(*d))(kf_f, 1, 2, 0)
        y_f = kv_f * kf_f[None, :, :, :]
        y = (lambda x, s, axes=(-2,-1): torch.fft.irfft2(x, s=s, dim=axes).to(torch.bfloat16))(y_f, (1, L), axes=(1, 2))[:, 0, :S, :]
        y = y + kv * ssm_D[None, None, :]
        return q * y

    def _B_ref(x, c_kv, w_q, w_uk, w_uv, w_o):
        B, Sq, M = x.shape[0], x.shape[1], x.shape[2]
        Sk = c_kv.shape[1]
        H, D = w_q.shape[1], w_q.shape[2]
        q = torch.einsum("bqm,mhd->bhqd", x, w_q)
        k = torch.einsum("bkc,chd->bhkd", c_kv, w_uk)
        v = torch.einsum("bkc,chd->bhkd", c_kv, w_uv)
        scores = torch.einsum("bhqd,bhkd->bhqk", q, k) * (D ** -0.5)
        off = Sk - Sq
        qi = torch.arange(Sq)[:, None]
        kj = torch.arange(Sk)[None, :]
        cmask = kj <= (qi + off)
        scores = torch.where(cmask[None, None], scores, -1e9)
        p = _softmax(scores, axis=-1)
        ctx = torch.einsum("bhqk,bhkd->bhqd", p, v)
        return torch.einsum("bhqd,hdm->bqm", ctx, w_o)

    a = _A_ref(A_q, A_k, A_v, A_shift_k, A_ssm_k, A_ssm_D)
    b = _B_ref(B_x, B_c_kv, B_w_q, B_w_uk, B_w_uv, B_w_o)
    return torch.concatenate([a, b], axis=-1)
```

```cpp
#include <hip/hip_runtime.h>
#include <hip/hip_bf16.h>
#include <stdint.h>
#include <utility>
#include <type_traits>

typedef __hip_bfloat16 bf16;
typedef __attribute__((ext_vector_type(4))) short bf16x4;
typedef __attribute__((ext_vector_type(2))) unsigned u32x2;

constexpr int SEQ = 2048;
constexpr int DA = 448;
constexpr int NH = 16;
constexpr int HD = 256;
constexpr int DC = 1024;
constexpr int OUTW = 896;

#define KST(kid, which)

__device__ __forceinline__ float bf2f(unsigned short u) { return __uint_as_float(((unsigned)u) << 16); }
__device__ __forceinline__ unsigned short f2bf(float f) { return __bfloat16_as_ushort(__float2bfloat16(f)); }
__device__ __forceinline__ float rbf(float f) { return bf2f(f2bf(f)); }
typedef __attribute__((ext_vector_type(2))) __bf16 bf16x2n;
typedef __attribute__((ext_vector_type(2))) float f32x2n;
__device__ __forceinline__ unsigned pk2(float a, float b) {
  return __builtin_bit_cast(unsigned, __builtin_convertvector((f32x2n){a, b}, bf16x2n));
}

__device__ __forceinline__ int off_b(int row, int ch) { return 256 * row + 16 * (ch ^ (((row & 3) << 2) | ((row >> 2) & 3))); }

#define DI __device__ __forceinline__
#define SCC_
#define SCC1
#define SCC2
#define SCC3
#define GAP() __builtin_amdgcn_sched_barrier(0)
template <int R> DI float vget() { float t; asm volatile("v_mov_b32 %0, v%c1" : "=v"(t) : "i"(R)); return t; }
template <int R> DI void vset(float x) { asm volatile("v_mov_b32 v%c0, %1" ::"i"(R), "v"(x)); }
template <int R> DI void vseti(unsigned x) { asm volatile("v_mov_b32 v%c0, %1" ::"i"(R), "v"(x)); }
template <int R> DI float acc_read1() { float t; asm volatile("v_accvgpr_read_b32 %0, a[%c1]" : "=v"(t) : "i"(R)); return t; }
template <class F, int... I> DI void static_for(F&& f, std::integer_sequence<int, I...>) {
  (f(std::integral_constant<int, I>{}), ...);
}
template <int... Is> using iseq = std::integer_sequence<int, Is...>;
template <int N> using make_iseq = std::make_integer_sequence<int, N>;
typedef unsigned long long u64;

constexpr unsigned O_K = 1u << 24, O_V = 2u << 24, O_C = 3u << 24, O_P = 4u << 24, O_F = O_P + 128 * 67584, O_S = O_F + 400 * 4352,
                   O_X = O_S + (1u << 24), O_Z = O_X + DA * SEQ * 2, O_W = O_Z + DA * SEQ * 2;
static_assert(O_W + 16 * 262144 <= 0x6400000);
constexpr unsigned fwo(int w) { return O_F + (w >> 4) * 4352 + (w & 15) * 8; }
constexpr unsigned OOB = 0x80000000u, M0X = 0x28000;
constexpr unsigned L_SP = 131072;
#define CMAX 10
#define NYA 15
#define NYB 15
#define MAP4 2
#define TSEQ 1
constexpr int O_SEQ[16] = {0x00, 0x01, 0x02, 0x10, 0x20, 0x30, 0x11, 0x21, 0x31, 0x12, 0x22, 0x32, 0x13, 0x23, 0x33, 0x03};
constexpr int o_k(int P) { int k = 0; for (int q = 0; q < P; ++q) k += O_SEQ[q] >= 16; return O_SEQ[P] >= 16 ? k : -1; }
#define Q44 1
#define KTYV 0x76543210fedcba98ull
#define LATEP 0
#define PKS 3
#define PPOS 99, 10, 9, 9, 9, 8
#define TABX 0xfedcb9875a463210ull, 0xefadc6982b574310ull, 0xfebad7693c285410ull, 0xefcba8764d392510ull
#define K2(c) (2 + (c))
#define POSTM 7
#define NCV 0
#define WP0 8
constexpr int ODD = NYA & 1;
static_assert((NYB & 1) == ODD);
constexpr int NYS[8] = {NYA, NYA, NYA, NYA, NYB, NYB, NYB, NYB};
enum { A_TAB = 0, A_FPREP = 2, A_FT2, A_NP, A_NPB = 6, A_VBB, A_MB2, A_VBA, A_MA2, A_VOO, A_SX, A_MX = 16, A_VOQ = 20, A_FG, A_NQ, A_N01, A_ZO, A_OQ,
       A_WSRC, A_WDST, A_PQ, A_PZ, A_M0F, A_GH, A_PC = 32, A_WLD = 44, A_FC, A_FCW, A_FCM, A_SS, A_FS = 51, A_FSW, A_FSM, A_SL, A_OUT = 57, A_OWN, A_FE = 62, A_GF };
enum { B_N = 0, B_ST, B_MS, B_CT, B_FW, B_FSET, B_NXT, B_SO, B_OP, B_MM, B_PM, C_FCM = 16, C_QT, B_LM, B_LP = 21, B_WD, C_PO, B_ACT = 32 };
enum { E_CHK = 1, E_PACK = 4, E_POLL = 8, E_POLL1 = 16, E_MASK = 32, E_FLAG = 64, E_WFLAG = 128, E_POLLZ = 256, E_LOAD = 512, E_POST = 1024, E_PLOAD = 2048, E_CHK1 = 4096 };
constexpr unsigned ev_code(unsigned a) {
  return a == 0 ? 0 : a == E_CHK ? 16 : a == E_FLAG ? 32 : a == (E_FLAG | E_POLL) ? 96 : a == E_POLL ? 56 : a == (E_WFLAG | E_POLLZ) ? 344 : a == E_WFLAG ? 80 : a == (E_FLAG | E_POLLZ) ? 448 : a == E_LOAD ? 400 : a == E_POST ? 512 : a == E_PLOAD ? 768 : a == (E_PLOAD | E_CHK1) ? 900 : a == E_CHK1 ? 872 :
         a == E_PACK ? 128 : a == E_POLL1 ? 192 : a == (E_POLL | E_POLL1 | E_MASK) ? 228 : a == (E_POLL1 | E_MASK) ? 240 : 1u << 31;
}
struct PTab { unsigned v[32][4][64][3]; bool bad; };
constexpr int p_unit(int j, int rr) { return (j & 1) && rr < 4 ? -1 : (j >> 1) * 28 + (j & 1) * 12 + rr; }
constexpr int o_head(int ks, int p) { return 2 * ks + (p >> 1) + 8 * (p & 1); }
constexpr PTab make_tab(int j) {
  PTab t{};
  for (int r = 0; r < 32; ++r) for (int wid = 0; wid < 4; ++wid) {
    auto& e = t.v[r][wid];
    const int rr = r & 15, hsg = rr & 1, pjg = rr >> 1, kv = pjg & 1, gw = j * 128;
    const bool isY = r >= 16;
    const int ks = j >> 1, p = MAP4 ? rr & 3 : (j & 1) * 2 + hsg, h = o_head(ks, p), hsel = h >> 3, ga = (h & 7) * 128;
    const int pj = MAP4 == 1 ? (rr >> 2) * 2 + (j & 1) : MAP4 == 2 ? (rr >> 2) + 4 * (j & 1) : pjg;
    const int mt = (rr >> 2) + (isY ? 4 : 0), cr = rr - (pjg > NCV ? 2 : 0), cch = j * 56 + cr * 4 + wid, NY = NYS[pj];
    const bool doA = !isY && pjg != NCV;
    const unsigned oG = (unsigned)(j + 8 * hsg) << 20;
    const int nx0 = 32 - 2 * pj - NY, cx = nx0 < CMAX ? nx0 : CMAX, ex = nx0 - cx, dg = 30 - 2 * pj;
    int seq[2][32] = {}, ns = 0;
    auto run = [&](int it, int t0, int t1) { for (int q = t0; q < t1; ++q) seq[it][ns++] = q; };
    const int x1 = TSEQ ? dg - ex : 16, c4 = 32 - 8 - NY < CMAX ? 32 - 8 - NY : CMAX;
    if (!isY) {
      run(0, 0, cx); run(0, x1, x1 + ex);
      ns = 0;
      const int s1 = TSEQ >= 2 && nx0 + TSEQ - 2 + 2 <= 2 * pj ? (nx0 < 4 ? 2 : nx0 + TSEQ - 2) : 0;
      run(1, s1, 2 * pj); run(1, 0, s1); run(1, 2 * pj, 2 * pj + 2);
    } else if (ex || !TSEQ) { run(0, cx, x1); run(0, x1 + ex, dg + 2); }
    else { run(0, c4 > cx ? c4 : cx, 16); run(0, cx, c4); run(0, 16, dg + 2); }
    const u64 own = h == j + 8 * hsg && isY ? 1ull << (hsel * 16 + kv * 8 + mt) : 0;
    u64 needB = 0;
    int c1 = 99;
    for (int i = (isY ? NY : nx0) - 1; i >= 0; --i) if (seq[0][i] >= 16) { needB |= 0x101ull << (hsel * 16 + (seq[0][i] >> 2)); c1 = i - 2; }
    needB &= ~own;
    const u64 needQ = (1ull << (32 + hsel * 8 + (pj >> 1))) | (1ull << (32 + hsel * 8 + ((15 - pj) >> 1)));
    const u64 needA = 0x0f0full << (hsel * 16);
    const unsigned oQ = (unsigned)h << 20, oK = oQ + O_K, oP = O_P + (h * 8 + pj) * 67584;
    const unsigned oQ0 = oQ + ((15 - pj) * 128 + wid * 32) * 512, oK0 = oK + seq[0][0] * 32768, lowb = ((wid & 1) + (wid >> 1) * 4) * 4096;
    auto w0 = [&](unsigned x) { return wid == 0 ? x : OOB; };
    const int cls = (rr + (rr >> 2)) & 3;
    const u64 tabX[4] = {TABX};
    const u64 tab = isY ? KTYV : tabX[cls];
    if ((tabX[cls] & 0xfff) != 0x10u + K2(cls) * 0x100) t.bad = true;
    e[C_QT][2] = 0x6543;
    e[A_TAB][0] = (unsigned)tab; e[A_TAB + 1][0] = (unsigned)(tab >> 32);
    const int pu = p_unit(j, rr);
    e[A_FPREP][0] = pu < 0 ? OOB : w0(fwo(6160 + pu));
    const int hq = Q44 ? o_head(ks, rr & 3) : j + 8 * hsg, tq = Q44 ? (rr >> 2) + 4 * (j & 1) : pjg;
    e[A_FT2][0] = isY ? w0(fwo((hq & 7) * 128 + 32 + (hq >> 3) * 8 + tq)) : OOB;
    const u64 np = isY ? needA | needQ : 0xffffull;
    e[A_NP][0] = (unsigned)np; e[A_NP + 1][0] = (unsigned)(np >> 32); e[A_NPB][0] = fwo(isY ? ga : 6160 + (cch >> 6) * 16);
    e[A_VBB][0] = oQ0; e[A_MB2][0] = isY ? 65536 + wid * 1024 : M0X;
    e[A_VBA][0] = oK0 + wid * 1024; e[A_MA2][0] = isY ? wid * 1024 : M0X;
    e[A_VOO][0] = oG + (kv ? O_V : O_K) + mt * 131072 + (wid >> 1) * 65536 + (wid & 1) * (kv ? 8192 : 4096);
    e[A_SX][0] = isY ? oK0 + lowb + 8192 : O_X + cch * 4096; e[A_SX + 1][0] = isY ? oQ0 + 12288 : cch * 4096;
    e[A_SX + 2][0] = oK0 + 32768 + lowb; e[A_SX + 3][0] = oK0 + O_K + wid * 8192;
    e[A_MX][0] = isY ? lowb + 8192 : doA ? 65536 + wid * 15488 : M0X; e[A_MX + 1][0] = isY ? 114688 + wid * 4096 : e[A_MX][0] + 5120;
    e[A_MX + 2][0] = isY ? 32768 + lowb : M0X; e[A_MX + 3][0] = isY ? 65536 + wid * 8192 : M0X;
    e[A_VOQ][0] = ((unsigned)hq << 20) + tq * 131072 + (wid >> 1) * 65536 + (wid & 1) * 4096;
    e[A_FG][0] = w0(fwo(gw + hsg * 16 + kv * 8 + mt));
    e[A_NQ][0] = (unsigned)(needQ >> 32); e[A_N01][0] = 0x0101u << (hsel * 16);
    e[A_ZO][0] = isY ? (pu < 0 ? OOB : O_X + (pu >> 4) * (64 * SEQ * 2) + (15 - (pu & 15)) * 256) : O_Z + j * (56 * SEQ * 2) + (cr * 4 + wid) * 64; e[A_OQ][0] = oQ0;
    const int qbl = isY ? 15 - pj : pj, u = 2 * pj, tile = qbl * 4 + p;
    e[A_WSRC][0] = ((o_head(ks, p ^ (u >> 2)) * 256 + (u & 3) * 64) * DA + p * 112) * 2;
    e[A_WLD][0] = O_W + h * 262144 + wid * 8192;
    e[A_WDST][0] = e[A_WLD][0] + u * 16384;
    e[A_PQ][0] = ((rr * 4 + wid) * 32 * DA + j * 56) * 2; e[A_PZ][0] = O_Z + (j * 64 + rr * 4 + wid) * 3584;
    e[A_M0F][0] = L_SP + wid * 8192; e[C_PO][2] = ((rr * 4 + wid) * 32 * OUTW + j * 56) * 2;
    e[A_GH][0] = fwo(gw + 64);
    auto qo = [&](int i) { return i == 3 ? ks : i + (i >= ks); };
    auto ctxo = [&](int o, int qb, int q) { return O_C + ((unsigned)o_head(ks, p ^ o) << 20) + qb * 65536 + q * 16384 + wid * 4096; };
    auto slab = [&](int tl, int q, int src) { return O_S + tl * 262144 + (q * 8 + src) * 8192 + wid * 2048; };
    for (int q = 0; q < 16; ++q) if (o_k(q) >= 0) e[A_PC + o_k(q)][0] = ctxo(O_SEQ[q] >> 4, qbl, qo(O_SEQ[q] & 3));
    e[A_FC][0] = w0(fwo(1024 + (qbl * 4 + ks) * 16 + p)); e[A_FCW][0] = fwo(1024 + (qbl * 4 + ks) * 16);
    e[A_FCM][0] = 15 & ~(1 << p);
    for (int q = 0; q < 3; ++q) { e[A_SS + q][0] = slab(tile, qo(q), 2 * ks); e[A_SL + q][0] = slab(tile, ks, 2 * qo(q)); }
    e[A_FS][0] = fwo(2048 + (tile * 4 + wid) * 16 + 2 * ks); e[A_FSW][0] = fwo(2048 + (tile * 4 + wid) * 16); e[A_FSM][0] = 0x55 & ~(1 << (2 * ks));
    e[A_OUT][0] = ((qbl * 128 + (ks < 2 ? 2 * ks : 11 - 2 * ks) * 16) * OUTW + DA + p * 112 + wid * 32) * 2;
    for (int q = 0; q < 4; ++q) e[A_OWN + q][0] = qo(q) * 16384;
    e[A_FE][0] = r == 28 && j == 4 ? w0(fwo(6144)) : OOB;
    e[A_GF][0] = fwo(ga);
    int wstep = isY || pj >= PKS ? 99 : 4, wpos = 0;
    for (int it = 0; it < 2; ++it) {
      const int R = 1 + it, n = isY ? NY : it ? 2 * pj + 2 : nx0, qb = isY || !it ? 15 - pj : pj;
      const bool last = isY || it;
      unsigned act[32] = {};
      int np_ = 0;
      auto poll = [&](int i, u64 m) {
        const int l = B_PM + 2 * np_++;
        e[l][R] = (unsigned)m; e[l + 1][R] = (unsigned)(m >> 32);
        if (act[i] & E_POLL) t.bad = true;
        act[i] |= E_POLL; act[i + 1] |= E_CHK;
      };
      if (!it || isY) act[1] |= E_FLAG;
      const bool late = isY && pj < LATEP;
      if (isY) {
        const int pa[8] = {99, 99, PPOS};
        int s_ = late ? n - 2 : pa[pj] + 15 - NY < n - 3 ? pa[pj] + 15 - NY : n - 3;
        int q = 1;
        if ((POSTM >> pj) & 1) {
          act[1] |= E_POLLZ; act[2] = E_LOAD;
          q = c1 == 4 ? 5 : 3;
          act[q] = E_POST;
        }
        while (s_ <= q || (s_ + 1 >= c1 - 1 && s_ <= c1)) ++s_;
        if (c1 < 4 || s_ > n - 3 + late) t.bad = true;
        if (c1 < s_) poll(c1 - 1, needB);
        poll(s_, 1ull << (48 + hsel * 8 + pj));
        act[s_ + 1] = late ? 0 : E_PLOAD;
        if (c1 > s_) poll(c1 - 1, needB);
      }
      else if (!it) { if (n > 2) poll(1, needA); if (c1 < 99) poll(c1 - 1, needB); }
      else if (nx0 <= 2) poll(1, needA);
      if (late) e[B_LM + 1][R] = 1u << (16 + hsel * 8 + pj);
      e[B_LP][R] = oP + wid * 8192;
      const int* sq = seq[isY ? 0 : it];
      if (sq[1] != sq[0] + 1 || (!isY && seq[1][0] + 1 >= 4 && nx0 < 4)) t.bad = true;
      if (last && n > 2) act[n - 3] |= E_POLL1 | E_MASK;
      if (!last && pj == 0) act[n - 2] |= E_POLL1;
      if (last) act[n - 2] |= E_CHK1;
      if (it && !isY && pj >= PKS) wstep = 5, wpos = WP0;
      const int wend = (POSTM >> pj) & 1 ? 6 : 8;
      const int ws_[4] = {E_PACK, wend == 6 ? E_WFLAG : E_WFLAG | E_POLLZ, E_LOAD, E_POST};
      for (int i = 0; i < (last ? n - 3 : n) && !isY; ++i, ++wpos)
        if (wstep < wend && wpos >= WP0 && !act[i] && i) act[i] = ws_[wstep++ - 4];
      if (it && wstep < wend) t.bad = true;
      e[B_WD][R] = pj >= PKS ? e[A_WDST][0] : OOB;
      e[B_FW][R] = wid < 2 ? fwo((j ^ wid) * 128 + 80 + wid * 16 + rr) : OOB;
      e[B_MS][R] = doA ? e[A_MX][0] + 10304 : M0X; e[B_CT][R] = (rr * 4 + wid) * 128;
      for (int i = 0; i < n; ++i) {
        const unsigned sk = i + 2 < n ? oK + sq[i + 2] * 32768 : last ? O_W + h * 262144 + 32768 : oK + (seq[1][0] + (i & 1)) * 32768;
        e[B_ACT + i][R] = sk | ev_code(act[i]);
        if (ev_code(act[i]) > 1023 || (sk & 1023)) t.bad = true;
      }
      e[B_N][R] = n; e[B_ST][R] = oK + sq[0] * 32768;
      e[B_FSET][R] = isY ? e[A_FG][0] : it ? w0(fwo(ga + 48 + hsel * 8 + pj)) : wid == 1 ? fwo(1024 + (qb * 4 + ks) * 16 + 8 + p) : doA ? w0(fwo(gw + 112 + cr)) : OOB;
      e[B_MM][R] = 256u << p;
      e[B_NXT][R] = last ? e[A_WLD][0] : oQ + (pj * 128 + wid * 32) * 512;
      e[B_SO][R] = last ? oQ + O_C + qb * 65536 : oP;
      e[B_OP][R] = oP + 65536 + wid * 256;
    }
  }
  return t;
}
constexpr PTab PT0 = make_tab(0), PT1 = make_tab(1), PT2 = make_tab(2), PT3 = make_tab(3), PT4 = make_tab(4), PT5 = make_tab(5), PT6 = make_tab(6), PT7 = make_tab(7);
static_assert(!PT0.bad && !PT7.bad);
static __device__ const PTab PTAB[8] = {PT0, PT1, PT2, PT3, PT4, PT5, PT6, PT7};

#define SPINB "0x100000"
#define SW "s[56:59]"
template <int S, int L, int ROW = 0> DI void sld() { asm volatile("v_readlane_b32 s%c0, v%c1, %c2" ::"i"(S), "i"(92 + ROW), "i"(L)); }
template <int L, int ROW = 0> DI unsigned prm() { unsigned x; asm volatile("v_readlane_b32 %0, v%c1, %c2\n\ts_nop 4" : "=s"(x) : "i"(92 + ROW), "i"(L)); return x; }
#define A_FLAG "s_mov_b64 exec, 1\n\tbuffer_store_dwordx2 v[82:83], off, " SW ", s63 sc1\n\ts_mov_b64 exec, -1\n\t"
#define A_POLL(V, M, B) "s_mov_b64 exec, " M "\n\tbuffer_load_dwordx2 " V ", v88, " SW ", " B " offen sc1\n\ts_mov_b64 exec, -1\n\t"
#define A_SPIN(V, M, B) "s_mov_b32 s95, " SPINB "\n\ts_mov_b64 exec, " M "\n" \
    "8:\n\tbuffer_load_dwordx2 " V ", v88, " SW ", " B " offen sc1\n\ts_waitcnt vmcnt(0)\n\tv_cmp_eq_u64 vcc, s[60:61], " V "\n\t" \
    "s_cmp_eq_u64 vcc, " M "\n\ts_cbranch_scc1 9f\n\ts_sleep 4\n\ts_sub_u32 s95, s95, 1\n\ts_cbranch_scc0 8b\n9:\n\ts_mov_b64 exec, -1\n\t"
#define A_CHECK(V, M, B) "v_cmp_eq_u64 vcc, s[60:61], " V "\n\ts_and_b64 vcc, vcc, " M "\n\ts_cmp_eq_u64 vcc, " M "\n\ts_cbranch_scc1 9f\n\t" A_SPIN(V, M, B)
#define A_CTOUCH(B, E, M) "v_readlane_b32 s63, v93, 3\n\ts_sub_u32 s96, s100, at_event - " B "\n\ts_subb_u32 s97, s101, 0\n\t" \
    "s_mov_b32 s98, " E " - " B "\n\ts_mov_b32 s99, s59\n\tv_lshl_add_u32 %0, v89, 9, s63\n\t" \
    "s_mov_b32 m0, 0x27f00\n\ts_mov_b64 exec, " M "\n\tbuffer_load_dword %0, s[96:99], 0 offen lds\n\ts_mov_b64 exec, -1\n\t"
template <int K> DI void code_touch() {
  unsigned t;
  if constexpr (K) asm volatile(A_CTOUCH("rsv_end", "at_tail", "0xff") : "=&v"(t) :: "scc", "memory");
  else asm volatile(A_CTOUCH("rsv_end", "rsv2_begin", "0x1f") : "=&v"(t) :: "scc", "memory");
}
template <int L, int ROW = 0> DI void set_flag() {
  asm volatile("v_readlane_b32 s63, v%c0, %c1\n\ts_nop 3\n\t" A_FLAG ::"i"(92 + ROW), "i"(L) : "memory");
}
template <int LO, int HI> DI void fl_mask() {
  if constexpr (LO >= 0) sld<96, LO>(); else asm volatile("s_mov_b32 s96, 0");
  if constexpr (HI >= 0) sld<97, HI>(); else asm volatile("s_mov_b32 s97, 0");
}
DI void fl_issue() { asm volatile(A_POLL("v[122:123]", "s[96:97]", "s62") ::: "memory"); }
DI void fl_check() { asm volatile(A_CHECK("v[122:123]", "s[96:97]", "s62") ::: "vcc", "scc", "memory"); }
DI void fl_wait() { asm volatile("s_nop 4\n\t" A_SPIN("v[122:123]", "s[96:97]", "s62") ::: "vcc", "scc", "memory"); }

typedef __attribute__((ext_vector_type(4))) unsigned u32x4c;
typedef __attribute__((ext_vector_type(4))) float f32x4c;
typedef __attribute__((ext_vector_type(2))) float f32x2c;
__device__ __forceinline__ float bfl(unsigned u) { return __uint_as_float(u << 16); }
__device__ __forceinline__ float bfh(unsigned u) { return __uint_as_float(u & 0xffff0000u); }

#define CODE_TOUCH(LB, LE, wx, NL, m0v) do { unsigned blo_, bhi_, elo_; \
    asm volatile("s_getpc_b64 s[100:101]\n\ts_add_u32 s100, s100, " LB "@rel32@lo+4\n\ts_addc_u32 s101, s101, " LB "@rel32@hi+12\n\t" \
                 "s_mov_b32 %0, s100\n\ts_mov_b32 %1, s101\n\t" \
                 "s_getpc_b64 s[100:101]\n\ts_add_u32 s100, s100, " LE "@rel32@lo+4\n\ts_mov_b32 %2, s100" \
                 : "=s"(blo_), "=s"(bhi_), "=s"(elo_) :: "scc"); \
    const unsigned n_ = elo_ - (blo_ & ~127u); \
    const __amdgpu_buffer_rsrc_t srd_ = __builtin_amdgcn_make_buffer_rsrc((void*)(((unsigned long long)bhi_ << 32) | (blo_ & ~127u)), (short)0, n_ < 262144u ? n_ : 0u, 0x00020000); \
    const unsigned l_ = threadIdx.x & 63, vo_ = l_ < (NL) ? ((wx) + l_ * 128u) * 128u : 0x80000000u; \
    asm volatile("s_mov_b32 m0, %0\n\ts_nop 4\n\tbuffer_load_dword %1, %2, 0 offen lds" ::"s"(m0v), "v"(vo_), "s"(srd_) : "memory"); } while (0)

#define STT(k)
#define SNK 31
#define GSC " sc1"
#define PL0 66
#define PLS 7
#define PCAP 0x55555555
#define PST 272
#define ECAP 3
#define EPACE_K 7
#define EPACE_V 6
typedef __attribute__((ext_vector_type(4))) float f32x4g;
typedef __attribute__((ext_vector_type(4))) unsigned u32x4g;
DI u32x4g pair16(f32x4g x, f32x4g y) {
  const unsigned a0 = pk2(x[0], x[1]), a1 = pk2(x[2], x[3]), b0 = pk2(y[0], y[1]), b1 = pk2(y[2], y[3]);
  auto r0 = __builtin_amdgcn_permlane16_swap(a0, b0, false, false), r1 = __builtin_amdgcn_permlane16_swap(a1, b1, false, false);
  return (u32x4g){r0[0], r1[0], r0[1], r1[1]};
}
constexpr int LDW = NH * HD;
struct GM { unsigned aA[2], aB[2][8]; };
#define GDP 8, 12, 24, 28, 40, 44, 56, 60
constexpr int GDPOS[8] = {GDP};
constexpr int g_j(int G) { for (int i = 0; i < 8; ++i) if (GDPOS[i] == (G & 63)) return i; return -1; }
constexpr int GD13 = 125 - GDPOS[3];
struct PA { unsigned vo, lw, lx, lr, vs; __amdgpu_buffer_rsrc_t sk, sv, sw; };
struct POp { int k, d, a, b; };
struct PSch { POp op[760]; int first[770], vm[770], n; };
constexpr PSch p_sched() {
  PSch z{};
  POp l[760] = {};
  int n = 0, early[760] = {};
  auto put = [&](int k, int d, int a, int b, int e = 0) { l[n] = POp{k, d, a, b}; early[n] = e; ++n; };
  for (int q = 0; q < 10; ++q) put(10, q < 6 ? q : q + 2, 0, 0, PL0 + PLS * q);
  for (int c = 0; c < 8; ++c) {
    const int I = c >> 1, u = c & 1;
    for (int t = 0; t < 4; ++t) put(t & 1, 112 + t, 76 + 2 * u + (t >> 1), 0, c ? 0 : PST);
    if (u && I < 3) put(11, I + 1, 0, 0);
    for (int q = 0; q < 7; ++q) put(u, 116 + q, 32 + 4 * q + I, 0);
    for (int e = 2; e >= 0; e -= 2) {
      put(2, 125, 117 + e, 115); put(2, 123, 116 + e, 115);
      for (int t = 1; t < 4; ++t) { put(4, 125, 117 + e + t, 115 - t); put(4, 123, 116 + e + t, 115 - t); }
      const int T = e ? 126 : 127;
      put(u, 124, 64 + 4 * e + I, 0); put(u, T, 60 + 4 * e + I, 0);
      put(2, 125, 125, 124); put(2, 123, 123, T);
      put(5, T, 125, 123);
    }
    put(12, c, 0, 0, (PST + 2) / 64 * 64 + 62);
  }
  const int nm = n;
  for (int q = 0; q < 4; ++q) put(13, q, 0, 0);
  for (int q = 0; q < 4; ++q) put(14, q, 0, 0);
  int g = 0, i = 0, hold = 0;
  for (; g < 768; ++g) {
    const int q = g & 15;
    int cap = g_j(g) >= 0 ? 0 : (PCAP >> (2 * q)) & 3;
    z.first[g] = z.n; z.vm[g + 1] = z.vm[g];
    if (g == PST - 16) z.op[z.n++] = POp{11, 0, 0, 0};
    if (g == PST - 20) z.op[z.n++] = POp{15, 0, 0, 0};
    if (g >= PST - 16 && g < PST - 13) for (int r = (g - PST + 16) * 7 / 3; r < (g - PST + 17) * 7 / 3; ++r) z.op[z.n++] = POp{16, r, 0, 0};
    while (cap > 0 && i < n && early[i] <= g && hold <= g) {
      const int k = l[i].k;
      if ((k == 11 || k == 13) && q > 2) { if (k == 13) break; }
      z.op[z.n++] = l[i];
      if (k == 10 || k == 14) { ++z.vm[g + 1]; cap = 0; hold = g + 3; }
      if (i == nm - 1) hold = (g + 3) / 64 * 64 + 64;
      if (i == nm + 3) hold = g / 16 * 16 + 16;
      --cap; ++i;
    }
  }
  z.first[768] = z.n;
  if (i < n) z.n = -1;
  return z;
}
constexpr PSch PS = p_sched();
constexpr int p_last() { int g = 0; while (PS.first[g] < PS.n) ++g; return g; }
static_assert(PS.n > 0 && PS.vm[(PST - 16) / 64 * 64 - 3 - GD13] == 10 && PST % 16 == 0);
template <int I> DI void p_op(const PA& p) {
  constexpr POp o = PS.op[I];
  if constexpr (o.k == 0) asm volatile("v_lshlrev_b32_e32 v%c0, 16, v%c1" ::"i"(o.d), "i"(o.a));
  else if constexpr (o.k == 1) asm volatile("v_and_b32_e32 v%c0, s94, v%c1" ::"i"(o.d), "i"(o.a));
  else if constexpr (o.k == 2) asm volatile("v_mul_f32_e32 v%c0, v%c1, v%c2" ::"i"(o.d), "i"(o.a), "i"(o.b));
  else if constexpr (o.k == 4) asm volatile("v_fmac_f32_e32 v%c0, v%c1, v%c2" ::"i"(o.d), "i"(o.a), "i"(o.b));
  else if constexpr (o.k == 5) asm volatile("v_cvt_pk_bf16_f32 v%c0, v%c1, v%c2" ::"i"(o.d), "i"(o.a), "i"(o.b));
  else if constexpr (o.k == 10 && o.d == 0)
    asm volatile("s_mov_b32 s93, m0\n\ts_add_u32 m0, s79, %1\n\ts_mov_b32 s94, 0xffff0000\n\tbuffer_load_dwordx4 v89, %0, 0 offen lds\n\ts_mov_b32 m0, s93"
                 ::"s"(p.sw), "i"(L_SP + 20480) : SCC_ "memory");
  else if constexpr (o.k == 10 && o.d < 6) {
    if constexpr (o.d == 1) asm volatile("v_and_b32 v113, 0x380, v89\n\tv_mul_u32_u24 v114, 21, v113\n\tv_sub_u32 v114, %0, v114\n\ts_mul_i32 s95, s79, 5" ::"v"(p.vo));
    asm volatile("v_add_u32 v112, %1, v114\n\ts_mov_b32 s93, m0\n\ts_add_u32 m0, s95, %2\n\ts_mov_b32 exec_lo, %3\n\ts_mov_b32 exec_hi, %4\n\t"
                 "buffer_load_dwordx4 v112, %0, 0 offen lds\n\ts_mov_b64 exec, -1\n\ts_mov_b32 m0, s93"
                 ::"s"(p.sk), "i"((o.d - 2) * 8 * DA * 2), "i"(L_SP + (o.d - 1) * 1024), "i"(o.d == 1 ? 0 : -1), "i"(o.d == 1 ? -256 : -1) : SCC_ "memory");
  } else if constexpr (o.k == 10)
    asm volatile("buffer_load_dwordx4 v[%c0:%c1], %2, %3, 0 offen offset:%c4" ::"i"(28 + 4 * o.d), "i"(31 + 4 * o.d), "v"(p.vo), "s"(p.sv), "i"((o.d & 3) * DA * 2) : "memory");
  else if constexpr (o.k == 15) asm volatile("v_mad_u32_u24 v113, v113, 3, v89\n\tv_add_u32 v113, s95, v113\n\tv_add_u32 v113, %0, v113" ::"i"(L_SP + 640));
  else if constexpr (o.k == 16) asm volatile("ds_read_b128 v[%c0:%c1], v113 offset:%c2" ::"i"(32 + 4 * o.d), "i"(35 + 4 * o.d), "i"(o.d * 128));
  else if constexpr (o.k == 11) asm volatile("ds_read_b128 v[76:79], %0 offset:%c1" ::"v"(p.lw), "i"(o.d * 16));
  else if constexpr (o.k == 12) asm volatile("ds_write_b64 %0, v[126:127] offset:%c1" ::"v"(p.lx), "i"(o.d * 256));
  else if constexpr (o.k == 13)
    asm volatile("v_xor_b32 v112, %3, %2\n\tds_read_b128 v[%c0:%c1], v112 offset:%c4" ::"i"(32 + 4 * o.d), "i"(35 + 4 * o.d), "v"(p.lr), "i"(o.d * 32), "i"(o.d * 4096));
  else
    asm volatile("buffer_store_dwordx4 v[%c0:%c1], %2, " SW ", s93 offen sc1\n\ts_add_u32 s93, s93, 0x10000" ::"i"(32 + 4 * o.d), "i"(35 + 4 * o.d), "v"(p.vs) : SCC_ "memory");
}
template <int B, int... Is> DI void p_ops_(const PA& p, iseq<Is...>) { (p_op<B + Is>(p), ...); }
template <int Gg> DI void p_ops(const PA& p) { p_ops_<PS.first[Gg]>(p, make_iseq<PS.first[Gg + 1] - PS.first[Gg]>{}); }
constexpr float CS = 0.0625f * 1.4426950408889634f;
struct EOp { int k, d, s, g; };
struct ESch { EOp op[640]; int first[260], st[260], n; bool ok; };
constexpr int e_load(int g) { return g >= 64 ? ((g & 7) == 4 ? g >> 3 : -1) : g % 5 != 4 || g > 59 ? -1 : g < 40 ? g / 5 : g / 5 + 8; }
constexpr int e_nld(int g0, int g1) { int n = 0; for (int g = g0; g < g1; ++g) n += e_load(g) >= 0; return n; }
constexpr int e_qrd(int g) { return g >= 32 && g < 48 && (g & 1) ? (g - 32) / 2 : g >= 104 && g < 120 && (g & 1) ? 8 + (g - 104) / 2 : -1; }
constexpr ESch e_sched(int MODE) {
  ESch t{}, z{};
  int cap[260] = {}, used[260] = {}, dead[640] = {};
  const int NG = MODE == 0 ? 256 : 128;
  for (int g = 0; g < NG; ++g) {
    const int q = g & 15;
    if (MODE == 0) cap[g] = g_j(g) >= 0 ? 1 : (q == 1 || q == 2 || q == 3 || q == 5 || q == 7 || q == 9) ? 2 : q == 14 ? 4 : 3;
    else cap[g] = g < 16 ? 1 : (q == 1 || q == 2 || e_load(g) >= 0 || e_qrd(g) >= 0) ? 1 : 2;
  }
  cap[NG] = 1000;
  int n = 0, g = 0;
  auto put = [&](int k, int d, int s_, int early, int dl) {
    if (g < early) g = early < NG ? early : NG;
    while (used[g] >= cap[g]) ++g;
    ++used[g]; if (k == 3) ++used[g];
    t.op[n] = EOp{k, d, s_, g}; dead[n] = dl; ++n;
  };
  auto av = [](int m, int nf) { return 16 * m + 2 * nf + 10; };
  for (int j = 0; j < (MODE == 0 ? 32 : 16); ++j) {
    const int m0 = MODE == 2 ? (j >> 3) * 2 : j >> 2, n0 = MODE == 2 ? j & 7 : (j & 3) * 2;
    const int m1 = MODE == 2 ? m0 + 1 : m0, n1 = MODE == 2 ? n0 : n0 + 1;
    const int T = j < 16 ? 128 + 8 * j : 112 + 8 * (j & 1), dl0 = 128 + 32 * (j >> 3) + 2 * (j & 7) - 1, dl1 = j < 16 ? dl0 : 999;
    const int pace = MODE == 0 ? 0 : (MODE == 1 ? 14 : 26) + j * (MODE == 1 ? EPACE_K : EPACE_V);
    if (j >= 16) for (int k = 0; k < 8; ++k) put(0, T + k, 8 * (j - 16) + k, k < 4 ? av(m0, n0) : av(m1, n1), dl0);
    for (int h = 0; h < 2; ++h) {
      const int e = h ? av(m1, n1) : av(m0, n0);
      if (MODE == 0) for (int k = 0; k < 4; ++k) put(1, T + 4 * h + k, 0, e, dl1);
      for (int k = 0; k < 2; ++k) put(2, T + 2 * h + k, T + 4 * h + 2 * k, e, dl1);
    }
    put(3, T, j, j < 16 || MODE ? pace : 0, dl1);
  }
  z.ok = true;
  for (int q = 0; q <= NG; ++q) {
    z.first[q] = z.n; z.st[q + 1] = z.st[q];
    for (int i = 0; i < n; ++i) if (t.op[i].g == q) {
      z.op[z.n++] = t.op[i];
      if (t.op[i].k == 3) ++z.st[q + 1];
      if (MODE == 0 && q > dead[i]) z.ok = false;
    }
  }
  z.first[NG + 1] = z.n;
  return z;
}
constexpr ESch ES[3] = {e_sched(0), e_sched(1), e_sched(2)};
static_assert(ES[0].ok && ES[0].first[256] == ES[0].n && ES[1].st[126] == 16 && ES[2].st[126] == 16);
template <int MODE, int I> DI void e_op() {
  constexpr EOp o = ES[MODE].op[I];
  if constexpr (o.k == 0) asm volatile("v_accvgpr_read_b32 v%c0, a[%c1]" ::"i"(o.d), "i"(o.s));
  else if constexpr (o.k == 1) asm volatile("v_mul_f32_e32 v%c0, s94, v%c0" ::"i"(o.d));
  else if constexpr (o.k == 2) asm volatile("v_cvt_pk_bf16_f32 v%c0, v%c1, v%c2" ::"i"(o.d), "i"(o.s), "i"(o.s + 1));
  else {
    constexpr int m = o.s >> 2;
    if constexpr ((o.s & 3) == 0)
      asm volatile("s_mov_b32 s93, %0" ::"i"(MODE == 2 ? (m >> 1) * 16384 + (m & 1) * 4096 : MODE == 1 ? m * 8192 : (m < 4 ? 2 * m : 15 - 2 * m) * 8192));
    asm volatile("buffer_store_dwordx4 v[%c0:%c1], v%c2, " SW ", s93 offen offset:%c3 sc1" ::"i"(o.d), "i"(o.d + 3), "i"(MODE ? 84 : 95), "i"((o.s & 3) * 1024) : "memory");
  }
}
template <int MODE, int B, int... Is> DI void e_ops_(iseq<Is...>) { (e_op<MODE, B + Is>(), ...); }
template <int MODE, int Gg> DI void e_ops() { e_ops_<MODE, ES[MODE].first[Gg]>(make_iseq<ES[MODE].first[Gg + 1] - ES[MODE].first[Gg]>{}); }
template <int P, int KS, int I> DI void g_wread(const GM& g) {
  constexpr int nf = I >> 1, hf = I & 1, r = 128 + 64 * P + 8 * nf + 4 * KS + 2 * hf;
  asm volatile("ds_read_b64_tr_b16 a[%c1:%c2], %0 offset:%c3" ::"v"(g.aB[hf][nf]), "i"(r), "i"(r + 1), "i"(P * 32768 + KS * 16384));
}
template <int P, int M, int KS> DI void g_aread(const GM& g) {
  constexpr int r = 96 + 8 * (M & 1) + 4 * KS;
  asm volatile("ds_read_b128 v[%c1:%c2], %0 offset:%c3" ::"v"(g.aA[KS]), "i"(r), "i"(r + 3), "i"(P * 32768 + M * 2048));
}
template <bool W, int V, int J> DI void g_dma() {
  if constexpr (W) {
    if constexpr (J == 0) asm volatile("buffer_load_dwordx4 v%c0, s[68:71], 0 offen" GSC " lds" ::"i"(V) : "memory");
    else asm volatile("buffer_load_dwordx4 v%c0, s[68:71], s%c1 offen" GSC " lds" ::"i"(V), "i"(79 + J) : "memory");
  } else {
    if constexpr (J == 0) asm volatile("buffer_load_dwordx4 v%c0, s[64:67], 0 offen" GSC " lds" ::"i"(V) : "memory");
    else asm volatile("buffer_load_dwordx4 v%c0, s[64:67], s%c1 offen" GSC " lds" ::"i"(V), "i"(71 + J) : "memory");
  }
}
template <int Q> DI void att_item() {
  if constexpr (Q == 0) sld<83, B_ST, 1>();
  if constexpr (Q == 1) asm volatile("s_mov_b32 s85, %0" ::"i"(B_PM));
  if constexpr (Q == 2) asm volatile("s_add_u32 s84, s83, 0x8000\n\ts_mov_b32 s86, %0" ::"i"(B_ACT) SCC1);
  if constexpr (Q >= 3 && Q < 6) asm volatile("v_mov_b64 v[%c0:%c1], 0" ::"i"(108 + 2 * Q), "i"(109 + 2 * Q));
  if constexpr (Q == 6) asm volatile("v_mov_b32 v120, 0x45800000");
}
template <int I> DI void g_pro1();
template <int K, int I> DI void g_part() {
  constexpr bool W = K < 2;
  asm volatile("s_add_u32 m0, s%c0, %1\n\ts_nop 0" ::"i"(96 + K), "i"(W ? I * 4096 : (I & 1) * 4096 + (I >> 1) * 16384) : "scc");
  g_dma<W, 115 + K, W ? I : (I & 1) + (I >> 1) * 4>();
}
template <int Y, int I> DI void g_tag() {
  if constexpr (I == 0) { if constexpr (Y) sld<88, C_QT, 2>(); else { sld<88, A_TAB>(); sld<89, A_TAB + 1>(); } }
  if constexpr (I == 1 && !Y) asm volatile("s_lshr_b64 s[88:89], s[88:89], 12" SCC3);
  if constexpr (I == 3) asm volatile("v_readfirstlane_b32 s60, v120");
  if constexpr (I == 5) asm volatile("v_readfirstlane_b32 s61, v121");
  if constexpr (I == 9) asm volatile("s_mul_i32 s61, s61, 0x4c957f2d\n\ts_mul_i32 s63, s60, 0x5851f42d");
  if constexpr (I == 11) asm volatile("s_add_u32 s61, s61, s63\n\ts_mul_hi_u32 s63, s60, 0x4c957f2d" SCC3);
  if constexpr (I == 13) asm volatile("s_add_u32 s61, s61, s63\n\ts_mul_i32 s60, s60, 0x4c957f2d" SCC3);
  if constexpr (I == 15) asm volatile("s_add_u32 s60, s60, 0xf767814f\n\ts_addc_u32 s61, s61, 0x14057b7e" SCC3);
  if constexpr (I == 17) asm volatile("v_mov_b64 v[82:83], s[60:61]");
}
template <bool TR, int G> DI void g_last_setup(int wid) {
  if constexpr (G == 0) { sld<72, A_SX>(); sld<76, A_MX>(); }
  if constexpr (G >= 1 && G < 4) sld<72 + G, A_SX + G>();
  if constexpr (G == 3) sld<63, A_VOO>();
  if constexpr (G >= 5 && G < 8) sld<72 + G, A_MX + G - 4>();
  if constexpr (G == 6) asm volatile("v_add_u32 v84, s63, v89");
  if constexpr (G == 7) asm volatile("s_lshl_b32 s63, %0, 10\n\ts_lshl_b32 s95, %0, 12" ::"s"(wid) SCC1);
  if constexpr (G == 8) asm volatile("v_add_u32 v85, %0, v84\n\tv_add_u32 v86, 0x10000, v89" ::"i"(TR ? 4096 : 8192));
  if constexpr (G == 9) asm volatile("v_add_u32 v87, s63, v86\n\tv_add_u32 v95, s95, v86");
  if constexpr (G == 10) asm volatile("s_lshl_b32 s80, %0, 13\n\ts_bitset1_b32 s80, 16" ::"s"(wid) SCC1);
  if constexpr (G == 11) asm volatile("s_add_u32 s81, s80, 0x8000\n\ts_mov_b32 s90, 0x8000" SCC3);
  if constexpr (G == 13) { sld<62, A_GF>(); asm volatile("s_mov_b32 s91, 0xc000"); }
  if constexpr (G == 14) { att_item<0>(); att_item<1>(); }
  if constexpr (G == 15) { att_item<2>(); sld<92, A_GH>(); }
  if constexpr (G == 12) { sld<87, B_MS, 1>(); asm volatile("s_add_u32 s82, s73, 2" SCC3); }
}
template <bool TR, int P, int H, int G, int MD, int PT> DI void g_gap(const GM& g, int wid, const PA& pa) {
  constexpr bool FIRST = MD == 1 || MD == 2, LAST = MD == 4;
  constexpr int m = 4 * H + (G >> 4), q = G & 15, n = (SNK & 8) && MD < 2 && (m & 1) ? 7 - (q >> 1) : q >> 1, ks = (SNK & 4 ? q ^ (q >> 1) : q) & 1;
  constexpr int a = 96 + 8 * (m & 1) + 4 * ks, b = 128 + 64 * P + 8 * n + 4 * ks, d0 = TR ? 64 * (m >> 1) + 8 * n + 4 * (m & 1) : 32 * m + 4 * n, d = m < 4 ? 128 + d0 : d0 - 128;
  constexpr bool ST0 = MD == 1 && H == 0;
  constexpr int J = LAST ? -1 : ST0 ? (G >= 48 && G % 4 == 0 ? G / 4 - 8 : -1) : g_j(G);
  constexpr int gj = (J & 3) ^ ((J & 3) >> 1);
  constexpr int EM = LAST ? 1 + TR : 0, eg = (MD == 2 ? 128 : 0) + 64 * H + G, L = LAST ? e_load(eg) : -1;
  if constexpr (MD == 1 && G == 32) asm volatile("s_waitcnt vmcnt(%0) lgkmcnt(%1)\n\ts_barrier" ::"i"(H ? 12 + (PT >= 0 ? PS.vm[eg] : 0) : 19), "i"(8 * H) : "memory");
  else if constexpr (q == 0 && G > 0) asm volatile("s_waitcnt lgkmcnt(%0)" ::"i"(LAST ? 0 : MD == 1 ? 8 * H : 4));
  if constexpr (J == 0) asm volatile("s_add_u32 m0, s79, %0" ::"i"((P ^ H ^ 1) * 32768 + (1 - H) * 8192) SCC1);
  else if constexpr (J == 4) asm volatile("s_add_u32 m0, s87, %0" ::"i"((P ^ H) * 32768 + (1 - H) * 16384) SCC1);
  else if constexpr (J > 0 && (J & 3) == 1) asm volatile("s_bitset1_b32 m0, 12");
  else if constexpr (J > 0 && (J & 3) == 2) asm volatile("s_bitset1_b32 m0, %0" ::"i"(J < 4 ? 14 : 13));
  else if constexpr (J > 0) asm volatile("s_bitset0_b32 m0, 12");
  if constexpr (L >= 0 && (L & 3) == 0) asm volatile("s_mov_b32 m0, s%c0" ::"i"(L < 16 ? 76 + (L >> 2) : 87));
  constexpr int QR = LAST ? e_qrd(eg) : -1;
  if constexpr (QR == 0) asm volatile("s_waitcnt vmcnt(%0)" ::"i"(8 + e_nld(0, eg) + ES[EM].st[eg]) : "memory");
  if constexpr (QR == 8) asm volatile("s_waitcnt vmcnt(%0)" ::"i"(e_nld(64, eg) + ES[EM].st[eg] - ES[EM].st[60]) : "memory");
#define GMF(D, X, Y, C) asm volatile("v_mfma_f32_16x16x32_bf16 " D "[%c0:%c1], " X ", " Y ", " C ::"i"(d), "i"(d + 3), "i"(a), "i"(a + 3), "i"(b), "i"(b + 3))
#define GMF2(D, C) do { if constexpr (TR) GMF(D, "v[%c2:%c3]", "a[%c4:%c5]", C); else GMF(D, "a[%c4:%c5]", "v[%c2:%c3]", C); } while (0)
  if constexpr (FIRST && !(q & 1)) { if constexpr (m < 4) GMF2("v", "0"); else GMF2("a", "0"); }
  else if constexpr (m < 4) GMF2("v", "v[%c0:%c1]");
  else GMF2("a", "a[%c0:%c1]");
#undef GMF2
#undef GMF
  if constexpr (J >= 0 && J < 4) g_dma<false, 84, (1 - H) * 2 + (gj & 1) + (gj >> 1) * 4>();
  else if constexpr (J >= 4) g_dma<true, 85, (1 - H) * 4 + gj>();
  if constexpr (L >= 0)
    asm volatile("buffer_load_dwordx4 v89, s[%c0:%c1], s%c2 offen offset:%c3 sc1 lds" ::"i"(L >= 4 && L < 8 || L >= 16 ? 68 : 56), "i"(L >= 4 && L < 8 || L >= 16 ? 71 : 59),
                 "i"(L < 16 ? 72 + (L >> 2) : 82), "i"((L & 3) * 1024) : "memory");
  if constexpr (QR >= 0)
    asm volatile("ds_read_b128 a[%c1:%c2], v%c0 offset:%c3" ::"i"(QR < 12 ? 87 : 95), "i"(128 + 4 * QR), "i"(131 + 4 * QR), "i"(QR < 12 ? QR * 4096 : 49152 + (QR - 12) * 1024));
  if constexpr ((q == 1 || q == 2) && !(LAST && m == 7)) g_aread<(m < 7 ? P : P ^ 1), (m + 1) & 7, q - 1>(g);
  if constexpr (!LAST) {
    if constexpr (MD == 1 && H == 1 && q >= 3 && q < 11) g_wread<P ^ 1, (G >> 5), ((G >> 4) & 1) * 8 + q - 3>(g);
    if constexpr (MD != 1 && q >= 3 && q < 11 && (q & 1)) g_wread<P ^ 1, H, (G >> 4) * 4 + (q - 3) / 2>(g);
    if constexpr (MD == 1 && eg >= 62 && eg < 80) g_tag<P, eg - 62>();
    if constexpr (H == 1 && G == 0) asm volatile("s_mov_b32 s90, s91");
    if constexpr (H == 1 && G == 4) asm volatile("v_lshl_add_u32 v84, s90, 7, v86\n\ts_bfe_u32 s91, s88, 0x40000" SCC3);
    if constexpr (H == 1 && G == 6) asm volatile("s_lshr_b64 s[88:89], s[88:89], 4\n\ts_lshl_b32 s92, s91, 19" SCC3);
    if constexpr (H == 1 && G == 10) asm volatile("v_add_u32 v85, s92, v87");
  } else if constexpr (eg < 16) g_last_setup<TR, eg>(wid);
  if constexpr (ST0 && G == 23) asm volatile("v_lshl_add_u32 v85, s91, 19, v87");
  if constexpr (ST0 && G < 36 && G % 3 == 0) g_part<G < 12 ? 2 : G < 24 ? 1 : 3, (G / 3) & 3>();
  if constexpr (ST0 && G >= 36 && G < 48 && G % 3 == 0) g_pro1<16 + G / 3>();
  if constexpr (MD == 3 && eg == 0) sld<63, A_VOQ>();
  if constexpr (MD == 3 && eg == 4) asm volatile("v_add_u32 v95, s63, v89\n\ts_mov_b32 s94, %0" ::"i"(__builtin_bit_cast(unsigned, CS)));
  if constexpr (MD >= 2) e_ops<EM, eg>();
  constexpr int pg = 128 * PT + 64 * H + G;
  if constexpr (PT == 2 && eg == 0) sld<93, A_ZO>();
  if constexpr (PT >= 0) p_ops<pg>(pa);
  if constexpr (G == 61 && !LAST) {
    constexpr int ns = MD >= 2 ? ES[0].st[eg + 1] - ES[0].st[eg >= GD13 ? eg - GD13 : 0] : PT >= 0 ? PS.vm[pg + 1] - PS.vm[pg >= GD13 ? pg - GD13 : 0] : 0;
    asm volatile("s_waitcnt vmcnt(%0) lgkmcnt(0)\n\ts_barrier" ::"i"(12 + ns) : "memory");
  } else if constexpr (G == 61) asm volatile("s_waitcnt lgkmcnt(0)\n\ts_barrier" ::: "memory");
  GAP();
}
template <bool TR, int P, int H, int MD, int PT, int... Gs> DI void g_half_(const GM& g, int wid, const PA& pa, iseq<Gs...>) { (g_gap<TR, P, H, Gs, MD, PT>(g, wid, pa), ...); }
template <bool TR, int P, int H, int MD> DI void g_half(const GM& g, int wid = 0) { g_half_<TR, P, H, MD, -1>(g, wid, PA{}, make_iseq<64>{}); }
template <int T, int H> DI void q_half(const GM& g, const PA& pa) { g_half_<false, (T & 1) ^ 1, H, T == 0, T>(g, 0, pa, make_iseq<64>{}); }
typedef __attribute__((ext_vector_type(16))) unsigned u32x16;
struct RSV { u32x16 a, b, c; };
DI void rsv_begin(RSV& r) { asm volatile("rsv_begin:" : "={v[32:47]}"(r.a), "={v[48:63]}"(r.b), "={v[64:79]}"(r.c)); }
DI void rsv_end(const RSV& r) { asm volatile("rsv_end:" ::"{v[32:47]}"(r.a), "{v[48:63]}"(r.b), "{v[64:79]}"(r.c)); }
template <int P, int... Is> DI void g_read0(const GM& g, iseq<Is...>) {
  (g_wread<P, 0, Is>(g), ...); (g_wread<P, 1, Is>(g), ...);
  g_aread<P, 0, 0>(g); g_aread<P, 0, 1>(g);
}
template <int I> DI void g_pro1() {
  constexpr int grp = I >> 2, i = I & 3, t = grp < 4 ? 0 : grp < 7 ? 1 : 2;
  constexpr bool isA = grp == 0 || grp == 3 || grp == 5;
  constexpr int jj = isA ? (grp == 3 ? 2 : 0) + (i & 1) + (i >> 1) * 4 : (grp == 2 || grp == 6 ? 4 : 0) + i;
  asm volatile("s_add_u32 m0, s%c0, %1\n\ts_nop 0" ::"i"(t == 1 ? 92 : 90), "i"((isA ? 0 : 65536) + jj * 4096) : "scc");
  g_dma<!isA, (isA ? 84 : 85) + (t ? 0 : 2), jj>();
}
template <int B, int... Is> DI void g_pro(iseq<Is...>) { (g_pro1<B + Is>(), ...); }
template <bool TR, bool SCL, class HO, class HE> DI void g_loop(const GM& g, HO&& odd, HE&& even) {
  constexpr int nkt = SCL ? 7 : 16, P0 = SCL ? 1 : 0;
#pragma unroll 1
  for (int t = 1;; t += 2) {
    g_half<TR, P0 ^ 1, 0, 0>(g);
    odd(t);
    GAP();
    g_half<TR, P0 ^ 1, 1, 0>(g);
    if (SCL && t + 2 >= nkt) break;
    g_half<TR, P0, 0, 0>(g);
    even(t + 1);
    GAP();
    g_half<TR, P0, 1, 0>(g);
    if (!SCL && t + 3 >= nkt) break;
  }
}

typedef __attribute__((ext_vector_type(4))) unsigned u32x4;
typedef __attribute__((ext_vector_type(4))) float f32x4;

constexpr int R_S = 128, R_P = 192, R_CI = 224, R_E = 96;
constexpr int R_TS = 112, R_RS = 114, R_M = 116;
constexpr int R_PD = 118, R_THR = 120, R_TMP = 121;
struct AC {
  u64 redo;
  unsigned rare;
};
template <int T, int D, int K> DI void d_op() {
  constexpr int t = T + 8 * (D & 1);
  if constexpr (K < 8) asm volatile("v_accvgpr_read_b32 v%c0, a[%c1]" ::"i"(t + K), "i"(8 * D + K));
  else if constexpr (K < 12) asm volatile("v_cvt_pk_bf16_f32 v%c0, v%c1, v%c2" ::"i"(t + K - 8), "i"(t + 2 * (K - 8)), "i"(t + 2 * (K - 8) + 1));
  else
    asm volatile("buffer_store_dwordx4 v[%c0:%c1], v%c2, " SW ", s%c3 offen offset:%c4 sc1" ::"i"(t), "i"(t + 3), "i"(84 + ((D >> 2) & 1)), "i"(90 + (D >> 3)),
                 "i"((D & 3) * 1024) : "memory");
}
template <int T, int O0, int... Os> DI void d_ops(iseq<Os...>) { (d_op<T, (O0 + Os) / 13, (O0 + Os) % 13>(), ...); }
template <int D> DI void d_store() { d_ops<160, 13 * D>(make_iseq<13>{}); }
template <int X, int F> DI void kread() {
  constexpr int r = 192 + 4 * (F & 15);
  asm volatile("ds_read_b128 a[%c0:%c1], v89 offset:%c2" ::"i"(r), "i"(r + 3), "i"(X * 32768 + F * 1024));
}
template <int X, int F> DI void vread() {
  constexpr int r = 192 + 4 * (F & 15);
  asm volatile("ds_read_b128 a[%c0:%c1], v86 offset:%c2" ::"i"(r), "i"(r + 3), "i"(X * 32768 + (F & 1) * 16384 + (F >> 1) * 1024));
}
template <int X, int... Fs> DI void kreads(iseq<Fs...>) { (kread<X, Fs>(), ...); }
template <int X, int... Fs> DI void vreads(iseq<Fs...>) { (vread<X, Fs>(), ...); }
template <int X, int J> DI void dma_m0() {
  if constexpr (J == 0) asm volatile("s_mov_b32 m0, s%c0" ::"i"(80 + X));
  else if constexpr (J == 4) asm volatile("s_or_b32 m0, s%c0, 0x1000" ::"i"(80 + X) SCC1);
  else if constexpr (J == 8) asm volatile("s_bitset0_b32 m0, 16");
  else if constexpr (J == 12) asm volatile("s_bitset0_b32 m0, 12");
}
template <int J> DI void dma_at() {
  asm volatile("buffer_load_dwordx4 v%c0, " SW ", s%c1 offen offset:%c2 sc1 lds" ::"i"(J >= 4 && J < 12 ? 91 : 90), "i"(J < 8 ? 82 : 84), "i"((J & 3) * 1024) : "memory");
}
template <int V, int S> DI void dma4() {
  asm volatile("buffer_load_dwordx4 v%c0, " SW ", s%c1 offen sc1 lds\n\tbuffer_load_dwordx4 v%c0, " SW ", s%c1 offen offset:1024 sc1 lds\n\t"
               "buffer_load_dwordx4 v%c0, " SW ", s%c1 offen offset:2048 sc1 lds\n\tbuffer_load_dwordx4 v%c0, " SW ", s%c1 offen offset:3072 sc1 lds"
               ::"i"(V), "i"(S) : "memory");
}
template <int Q> DI void k01_dma() {
  asm volatile("s_add_u32 m0, s80, %0\n\ts_nop 0" ::"i"((Q >> 1) * 32768 + (Q & 1) * 4096 - 65536) SCC1);
  dma4<90 + (Q & 1), 83 + (Q >> 1)>();
}
template <int R, int I, int ST = 4, int V = 89> DI void f_load() {
  asm volatile("buffer_load_dwordx4 a[%c0:%c1], v%c3, " SW ", s72 offen offset:%c2 sc1" ::"i"(R + ST * I), "i"(R + ST * I + 3), "i"((I & 3) * 1024), "i"(V) : "memory");
}
DI void f_next() { asm volatile("s_add_u32 s72, s72, 0x1000" SCC3); }
constexpr int o_wreg(int o, int f) { return o == 0 ? 128 + 4 * f : o == 3 ? 192 + 4 * f : 8 * f + 4 * (o - 1); }
template <int F, int LASTF = 3> DI void w_load() {
  f_load<o_wreg(F >> 4, 0), F & 15, o_wreg(F >> 4, 1) - o_wreg(F >> 4, 0), (F & 1) ? 84 : 89>();
  if constexpr ((F & 3) >= LASTF) asm volatile("s_add_u32 s72, s72, %0" ::"i"(F < 4 ? 0x1000 : F < 8 ? 0xf000 : F & 4 ? 0x7000 : 0x1000) SCC1);
}
#define NWF 40
constexpr int CV_KP = 5120 - 32, CV_KS = 10304 - 32;
struct CV { unsigned pa[4], pb, lw, vzo; };
constexpr int cv_J(int q) { int J = 7; while (q > J) { q -= J + 1; --J; } return J; }
constexpr int cv_i(int q) { int J = 7; while (q > J) { q -= J + 1; --J; } return q; }
template <int r_, int N> DI void cv_rd(const CV& c) {
  constexpr int R = (r_ + 1) & 7;
  if constexpr (N < 16) {
    constexpr int i = N >> 1, o = 16 * R + 128 * (i & 1) + 2 * (N & 1), r = 192 + 32 * (r_ & 1) + 2 * N;
    asm volatile("ds_read2_b32 a[%c1:%c2], %0 offset0:%c3 offset1:%c4" ::"v"(c.pa[i >> 1]), "i"(r), "i"(r + 1), "i"(o), "i"(o + 1));
  } else {
    constexpr int m = N - 16, r = 160 + 32 * (r_ & 1) + 4 * m;
    asm volatile("ds_read_b128 v[%c1:%c2], %0 offset:%c3" ::"v"(c.pb), "i"(r), "i"(r + 3), "i"(64 * R + 512 * (7 - m)));
  }
}
template <int O> DI void cv_z(const CV& c) {
  constexpr int pr = O / 7, o = O % 7, w = 128 + 4 * (7 - 2 * pr), y = w + 2 * (o & 1) - 4 * (o >> 1);
  if constexpr (o == 4) asm volatile("s_add_u32 s63, s65, %2\n\tv_permlane16_swap_b32_e32 v%c0, v%c1" ::"i"(w), "i"(w + 2), "i"((6 - 2 * pr) * 8 * 3584) SCC1);
  else if constexpr (o == 5) asm volatile("v_permlane16_swap_b32_e32 v%c0, v%c1" ::"i"(w + 1), "i"(w + 3));
  else if constexpr (o == 6) asm volatile("buffer_store_dwordx4 v[%c0:%c1], %2, " SW ", s63 offen sc1" ::"i"(w), "i"(w + 3), "v"(c.vzo) : "memory");
  else asm volatile("v_cvt_pk_bf16_f32 v%c0, v%c1, v%c2" ::"i"(w + o), "i"(y), "i"(y + 1));
}
#define CVS 6
#define CVF 150
#define CVP 206
#define CVK 250
constexpr int CVQ = 16 * CVS + 2;
constexpr int cv_vm(int T) {
  return T < 16 * CVS && T % CVS == CVS - 1 ? 1 : T >= CVQ && T < CVQ + 96 && (T - CVQ) % 6 == 0 ? 2 : T == CVF ? 4 : T == CVP ? 5 : T > CVK && T <= CVK + 48 && (T - CVK) % 3 == 1 ? 7 : 0;
}
constexpr int cv_cnt(int T0, int T1, int kind = 0) { int n = 0; for (int T = T0; T < T1; ++T) n += kind ? cv_vm(T) == kind : cv_vm(T) != 0; return n; }
static_assert(16 * CVS + 4 <= 108 && cv_vm(CVF) == 4 && cv_vm(CVP) == 5 && CVQ + 96 < CVK);
constexpr int cv_z0(int T) { return T < 271 ? 0 : T < 278 ? T - 271 : T < 281 ? 7 : T - 274; }
template <int I> DI void k01_dma1() {
  if constexpr ((I & 3) == 0) asm volatile("s_add_u32 m0, s80, %0\n\ts_nop 0" ::"i"((I >> 3) * 32768 + ((I >> 2) & 1) * 4096 - 65536) SCC1);
  asm volatile("buffer_load_dwordx4 v%c0, " SW ", s%c1 offen offset:%c2 sc1 lds" ::"i"(90 + ((I >> 2) & 1)), "i"(83 + (I >> 3)), "i"((I & 3) * 1024) : "memory");
}
template <int T> DI void cv_slot(const CV& c) {
  constexpr int r = T / 36, Q = T % 36, i = cv_i(Q), J = cv_J(Q), d = 128 + 4 * J, a = 192 + 32 * (r & 1) + 4 * i, b = 160 + 32 * (r & 1) + 4 * (J - i), vm = cv_vm(T);
  if constexpr (Q == 0) asm volatile("s_waitcnt lgkmcnt(0)");
  if constexpr (r == 0 && i == 0)
    asm volatile("v_mfma_f32_16x16x32_bf16 v[%c0:%c1], a[%c2:%c3], v[%c4:%c5], 0" ::"i"(d), "i"(d + 3), "i"(a), "i"(a + 3), "i"(b), "i"(b + 3));
  else
    asm volatile("v_mfma_f32_16x16x32_bf16 v[%c0:%c1], a[%c2:%c3], v[%c4:%c5], v[%c0:%c1]" ::"i"(d), "i"(d + 3), "i"(a), "i"(a + 3), "i"(b), "i"(b + 3));
  if constexpr (r == 7 && i == 0) asm volatile("v_mfma_f32_16x16x32_bf16 v[%c0:%c1], v[96:99], v[%c2:%c3], v[%c0:%c1]" ::"i"(d), "i"(d + 3), "i"(b), "i"(b + 3));
  if constexpr (Q < 24 && r < 7) cv_rd<r + 1, Q>(c);
  if constexpr (T == 32) asm volatile("ds_read_u16 v121, %0 offset:5120" ::"v"(c.lw));
  if constexpr (T == 33) asm volatile("ds_write_b128 %0, v[114:117] offset:%c1\n\tds_write_b128 %0, v[114:117] offset:%c2" ::"v"(c.lw), "i"(CV_KS), "i"(CV_KS + 16));
  if constexpr (T == 62) asm volatile("ds_write_b16 %0, v121 offset:%c1" ::"v"(c.lw), "i"(CV_KS + 30));
  if constexpr (T == 34) sld<72, A_OQ>();
  if constexpr (T == 35) sld<65, A_ZO>();
  if constexpr (T == CVQ - 2) { asm volatile("s_waitcnt vmcnt(16)" ::: "memory"); fl_check(); }
  if constexpr (T < 16 * CVS) d_ops<224, 13 * (T / CVS) + (T % CVS * 13) / CVS>(make_iseq<((T % CVS + 1) * 13) / CVS - (T % CVS * 13) / CVS>{});
  if constexpr (vm == 2) { f_load<128, (T - CVQ) / 6>(); if constexpr ((T - CVQ) / 6 % 4 == 3) f_next(); }
  if constexpr (vm == 4) {
    asm volatile("s_waitcnt vmcnt(%0)\n\ts_barrier" ::"i"(cv_cnt(16 * CVS, T)) : "memory");
    set_flag<A_FG>();
  }
  if constexpr (T == CVP - 3) fl_mask<A_N01, -1>();
  if constexpr (vm == 5) fl_issue();
  if constexpr (T == CVK) { asm volatile("s_waitcnt vmcnt(%0)" ::"i"(cv_cnt(CVP + 1, T)) : "memory"); fl_check(); }
  if constexpr (vm == 7) k01_dma1<cv_cnt(CVK, T, 7)>();
  if constexpr (cv_z0(T + 1) > cv_z0(T)) cv_z<cv_z0(T)>(c);
  GAP();
}
template <int... T> DI void cv_slots(const CV& c, iseq<T...>) { (cv_slot<T>(c), ...); }
template <int... N> DI void cv_rd0(const CV& c, iseq<N...>) { (cv_rd<0, N>(c), ...); }
template <int O> DI void cv_tail(const CV& c) {
  constexpr int k = cv_cnt(CVK, 288, 7) + O / 6;
  if constexpr (O % 6 == 0 && k < 16) k01_dma1<k>();
  cv_z<cv_z0(288) + O>(c);
}
template <int... O> DI void cv_tails(const CV& c, iseq<O...>) { (cv_tail<O>(c), ...); }
static_assert(cv_cnt(CVK, 288, 7) >= 8 && cv_cnt(CVK, 288, 7) + 3 >= 16);
constexpr int cv_end() {
  int T = CVK; while (cv_cnt(CVK, T + 1, 7) < 8) ++T;
  int n = 8; for (int o = cv_z0(T); o < 28; ++o) n += o % 7 == 6;
  return n;
}

template <int Q> DI void acc_zero() {
  asm volatile("v_mfma_f32_32x32x16_bf16 a[%c0:%c1], v[208:211], v[208:211], 0" ::"i"(16 * Q), "i"(16 * Q + 15));
}
struct SMS { int ex[128], op0[128], nop[128], last; };
constexpr SMS sm_sched() {
  SMS z{};
  int et[32] = {}, e = 0;
  for (int t = 0; t < 128; ++t) {
    const int g = t & 15;
    const bool x = t < 48 ? (g >= 8 && (g & 1)) : (g >= 8 && g != 10 && g != 13 && g != 15);
    z.ex[t] = x && e < 32 ? e : -1;
    if (z.ex[t] >= 0) et[e++] = t;
  }
  int o = 0;
  for (int t = 0; t < 128; ++t) {
    const int g = t & 15;
    int cap = g < 8 ? (g == 0 ? 0 : 1) : (z.ex[t] >= 0 || t < 48 ? 0 : 2);
    z.op0[t] = o; z.nop[t] = 0;
    while (cap > 0 && o < 48 && et[2 * (o / 3) + (o % 3 == 0 ? 0 : 1)] + 2 <= t) {
      const int el = 2 * (o / 3) + o % 3;
      if (!(o % 3 < 2 && (el == 0 || el == 4))) --cap;
      ++o; ++z.nop[t]; z.last = t;
    }
  }
  return z;
}
constexpr SMS SM = sm_sched();
static_assert(SM.last <= 115);
constexpr int p_reg(int Y, int k) { return R_P + 16 * Y + 4 * ((k >> 3) * 2 + ((k >> 1) & 1)) + ((k >> 2) & 1) * 2 + (k & 1); }
template <int Y, int O> DI void sm_op() {
  constexpr int k = O / 3, e = 2 * k + O % 3, ts = R_TS + ((e >> 2) & 1);
  if constexpr (O % 3 == 2) asm volatile("v_cvt_pk_bf16_f32 v%c0, v%c1, v%c2" ::"i"(p_reg(Y, k)), "i"(R_E + ((2 * k) & 15)), "i"(R_E + ((2 * k + 1) & 15)));
  else if constexpr (e == 1 || e == 5) asm volatile("v_add_f32_e32 v%c0, v%c1, v%c2" ::"i"(ts), "i"(R_E + e - 1), "i"(R_E + e));
  else if constexpr (e != 0 && e != 4) asm volatile("v_add_f32_e32 v%c0, v%c0, v%c1" ::"i"(ts), "i"(R_E + (e & 15)));
}
template <int Y, int T> DI void sm_gap(u64& redo) {
  if constexpr (SM.ex[T] >= 0) asm volatile("v_exp_f32 v%c0, v%c1" ::"i"(R_E + (SM.ex[T] & 15)), "i"(R_S + 32 * Y + SM.ex[T]));
  if constexpr (SM.nop[T] > 0) sm_op<Y, SM.op0[T]>();
  if constexpr (SM.nop[T] > 1) sm_op<Y, SM.op0[T] + 1>();
  if constexpr (SM.nop[T] > 2) sm_op<Y, SM.op0[T] + 2>();
  if constexpr (T == SM.last + 1) asm volatile("v_add_f32_e32 v%c0, v%c1, v%c2" ::"i"(R_TMP), "i"(R_TS), "i"(R_TS + 1));
  if constexpr (T == SM.last + 2) asm volatile("v_cmp_nle_f32_e64 %0, v%c1, v%c2" : "=s"(redo) : "i"(R_TMP), "i"(R_THR));
}
#define EVG 35
template <int X, int KB, int QB, int S_, bool FIRST, bool HEAD = S_ == 0> DI void qk_mfma() {
  constexpr int r = 192 + 4 * ((KB & 1) * 8 + S_), q = 128 + 4 * (QB * 8 + S_);
  constexpr int d = R_S + 32 * X + 4 * (KB * 2 + QB), ci = HEAD ? R_CI + 4 * (KB * 2 + QB) : d;
  if constexpr (FIRST && HEAD)
    asm volatile("v_mfma_f32_16x16x32_bf16 v[%c0:%c1], a[%c2:%c3], a[%c4:%c5], 0" ::"i"(d), "i"(d + 3), "i"(r), "i"(r + 3), "i"(q), "i"(q + 3));
  else
    asm volatile("v_mfma_f32_16x16x32_bf16 v[%c0:%c1], a[%c2:%c3], a[%c4:%c5], v[%c6:%c7]" ::"i"(d), "i"(d + 3), "i"(r), "i"(r + 3), "i"(q), "i"(q + 3),
                 "i"(ci), "i"(ci + 3));
}
template <int X, int FIRST, bool NOQK, int G> DI void gapA(AC& c) {
  constexpr int Xp = X ^ 1, kb = G >> 4, g = G & 15;
  if constexpr (NOQK && G < 48 && G % 3 == 0) { f_load<128, G / 3>(); if constexpr (G % 12 == 9) f_next(); }
  if constexpr (G == 48) asm volatile("s_waitcnt vmcnt(%0) lgkmcnt(0)\n\ts_barrier" ::"i"(NOQK ? 16 : FIRST == 2 ? 63 : FIRST == 1 ? 9 : FIRST == 3 ? 8 : 0) : "memory");
  else if constexpr (g == 0 && !NOQK) asm volatile("s_waitcnt lgkmcnt(0)");
  if constexpr (!NOQK) {
    if constexpr (SNK & 1) qk_mfma<X, kb, (g >> 3) ^ (kb & 1), g < 8 ? g : 15 - g, FIRST != 0, !(g & 7)>();
    else qk_mfma<X, kb, (g >> 3), g & 7, FIRST != 0>();
    if constexpr (g < 8 && kb < 3) kread<X, (kb + 1) * 8 + g>();
    if constexpr (G == 54) asm volatile("s_add_u32 s82, s83, 0x1000000" SCC3);
    if constexpr (G == 55) dma_m0<X, 0>();
    if constexpr (G >= 56 && !(G & 1) && FIRST != 2) dma_at<(G - 56) / 2>();
    if constexpr (G == EVG) asm volatile("v_readlane_b32 s87, v93, s86");
    if constexpr (G == EVG + 2) asm volatile("s_add_u32 s86, s86, 1" SCC3);
    if constexpr (G == 57) asm volatile("s_mov_b32 s83, s84\n\ts_andn2_b32 s84, s87, 0x3ff" SCC3);
    if constexpr (G == 59 && FIRST) asm volatile("s_and_b32 %0, s87, 0x3ff" : "=s"(c.rare) SCC2);
    if constexpr (G == 59 && !FIRST) asm volatile("s_and_b32 s63, s87, 0x3ff\n\ts_cmp_lg_u64 %0, 0" ::"s"(c.redo) SCC1);
    if constexpr (G == 61 && !FIRST) asm volatile("s_addc_u32 %0, s63, 0" : "=s"(c.rare) SCC2);
  } else if constexpr (SM.nop[64 + G] > 0) asm volatile("s_nop 0");
  if constexpr (!FIRST && g < 8 && kb == 3) vread<Xp, g>();
  if constexpr (FIRST == 2) d_ops<160, 13 * (G / 8) + (G % 8 * 13) / 8>(make_iseq<((G % 8 + 1) * 13) / 8 - (G % 8 * 13) / 8>{});
  if constexpr (FIRST == 2 && G == 34)
    asm volatile("v_and_b32 v124, 0xf0, v89\n\ts_waitcnt vmcnt(8)\n\t" A_CHECK("v[122:123]", "s[96:97]", "s72")
                 "buffer_load_dwordx2 v[122:123], v124, " SW ", s73 offen offset:1024 sc1" ::: "vcc", "scc", "memory");
  if constexpr (FIRST == 1 && G >= 8 && G < 40 && G % 4 == 1)
    asm volatile("s_mov_b32 m0, s75\n\ts_add_u32 s75, s75, 0x1000\n\tbuffer_load_dwordx4 v84, s[64:67], s94 offen lds\n\ts_add_u32 s94, s94, 0x3800" ::: SCC_ "memory");
  if constexpr (FIRST == 1 && G == 42) asm volatile("s_mov_b32 m0, 0x1f800\n\ts_mov_b64 s[64:65], s[88:89]\n\ts_mov_b32 s66, 0x1c0000\n\tbuffer_load_dword v121, s[64:67], 0 offen lds" ::: "memory");
  if constexpr (FIRST == 3) {
    constexpr int n = (G >> 4) * 4 + (G & 3), f = G < 32 ? (G - 17) / 4 : 4 + (G - 33) / 3;
    if constexpr (G == 2) { asm volatile("v_xor_b32 v84, 32, v85"); sld<94, B_WD, 1>(); }
    if constexpr (G < 32 && (G & 12) == 8)
      asm volatile("ds_read_b64_tr_b16 v[%c0:%c1], v%c4 offset:%c5\n\tds_read_b64_tr_b16 v[%c2:%c3], v%c4 offset:%c6" ::"i"(224 + 4 * n), "i"(225 + 4 * n), "i"(226 + 4 * n),
                   "i"(227 + 4 * n), "i"(n & 1 ? 84 : 85), "i"((n >> 1) * 8192), "i"((n >> 1) * 8192 + 4096));
    if constexpr (G == 32) asm volatile("s_add_u32 s94, s94, 0x1000" SCC3);
    if constexpr (G >= 17 && G < 45 && (G < 32 ? G % 4 == 1 : G % 3 == 0 && G > 32))
      asm volatile("buffer_store_dwordx4 v[%c0:%c1], v89, " SW ", s94 offen offset:%c2 sc1" ::"i"(224 + 4 * f), "i"(227 + 4 * f), "i"((f & 3) * 1024) : "memory");
  }
  if constexpr (FIRST == 3 && G == 42) sld<63, B_FSET, 1>();
  if constexpr (FIRST == 3 && G == 48) asm volatile(A_FLAG ::: "memory");
  if constexpr (FIRST && G == 44) asm volatile("v_mov_b64 v[208:209], 0\n\tv_mov_b64 v[210:211], 0");
  if constexpr (!FIRST) sm_gap<Xp, 64 + G>(c.redo);
  GAP();
}
template <int R> DI void rescale1(float alpha) {
  float t;
  asm volatile("v_accvgpr_read_b32 %0, a[%c2]\n\tv_mul_f32 %0, %0, %1\n\tv_accvgpr_write_b32 a[%c2], %0" : "=&v"(t) : "v"(alpha), "i"(R));
}
template <int Q0, int R> DI void rescale_q(const float (&alpha)[2]) { if constexpr (((R >> 2) & 1) >= Q0) rescale1<R>(alpha[(R >> 2) & 1]); }
template <int Q0, int... Rs> DI void rescale_all(const float (&alpha)[2], iseq<Rs...>) { (rescale_q<Q0, Rs>(alpha), ...); }
template <bool MAX> DI float row_reduce(float v) {
  auto p = __builtin_amdgcn_permlane32_swap(__float_as_uint(v), __float_as_uint(v), false, false);
  v = MAX ? fmaxf(__uint_as_float(p[0]), __uint_as_float(p[1])) : __uint_as_float(p[0]) + __uint_as_float(p[1]);
  p = __builtin_amdgcn_permlane16_swap(__float_as_uint(v), __float_as_uint(v), false, false);
  return MAX ? fmaxf(__uint_as_float(p[0]), __uint_as_float(p[1])) : __uint_as_float(p[0]) + __uint_as_float(p[1]);
}
template <int Y, int QB> DI float row_max() {
  constexpr int r = R_S + 32 * Y + 4 * QB;
  float mx;
  asm volatile("v_max_f32 %0, v%c1, v%c2\n\tv_max3_f32 %0, %0, v%c3, v%c4" : "=&v"(mx) : "i"(r), "i"(r + 1), "i"(r + 2), "i"(r + 3));
  static_for([&](auto K) {
    constexpr int q = r + 8 * (K + 1);
    float t = mx;
    asm volatile("v_max3_f32 %0, %0, v%c1, v%c2\n\tv_max3_f32 %0, %0, v%c3, v%c4" : "+v"(t) : "i"(q), "i"(q + 1), "i"(q + 2), "i"(q + 3));
    mx = t;
  }, make_iseq<3>{});
  return row_reduce<true>(mx);
}
template <int R> DI void v_sub(float d) { asm volatile("v_sub_f32 v%c0, v%c0, %1" ::"i"(R), "v"(d)); }
template <int B> DI void s_shift(const float (&d)[2]) { static_for([&](auto E) { v_sub<B + E>(d[(E >> 2) & 1]); }, make_iseq<32>{}); }
DI void m_shift(const float (&d)[2]) {
  s_shift<R_CI>(d);
  v_sub<R_M>(-d[0]); v_sub<R_M + 1>(-d[1]);
}
#define A_MASK(R0, R1, R2, R3, K0, K1, K2, K3) \
    "v_cmp_gt_i32_e64 s[64:65], " K0 ", v95\n\tv_cmp_gt_i32_e64 s[66:67], " K1 ", v95\n\tv_cmp_gt_i32_e64 s[68:69], " K2 ", v95\n\tv_cmp_gt_i32_e64 s[70:71], " K3 ", v95\n\t" \
    "v_cndmask_b32_e64 " R0 ", -v116, v127, s[64:65]\n\tv_cndmask_b32_e64 " R1 ", -v116, v127, s[66:67]\n\t" \
    "v_cndmask_b32_e64 " R2 ", -v116, v127, s[68:69]\n\tv_cndmask_b32_e64 " R3 ", -v116, v127, s[70:71]\n\t"
template <int K, int T> DI void mask_cmp() {
  asm volatile("v_cmp_gt_i32_e64 s[64:65], %c0, v%c4\n\tv_cmp_gt_i32_e64 s[66:67], %c1, v%c4\n\tv_cmp_gt_i32_e64 s[68:69], %c2, v%c4\n\tv_cmp_gt_i32_e64 s[70:71], %c3, v%c4"
               ::"i"(K), "i"(K + 1), "i"(K + 2), "i"(K + 3), "i"(T));
}
template <int R, int S> DI void mask_sel() {
  if constexpr (S < 0)
    asm volatile("v_cndmask_b32_e64 v%c0, v%c0, v127, s[64:65]\n\tv_cndmask_b32_e64 v%c1, v%c1, v127, s[66:67]\n\t"
                 "v_cndmask_b32_e64 v%c2, v%c2, v127, s[68:69]\n\tv_cndmask_b32_e64 v%c3, v%c3, v127, s[70:71]" ::"i"(R), "i"(R + 1), "i"(R + 2), "i"(R + 3));
  else
    asm volatile("v_cndmask_b32_e64 v%c0, -v%c4, v127, s[64:65]\n\tv_cndmask_b32_e64 v%c1, -v%c4, v127, s[66:67]\n\t"
                 "v_cndmask_b32_e64 v%c2, -v%c4, v127, s[68:69]\n\tv_cndmask_b32_e64 v%c3, -v%c4, v127, s[70:71]" ::"i"(R), "i"(R + 1), "i"(R + 2), "i"(R + 3), "i"(S));
}
template <int X, int Q0 = 0> DI void sm_redo() {
  constexpr int Xp = X ^ 1;
  float d[2] = {0.f, 0.f}, dt[2] = {0.f, 0.f}, alpha[2] = {1.f, 1.f}, ts[2] = {0.f, 0.f};
  asm volatile("s_nop 7");
  static_for([&](auto Q) {
    constexpr int qb = Q + Q0;
    const float pd = vget<R_PD + qb>();
    d[qb] = fmaxf(row_max<Xp, qb>() - pd, 0.f);
    dt[qb] = d[qb] + pd;
    vset<R_PD + qb>(d[qb]);
    alpha[qb] = __builtin_amdgcn_exp2f(-d[qb]);
    vset<R_RS + qb>(vget<R_RS + qb>() * alpha[qb]);
  }, make_iseq<2 - Q0>{});
  vset<R_THR>(d[0] > 0.f || d[1] > 0.f ? -1.f : 4096.f);
  m_shift(d);
  static_for([&](auto K) {
    constexpr int k = K, qb = (k >> 1) & 1;
    if constexpr (qb >= Q0) {
      const float e0 = __builtin_amdgcn_exp2f(vget<R_S + 32 * Xp + 2 * k>() - dt[qb]), e1 = __builtin_amdgcn_exp2f(vget<R_S + 32 * Xp + 2 * k + 1>() - dt[qb]);
      ts[qb] += e0; ts[qb] += e1;
      vset<p_reg(Xp, k)>(__uint_as_float(pk2(e0, e1)));
    }
  }, make_iseq<16>{});
  vset<R_TS>(ts[0]); vset<R_TS + 1>(ts[1]);
  rescale_all<Q0>(alpha, make_iseq<128>{});
  asm volatile("s_nop 1" ::: "memory");
  GAP();
}
template <int X, int FIRST, bool NOQK, int... Gs> DI void phaseA_(AC& c, iseq<Gs...>) {
  (gapA<X, FIRST, NOQK, Gs>(c), ...);
}
template <int X, int FIRST, bool NOQK> DI void phaseA(AC& c) { phaseA_<X, FIRST, NOQK>(c, make_iseq<64>{}); }

template <int Y, int F, int QB> DI void pv_mfma() {
  constexpr int r = 192 + 4 * (F & 15), o = 4 * ((F >> 1) * 2 + QB), p = R_P + 16 * Y + 4 * ((F & 1) * 2 + QB);
  asm volatile("v_mfma_f32_16x16x32_bf16 a[%c0:%c1], a[%c2:%c3], v[%c4:%c5], a[%c0:%c1]" ::"i"(o), "i"(o + 3), "i"(r), "i"(r + 3), "i"(p), "i"(p + 3));
}
template <int X, bool SMX, int G> DI void gapB(AC& c) {
  constexpr int Xp = X ^ 1, blk = G >> 4, g = G & 15;
  if constexpr (g == 0) asm volatile("s_waitcnt lgkmcnt(0)");
  if constexpr (SMX && g == 7 && blk < 3) dma_m0<X, blk * 4 + 4>();
  if constexpr (SNK & 2) pv_mfma<Xp, (G >> 2) * 2 + ((g ^ (g >> 1)) & 1), ((g >> 1) ^ (g >> 2)) & 1>();
  else pv_mfma<Xp, (G >> 2) * 2 + (g & 1), (g >> 1) & 1>();
  if constexpr (SMX && (G == 1 || G == 2)) asm volatile("v_add_f32_e32 v%c0, v%c0, v%c1" ::"i"(R_RS + G - 1), "i"(R_TS + G - 1));
  if constexpr (g < 8) {
    if constexpr (blk < 3) vread<Xp, (blk + 1) * 8 + g>();
    else if constexpr (SMX) kread<Xp, g>();
  }
  if constexpr (SMX) {
    if constexpr (g >= 8 && !(g & 1) && blk < 3) dma_at<blk * 4 + 4 + (g - 8) / 2>();
    sm_gap<X, G>(c.redo);
    GAP();
  }
}
template <int X, int... Gs> DI void phaseB_(AC& c, iseq<Gs...>) { (gapB<X, true, Gs>(c), ...); }
template <int X> DI void phaseB(AC& c) { phaseB_<X>(c, make_iseq<64>{}); }
constexpr int sm_nex() { int n = 0; for (int t = 0; t < 64; ++t) n += SM.ex[t] >= 0; return n; }
template <bool ST> DI void phaseB0(bool diag0, unsigned vml) {
  float d[2];
  dma_m0<0, 4>();
  if constexpr (ST) {
    asm volatile("s_waitcnt vmcnt(4)" ::: "memory");
    dma_at<4>(); d_store<8>();
    d[0] = vget<122>(); d[1] = vget<123>();
    dma_at<5>();
  } else {
    kreads<1>(make_iseq<8>{});
    dma_at<4>();
    asm volatile("s_nop 3");
    if (__builtin_expect(diag0, 0))
      static_for([&](auto I) { constexpr int i = I; mask_cmp<16 * i, 95>(); mask_sel<R_S + 8 * i, -1>(); }, make_iseq<4>{});
    d[0] = row_max<0, 0>(); GAP(); dma_at<5>();
    d[1] = row_max<0, 1>(); GAP(); dma_at<6>();
#pragma unroll
    for (int qb = 0; qb < 2; ++qb) if (!(d[qb] > -3.0e38f)) d[qb] = 0.f;
    asm volatile("buffer_store_dwordx2 %0, %1, " SW ", s73 offen offset:1024 sc1" ::"v"((f32x2c){d[0], d[1]}), "v"(vml) : "memory");
  }
  vset<R_M>(d[0]); vset<R_M + 1>(d[1]);
  const f32x2c nd[2] = {{-d[0], -d[0]}, {-d[1], -d[1]}};
  static_for([&](auto E) {
    constexpr int e = E;
    const f32x2c v = nd[(e >> 1) & 1];
    asm volatile("v_pk_add_f32 v[%c0:%c1], v[%c0:%c1], %4\n\tv_mov_b64 v[%c2:%c3], %4" ::"i"(R_S + 2 * e), "i"(R_S + 2 * e + 1), "i"(R_CI + 2 * e), "i"(R_CI + 2 * e + 1), "v"(v));
    if constexpr (ST && e == 0) d_store<9>();
    if constexpr (ST && e == 2) dma_at<6>();
    if constexpr (e == (ST ? 5 : 3)) dma_at<7>();
    if constexpr (e == 6) dma_m0<0, 8>();
    if constexpr (e == 7 || e == 11 || e == 15) dma_at<8 + (e - 7) / 4>();
    if constexpr (ST && (e == 4 || e == 9)) d_store<10 + e / 8>();
  }, make_iseq<16>{});
  static_assert(sm_nex() >= 16 && SM.op0[64] >= 3 * ((sm_nex() - 15) / 2) && SM.op0[64] <= 24);
  static_for([&](auto E) {
    constexpr int e = E;
    asm volatile("v_exp_f32 v%c0, v%c1" ::"i"(R_E + e), "i"(R_S + e));
    if constexpr (e % 3 == 0 && e < 15) acc_zero<e / 3>();
    if constexpr (e == 3) dma_at<11>();
    if constexpr (e == 6) dma_m0<0, 12>();
    if constexpr (e == 7 || e == 11 || e == 15) dma_at<12 + (e - 7) / 4>();
    if constexpr (ST && (e == 1 || e == 9)) d_store<12 + e / 8>();
  }, make_iseq<16>{});
  if constexpr (ST) {
    d_store<14>();
    asm volatile("s_barrier" ::: "memory");
    kreads<1>(make_iseq<8>{});
  }
  static_for([&](auto O) { sm_op<0, O>(); if constexpr (O % 7 == 1 && O < (ST ? 14 : 21)) acc_zero<5 + O / 7>(); }, make_iseq<SM.op0[64]>{});
  dma_at<15>();
  if constexpr (ST) { d_store<15>(); acc_zero<7>(); }
  static_for([&](auto E) { asm volatile("v_exp_f32 v%c0, v%c1" ::"i"(R_E + (int)E), "i"(R_S + 16 + (int)E)); }, make_iseq<sm_nex() - 16>{});
  GAP();
}
struct EP { float fa[2], fb[2]; };
template <int I, int E, int MERGE, int TB = 96> DI void epi_pair(const EP& p) {
  constexpr int qb = I & 1, r = 16 * (I >> 1) + 4 * qb + (E >> 1) * 8 + (E & 1) * 2, t = TB + 2 * E, w = 128 + 4 * I + E;
  if constexpr (MERGE == 2) asm volatile("v_accvgpr_read_b32 v%c0, a[%c2]\n\tv_accvgpr_read_b32 v%c1, a[%c3]" ::"i"(t), "i"(t + 1), "i"(r), "i"(r + 1));
  else
    asm volatile("v_accvgpr_read_b32 v%c0, a[%c2]\n\tv_accvgpr_read_b32 v%c1, a[%c3]\n\tv_mul_f32 v%c0, v%c0, %4\n\tv_mul_f32 v%c1, v%c1, %4"
                 ::"i"(t), "i"(t + 1), "i"(r), "i"(r + 1), "v"(p.fa[qb]));
  if constexpr (MERGE == 1)
    asm volatile("v_lshlrev_b32 v104, 16, v%c2\n\tv_and_b32 v105, 0xffff0000, v%c2\n\tv_fmac_f32 v%c0, v104, %3\n\tv_fmac_f32 v%c1, v105, %3"
                 ::"i"(t), "i"(t + 1), "i"(w), "v"(p.fb[qb]));
  asm volatile("v_cvt_pk_bf16_f32 v%c0, v%c1, v%c2" ::"i"(w), "i"(t), "i"(t + 1));
}
template <int I> DI void ctx_store() {
  asm volatile("buffer_store_dwordx4 v[%c0:%c1], v85, " SW ", s76 offen offset:%c2 sc1\n\tds_write_b128 v85, v[%c0:%c1] offset:%c3"
               ::"i"(128 + 4 * I), "i"(131 + 4 * I), "i"((I & 2) * 1024), "i"((I & 1) * 32768 + (I >> 1) * 2048) : "memory");
  if constexpr (I & 2) asm volatile("s_add_u32 s76, s76, %0" ::"i"(I == 14 ? 0x5000 : 0x1000) SCC1);
}
constexpr int mg_reg(int L, int I) { return I >= 8 ? 8 + 4 * I : I >= 4 ? R_CI + 8 * (I - 4) : R_S + 32 * L + 8 * I; }
template <int L, bool MG, int G> DI void gapAD(AC& c) {
  constexpr int kb = G >> 3, s = G & 7, db = ((G >> 1) + 8) & 15, o = 4 * (db * 2 + (G & 1)), a = 72 + 2 * (db & 1), b = mg_reg(L, (db >> 1) * 2 + (G & 1));
  if constexpr (G == 0) asm volatile("s_waitcnt lgkmcnt(0)");
  else if constexpr (G >= 8 && G < 28 && !(G & 1)) asm volatile("s_waitcnt lgkmcnt(%0)" ::"i"(G <= 24 ? 6 : 4));
  else if constexpr (G == 28) asm volatile("s_waitcnt vmcnt(16) lgkmcnt(0)\n\ts_barrier" ::: "memory");
  qk_mfma<L, kb, 1, s, false>();
  if constexpr (MG && G < 8) asm volatile("ds_read_b128 v[%c0:%c1], v85 offset:%c2" ::"i"(mg_reg(L, G)), "i"(mg_reg(L, G) + 3), "i"(G * 1024));
  if constexpr (G < 24) kread<L, 8 + G>();
  if constexpr (G < 24 && s == 1) mask_cmp<16 * (kb + 1), 126>();
  if constexpr (G < 24 && s == 3) mask_sel<R_CI + 8 * (kb + 1) + 4, R_M + 1>();
  if constexpr (G >= 4 && G < 28 && G % 3 == 2) w_load<(G - 4) / 3>();
  if constexpr (G == 25) asm volatile("s_add_u32 s82, s83, 0x1000000" SCC3);
  if constexpr (G == 27) dma_m0<L, 0>();
  if constexpr (G >= 28) { vread<L ^ 1, 2 * (G - 28)>(); vread<L ^ 1, 2 * (G - 28) + 1>(); dma_at<G - 28>(); }
  sm_gap<L ^ 1, 64 + 2 * G>(c.redo);
  if constexpr (MG) asm volatile("v_mfma_f32_16x16x32_bf16 a[%c0:%c1], v[%c2:%c3], v[%c4:%c5], a[%c0:%c1]" ::"i"(o), "i"(o + 3), "i"(a), "i"(a + 3), "i"(b), "i"(b + 3));
  sm_gap<L ^ 1, 65 + 2 * G>(c.redo);
  GAP();
}
template <int L, bool MG, int... Gs> DI void phaseAD(AC& c, iseq<Gs...>) {
  sld<72, B_NXT, 1>();
  mask_cmp<0, 126>(); mask_sel<R_CI + 4, R_M + 1>();
  (gapAD<L, MG, Gs>(c), ...);
}
#define BDX 8
constexpr int bd_w(bool EPI, int G) { return EPI && G >= 19 && G % 4 == 3 && (G - 19) / 4 < BDX ? 16 + (G - 19) / 4 : -1; }
template <int L, bool EPI, int G> DI void gapBD(AC& c, const EP& p) {
  constexpr int g = G & 15, e = (G >> 4) * 8 + (g - 8), o = e - 3;
  if constexpr (G == 1) dma_m0<L, 4>();
  if constexpr (G == 8) asm volatile("s_waitcnt vmcnt(14)" ::: "memory");
  if constexpr (G == 16) asm volatile("s_waitcnt lgkmcnt(0)\n\ts_barrier" ::: "memory");
  gapB<L, false, G>(c);
  if constexpr (G >= 8 && G < 12)
    asm volatile("ds_read_b128 a[%c0:%c1], v90 offset:%c4\n\tds_read_b128 a[%c2:%c3], v90 offset:%c5" ::"i"(96 + 8 * G), "i"(99 + 8 * G), "i"(100 + 8 * G), "i"(103 + 8 * G),
                 "i"((L ^ 1) * 32768 + (G - 8) * 2048), "i"((L ^ 1) * 32768 + (G - 8) * 2048 + 1024));
  if constexpr (G == 1 || G == 2) asm volatile("v_add_f32_e32 v%c0, v%c0, v%c1" ::"i"(R_RS + G - 1), "i"(R_TS + G - 1));
  if constexpr (G >= 3 && G < 19 && G % 4 == 3) dma_at<4 + (G - 3) / 4>();
  if constexpr (!EPI && G == 17)
    asm volatile("s_cmp_eq_u32 s97, 0\n\ts_cbranch_scc1 1f\n\ts_add_u32 s64, s100, 4 * 800\n\ts_addc_u32 s65, s101, 0\n\ts_swappc_b64 s[98:99], s[64:65]\n1:" ::: SCC_ "memory");
  if constexpr (bd_w(EPI, G) >= 0) w_load<bd_w(EPI, G)>();
  if constexpr (g >= 8 && e < 16) asm volatile("v_exp_f32 v%c0, v%c1" ::"i"(R_E + e), "i"(R_S + 32 * L + 4 + 8 * (e >> 2) + (e & 3)));
  if constexpr (g >= 8 && o >= 1 && o < 16) {
    if constexpr (o == 1) asm volatile("v_add_f32_e32 v%c0, v%c1, v%c2" ::"i"(R_TS + 1), "i"(R_E), "i"(R_E + 1));
    else asm volatile("v_add_f32_e32 v%c0, v%c0, v%c1" ::"i"(R_TS + 1), "i"(R_E + o));
    if constexpr (o & 1) asm volatile("v_cvt_pk_bf16_f32 v%c0, v%c1, v%c2" ::"i"(p_reg(L, (o >> 2) * 4 + 2 + ((o >> 1) & 1))), "i"(R_E + o - 1), "i"(R_E + o));
  }
  if constexpr (G == 45) asm volatile("v_cmp_nle_f32_e64 %0, v%c1, v%c2" : "=s"(c.redo) : "i"(R_TS + 1), "i"(R_THR));
  if constexpr (EPI && G >= 10 && G < 58 && !(G & 1)) epi_pair<((G - 10) >> 3) * 2, ((G - 10) >> 1) & 3, 0, 224>(p);
  if constexpr (EPI && G >= 17 && G < 58 && (G & 7) == 1) ctx_store<((G - 17) >> 3) * 2>();
  GAP();
}
template <int L, bool EPI, int... Gs> DI void phaseBD(AC& c, const EP& p, iseq<Gs...>) {
  if constexpr (!EPI) { sld<96, B_LM, 1>(); sld<97, B_LM + 1, 1>(); asm volatile("s_nop 0"); fl_check(); }
  (gapBD<L, EPI, Gs>(c, p), ...);
}

#define ACLOB_ALL "a0","a1","a2","a3","a4","a5","a6","a7","a8","a9","a10","a11","a12","a13","a14","a15","a16","a17","a18","a19","a20","a21","a22","a23","a24","a25","a26","a27","a28","a29","a30","a31","a32","a33","a34","a35","a36","a37","a38","a39","a40","a41","a42","a43","a44","a45","a46","a47","a48","a49","a50","a51","a52","a53","a54","a55","a56","a57","a58","a59","a60","a61","a62","a63","a64","a65","a66","a67","a68","a69","a70","a71","a72","a73","a74","a75","a76","a77","a78","a79","a80","a81","a82","a83","a84","a85","a86","a87","a88","a89","a90","a91","a92","a93","a94","a95","a96","a97","a98","a99","a100","a101","a102","a103","a104","a105","a106","a107","a108","a109","a110","a111","a112","a113","a114","a115","a116","a117","a118","a119","a120","a121","a122","a123","a124","a125","a126","a127","a128","a129","a130","a131","a132","a133","a134","a135","a136","a137","a138","a139","a140","a141","a142","a143","a144","a145","a146","a147","a148","a149","a150","a151","a152","a153","a154","a155","a156","a157","a158","a159","a160","a161","a162","a163","a164","a165","a166","a167","a168","a169","a170","a171","a172","a173","a174","a175","a176","a177","a178","a179","a180","a181","a182","a183","a184","a185","a186","a187","a188","a189","a190","a191","a192","a193","a194","a195","a196","a197","a198","a199","a200","a201","a202","a203","a204","a205","a206","a207","a208","a209","a210","a211","a212","a213","a214","a215","a216","a217","a218","a219","a220","a221","a222","a223","a224","a225","a226","a227","a228","a229","a230","a231","a232","a233","a234","a235","a236","a237","a238","a239","a240","a241","a242","a243","a244","a245","a246","a247","a248","a249","a250","a251","a252","a253","a254","a255"

template <int I> DI void epi_store() {
  asm volatile("buffer_store_dwordx4 v[%c0:%c1], v90, " SW ", s%c2 offen offset:%c3 sc1" ::"i"(128 + 4 * I), "i"(131 + 4 * I), "i"(76 + (I >> 2)), "i"((I & 3) * 1024) : "memory");
}
template <int HI> DI void epi_init() {
  asm volatile("v_readlane_b32 s76, v93, %c0\n\ts_nop 0\n\ts_add_u32 s77, s76, 0x1000\n\ts_add_u32 s78, s76, %1\n\ts_add_u32 s79, s78, 0x1000" ::"i"(B_SO), "i"(HI) SCC1);
}
template <int I> DI void epi_pread() {
  asm volatile("ds_read_b128 v[%c0:%c1], v107 offset:%c2" ::"i"(128 + 4 * I), "i"(131 + 4 * I), "i"(I * 1024));
}
template <int... Is> DI void epi_preads(iseq<Is...>) { (epi_pread<Is>(), ...); }
template <int G> DI void gapF(AC& c, const EP& p) {
  if constexpr (G < 64) gapB<ODD, false, G>(c);
  if constexpr (G == 1) asm volatile("v_mov_b32 v93, v94\n\ts_brev_b32 s73, 1");
  if constexpr (G >= 3 && G < 10) att_item<G - 3>();
  if constexpr (G >= 10) epi_pair<(G - 10) / 4, (G - 10) & 3, 2>(p);
  if constexpr (G >= 14 && ((G - 14) & 3) == 0) epi_store<(G - 14) / 4>();
  if constexpr (G == 73) epi_store<15>();
  GAP();
}
template <int... Gs> DI void phaseF(AC& c, const EP& p, iseq<Gs...>) { (gapF<Gs>(c, p), ...); }
constexpr int fd_w(bool EPI, int G) {
  const int f0 = EPI ? 16 + BDX : 16, n = NWF - f0;
  for (int i = 0; i < n; ++i) if (G == (EPI ? 2 + (i * 37) / n : i < 16 ? 2 + 2 * i : 4 + 2 * i)) return f0 + i;
  return -1;
}
#define FDA 50
#define FDE 20
constexpr int fd_cnt(bool EPI, int G0, int G1) { int n = 0; for (int G = G0; G < G1; ++G) n += (fd_w(EPI, G) >= 0) + ((G & 3) == 3); return n; }
constexpr bool fd_ok() {
  for (int G = 0; G < 64; ++G) {
    const int a = fd_w(0, G), b = fd_w(1, G);
    if ((a >= 16 && G < 2 * (a & 15) + (a >= 32 ? 34 : 2)) || (b >= 28 && G < (b >= 32 ? 2 * (b - 32) + 10 : 2 * (b - 28) + 2))) return false;
  }
  return true;
}
static_assert(fd_ok() && NWF == 40 && BDX <= 12);
template <int L, bool EPI, int G> DI void gapFD(const EP& p) {
  constexpr int I = EPI ? (G < 8 ? 12 + (G >> 2) * 2 : ((G - 8) >> 2) * 2 + 1) : G < 32 ? (G >> 2) * 2 : ((G - 32) >> 2) * 2 + 1;
  constexpr int u = EPI ? G : G - 2, uf = SNK & 2 ? u ^ ((u >> 1) & 1) : u;
  if constexpr (u >= 16 && u < 32 && (u & 3) == 0) asm volatile("s_waitcnt lgkmcnt(%0)" ::"i"(28 - u));
  if constexpr (u >= 0 && u < 32) pv_mfma<L, uf, 1>();
  if constexpr (u >= 0 && u < 16) vread<L, uf + 16>();
  constexpr int F = fd_w(EPI, G);
  if constexpr (F >= 0) w_load<F>();
  epi_pair<I, G & 3, !EPI>(p);
  if constexpr ((G & 3) == 3) ctx_store<I>();
  if constexpr (G == (EPI ? FDE : FDA)) {
    asm volatile("s_waitcnt vmcnt(%0)\n\ts_barrier" ::"i"(fd_cnt(EPI, EPI ? 8 : 32, G + 1)) : "memory");
    set_flag<A_FC>();
  }
  GAP();
}
template <int L, bool EPI, int... Gs> DI void phaseFD(const EP& p, iseq<Gs...>) { (gapFD<L, EPI, Gs>(p), ...); }
#define A_CT2 "v_readlane_b32 s63, v93, 3\n\ts_sub_u32 s64, s100, at_event - at_tail\n\ts_subb_u32 s65, s101, 0\n\ts_mov_b32 s66, at_event - at_tail\n\ts_mov_b32 s67, s59\n\t" \
    "v_lshl_add_u32 v78, v89, 9, s63\n\ts_mov_b64 exec, 0x1f\n\tbuffer_load_dword v84, v78, s[64:67], 0 offen\n\ts_mov_b64 exec, -1\n\t"
#define A_CT4 "s_mov_b64 s[64:65], s[100:101]\n\ts_movk_i32 s66, 0xf00\n\ts_mov_b32 s67, s59\n\tv_lshlrev_b32 v78, 3, v89\n\tbuffer_load_dword v84, v78, s[64:67], 0 offen\n\t"
#define A_RET "s_setpc_b64 s[98:99]\n\t"
#define A_EFLAG(L) "v_readlane_b32 s63, v93, " #L "\n\ts_nop 3\n\t" A_FLAG
#define A_EPOLL "v_readlane_b32 s96, v93, s85\n\ts_add_u32 s85, s85, 1\n\tv_readlane_b32 s97, v93, s85\n\ts_add_u32 s85, s85, 1\n\t" A_POLL("v[122:123]", "s[96:97]", "s62")
#define A_Q1 "buffer_load_dwordx4 v87, s[64:67], s95 offen lds\n\ts_add_u32 m0, m0, 0x400\n\ts_add_u32 s95, s95, 0x1c00\n\t"
#define A_EPOLL1 "s_mov_b32 exec_lo, 0xffff0000\n\ts_mov_b32 exec_hi, 0xffff\n\tbuffer_load_dwordx2 v[124:125], v88, " SW ", s92 offen sc1\n\ts_mov_b64 exec, -1\n\t" A_CT2
#define A_Z1(K, O) "v_xor_b32 v49, " K ", v48\n\tbuffer_load_dwordx4 v49, " SW ", s63 offen offset:" O " sc1 lds\n\t"
#define A_EQ "v_readlane_b32 s68, v92, 30\n\tv_readlane_b32 s95, v92, 28\n\ts_mov_b64 s[64:65], s[88:89]\n\ts_mov_b32 s66, 0x1c0000\n\ts_mov_b32 s67, s59\n\t" \
    "s_add_u32 m0, s68, 0x1000\n\ts_nop 1\n\t" A_Q1 A_Q1 A_Q1 A_Q1
#define A_ELOAD "v_readlane_b32 s68, v92, 30\n\tv_readlane_b32 s63, v92, 29\n\tv_lshrrev_b32 v48, 5, v89\n\ts_mov_b32 s96, 0\n\ts_mov_b32 s97, 0x3fff0000\n\t" \
    "v_and_b32 v48, 16, v48\n\t" A_CHECK("v[124:125]", "s[96:97]", "s92") "v_xor_b32 v48, v48, v89\n\ts_mov_b32 m0, s68\n\t" \
    A_Z1("0", "0") A_Z1("32", "1024") A_Z1("64", "2048") "s_mov_b32 exec_hi, 0\n\t" A_Z1("0x60", "3072") "s_mov_b32 exec_hi, -1\n\t"
#define A_PZ2(Z0, Z1, O) "ds_read_u16_d16_hi " Z0 ", v46 offset:" O "\n\tds_read_u16_d16_hi " Z1 ", v47 offset:" O "\n\t"
#define A_PRD(X, Q, O, Z0, Z1, Z2, Z3, Z4, Z5, Z6, Z7) "v_xor_b32 v46, " X ", v44\n\tds_read_b128 " Q ", v45 offset:" O "\n\tv_xor_b32 v47, 64, v46\n\t" \
    A_PZ2(Z0, Z1, "0") A_PZ2(Z2, Z3, "128") A_PZ2(Z4, Z5, "256") A_PZ2(Z6, Z7, "384")
#define A_PM2(Q, Z0, Z1) "v_lshlrev_b32 v40, 16, " Q "\n\tv_and_b32 v41, 0xffff0000, " Q "\n\tv_mul_f32 v40, v40, " Z0 "\n\tv_mul_f32 v41, v41, " Z1 "\n\tv_cvt_pk_bf16_f32 " Q ", v40, v41\n\t"
#define A_PST(Q) "buffer_store_dwordx4 " Q ", v43, s[64:67], s63 offen\n\ts_add_u32 s63, s63, 0x3800\n\t"
#define A_PA "v64", "v65", "v66", "v67", "v68", "v69", "v70", "v71"
#define A_PB "v72", "v73", "v74", "v75", "v76", "v77", "v78", "v79"
#define A_PRD_(...) A_PRD(__VA_ARGS__)
#define A_EPOST "v_readlane_b32 s68, v92, 30\n\tv_lshrrev_b32 v40, 4, v89\n\tv_readlane_b32 s64, v92, 0\n\tv_mul_u32_u24 v41, 37, v40\n\tv_readlane_b32 s65, v92, 1\n\t" \
    "v_lshrrev_b32 v41, 8, v41\n\tv_readlane_b32 s63, v94, 23\n\tv_mad_i32_i24 v42, v41, -7, v40\n\ts_movk_i32 s66, 0x380\n\ts_movk_i32 s67, 0x210\n\t" \
    "v_mad_u32_u24 v43, v41, s66, v87\n\tv_lshl_add_u32 v44, v41, 1, s68\n\tv_add_u32 v45, s68, v89\n\tv_mad_u32_u24 v44, v42, s67, v44\n\t" \
    "v_mov_b64 v[64:65], 0\n\tv_mov_b64 v[66:67], 0\n\tv_mov_b64 v[68:69], 0\n\tv_mov_b64 v[70:71], 0\n\t" \
    "v_mov_b64 v[72:73], 0\n\tv_mov_b64 v[74:75], 0\n\tv_mov_b64 v[76:77], 0\n\tv_mov_b64 v[78:79], 0\n\ts_mov_b32 s66, 0x380000\n\ts_mov_b32 s67, s59\n\t" \
    A_PRD_("0", "v[48:51]", "4096", A_PA) A_PRD_("16", "v[52:55]", "5120", A_PB) "s_waitcnt lgkmcnt(9)\n\t" \
    A_PM2("v48", "v64", "v65") A_PM2("v49", "v66", "v67") A_PM2("v50", "v68", "v69") A_PM2("v51", "v70", "v71") A_PST("v[48:51]") \
    A_PRD_("32", "v[56:59]", "6144", A_PA) "s_waitcnt lgkmcnt(9)\n\t" \
    A_PM2("v52", "v72", "v73") A_PM2("v53", "v74", "v75") A_PM2("v54", "v76", "v77") A_PM2("v55", "v78", "v79") A_PST("v[52:55]") \
    A_PRD_("48", "v[60:63]", "7168", A_PB) "s_waitcnt lgkmcnt(9)\n\t" \
    A_PM2("v56", "v64", "v65") A_PM2("v57", "v66", "v67") A_PM2("v58", "v68", "v69") A_PM2("v59", "v70", "v71") A_PST("v[56:59]") "s_waitcnt lgkmcnt(0)\n\t" \
    A_PM2("v60", "v72", "v73") A_PM2("v61", "v74", "v75") A_PM2("v62", "v76", "v77") A_PM2("v63", "v78", "v79") A_PST("v[60:63]")
#define A_TR(R0, R1, R2, R3, O0, O1) "ds_read_b64_tr_b16 " R0 ", v85 offset:" O0 "\n\tds_read_b64_tr_b16 " R1 ", v85 offset:" O1 "\n\t" \
    "ds_read_b64_tr_b16 " R2 ", v84 offset:" O0 "\n\tds_read_b64_tr_b16 " R3 ", v84 offset:" O1 "\n\t"
#define A_WS(R, S, O) "buffer_store_dwordx4 " R ", v89, " SW ", " S " offen offset:" O " sc1\n\t"
#define A_PL1(O) "buffer_load_dwordx4 v89, " SW ", s63 offen offset:" O " sc1 lds\n\t"
#define A_PL2(R, O) "buffer_load_dwordx4 " R ", v89, " SW ", s95 offen offset:" O " sc1\n\t"
#define A_EPLOAD "v_readlane_b32 s68, v92, 30\n\tv_readlane_b32 s63, v93, 21\n\ts_mov_b32 m0, s68\n\ts_add_u32 s95, s63, 0x8000\n\ts_nop 2\n\t" \
    A_PL1("0") A_PL1("1024") A_PL1("2048") A_PL1("3072") "s_bitset1_b32 m0, 12\n\ts_add_u32 s63, s63, 0x1000\n\t" A_PL1("0") A_PL1("1024") A_PL1("2048") A_PL1("3072") \
    A_PL2("v[40:43]", "0") A_PL2("v[44:47]", "1024") A_PL2("v[48:51]", "2048") A_PL2("v[52:55]", "3072") "s_add_u32 s95, s95, 0x1000\n\t" \
    A_PL2("v[56:59]", "0") A_PL2("v[60:63]", "1024") A_PL2("v[64:67]", "2048") A_PL2("v[68:71]", "3072") \
    "v_and_b32 v78, 0xf0, v89\n\tbuffer_load_dwordx4 v[72:75], v78, " SW ", s73 offen sc1\n\t"
#define A_EPOLLZ "s_mov_b32 exec_lo, 0\n\ts_mov_b32 exec_hi, 0x3fff0000\n\tbuffer_load_dwordx2 v[124:125], v88, " SW ", s92 offen sc1\n\ts_mov_b64 exec, -1\n\t"
#define A_ECHK1 "s_mov_b32 s70, 0xffff0000\n\ts_mov_b32 s71, 0xffff\n\t" A_CHECK("v[124:125]", "s[70:71]", "s92")
#define A_AT(N) ".fill (at_event + 4 * " #N " - .) / 4, 4, 0xbf800000\n\t"
#define A_EVENT \
    ".p2align 6\nat_event:\n\t" A_RET \
    A_AT(16) "v_cmp_eq_u64 vcc, s[60:61], v[122:123]\n\ts_and_b64 vcc, vcc, s[96:97]\n\ts_cmp_eq_u64 vcc, s[96:97]\n\ts_cbranch_scc0 7f\n\t" A_RET \
    A_AT(32) A_EFLAG(5) A_CT4 A_RET \
    A_AT(48) A_EFLAG(5) A_AT(56) A_EPOLL A_RET \
    A_AT(80) A_EFLAG(4) A_RET \
    A_AT(96) A_EFLAG(5) A_CT4 A_EPOLL A_RET \
    A_AT(128) "v_xor_b32 v84, 32, v85\n\tv_readlane_b32 s63, v92, 27\n\t" \
    A_TR("v[48:49]", "v[50:51]", "v[52:53]", "v[54:55]", "0", "4096") A_TR("v[56:57]", "v[58:59]", "v[60:61]", "v[62:63]", "8192", "12288") \
    A_TR("v[64:65]", "v[66:67]", "v[68:69]", "v[70:71]", "16384", "20480") A_TR("v[72:73]", "v[74:75]", "v[76:77]", "v[78:79]", "24576", "28672") \
    "s_add_u32 s95, s63, 0x1000\n\ts_waitcnt lgkmcnt(8)\n\t" \
    A_WS("v[48:51]", "s63", "0") A_WS("v[52:55]", "s63", "1024") A_WS("v[56:59]", "s63", "2048") A_WS("v[60:63]", "s63", "3072") "s_waitcnt lgkmcnt(0)\n\t" \
    A_WS("v[64:67]", "s95", "0") A_WS("v[68:71]", "s95", "1024") A_WS("v[72:75]", "s95", "2048") A_WS("v[76:79]", "s95", "3072") A_RET \
    A_AT(192) A_EPOLL1 A_RET \
    A_AT(228) A_EPOLL A_AT(240) A_EPOLL1 \
    A_MASK("v224", "v225", "v226", "v227", "0", "1", "2", "3") \
    A_MASK("v232", "v233", "v234", "v235", "16", "17", "18", "19") \
    A_MASK("v240", "v241", "v242", "v243", "32", "33", "34", "35") \
    A_MASK("v248", "v249", "v250", "v251", "48", "49", "50", "51") A_RET \
    "7:\n\t" A_SPIN("v[122:123]", "s[96:97]", "s62") A_RET \
    A_AT(344) A_EFLAG(4) A_EPOLLZ A_EQ A_RET \
    A_AT(400) A_ELOAD A_RET \
    A_AT(448) A_EFLAG(5) A_CT4 A_EPOLLZ A_EQ A_RET \
    A_AT(512) A_EPOST A_RET \
    A_AT(768) "5:\n\t" A_CHECK("v[122:123]", "s[96:97]", "s62") "s_branch 6f\n\t" A_AT(800) "6:\n\t" A_EPLOAD A_RET \
    A_AT(872) A_ECHK1 A_RET \
    A_AT(900) A_ECHK1 "s_branch 5b\n\t"
static_assert(B_CT == 3 && B_FSET == 5 && B_FW == 4 && A_M0F == 30 && A_PQ == 28 && A_PZ == 29 && A_WDST == 27 && C_PO == 23 && B_LP == 21);
DI void event() {
  asm volatile("s_and_b32 s63, s87, 0x3ff\n\ts_lshl2_add_u32 s64, s63, s100\n\ts_addc_u32 s65, s101, 0\n\ts_swappc_b64 s[98:99], s[64:65]" ::: "vcc", SCC_ "memory",
               "v78", "v79");
  GAP();
}

#define OFU 20
static_assert(OFU >= 4 && OFU < 28 && OFU % 2 == 0);
#define OGB 14
constexpr int o_qo(int P) { return O_SEQ[P] & 3; }
constexpr int o_o(int P) { return O_SEQ[P] >> 4; }
constexpr int o_ks(int k) { return k % 7 < 4 ? 4 + k % 7 : k % 7 - 4; }
constexpr int o_sq(int P) {
  for (int q = P; q < 16; ++q) if (P < 1 || o_qo(q) == o_qo(P - 1)) return -1;
  return o_qo(P - 1);
}
constexpr int o_last() { int l = 0; for (int P = 0; P < 16; ++P) if (o_sq(P) >= 0) l = P; return l; }
constexpr bool o_first(int P) { for (int q = 0; q < P; ++q) if (o_qo(q) == o_qo(P)) return false; return true; }
template <int P, int MB, int KK> DI void o_read() {
  constexpr int r = 128 + 4 * (8 * MB + KK), sl = o_k(P) < 0 ? o_qo(P) : o_ks(o_k(P));
  asm volatile("ds_read_b128 v[%c0:%c1], v%c2 offset:%c3" ::"i"(r), "i"(r + 3), "i"(sl < 4 ? 96 + sl : 86), "i"((sl < 4 ? 0 : (sl & 3) * 16384) + (2 * KK + MB) * 1024));
}
template <int... Is> DI void o_read0(iseq<Is...>) { (o_read<0, (Is >> 3), Is & 7>(), ...); }
constexpr int o_gb(int NF) { return NF == 2 ? OGB : 0; }
#define OTAB {40, 44, 48, -1}, {52, -1, 0, 1}, {56, -1, 2, 60}, {-1, -1, 3, 4}, {-1, -1, 5, 6}, {-1, -1, 7, -1}, {-1, -1, 8, -1}, {-1, -1, 9, -1}, {-1, -1, 10, -1}, {-1, -1, 11, -1}
constexpr int O_TAB[][4] = {OTAB};
constexpr int o_op(int P, int U, int NF) {
  const int c = U / 8 == 1 && o_sq(P) >= 0 ? 99 : P < (int)(sizeof(O_TAB) / 16) ? O_TAB[P][U / 8] : -1, i = (U & 7) / 2;
  if (c < 0 || (U & 1)) return 0;
  if (c < 32) return 200 + 4 * c + i;
  if (c == 99) return i == 3 || (i == 1 && NF == 2) ? 300 + i : 0;
  return NF == 1 && (i & 1) ? 0 : 100 + c + i;
}
constexpr int o_vm(int P, int NF) {
  int n = 0;
  for (int i = 0; i <= P; ++i) {
    for (int U = 0; U < (i < P ? 32 : o_gb(NF)); ++U) {
      const int op = o_op(i, U, NF);
      if (op) ++n;
      if (op >= 200 && op < 300 && (op - 200) / 4 == o_k(P + 1)) n = 0;
      if (op >= 100 && op < 200 && (op - 100) / 16 == o_o(P + 1)) n = 0;
    }
  }
  return n;
}
constexpr bool o_ok() {
  for (int P = 1; P < 16; ++P) {
    for (int i = P - 1; i < 16; ++i) for (int U = 0; U < 32; ++U) {
      const int op = o_op(i, U, 2);
      if (i > P - 1 || U >= 0) {
        if (op >= 200 && op < 300 && (op - 200) / 4 == o_k(P)) return false;
        if (op >= 100 && op < 200 && (op - 100) / 16 == o_o(P)) return false;
      }
    }
  }
  for (int i = 0; i < 16; ++i) for (int U = 0; U < 32; ++U) {
    const int op = o_op(i, U, 2);
    if (op >= 200 && op < 300) {
      const int k = (op - 200) / 4, sl = o_ks(k);
      int prev = -1;
      for (int P = 0; P < 16; ++P) if ((o_k(P) < 0 ? o_qo(P) : o_ks(o_k(P))) == sl && (o_k(P) < k)) prev = P;
      if (prev >= 0 && !(i > prev + 1 || (i == prev + 1 && U >= 16))) return false;
    }
  }
  return true;
}
static_assert(NWF == 40 && o_ok());
template <int K> DI void o_m0() {
  constexpr int sl = o_ks(K);
  if constexpr (sl < 4) asm volatile("s_add_u32 m0, s80, s%c0" ::"i"(64 + sl) SCC1);
  else asm volatile("s_add_u32 m0, s80, %0" ::"i"(sl * 16384) SCC1);
}
template <int NF, int SR, int C> DI void o_pack() {
  if constexpr (NF == 2 || !(C & 2))
    asm volatile("v_cvt_pk_bf16_f32 v%c0, v%c1, v%c2" ::"i"(NF == 2 ? SR + C : SR + (C >> 2) * 2 + (C & 1)), "i"(SR + 2 * C), "i"(SR + 2 * C + 1));
}
template <int SR, int F> DI void o_sst() {
  asm volatile("buffer_store_dwordx4 v[%c0:%c1], v89, " SW ", s77 offen offset:%c2 sc1" ::"i"(SR + 4 * F), "i"(SR + 4 * F + 3), "i"(F * 1024) : "memory");
}
template <int I, int V> DI void o_slab() {
  asm volatile("buffer_load_dwordx4 v[%c0:%c1], v%c4, " SW ", s%c2 offen offset:%c3 sc1" ::"i"(192 + 4 * I), "i"(195 + 4 * I), "i"(64 + I / 2), "i"((I & 1) * 1024), "i"(V) : "memory");
}
template <int NF, int P, int G> DI void o_gap() {
  constexpr int NG = 16 * NF, ch = G >> 3, mb = ch / NF, nfi = (SNK & 16) && NF == 2 ? (ch ^ (ch >> 1)) & 1 : ch % NF, kk = (SNK & 16) && NF == 2 && (ch & 1) ? 7 - (G & 7) : G & 7, sq = o_sq(P), sr = 192 + 16 * sq;
  constexpr int w = o_wreg(o_o(P), 2 * kk + nfi), b = 128 + 4 * (8 * mb + kk), d = 192 + 16 * o_qo(P) + 4 * (2 * mb + nfi);
  constexpr bool nxt = P != 15;
  constexpr int U = NF == 2 ? G : 2 * G, op = o_op(P, U, NF), opn = U < 24 ? o_op(P, U + 8, NF) : nxt ? o_op(P + 1, U - 24, NF) : 0;
  if constexpr (G == 0) asm volatile("s_waitcnt lgkmcnt(8)" ::: "memory");
  if constexpr (G == NG / 2) asm volatile("s_waitcnt lgkmcnt(%0)" ::"i"(nxt && NF == 1 ? 8 : 0) : "memory");
  if constexpr (nxt && G == o_gb(NF) && P >= 1 && (o_k(nxt ? P + 1 : P) >= 0 || P < 3)) asm volatile("s_waitcnt vmcnt(%0)\n\ts_barrier" ::"i"(o_vm(P, NF)) : "memory");
  if constexpr (P == 1 && G == o_gb(NF))
    asm volatile("v_readlane_b32 s63, v92, %c0\n\ts_nop 0\n\ts_add_u32 s63, s63, 32\n\t" A_FLAG ::"i"(A_FC) : SCC_ "memory");
  if constexpr (P == o_last() + 1 && U == OFU) {
    asm volatile("s_waitcnt vmcnt(0)" ::: "memory");
    set_flag<A_FS>();
  }
  if constexpr (op >= 200 && op < 300 && (op & 3) == 0) o_m0<(op - 200) / 4>();
  if constexpr (o_first(P) && !(G & 7))
    asm volatile("v_mfma_f32_16x16x32_bf16 v[%c0:%c1], a[%c2:%c3], v[%c4:%c5], 0" ::"i"(d), "i"(d + 3), "i"(w), "i"(w + 3), "i"(b), "i"(b + 3));
  else
    asm volatile("v_mfma_f32_16x16x32_bf16 v[%c0:%c1], a[%c2:%c3], v[%c4:%c5], v[%c0:%c1]" ::"i"(d), "i"(d + 3), "i"(w), "i"(w + 3), "i"(b), "i"(b + 3));
  if constexpr (nxt && G >= NG - 16) o_read<P + 1, (G >> 3) & 1, kk>();
  if constexpr (op >= 100 && op < 200) w_load<op - 100, 1 + NF>();
  if constexpr (op >= 200 && op < 300)
    asm volatile("buffer_load_dwordx4 v89, " SW ", s%c0 offen offset:%c1 sc1 lds" ::"i"(73 + U / 8), "i"((op & 3) * 1024) : "memory");
  if constexpr (sq >= 0) {
    if constexpr (G == 0) sld<77, A_SS + sq>();
    if constexpr (NF == 2 && G >= 6 && G < 14) o_pack<2, sr, G - 6>();
    if constexpr (NF == 1 && (G == 4 || G == 5)) { o_pack<1, sr, 4 * (G - 4)>(); o_pack<1, sr, 4 * (G - 4) + 1>(); }
    if constexpr (op >= 300) o_sst<sr, NF == 2 ? (op - 300) / 2 : 0>();
  }
  if constexpr (opn >= 200 && opn < 300 && (opn & 3) == 0) sld<73 + ((U + 8) & 31) / 8, A_PC + (opn - 200) / 4>();
  if constexpr (P == 12 && G == NG - 4) { sld<62, A_FSW>(); fl_mask<A_FSM, -1>(); }
  if constexpr (P == 13 && G == 1) fl_issue();
  if constexpr (P == 13 && G >= 2 && G < 5) sld<62 + G, A_SL + G - 2>();
  if constexpr (P == 13 && G == NG - 2)
    asm volatile("v_cmp_eq_u64 vcc, s[60:61], v[122:123]\n\ts_and_b64 vcc, vcc, s[96:97]\n\ts_cmp_eq_u64 vcc, s[96:97]\n\ts_cselect_b32 s67, 0, 0x80000000\n\t"
                 "v_or_b32 v100, s67, v89" ::: "vcc", SCC_ "memory");
  if constexpr (P == 14 && G % NF == 0 && G >= 2 * NF && G < 14 * NF && !((G / NF) & 1) && (NF == 2 || !((G / NF) & 2) == false)) o_slab<G / NF / 2 - 1, 100>();
  GAP();
}
template <int NF, int P, int... Gs> DI void o_interval_(iseq<Gs...>) {
  (o_gap<NF, P, Gs>(), ...);
}
template <int NF, int P> DI void o_interval() { o_interval_<NF, P>(make_iseq<16 * NF>{}); }
template <int NF> DI void o_slabs() {
  unsigned late;
  asm volatile("s_mov_b32 %0, s67" : "=s"(late));
  if (late) {
    fl_wait();
    static_for([&](auto I) { if constexpr (NF == 2 || !(I & 1)) o_slab<I, 89>(); }, make_iseq<6>{});
  }
  GAP();
}
template <int NF> DI void o_sum() {
  static_for([&](auto I) {
    constexpr int i = I, q = i >> 2, f = i & 3, b = 192 + 8 * q + (NF == 2 ? 4 * (f >> 1) : 0), a = 72 + 2 * (NF == 2 ? f & 1 : f >> 1);
    if constexpr (f == 0) asm volatile("s_waitcnt vmcnt(%0)" ::"i"((2 - q) * NF) : "memory");
    if constexpr (NF == 2 || !(f & 1))
      asm volatile("v_mfma_f32_16x16x32_bf16 v[%c0:%c1], v[%c2:%c3], v[%c4:%c5], v[%c0:%c1]" ::"i"(240 + 4 * f), "i"(243 + 4 * f), "i"(a), "i"(a + 3), "i"(b), "i"(b + 3));
  }, make_iseq<12>{});
  asm volatile("s_nop 7");
}

#define NSG 64
#define KWAIT "s_waitcnt lgkmcnt(0)\n\t"
#define TOUCH0 0x20
#define TOUCH1 0x20
__global__ __launch_bounds__(256, 1) __attribute__((amdgpu_num_vgpr(82), amdgpu_num_sgpr(NSG)))
void fused_kernel(const bf16* a0, const bf16* a1, const bf16* a2, const bf16* a3, const bf16* a4, char* ws, const bf16* __restrict__ ak, const bf16* __restrict__ av,
                  const bf16* __restrict__ ash, const bf16* __restrict__ assmk, const bf16* __restrict__ assmD, const bf16* __restrict__ wo,
                  const bf16* __restrict__ aq, bf16* __restrict__ out) {
  int b = blockIdx.x;
  asm volatile("s_load_dwordx8 s[92:99], %1, 0\n\ts_load_dwordx2 s[60:61], %1, 32\n\ts_load_dword s62, %1, 64" : "+s"(b) : "s"(__builtin_amdgcn_kernarg_segment_ptr()));
  const bf16* ckv;
  const int bp = b >> 3;
  extern __shared__ __attribute__((aligned(16))) char lds[];
  asm volatile("at_code_begin:" ::: ACLOB_ALL, "v255", "s101");
  KST(0, 0);
  const int tid = threadIdx.x;
  int lane = tid & 63;
  const int wid = __builtin_amdgcn_readfirstlane(tid >> 6);
  const int j = b & 7, r = b >> 3, rr = r & 15, pj = rr >> 1, kv = pj & 1;
  const bool isY = r >= 16, doA = !isY && pj != NCV;
  const int cr = rr - (pj > NCV ? 2 : 0);
  const bf16* wkv;
  const int dq = Q44 ? (j & 1) - ((rr >> 1) & 1) : 0;
  CODE_TOUCH("at_code_begin", "pf_code_end", bp * 4 + wid, 8, L_SP + 16384);
  unsigned vq, vw, tb0, vbA2, vD;
  const char* tp;
  {
    GM g;
    {
      const int lda = isY ? DA : DC, row = wid * 8 + (lane >> 3), sw8 = ((lane & 7) ^ (((wid & 1) * 4 + (lane >> 4)) & 7)) * 8;
      const unsigned vbA = (((Q44 ? (rr >> 2) + (isY ? 4 * (j & 1) : 0) : isY ? pj : rr >> 2) * 256 + row) * lda + sw8) * 2;
      const unsigned vbB = (4 * wid + (lane >> 4)) * LDW * 2 + 16 * ((lane & 15) ^ (((lane >> 4) << 2) | wid)) + (j + 8 * (rr & 1) - (isY ? dq : 0)) * 512;
      asm volatile("s_lshl_b32 s66, %2, 12\n\ts_mov_b32 s67, 0x20000\n\tv_mov_b32 v86, %3\n\t"
                   "s_lshl_b32 s72, %2, 6\n\ts_lshl_b32 s75, %2, 8\n\ts_add_u32 s76, s75, s72\n\ts_mov_b32 s90, %5\n\t"
                   "s_lshl_b32 s70, %2, 13\n\ts_mov_b32 s71, 0x20000\n\tv_mov_b32 v87, %4\n\t"
                   "s_mov_b32 s80, 0x20000\n\ts_mov_b32 s81, 0x100\n\ts_mov_b32 s82, 0x20100\n\ts_mov_b32 s83, 0x40000\n\t"
                   "s_mov_b32 s84, 0x60000\n\ts_mov_b32 s85, 0x40100\n\ts_mov_b32 s86, 0x60100\n\t"
                   "s_lshl_b32 s73, s72, 1\n\ts_add_u32 s74, s73, s72\n\ts_add_u32 s77, s75, s73\n\ts_add_u32 s78, s75, s74\n\t"
                   "s_and_b32 s79, s90, 0xfff\n\ts_add_u32 s87, s79, 0x10000\n\t"
                   "v_add_u32 v84, 0x80, v86\n\tv_add_u32 v85, 0x80000, v87\n\ts_mov_b32 s58, 0x6400000\n\ts_mov_b32 s59, 0x20000\n\ts_mov_b32 s91, %7\n\t"
                   "s_bitcmp1_b32 %6, 4\n\t" KWAIT "s_cselect_b64 %1, s[98:99], s[96:97]\n\ts_bitcmp1_b32 %6, 7\n\t"
                   "s_cselect_b64 s[64:65], s[94:95], s[92:93]\n\ts_cselect_b64 s[68:69], s[60:61], %1\n\ts_mov_b64 %0, s[92:93]\n\ts_xor_b32 s92, s90, 0x8000"
                   : "=&s"(ckv), "=&s"(wkv) : "s"(lda), "v"(vbA), "v"(vbB), "s"(wid * 1024 + (isY ? 32768 : 0)), "s"(b),
                     "s"(isY ? 2 : K2((rr + (rr >> 2)) & 3)) : "scc");
      g_pro<0>(make_iseq<12>{});
      asm volatile("" : "+s"(ws), "+s"(ak), "+s"(av), "+s"(ash), "+s"(assmk), "+s"(assmD), "+s"(wo), "+s"(aq), "+s"(out));
    }
    auto addrs = [&]() __attribute__((always_inline)) {
      asm volatile("" : "+v"(lane));
      const int fr = lane & 15, fq = lane >> 4, q = (lane & 15) >> 2, p = lane & 3;
#pragma unroll
      for (int k2 = 0; k2 < 2; ++k2)
        g.aA[k2] = (wid >> 1) * 16384 + (fr >> 3) * 1024 + (fr & 7) * 128 + (((4 * k2 + fq) ^ (fr >> 1)) * 16);
#pragma unroll
      for (int hf = 0; hf < 2; ++hf) {
        const unsigned a0 = 65536 + (wid & 1) * 8192 + off_b(8 * fq + 4 * hf + q, p >> 1) + 8 * (p & 1);
#pragma unroll
        for (int nf = 0; nf < 8; ++nf) { g.aB[hf][nf] = a0 ^ (nf * 32); asm volatile("" : "+v"(g.aB[hf][nf])); }
      }
      GAP();
    };
    auto consts2 = [&]() __attribute__((always_inline)) {
      const int tl = (lane * 37) >> 8, ck = lane - tl * 7;
      vq = lane < 56 ? (tl * DA + ck * 8) * 2 : OOB;
      asm volatile("" : "+v"(vq));
    };
    auto consts = [&](auto Yc) __attribute__((always_inline)) {
      const int fr = lane & 15, fq = lane >> 4;
      if constexpr (Yc) {
        vbA2 = ((((rr >> 2) + 4) * 256 + wid * 8 + (lane >> 3)) * DC + ((lane & 7) ^ (((wid & 1) * 4 + fq) & 7)) * 8) * 2;
        asm volatile("" : "+v"(vbA2));
      } else {
        consts2();
        vw = (4 * wid + fq) * DA * 2 + 16 * (fr ^ ((fq << 2) | wid));
        tb0 = L_SP + off_b(4 * fq + (fr >> 2), wid * 4 + ((lane & 3) >> 1)) + 8 * (lane & 1);
        tp = (lane < 32 && doA ? (const char*)assmk + (j * 56 + cr * 4 + wid) * 4096 : (const char*)ckv + 0x200000 + ((b & 127) * 4 + wid) * 4096) + (lane & 31) * 128;
        asm volatile("" : "+v"(vw), "+v"(tb0), "+v"(tp));
      }
      GAP();
    };
    addrs();
    PA pa;
    const int pu = p_unit(j, rr);
    if (isY) {
      const int rp = tid >> 3, c = tid & 7, ch = tid >> 4, q = tid & 15;
      pa.vo = pu < 0 ? OOB : (((pu & 15) * 128 + rp * 4) * DA + (pu >> 4) * 64 + c * 8) * 2;
      pa.lw = L_SP + 20480 + wid * 1024 + c * 64;
      pa.lx = L_SP + c * 2048 + (((31 - rp) * 8) ^ (c << 4));
      pa.lr = L_SP + ch * 256 + 16 * (q ^ (ch >> 3));
      pa.vs = ch * (SEQ * 2) + q * 16;
      asm volatile("" : "+v"(pa.vo), "+v"(pa.lw), "+v"(pa.lx), "+v"(pa.lr), "+v"(pa.vs));
      consts(std::true_type{});
      asm volatile("" : "=v"(vq), "=v"(vw), "=v"(tb0), "=v"(tp));
    } else {
      consts(std::false_type{});
      asm volatile("" : "=v"(pa.vo), "=v"(pa.lw), "=v"(pa.lx), "=v"(pa.lr), "=v"(pa.vs), "=v"(vbA2));
    }
    {
      const int fr = lane & 15, fq = lane >> 4;
      vseti<88>(fq * 4352 + fr * 8); vseti<89>(lane * 16); vseti<90>(wid * 8192 + lane * 16); vseti<91>(wid * 8192 + lane * 16 + 4096);
      asm volatile("s_getpc_b64 s[100:101]\n\ts_add_u32 s100, s100, at_event@rel32@lo+4\n\ts_addc_u32 s101, s101, at_event@rel32@hi+12" ::: "scc");
      asm volatile("s_lshl_b32 s63, %0, 18\n\tv_add_u32 v115, s63, v85\n\ts_xor_b32 s63, s63, 0x40000\n\tv_add_u32 v116, s63, v85\n\t"
                   "s_mul_i32 s63, %0, s73\n\tv_add_u32 v117, s63, v84\n\ts_sub_u32 s63, s73, s63\n\tv_add_u32 v118, s63, v84\n\t"
                   "s_lshl_b32 s63, %0, 13\n\ts_add_u32 s98, s92, s63\n\ts_xor_b32 s99, s98, 0x2000\n\t"
                   "s_add_u32 s96, s98, s63\n\ts_bitset1_b32 s96, 16\n\ts_xor_b32 s97, s96, 0x4000" ::"s"((rr + (rr >> 2)) & 1) : "scc");
      g_pro<12>(make_iseq<4>{});
      g_part<0, 0>(); g_part<0, 1>(); g_part<0, 2>(); g_part<0, 3>();
      {
        const char* xp;
        asm volatile("s_mov_b64 %0, s[94:95]" : "=s"(xp));
        asm volatile("s_mov_b32 m0, %1\n\ts_mov_b64 exec, 0x3fff\n\tglobal_load_lds_dword %0, off\n\ts_mov_b64 exec, -1"
                     ::"v"(xp + ((b * 4 + wid) * 14 + lane) * 128), "i"(L_SP + 16384) : "memory");
      }
      asm volatile("s_mov_b64 s[56:57], %0\n\ts_mov_b32 s63, %2\n\t"
                   "global_load_dwordx3 v[92:94], %1, off\n\tglobal_load_ushort v95, %3, off\n\tbuffer_load_dwordx2 v[120:121], off, " SW ", s63 sc1"
                   ::"s"(ws), "v"(&PTAB[j].v[r][0][tid][0]), "i"(fwo(6144)), "v"(assmD + j * 56 + (doA ? cr * 4 + wid : 0)) : "memory");
      pa.sk = __builtin_amdgcn_make_buffer_rsrc((void*)ak, (short)0, SEQ * DA * 2, 0x00020000);
      pa.sv = __builtin_amdgcn_make_buffer_rsrc((void*)av, (short)0, SEQ * DA * 2, 0x00020000);
      pa.sw = __builtin_amdgcn_make_buffer_rsrc((void*)(ash + (pu >> 4) * 256), (short)0, pu < 0 ? 0 : 512, 0x00020000);
      asm volatile("" : "+s"(pa.sk), "+s"(pa.sv), "+s"(pa.sw));
    }
    auto start = [&](auto Pc) __attribute__((always_inline)) {
      constexpr int P = Pc;
      asm volatile("s_waitcnt vmcnt(12) lgkmcnt(0)\n\ts_barrier" ::: "memory");
      g_read0<P>(g, make_iseq<16>{});
      asm volatile("s_waitcnt lgkmcnt(0)\n\ts_barrier" ::: "memory");
      GAP();
    };
    if (isY) {
      start(std::integral_constant<int, 1>{});
      RSV rsv;
      rsv_begin(rsv);
      static_for([&](auto Tc) __attribute__((always_inline)) {
        constexpr int T = Tc;
        q_half<T, 0>(g, pa);
        if constexpr (T == 4) {
          const bf16* w_ = wkv; const int d_ = dq * 512;
          sld<88, A_TAB>(); sld<89, A_TAB + 1>();
          asm volatile("s_mov_b64 s[68:69], %0\n\ts_mov_b32 s70, 0x800000\n\tv_add_u32 v87, %1, v87" ::"s"(w_), "s"(d_));
          code_touch<0>();
        }
        if constexpr (T == 5) {
          const bf16* c_ = ckv; const unsigned v_ = vbA2;
          asm volatile("s_mov_b64 s[64:65], %0\n\ts_mov_b32 s66, 0x400000\n\tv_mov_b32 v86, %1\n\ts_mov_b32 s72, 0x10000\n\ts_mov_b32 s73, 0x20000\n\t"
                       "s_mov_b32 s74, 0x30000\n\ts_mov_b32 s75, 0x40000\n\ts_mov_b32 s76, 0x50000\n\ts_mov_b32 s77, 0x60000\n\ts_mov_b32 s78, 0x70000"
                       ::"s"(c_), "v"(v_));
        }
        GAP();
        q_half<T, 1>(g, pa);
      }, make_iseq<6>{});
      rsv_end(rsv);
      asm volatile("v_lshrrev_b32 %0, 4, v89" : "=v"(lane));
      consts2();
      asm volatile("" : "=v"(vw), "=v"(tb0), "=v"(tp), "=v"(vD));
      GAP();
      STT(0);
      g_half<false, 1, 0, 3>(g);
      g_half<false, 1, 1, 3>(g);
      set_flag<A_FPREP>();
      KST(0, 3);
    }
    unsigned om = 0x7000u | (isY ? 4u : (wid & 1) ? TOUCH1 : TOUCH0);
    asm volatile("" : "+s"(om));
    auto pollG = [&]() __attribute__((always_inline)) {
      sld<62, A_NPB>();
      fl_mask<A_NP, A_NP + 1>();
      asm volatile("s_nop 1");
      fl_issue();
    };
    auto oddG = [&](int t) __attribute__((always_inline)) {
      if (__builtin_expect((om >> t) & 1, 0)) {
        if (t != 13) {
          asm volatile("s_mov_b32 m0, %1\n\ts_nop 0\n\tglobal_load_lds_dword %0, off" ::"v"(tp), "i"(L_SP) : "memory");
        } else {
          fl_check();
          sld<63, A_VBB>(); sld<87, A_MB2>();
          asm volatile("s_mov_b64 s[68:69], %0\n\ts_mov_b32 s70, %1\n\ts_mov_b64 s[88:89], 0\n\tv_add_u32 v87, s63, v89\n\t"
                       "s_movk_i32 s80, 0x400\n\ts_movk_i32 s81, 0x800\n\ts_movk_i32 s82, 0xc00\n\ts_movk_i32 s83, 0x1000\n\t"
                       "s_movk_i32 s84, 0x1400\n\ts_movk_i32 s85, 0x1800\n\ts_movk_i32 s86, 0x1c00"
                       ::"s"(isY ? (const void*)ws : (const void*)assmk), "s"(isY ? 0x6400000 : DA * SEQ * 2));
        }
      }
    };
    auto evenG = [&](int t) __attribute__((always_inline)) {
      if (__builtin_expect((om >> t) & 1, 0)) {
        if (t == 2) set_flag<A_FT2>();
        else if (t == 12) pollG();
        else {
          sld<63, A_VBA>(); sld<79, A_MA2>();
          asm volatile("s_mov_b64 s[64:65], s[56:57]\n\ts_mov_b32 s66, s58\n\tv_add_u32 v87, 0x2000, v87\n\tv_add_u32 v86, s63, v89\n\t"
                       "s_movk_i32 s72, 0x1000\n\ts_movk_i32 s75, 0x4000\n\ts_movk_i32 s76, 0x5000");
          code_touch<1>();
        }
      }
    };
    auto tileG = [&](auto TRc) __attribute__((always_inline)) {
      constexpr bool TR = TRc;
      if (isY) {
        STT(5);
        g_half<TR, 0, 0, 2>(g);
        g_half<TR, 0, 1, 2>(g);
      } else {
        start(std::integral_constant<int, 0>{});
        g_half<TR, 0, 0, 1>(g);
        g_half<TR, 0, 1, 1>(g);
        vD = __float_as_uint(vget<95>());
      }
      g_loop<TR, false>(g, oddG, evenG);
      STT(10); STT(11);
      g_half<TR, 1, 0, 4>(g, wid);
      g_half<TR, 1, 1, 4>(g, wid);
      GAP();
    };
    if (kv) tileG(std::true_type{});
    else tileG(std::false_type{});
    STT(16);
  }
  att_item<3>(); att_item<4>(); att_item<5>(); att_item<6>();
  vseti<95>(wid * 16 + (lane & 15) - 4 * (lane >> 4)); vseti<126>(48 - wid * 16 + (lane & 15) - 4 * (lane >> 4)); vseti<127>(0xff800000u);
  asm volatile("s_mov_b64 s[88:89], %0\n\tv_mov_b32 v87, %1\n\ts_mov_b64 s[96:97], %2\n\tv_writelane_b32 v92, s96, 0\n\tv_writelane_b32 v92, s97, 1" ::"s"(aq), "v"(vq), "s"(out));
  KST(0, 4);
  if (isY) {
    sld<72, A_FCW>(); sld<96, B_MM, 1>(); sld<73, B_OP, 1>();
    asm volatile("s_mov_b32 s97, 0\n\ts_add_u32 m0, s78, 0x2000\n\ts_add_u32 s74, s74, 0x2000\n\t" A_POLL("v[122:123]", "s[96:97]", "s72") ::: "scc", "memory");
    dma4<89, 74>();
  } else if (doA) {
    const int l16 = lane & 15, fq = lane >> 4;
    CV c;
    c.lw = 65536 + wid * 15488;
    asm volatile("ds_write_b128 %0, v[114:117] offset:4096" ::"v"(c.lw + lane * 16));
    fl_mask<-1, A_NQ>();
#pragma unroll
    for (int i = 0; i < 4; ++i) c.pa[i] = c.lw + (((l16 + 1) & 1) ? CV_KS : CV_KP) + 4 * ((8 * fq + l16 + 1) >> 1) + 1024 * i;
    c.pb = c.lw + (2032 - 16 * l16 + 8 * fq) * 2 - 512 * 7;
    KST(0, 5);
    cv_rd0(c, make_iseq<24>{});
    static_for([&](auto I) { vseti<96 + I>(15 - l16 >> 1 == 4 * fq + I ? vD << (l16 & 1 ? 0 : 16) : 0u); }, make_iseq<4>{});
    fl_issue();
    c.vzo = (l16 >> 1) * 3584 + (l16 & 1) * 32 + (fq >> 1) * 16 + (1 - (fq & 1)) * 8 * 3584;
    GAP();
    cv_slots(c, make_iseq<144>{});
    static_for([&](auto T) { cv_slot<144 + T>(c); }, make_iseq<144>{});
    cv_tails(c, make_iseq<28 - cv_z0(288)>{});
    KST(0, 6);
    asm volatile("s_waitcnt vmcnt(%0) lgkmcnt(0)\n\ts_barrier" ::"i"(cv_end()) : "memory");
  } else {
    d_ops<160, 0>(make_iseq<208>{});
    fl_mask<-1, A_NQ>();
    sld<72, A_OQ>();
    fl_wait();
    asm volatile("s_waitcnt lgkmcnt(0)\n\ts_barrier" ::: "memory");
    set_flag<A_FG>();
    static_for([&](auto I) { f_load<128, I>(); if constexpr ((I & 3) == 3) f_next(); }, make_iseq<16>{});
    KST(0, 5);
    fl_mask<A_N01, -1>();
    fl_wait();
    k01_dma<0>(); k01_dma<1>(); k01_dma<2>(); k01_dma<3>();
    KST(0, 6);
    asm volatile("s_waitcnt vmcnt(8)\n\ts_barrier" ::: "memory");
  }
  if (!isY) {
    sld<94, A_WSRC>(); sld<73, B_OP, 1>();
    asm volatile("s_mov_b64 s[64:65], %0\n\ts_mov_b32 s66, 0x380000\n\ts_mov_b32 s67, s59\n\ts_mov_b32 s75, %1\n\t"
                 "v_mov_b32 v84, %2\n\tv_mov_b32 v85, %3\n\tv_mov_b32 v121, %4"
                 ::"s"(wo), "s"(L_SP + wid * 1024), "v"(vw), "v"(tb0), "v"(lane < 28 ? ((b * 4 + wid) * 28 + lane) * 128 : OOB));
  }
  u32x16 rs0, rs1; u32x4 rs2; u32x2 rs3;
#define STA(k)
  {
    AC c;
    asm volatile("rsv2_begin:" : "={v[40:55]}"(rs0), "={v[56:71]}"(rs1), "={v[72:75]}"(rs2), "={v[76:77]}"(rs3));
    const unsigned vml = (lane & 15) * 16;
#pragma unroll 1
    for (int it = r >> 4;; ++it) {
      if (it == r >> 4) KST(0, 8); else KST(0, 11);
      kreads<0>(make_iseq<8>{});
      const int n = prm<B_N, 1>();
      GAP();
      STA(0);
      if (isY) {
        phaseA<0, 2, false>(c);
        STA(1); STA(2);
        phaseB0<true>(false, vml);
      } else {
        if (it) phaseA<0, 3, false>(c);
        else phaseA<0, 1, false>(c);
        STA(1);
        if (c.rare) event();
        STA(2);
        phaseB0<false>(it && n == 2, vml);
      }
      STA(3);
      const int nl = n - it;
#pragma unroll 1
      for (int i = 1; i != nl; i += 2) {
        phaseA<1, 0, false>(c);
        STA(9 + i);
        if (__builtin_expect(c.rare != 0, 0)) { if (c.redo) sm_redo<1>(); event(); }
        phaseB<1>(c);
        if (i + 1 == nl) break;
        phaseA<0, 0, false>(c);
        STA(10 + i);
        if (__builtin_expect(c.rare != 0, 0)) { if (c.redo) sm_redo<0>(); event(); }
        phaseB<0>(c);
      }
      STA(4);
      if (it) break;
      sld<72, B_NXT, 1>(); sld<73, B_OP, 1>();
      epi_init<32768>();
      phaseA<ODD, 0, true>(c);
      if (__builtin_expect(c.redo != 0, 0)) sm_redo<ODD>();
      KST(0, 9);
      const float l[2] = {row_reduce<false>(vget<R_RS>() + vget<R_TS>()), row_reduce<false>(vget<R_RS + 1>() + vget<R_TS + 1>())};
      const f32x4 ml = {vget<R_M>(), l[0], vget<R_M + 1>(), l[1]};
      asm volatile("buffer_store_dwordx4 %0, %1, " SW ", s73 offen sc1" ::"v"(ml), "v"(vml) : "memory");
      phaseF(c, EP{}, make_iseq<74>{});
      asm volatile("s_waitcnt vmcnt(16)" ::: "memory");
      KST(0, 10);
    }
    asm volatile("at_tail:");
    vseti<84>(wid == 3 ? OOB : lane * 16);
    auto sel = [&]() __attribute__((always_inline)) {
      const unsigned one = (lane >> 4) == ((lane & 15) >> 2) ? 0x3f80u << ((lane & 1) * 16) : 0u, s0 = lane & 2 ? 0u : one, s1 = lane & 2 ? one : 0u;
      asm volatile("v_mov_b32 v72, %0\n\tv_mov_b32 v73, %1\n\tv_mov_b64 v[74:75], 0\n\tv_mov_b32 v76, %0\n\tv_mov_b32 v77, %1" ::"v"(s0), "v"(s1));
    };
    auto fin = [&](auto Lc, auto Mc) __attribute__((always_inline)) {
      constexpr int L = Lc, M = Mc;
      EP p;
      phaseAD<L, M == 1>(c, make_iseq<32>{});
      if (__builtin_expect(c.redo != 0, 0)) sm_redo<L>();
      STA(5);
      sld<76, B_SO, 1>();
      vseti<85>((wid >> 1) * 16384 + (wid & 1) * 1024 + lane * 16);
      if constexpr (M < 2) p.fa[0] = __builtin_amdgcn_rcpf(row_reduce<false>(vget<R_RS>() + vget<R_TS>()));
      GAP();
      phaseBD<L, M < 2>(c, p, make_iseq<64>{});
      if (__builtin_expect(c.redo != 0, 0)) sm_redo<L ^ 1, 1>();
      STA(6);
      KST(0, 12);
      asm volatile("s_waitcnt vmcnt(%0)\n\ts_barrier" ::"i"(M < 2 ? 6 + BDX : 0) : "memory");
      if constexpr (M == 2) { asm volatile("v_add_u32 v107, %0, v90" ::"i"(L_SP)); epi_preads(make_iseq<8>{}); }
      vreads<L>(make_iseq<16>{});
      const float l1 = row_reduce<false>(vget<R_RS + 1>() + vget<R_TS + 1>());
      if constexpr (M < 2) {
        p.fa[1] = __builtin_amdgcn_rcpf(l1);
        asm volatile("s_waitcnt lgkmcnt(0)" ::: "memory");
        phaseFD<L, true>(p, make_iseq<40>{});
      } else {
        const float l[2] = {row_reduce<false>(vget<R_RS>()), l1}, cm[2] = {vget<R_M>(), vget<R_M + 1>()};
        const f32x4 ml = {vget<72>(), vget<73>(), vget<74>(), vget<75>()};
#pragma unroll
        for (int e = 0; e < 2; ++e) {
          const float mm = fmaxf(cm[e], ml[2 * e]);
          const float wa = __builtin_amdgcn_exp2f(cm[e] - mm), wb = __builtin_amdgcn_exp2f(ml[2 * e] - mm);
          const float inv = __builtin_amdgcn_rcpf(l[e] * wa + ml[2 * e + 1] * wb);
          p.fa[e] = wa * inv; p.fb[e] = wb * inv;
        }
        sel();
        static_for([&](auto I) { asm volatile("v_mov_b32 v%c0, v%c1" ::"i"(160 + (int)I), "i"(40 + (int)I)); }, make_iseq<32>{});
        asm volatile("s_waitcnt lgkmcnt(0)" ::: "memory");
        phaseFD<L, false>(p, make_iseq<64>{});
      }
    };
    using I0 = std::integral_constant<int, 0>; using I1 = std::integral_constant<int, 1>; using I2 = std::integral_constant<int, 2>;
    using LY = std::integral_constant<int, ODD ? 0 : 1>;
    if (isY) {
      const unsigned late = prm<B_LM + 1, 1>();
      asm volatile("s_waitcnt vmcnt(12)" ::: "memory");
      const float cm[2] = {vget<R_M>(), vget<R_M + 1>()};
      const f32x4 ml = {vget<72>(), vget<73>(), vget<74>(), vget<75>()};
      if (__builtin_expect(!late && __builtin_amdgcn_ballot_w64(cm[0] != ml[0] || cm[1] != ml[2]) == 0, 1)) {
        vset<R_RS>(vget<R_RS>() + 0.25f * ml[1]); vset<R_RS + 1>(vget<R_RS + 1>() + 0.25f * ml[3]);
        sel();
        asm volatile("v_add_u32 v85, %0, v90" ::"i"(L_SP));
        fin(LY{}, I1{});
      } else fin(LY{}, I2{});
    } else { sel(); fin(I1{}, I0{}); }
    KST(0, 13);
  }
  {
    sld<64, A_OWN>(); sld<65, A_OWN + 1>(); sld<66, A_OWN + 2>(); sld<67, A_OWN + 3>();
    asm volatile("s_lshl_b32 s80, %0, 12\n\tv_add_u32 v96, s64, v89\n\tv_add_u32 v97, s65, v89\n\tv_add_u32 v98, s66, v89\n\tv_add_u32 v99, s67, v89\n\t"
                 "s_waitcnt lgkmcnt(0)\n\ts_barrier" ::"s"(wid) : "scc", "memory");
    o_read0(make_iseq<16>{});
    {
      const int fr = lane & 15, fq = lane >> 4;
      const unsigned vo = wid < 3 || !(fq & 1) ? prm<A_OUT>() + (fr * OUTW + (fq & 1) * 16 + (fq >> 1) * 8) * 2 : OOB;
      asm volatile("v_mov_b32 v101, %0\n\tv_add_u32 v102, %1, %0\n\ts_mov_b64 s[88:89], %2\n\ts_mov_b32 s90, 0x380000\n\ts_mov_b32 s91, s59"
                   ::"v"(vo), "s"((j < 4 ? 16 : -16) * OUTW * 2), "s"(out));
      GAP();
    }
    if (wid < 3) {
      static_for([&](auto P) {
        o_interval<2, P>();
        if constexpr (P == 1) KST(0, 27);
        if constexpr (P == 3) KST(0, 28);
      }, make_iseq<15>{});
      o_interval<2, 15>();
      o_slabs<2>();
      o_sum<2>();
    } else {
      sld<62, A_FCW>();
      fl_mask<A_FCM, -1>();
      GAP();
      static_for([&](auto Pc) {
        constexpr int P = Pc;
        o_interval<1, P>();
        if constexpr (P == 0 || P == 2) { fl_wait(); asm volatile("s_lshl_b32 s96, s96, 4" ::: "scc"); }
      }, make_iseq<15>{});
      o_interval<1, 15>();
      o_slabs<1>();
      o_sum<1>();
    }
    asm volatile("rsv2_end:" ::"{v[40:55]}"(rs0), "{v[56:71]}"(rs1), "{v[72:75]}"(rs2), "{v[76:77]}"(rs3));
    KST(0, 29);
#define A_CV(D, S0, S1) "v_cvt_pk_bf16_f32 v" #D ", v" #S0 ", v" #S1 "\n\t"
    asm volatile(A_CV(224, 240, 241) A_CV(226, 244, 245) A_CV(225, 242, 243) A_CV(227, 246, 247) A_CV(228, 248, 249) A_CV(230, 252, 253) A_CV(229, 250, 251) A_CV(231, 254, 255)
                 "v_permlane16_swap_b32_e32 v224, v226\n\tv_permlane16_swap_b32_e32 v225, v227\n\tv_permlane16_swap_b32_e32 v228, v230\n\tv_permlane16_swap_b32_e32 v229, v231\n\t"
                 "buffer_store_dwordx4 v[224:227], v101, s[88:91], 0 offen\n\tbuffer_store_dwordx4 v[228:231], v102, s[88:91], 0 offen" ::: "memory");
    set_flag<A_FE>();
  }
  KST(0, 1);
  asm volatile("s_endpgm\n" A_EVENT "\npf_code_end:" ::: "memory");
}

extern "C" void kernel_launch(void* const* d_in, const int* in_sizes, int n_in, void* d_out, int out_size, void* d_ws,
                              size_t ws_size, hipStream_t stream) {
  const bf16* Aq = (const bf16*)d_in[0];
  const bf16* Ak = (const bf16*)d_in[1];
  const bf16* Av = (const bf16*)d_in[2];
  const bf16* Ash = (const bf16*)d_in[3];
  const bf16* Assmk = (const bf16*)d_in[4];
  const bf16* AssmD = (const bf16*)d_in[5];
  const bf16* Bx = (const bf16*)d_in[6];
  const bf16* Bckv = (const bf16*)d_in[7];
  const bf16* Bwq = (const bf16*)d_in[8];
  const bf16* Bwuk = (const bf16*)d_in[9];
  const bf16* Bwuv = (const bf16*)d_in[10];
  const bf16* Bwo = (const bf16*)d_in[11];
  bf16* out = (bf16*)d_out;
  (void)hipFuncSetAttribute((const void*)fused_kernel, hipFuncAttributeMaxDynamicSharedMemorySize, 163840);
  fused_kernel<<<256, 256, 163840, stream>>>(Bckv, Bx, Bwuk, Bwuv, Bwq, (char*)d_ws + ((ws_size - 0x8000000) & ~(size_t)0x1fffff), Ak, Av, Ash, Assmk, AssmD, Bwo, Aq, out);
}
```

```cpp
#include <hip/hip_runtime.h>
#include <hip/hip_bf16.h>
#include <stdint.h>
#include <utility>
#include <type_traits>

typedef __hip_bfloat16 bf16;
typedef __attribute__((ext_vector_type(4))) short bf16x4;
typedef __attribute__((ext_vector_type(2))) unsigned u32x2;

constexpr int SEQ = 2048;
constexpr int DA = 448;
constexpr int NH = 16;
constexpr int HD = 256;
constexpr int DC = 1024;
constexpr int OUTW = 896;

#define KST(kid, which)

__device__ __forceinline__ float bf2f(unsigned short u) { return __uint_as_float(((unsigned)u) << 16); }
__device__ __forceinline__ unsigned short f2bf(float f) { return __bfloat16_as_ushort(__float2bfloat16(f)); }
__device__ __forceinline__ float rbf(float f) { return bf2f(f2bf(f)); }
typedef __attribute__((ext_vector_type(2))) __bf16 bf16x2n;
typedef __attribute__((ext_vector_type(2))) float f32x2n;
__device__ __forceinline__ unsigned pk2(float a, float b) {
  return __builtin_bit_cast(unsigned, __builtin_convertvector((f32x2n){a, b}, bf16x2n));
}

__device__ __forceinline__ int off_b(int row, int ch) { return 256 * row + 16 * (ch ^ (((row & 3) << 2) | ((row >> 2) & 3))); }

#define DI __device__ __forceinline__
#define SCC_
#define SCC1
#define SCC2
#define SCC3
#define GAP() __builtin_amdgcn_sched_barrier(0)
template <int R> DI float vget() { float t; asm volatile("v_mov_b32 %0, v%c1" : "=v"(t) : "i"(R)); return t; }
template <int R> DI void vset(float x) { asm volatile("v_mov_b32 v%c0, %1" ::"i"(R), "v"(x)); }
template <int R> DI void vseti(unsigned x) { asm volatile("v_mov_b32 v%c0, %1" ::"i"(R), "v"(x)); }
template <int R> DI float acc_read1() { float t; asm volatile("v_accvgpr_read_b32 %0, a[%c1]" : "=v"(t) : "i"(R)); return t; }
template <class F, int... I> DI void static_for(F&& f, std::integer_sequence<int, I...>) {
  (f(std::integral_constant<int, I>{}), ...);
}
template <int... Is> using iseq = std::integer_sequence<int, Is...>;
template <int N> using make_iseq = std::make_integer_sequence<int, N>;
typedef unsigned long long u64;

constexpr unsigned O_K = 1u << 24, O_V = 2u << 24, O_C = 3u << 24, O_P = 4u << 24, O_F = O_P + 128 * 67584, O_S = O_F + 400 * 4352,
                   O_X = O_S + (1u << 24), O_Z = O_X + DA * SEQ * 2, O_W = O_Z + DA * SEQ * 2;
static_assert(O_W + 16 * 262144 <= 0x6400000);
constexpr unsigned fwo(int w) { return O_F + (w >> 4) * 4352 + (w & 15) * 8; }
constexpr unsigned OOB = 0x80000000u, M0X = 0x28000;
constexpr unsigned L_SP = 131072;
#define CMAX 10
#define NYA 15
#define NYB 15
#define MAP4 2
#define TSEQ 1
constexpr int O_SEQ[16] = {0x00, 0x01, 0x02, 0x10, 0x20, 0x30, 0x11, 0x21, 0x31, 0x12, 0x22, 0x32, 0x13, 0x23, 0x33, 0x03};
constexpr int o_k(int P) { int k = 0; for (int q = 0; q < P; ++q) k += O_SEQ[q] >= 16; return O_SEQ[P] >= 16 ? k : -1; }
#define Q44 1
#define KTYV 0x76543210fedcba98ull
#define LATEP 0
#define PKS 3
#define PPOS 99, 10, 9, 9, 9, 8
#define TABX 0xfedcb9875a463210ull, 0xefadc6982b574310ull, 0xfebad7693c285410ull, 0xefcba8764d392510ull
#define K2(c) (2 + (c))
#define POSTM 7
#define NCV 0
#define WP0 8
constexpr int ODD = NYA & 1;
static_assert((NYB & 1) == ODD);
constexpr int NYS[8] = {NYA, NYA, NYA, NYA, NYB, NYB, NYB, NYB};
enum { A_TAB = 0, A_FPREP = 2, A_FT2, A_NP, A_NPB = 6, A_VBB, A_MB2, A_VBA, A_MA2, A_VOO, A_SX, A_MX = 16, A_VOQ = 20, A_FG, A_NQ, A_N01, A_ZO, A_OQ,
       A_WSRC, A_WDST, A_PQ, A_PZ, A_M0F, A_GH, A_PC = 32, A_WLD = 44, A_FC, A_FCW, A_FCM, A_SS, A_FS = 51, A_FSW, A_FSM, A_SL, A_OUT = 57, A_OWN, A_FE = 62, A_GF };
enum { B_N = 0, B_ST, B_MS, B_CT, B_FW, B_FSET, B_NXT, B_SO, B_OP, B_MM, B_PM, C_FCM = 16, C_QT, B_LM, B_LP = 21, B_WD, C_PO, B_ACT = 32 };
enum { E_CHK = 1, E_PACK = 4, E_POLL = 8, E_POLL1 = 16, E_MASK = 32, E_FLAG = 64, E_WFLAG = 128, E_POLLZ = 256, E_LOAD = 512, E_POST = 1024, E_PLOAD = 2048, E_CHK1 = 4096 };
constexpr unsigned ev_code(unsigned a) {
  return a == 0 ? 0 : a == E_CHK ? 16 : a == E_FLAG ? 32 : a == (E_FLAG | E_POLL) ? 96 : a == E_POLL ? 56 : a == (E_WFLAG | E_POLLZ) ? 344 : a == E_WFLAG ? 80 : a == (E_FLAG | E_POLLZ) ? 448 : a == E_LOAD ? 400 : a == E_POST ? 512 : a == E_PLOAD ? 768 : a == (E_PLOAD | E_CHK1) ? 900 : a == E_CHK1 ? 872 :
         a == E_PACK ? 128 : a == E_POLL1 ? 192 : a == (E_POLL | E_POLL1 | E_MASK) ? 228 : a == (E_POLL1 | E_MASK) ? 240 : 1u << 31;
}
struct PTab { unsigned v[32][4][64][3]; bool bad; };
constexpr int p_unit(int j, int rr) { return (j & 1) && rr < 4 ? -1 : (j >> 1) * 28 + (j & 1) * 12 + rr; }
constexpr int o_head(int ks, int p) { return 2 * ks + (p >> 1) + 8 * (p & 1); }
constexpr PTab make_tab(int j) {
  PTab t{};
  for (int r = 0; r < 32; ++r) for (int wid = 0; wid < 4; ++wid) {
    auto& e = t.v[r][wid];
    const int rr = r & 15, hsg = rr & 1, pjg = rr >> 1, kv = pjg & 1, gw = j * 128;
    const bool isY = r >= 16;
    const int ks = j >> 1, p = MAP4 ? rr & 3 : (j & 1) * 2 + hsg, h = o_head(ks, p), hsel = h >> 3, ga = (h & 7) * 128;
    const int pj = MAP4 == 1 ? (rr >> 2) * 2 + (j & 1) : MAP4 == 2 ? (rr >> 2) + 4 * (j & 1) : pjg;
    const int mt = (rr >> 2) + (isY ? 4 : 0), cr = rr - (pjg > NCV ? 2 : 0), cch = j * 56 + cr * 4 + wid, NY = NYS[pj];
    const bool doA = !isY && pjg != NCV;
    const unsigned oG = (unsigned)(j + 8 * hsg) << 20;
    const int nx0 = 32 - 2 * pj - NY, cx = nx0 < CMAX ? nx0 : CMAX, ex = nx0 - cx, dg = 30 - 2 * pj;
    int seq[2][32] = {}, ns = 0;
    auto run = [&](int it, int t0, int t1) { for (int q = t0; q < t1; ++q) seq[it][ns++] = q; };
    const int x1 = TSEQ ? dg - ex : 16, c4 = 32 - 8 - NY < CMAX ? 32 - 8 - NY : CMAX;
    if (!isY) {
      run(0, 0, cx); run(0, x1, x1 + ex);
      ns = 0;
      const int s1 = TSEQ >= 2 && nx0 + TSEQ - 2 + 2 <= 2 * pj ? (nx0 < 4 ? 2 : nx0 + TSEQ - 2) : 0;
      run(1, s1, 2 * pj); run(1, 0, s1); run(1, 2 * pj, 2 * pj + 2);
    } else if (ex || !TSEQ) { run(0, cx, x1); run(0, x1 + ex, dg + 2); }
    else { run(0, c4 > cx ? c4 : cx, 16); run(0, cx, c4); run(0, 16, dg + 2); }
    const u64 own = h == j + 8 * hsg && isY ? 1ull << (hsel * 16 + kv * 8 + mt) : 0;
    u64 needB = 0;
    int c1 = 99;
    for (int i = (isY ? NY : nx0) - 1; i >= 0; --i) if (seq[0][i] >= 16) { needB |= 0x101ull << (hsel * 16 + (seq[0][i] >> 2)); c1 = i - 2; }
    needB &= ~own;
    const u64 needQ = (1ull << (32 + hsel * 8 + (pj >> 1))) | (1ull << (32 + hsel * 8 + ((15 - pj) >> 1)));
    const u64 needA = 0x0f0full << (hsel * 16);
    const unsigned oQ = (unsigned)h << 20, oK = oQ + O_K, oP = O_P + (h * 8 + pj) * 67584;
    const unsigned oQ0 = oQ + ((15 - pj) * 128 + wid * 32) * 512, oK0 = oK + seq[0][0] * 32768, lowb = ((wid & 1) + (wid >> 1) * 4) * 4096;
    auto w0 = [&](unsigned x) { return wid == 0 ? x : OOB; };
    const int cls = (rr + (rr >> 2)) & 3;
    const u64 tabX[4] = {TABX};
    const u64 tab = isY ? KTYV : tabX[cls];
    if ((tabX[cls] & 0xfff) != 0x10u + K2(cls) * 0x100) t.bad = true;
    e[C_QT][2] = 0x6543;
    e[A_TAB][0] = (unsigned)tab; e[A_TAB + 1][0] = (unsigned)(tab >> 32);
    const int pu = p_unit(j, rr);
    e[A_FPREP][0] = pu < 0 ? OOB : w0(fwo(6160 + pu));
    const int hq = Q44 ? o_head(ks, rr & 3) : j + 8 * hsg, tq = Q44 ? (rr >> 2) + 4 * (j & 1) : pjg;
    e[A_FT2][0] = isY ? w0(fwo((hq & 7) * 128 + 32 + (hq >> 3) * 8 + tq)) : OOB;
    const u64 np = isY ? needA | needQ : 0xffffull;
    e[A_NP][0] = (unsigned)np; e[A_NP + 1][0] = (unsigned)(np >> 32); e[A_NPB][0] = fwo(isY ? ga : 6160 + (cch >> 6) * 16);
    e[A_VBB][0] = oQ0; e[A_MB2][0] = isY ? 65536 + wid * 1024 : M0X;
    e[A_VBA][0] = oK0 + wid * 1024; e[A_MA2][0] = isY ? wid * 1024 : M0X;
    e[A_VOO][0] = oG + (kv ? O_V : O_K) + mt * 131072 + (wid >> 1) * 65536 + (wid & 1) * (kv ? 8192 : 4096);
    e[A_SX][0] = isY ? oK0 + lowb + 8192 : O_X + cch * 4096; e[A_SX + 1][0] = isY ? oQ0 + 12288 : cch * 4096;
    e[A_SX + 2][0] = oK0 + 32768 + lowb; e[A_SX + 3][0] = oK0 + O_K + wid * 8192;
    e[A_MX][0] = isY ? lowb + 8192 : doA ? 65536 + wid * 15488 : M0X; e[A_MX + 1][0] = isY ? 114688 + wid * 4096 : e[A_MX][0] + 5120;
    e[A_MX + 2][0] = isY ? 32768 + lowb : M0X; e[A_MX + 3][0] = isY ? 65536 + wid * 8192 : M0X;
    e[A_VOQ][0] = ((unsigned)hq << 20) + tq * 131072 + (wid >> 1) * 65536 + (wid & 1) * 4096;
    e[A_FG][0] = w0(fwo(gw + hsg * 16 + kv * 8 + mt));
    e[A_NQ][0] = (unsigned)(needQ >> 32); e[A_N01][0] = 0x0101u << (hsel * 16);
    e[A_ZO][0] = isY ? (pu < 0 ? OOB : O_X + (pu >> 4) * (64 * SEQ * 2) + (15 - (pu & 15)) * 256) : O_Z + j * (56 * SEQ * 2) + (cr * 4 + wid) * 64; e[A_OQ][0] = oQ0;
    const int qbl = isY ? 15 - pj : pj, u = 2 * pj, tile = qbl * 4 + p;
    e[A_WSRC][0] = ((o_head(ks, p ^ (u >> 2)) * 256 + (u & 3) * 64) * DA + p * 112) * 2;
    e[A_WLD][0] = O_W + h * 262144 + wid * 8192;
    e[A_WDST][0] = e[A_WLD][0] + u * 16384;
    e[A_PQ][0] = ((rr * 4 + wid) * 32 * DA + j * 56) * 2; e[A_PZ][0] = O_Z + (j * 64 + rr * 4 + wid) * 3584;
    e[A_M0F][0] = L_SP + wid * 8192; e[C_PO][2] = ((rr * 4 + wid) * 32 * OUTW + j * 56) * 2;
    e[A_GH][0] = fwo(gw + 64);
    auto qo = [&](int i) { return i == 3 ? ks : i + (i >= ks); };
    auto ctxo = [&](int o, int qb, int q) { return O_C + ((unsigned)o_head(ks, p ^ o) << 20) + qb * 65536 + q * 16384 + wid * 4096; };
    auto slab = [&](int tl, int q, int src) { return O_S + tl * 262144 + (q * 8 + src) * 8192 + wid * 2048; };
    for (int q = 0; q < 16; ++q) if (o_k(q) >= 0) e[A_PC + o_k(q)][0] = ctxo(O_SEQ[q] >> 4, qbl, qo(O_SEQ[q] & 3));
    e[A_FC][0] = w0(fwo(1024 + (qbl * 4 + ks) * 16 + p)); e[A_FCW][0] = fwo(1024 + (qbl * 4 + ks) * 16);
    e[A_FCM][0] = 15 & ~(1 << p);
    for (int q = 0; q < 3; ++q) { e[A_SS + q][0] = slab(tile, qo(q), 2 * ks); e[A_SL + q][0] = slab(tile, ks, 2 * qo(q)); }
    e[A_FS][0] = fwo(2048 + (tile * 4 + wid) * 16 + 2 * ks); e[A_FSW][0] = fwo(2048 + (tile * 4 + wid) * 16); e[A_FSM][0] = 0x55 & ~(1 << (2 * ks));
    e[A_OUT][0] = ((qbl * 128 + (ks < 2 ? 2 * ks : 11 - 2 * ks) * 16) * OUTW + DA + p * 112 + wid * 32) * 2;
    for (int q = 0; q < 4; ++q) e[A_OWN + q][0] = qo(q) * 16384;
    e[A_FE][0] = r == 28 && j == 4 ? w0(fwo(6144)) : OOB;
    e[A_GF][0] = fwo(ga);
    int wstep = isY || pj >= PKS ? 99 : 4, wpos = 0;
    for (int it = 0; it < 2; ++it) {
      const int R = 1 + it, n = isY ? NY : it ? 2 * pj + 2 : nx0, qb = isY || !it ? 15 - pj : pj;
      const bool last = isY || it;
      unsigned act[32] = {};
      int np_ = 0;
      auto poll = [&](int i, u64 m) {
        const int l = B_PM + 2 * np_++;
        e[l][R] = (unsigned)m; e[l + 1][R] = (unsigned)(m >> 32);
        if (act[i] & E_POLL) t.bad = true;
        act[i] |= E_POLL; act[i + 1] |= E_CHK;
      };
      if (!it || isY) act[1] |= E_FLAG;
      const bool late = isY && pj < LATEP;
      if (isY) {
        const int pa[8] = {99, 99, PPOS};
        int s_ = late ? n - 2 : pa[pj] + 15 - NY < n - 3 ? pa[pj] + 15 - NY : n - 3;
        int q = 1;
        if ((POSTM >> pj) & 1) {
          act[1] |= E_POLLZ; act[2] = E_LOAD;
          q = c1 == 4 ? 5 : 3;
          act[q] = E_POST;
        }
        while (s_ <= q || (s_ + 1 >= c1 - 1 && s_ <= c1)) ++s_;
        if (c1 < 4 || s_ > n - 3 + late) t.bad = true;
        if (c1 < s_) poll(c1 - 1, needB);
        poll(s_, 1ull << (48 + hsel * 8 + pj));
        act[s_ + 1] = late ? 0 : E_PLOAD;
        if (c1 > s_) poll(c1 - 1, needB);
      }
      else if (!it) { if (n > 2) poll(1, needA); if (c1 < 99) poll(c1 - 1, needB); }
      else if (nx0 <= 2) poll(1, needA);
      if (late) e[B_LM + 1][R] = 1u << (16 + hsel * 8 + pj);
      e[B_LP][R] = oP + wid * 8192;
      const int* sq = seq[isY ? 0 : it];
      if (sq[1] != sq[0] + 1 || (!isY && seq[1][0] + 1 >= 4 && nx0 < 4)) t.bad = true;
      if (last && n > 2) act[n - 3] |= E_POLL1 | E_MASK;
      if (!last && pj == 0) act[n - 2] |= E_POLL1;
      if (last) act[n - 2] |= E_CHK1;
      if (it && !isY && pj >= PKS) wstep = 5, wpos = WP0;
      const int wend = (POSTM >> pj) & 1 ? 6 : 8;
      const int ws_[4] = {E_PACK, wend == 6 ? E_WFLAG : E_WFLAG | E_POLLZ, E_LOAD, E_POST};
      for (int i = 0; i < (last ? n - 3 : n) && !isY; ++i, ++wpos)
        if (wstep < wend && wpos >= WP0 && !act[i] && i) act[i] = ws_[wstep++ - 4];
      if (it && wstep < wend) t.bad = true;
      e[B_WD][R] = pj >= PKS ? e[A_WDST][0] : OOB;
      e[B_FW][R] = wid < 2 ? fwo((j ^ wid) * 128 + 80 + wid * 16 + rr) : OOB;
      e[B_MS][R] = doA ? e[A_MX][0] + 10304 : M0X; e[B_CT][R] = (rr * 4 + wid) * 128;
      for (int i = 0; i < n; ++i) {
        const unsigned sk = i + 2 < n ? oK + sq[i + 2] * 32768 : last ? O_W + h * 262144 + 32768 : oK + (seq[1][0] + (i & 1)) * 32768;
        e[B_ACT + i][R] = sk | ev_code(act[i]);
        if (ev_code(act[i]) > 1023 || (sk & 1023)) t.bad = true;
      }
      e[B_N][R] = n; e[B_ST][R] = oK + sq[0] * 32768;
      e[B_FSET][R] = isY ? e[A_FG][0] : it ? w0(fwo(ga + 48 + hsel * 8 + pj)) : wid == 1 ? fwo(1024 + (qb * 4 + ks) * 16 + 8 + p) : doA ? w0(fwo(gw + 112 + cr)) : OOB;
      e[B_MM][R] = 256u << p;
      e[B_NXT][R] = last ? e[A_WLD][0] : oQ + (pj * 128 + wid * 32) * 512;
      e[B_SO][R] = last ? oQ + O_C + qb * 65536 : oP;
      e[B_OP][R] = oP + 65536 + wid * 256;
    }
  }
  return t;
}
constexpr PTab PT0 = make_tab(0), PT1 = make_tab(1), PT2 = make_tab(2), PT3 = make_tab(3), PT4 = make_tab(4), PT5 = make_tab(5), PT6 = make_tab(6), PT7 = make_tab(7);
static_assert(!PT0.bad && !PT7.bad);
static __device__ const PTab PTAB[8] = {PT0, PT1, PT2, PT3, PT4, PT5, PT6, PT7};

#define SPINB "0x100000"
#define SW "s[56:59]"
template <int S, int L, int ROW = 0> DI void sld() { asm volatile("v_readlane_b32 s%c0, v%c1, %c2" ::"i"(S), "i"(92 + ROW), "i"(L)); }
template <int L, int ROW = 0> DI unsigned prm() { unsigned x; asm volatile("v_readlane_b32 %0, v%c1, %c2\n\ts_nop 4" : "=s"(x) : "i"(92 + ROW), "i"(L)); return x; }
#define A_FLAG "s_mov_b64 exec, 1\n\tbuffer_store_dwordx2 v[82:83], off, " SW ", s63 sc1\n\ts_mov_b64 exec, -1\n\t"
#define A_POLL(V, M, B) "s_mov_b64 exec, " M "\n\tbuffer_load_dwordx2 " V ", v88, " SW ", " B " offen sc1\n\ts_mov_b64 exec, -1\n\t"
#define A_SPIN(V, M, B) "s_mov_b32 s95, " SPINB "\n\ts_mov_b64 exec, " M "\n" \
    "8:\n\tbuffer_load_dwordx2 " V ", v88, " SW ", " B " offen sc1\n\ts_waitcnt vmcnt(0)\n\tv_cmp_eq_u64 vcc, s[60:61], " V "\n\t" \
    "s_cmp_eq_u64 vcc, " M "\n\ts_cbranch_scc1 9f\n\ts_sleep 4\n\ts_sub_u32 s95, s95, 1\n\ts_cbranch_scc0 8b\n9:\n\ts_mov_b64 exec, -1\n\t"
#define A_CHECK(V, M, B) "v_cmp_eq_u64 vcc, s[60:61], " V "\n\ts_and_b64 vcc, vcc, " M "\n\ts_cmp_eq_u64 vcc, " M "\n\ts_cbranch_scc1 9f\n\t" A_SPIN(V, M, B)
#define A_CTOUCH(B, E, M) "v_readlane_b32 s63, v93, 3\n\ts_sub_u32 s96, s100, at_event - " B "\n\ts_subb_u32 s97, s101, 0\n\t" \
    "s_mov_b32 s98, " E " - " B "\n\ts_mov_b32 s99, s59\n\tv_lshl_add_u32 %0, v89, 9, s63\n\t" \
    "s_mov_b32 m0, 0x27f00\n\ts_mov_b64 exec, " M "\n\tbuffer_load_dword %0, s[96:99], 0 offen lds\n\ts_mov_b64 exec, -1\n\t"
template <int K> DI void code_touch() {
  unsigned t;
  if constexpr (K) asm volatile(A_CTOUCH("rsv_end", "at_tail", "0xff") : "=&v"(t) :: "scc", "memory");
  else asm volatile(A_CTOUCH("rsv_end", "rsv2_begin", "0x1f") : "=&v"(t) :: "scc", "memory");
}
template <int L, int ROW = 0> DI void set_flag() {
  asm volatile("v_readlane_b32 s63, v%c0, %c1\n\ts_nop 3\n\t" A_FLAG ::"i"(92 + ROW), "i"(L) : "memory");
}
template <int LO, int HI> DI void fl_mask() {
  if constexpr (LO >= 0) sld<96, LO>(); else asm volatile("s_mov_b32 s96, 0");
  if constexpr (HI >= 0) sld<97, HI>(); else asm volatile("s_mov_b32 s97, 0");
}
DI void fl_issue() { asm volatile(A_POLL("v[122:123]", "s[96:97]", "s62") ::: "memory"); }
DI void fl_check() { asm volatile(A_CHECK("v[122:123]", "s[96:97]", "s62") ::: "vcc", "scc", "memory"); }
DI void fl_wait() { asm volatile("s_nop 4\n\t" A_SPIN("v[122:123]", "s[96:97]", "s62") ::: "vcc", "scc", "memory"); }

typedef __attribute__((ext_vector_type(4))) unsigned u32x4c;
typedef __attribute__((ext_vector_type(4))) float f32x4c;
typedef __attribute__((ext_vector_type(2))) float f32x2c;
__device__ __forceinline__ float bfl(unsigned u) { return __uint_as_float(u << 16); }
__device__ __forceinline__ float bfh(unsigned u) { return __uint_as_float(u & 0xffff0000u); }

#define CODE_TOUCH(LB, LE, wx, NL, m0v) do { unsigned blo_, bhi_, elo_; \
    asm volatile("s_getpc_b64 s[100:101]\n\ts_add_u32 s100, s100, " LB "@rel32@lo+4\n\ts_addc_u32 s101, s101, " LB "@rel32@hi+12\n\t" \
                 "s_mov_b32 %0, s100\n\ts_mov_b32 %1, s101\n\t" \
                 "s_getpc_b64 s[100:101]\n\ts_add_u32 s100, s100, " LE "@rel32@lo+4\n\ts_mov_b32 %2, s100" \
                 : "=s"(blo_), "=s"(bhi_), "=s"(elo_) :: "scc"); \
    const unsigned n_ = elo_ - (blo_ & ~127u); \
    const __amdgpu_buffer_rsrc_t srd_ = __builtin_amdgcn_make_buffer_rsrc((void*)(((unsigned long long)bhi_ << 32) | (blo_ & ~127u)), (short)0, n_ < 262144u ? n_ : 0u, 0x00020000); \
    const unsigned l_ = threadIdx.x & 63, vo_ = l_ < (NL) ? ((wx) + l_ * 128u) * 128u : 0x80000000u; \
    asm volatile("s_mov_b32 m0, %0\n\ts_nop 4\n\tbuffer_load_dword %1, %2, 0 offen lds" ::"s"(m0v), "v"(vo_), "s"(srd_) : "memory"); } while (0)

#define STT(k)
#define SNK 31
#define GSC " sc1"
#define PL0 66
#define PLS 7
#define PCAP 0x55555555
#define PST 272
#define ECAP 3
#define EPACE_K 7
#define EPACE_V 6
typedef __attribute__((ext_vector_type(4))) float f32x4g;
typedef __attribute__((ext_vector_type(4))) unsigned u32x4g;
DI u32x4g pair16(f32x4g x, f32x4g y) {
  const unsigned a0 = pk2(x[0], x[1]), a1 = pk2(x[2], x[3]), b0 = pk2(y[0], y[1]), b1 = pk2(y[2], y[3]);
  auto r0 = __builtin_amdgcn_permlane16_swap(a0, b0, false, false), r1 = __builtin_amdgcn_permlane16_swap(a1, b1, false, false);
  return (u32x4g){r0[0], r1[0], r0[1], r1[1]};
}
constexpr int LDW = NH * HD;
struct GM { unsigned aA[2], aB[2][8]; };
#define GDP 8, 12, 24, 28, 40, 44, 56, 60
constexpr int GDPOS[8] = {GDP};
constexpr int g_j(int G) { for (int i = 0; i < 8; ++i) if (GDPOS[i] == (G & 63)) return i; return -1; }
constexpr int GD13 = 125 - GDPOS[3];
struct PA { unsigned vo, lw, lx, lr, vs; __amdgpu_buffer_rsrc_t sk, sv, sw; };
struct POp { int k, d, a, b; };
struct PSch { POp op[760]; int first[770], vm[770], n; };
constexpr PSch p_sched() {
  PSch z{};
  POp l[760] = {};
  int n = 0, early[760] = {};
  auto put = [&](int k, int d, int a, int b, int e = 0) { l[n] = POp{k, d, a, b}; early[n] = e; ++n; };
  for (int q = 0; q < 10; ++q) put(10, q < 6 ? q : q + 2, 0, 0, PL0 + PLS * q);
  for (int c = 0; c < 8; ++c) {
    const int I = c >> 1, u = c & 1;
    for (int t = 0; t < 4; ++t) put(t & 1, 112 + t, 76 + 2 * u + (t >> 1), 0, c ? 0 : PST);
    if (u && I < 3) put(11, I + 1, 0, 0);
    for (int q = 0; q < 7; ++q) put(u, 116 + q, 32 + 4 * q + I, 0);
    for (int e = 2; e >= 0; e -= 2) {
      put(2, 125, 117 + e, 115); put(2, 123, 116 + e, 115);
      for (int t = 1; t < 4; ++t) { put(4, 125, 117 + e + t, 115 - t); put(4, 123, 116 + e + t, 115 - t); }
      const int T = e ? 126 : 127;
      put(u, 124, 64 + 4 * e + I, 0); put(u, T, 60 + 4 * e + I, 0);
      put(2, 125, 125, 124); put(2, 123, 123, T);
      put(5, T, 125, 123);
    }
    put(12, c, 0, 0, (PST + 2) / 64 * 64 + 62);
  }
  const int nm = n;
  for (int q = 0; q < 4; ++q) put(13, q, 0, 0);
  for (int q = 0; q < 4; ++q) put(14, q, 0, 0);
  int g = 0, i = 0, hold = 0;
  for (; g < 768; ++g) {
    const int q = g & 15;
    int cap = g_j(g) >= 0 ? 0 : (PCAP >> (2 * q)) & 3;
    z.first[g] = z.n; z.vm[g + 1] = z.vm[g];
    if (g == PST - 16) z.op[z.n++] = POp{11, 0, 0, 0};
    if (g == PST - 20) z.op[z.n++] = POp{15, 0, 0, 0};
    if (g >= PST - 16 && g < PST - 13) for (int r = (g - PST + 16) * 7 / 3; r < (g - PST + 17) * 7 / 3; ++r) z.op[z.n++] = POp{16, r, 0, 0};
    while (cap > 0 && i < n && early[i] <= g && hold <= g) {
      const int k = l[i].k;
      if ((k == 11 || k == 13) && q > 2) { if (k == 13) break; }
      z.op[z.n++] = l[i];
      if (k == 10 || k == 14) { ++z.vm[g + 1]; cap = 0; hold = g + 3; }
      if (i == nm - 1) hold = (g + 3) / 64 * 64 + 64;
      if (i == nm + 3) hold = g / 16 * 16 + 16;
      --cap; ++i;
    }
  }
  z.first[768] = z.n;
  if (i < n) z.n = -1;
  return z;
}
constexpr PSch PS = p_sched();
constexpr int p_last() { int g = 0; while (PS.first[g] < PS.n) ++g; return g; }
static_assert(PS.n > 0 && PS.vm[(PST - 16) / 64 * 64 - 3 - GD13] == 10 && PST % 16 == 0);
template <int I> DI void p_op(const PA& p) {
  constexpr POp o = PS.op[I];
  if constexpr (o.k == 0) asm volatile("v_lshlrev_b32_e32 v%c0, 16, v%c1" ::"i"(o.d), "i"(o.a));
  else if constexpr (o.k == 1) asm volatile("v_and_b32_e32 v%c0, s94, v%c1" ::"i"(o.d), "i"(o.a));
  else if constexpr (o.k == 2) asm volatile("v_mul_f32_e32 v%c0, v%c1, v%c2" ::"i"(o.d), "i"(o.a), "i"(o.b));
  else if constexpr (o.k == 4) asm volatile("v_fmac_f32_e32 v%c0, v%c1, v%c2" ::"i"(o.d), "i"(o.a), "i"(o.b));
  else if constexpr (o.k == 5) asm volatile("v_cvt_pk_bf16_f32 v%c0, v%c1, v%c2" ::"i"(o.d), "i"(o.a), "i"(o.b));
  else if constexpr (o.k == 10 && o.d == 0)
    asm volatile("s_mov_b32 s93, m0\n\ts_add_u32 m0, s79, %1\n\ts_mov_b32 s94, 0xffff0000\n\tbuffer_load_dwordx4 v89, %0, 0 offen lds\n\ts_mov_b32 m0, s93"
                 ::"s"(p.sw), "i"(L_SP + 20480) : SCC_ "memory");
  else if constexpr (o.k == 10 && o.d < 6) {
    if constexpr (o.d == 1) asm volatile("v_and_b32 v113, 0x380, v89\n\tv_mul_u32_u24 v114, 21, v113\n\tv_sub_u32 v114, %0, v114\n\ts_mul_i32 s95, s79, 5" ::"v"(p.vo));
    asm volatile("v_add_u32 v112, %1, v114\n\ts_mov_b32 s93, m0\n\ts_add_u32 m0, s95, %2\n\ts_mov_b32 exec_lo, %3\n\ts_mov_b32 exec_hi, %4\n\t"
                 "buffer_load_dwordx4 v112, %0, 0 offen lds\n\ts_mov_b64 exec, -1\n\ts_mov_b32 m0, s93"
                 ::"s"(p.sk), "i"((o.d - 2) * 8 * DA * 2), "i"(L_SP + (o.d - 1) * 1024), "i"(o.d == 1 ? 0 : -1), "i"(o.d == 1 ? -256 : -1) : SCC_ "memory");
  } else if constexpr (o.k == 10)
    asm volatile("buffer_load_dwordx4 v[%c0:%c1], %2, %3, 0 offen offset:%c4" ::"i"(28 + 4 * o.d), "i"(31 + 4 * o.d), "v"(p.vo), "s"(p.sv), "i"((o.d & 3) * DA * 2) : "memory");
  else if constexpr (o.k == 15) asm volatile("v_mad_u32_u24 v113, v113, 3, v89\n\tv_add_u32 v113, s95, v113\n\tv_add_u32 v113, %0, v113" ::"i"(L_SP + 640));
  else if constexpr (o.k == 16) asm volatile("ds_read_b128 v[%c0:%c1], v113 offset:%c2" ::"i"(32 + 4 * o.d), "i"(35 + 4 * o.d), "i"(o.d * 128));
  else if constexpr (o.k == 11) asm volatile("ds_read_b128 v[76:79], %0 offset:%c1" ::"v"(p.lw), "i"(o.d * 16));
  else if constexpr (o.k == 12) asm volatile("ds_write_b64 %0, v[126:127] offset:%c1" ::"v"(p.lx), "i"(o.d * 256));
  else if constexpr (o.k == 13)
    asm volatile("v_xor_b32 v112, %3, %2\n\tds_read_b128 v[%c0:%c1], v112 offset:%c4" ::"i"(32 + 4 * o.d), "i"(35 + 4 * o.d), "v"(p.lr), "i"(o.d * 32), "i"(o.d * 4096));
  else
    asm volatile("buffer_store_dwordx4 v[%c0:%c1], %2, " SW ", s93 offen sc1\n\ts_add_u32 s93, s93, 0x10000" ::"i"(32 + 4 * o.d), "i"(35 + 4 * o.d), "v"(p.vs) : SCC_ "memory");
}
template <int B, int... Is> DI void p_ops_(const PA& p, iseq<Is...>) { (p_op<B + Is>(p), ...); }
template <int Gg> DI void p_ops(const PA& p) { p_ops_<PS.first[Gg]>(p, make_iseq<PS.first[Gg + 1] - PS.first[Gg]>{}); }
constexpr float CS = 0.0625f * 1.4426950408889634f;
struct EOp { int k, d, s, g; };
struct ESch { EOp op[640]; int first[260], st[260], n; bool ok; };
constexpr int e_load(int g) { return g >= 64 ? ((g & 7) == 4 ? g >> 3 : -1) : g % 5 != 4 || g > 59 ? -1 : g < 40 ? g / 5 : g / 5 + 8; }
constexpr int e_nld(int g0, int g1) { int n = 0; for (int g = g0; g < g1; ++g) n += e_load(g) >= 0; return n; }
constexpr int e_qrd(int g) { return g >= 32 && g < 48 && (g & 1) ? (g - 32) / 2 : g >= 104 && g < 120 && (g & 1) ? 8 + (g - 104) / 2 : -1; }
constexpr ESch e_sched(int MODE) {
  ESch t{}, z{};
  int cap[260] = {}, used[260] = {}, dead[640] = {};
  const int NG = MODE == 0 ? 256 : 128;
  for (int g = 0; g < NG; ++g) {
    const int q = g & 15;
    if (MODE == 0) cap[g] = g_j(g) >= 0 ? 1 : (q == 1 || q == 2 || q == 3 || q == 5 || q == 7 || q == 9) ? 2 : q == 14 ? 4 : 3;
    else cap[g] = g < 16 ? 1 : (q == 1 || q == 2 || e_load(g) >= 0 || e_qrd(g) >= 0) ? 1 : 2;
  }
  cap[NG] = 1000;
  int n = 0, g = 0;
  auto put = [&](int k, int d, int s_, int early, int dl) {
    if (g < early) g = early < NG ? early : NG;
    while (used[g] >= cap[g]) ++g;
    ++used[g]; if (k == 3) ++used[g];
    t.op[n] = EOp{k, d, s_, g}; dead[n] = dl; ++n;
  };
  auto av = [](int m, int nf) { return 16 * m + 2 * nf + 10; };
  for (int j = 0; j < (MODE == 0 ? 32 : 16); ++j) {
    const int m0 = MODE == 2 ? (j >> 3) * 2 : j >> 2, n0 = MODE == 2 ? j & 7 : (j & 3) * 2;
    const int m1 = MODE == 2 ? m0 + 1 : m0, n1 = MODE == 2 ? n0 : n0 + 1;
    const int T = j < 16 ? 128 + 8 * j : 112 + 8 * (j & 1), dl0 = 128 + 32 * (j >> 3) + 2 * (j & 7) - 1, dl1 = j < 16 ? dl0 : 999;
    const int pace = MODE == 0 ? 0 : (MODE == 1 ? 14 : 26) + j * (MODE == 1 ? EPACE_K : EPACE_V);
    if (j >= 16) for (int k = 0; k < 8; ++k) put(0, T + k, 8 * (j - 16) + k, k < 4 ? av(m0, n0) : av(m1, n1), dl0);
    for (int h = 0; h < 2; ++h) {
      const int e = h ? av(m1, n1) : av(m0, n0);
      if (MODE == 0) for (int k = 0; k < 4; ++k) put(1, T + 4 * h + k, 0, e, dl1);
      for (int k = 0; k < 2; ++k) put(2, T + 2 * h + k, T + 4 * h + 2 * k, e, dl1);
    }
    put(3, T, j, j < 16 || MODE ? pace : 0, dl1);
  }
  z.ok = true;
  for (int q = 0; q <= NG; ++q) {
    z.first[q] = z.n; z.st[q + 1] = z.st[q];
    for (int i = 0; i < n; ++i) if (t.op[i].g == q) {
      z.op[z.n++] = t.op[i];
      if (t.op[i].k == 3) ++z.st[q + 1];
      if (MODE == 0 && q > dead[i]) z.ok = false;
    }
  }
  z.first[NG + 1] = z.n;
  return z;
}
constexpr ESch ES[3] = {e_sched(0), e_sched(1), e_sched(2)};
static_assert(ES[0].ok && ES[0].first[256] == ES[0].n && ES[1].st[126] == 16 && ES[2].st[126] == 16);
template <int MODE, int I> DI void e_op() {
  constexpr EOp o = ES[MODE].op[I];
  if constexpr (o.k == 0) asm volatile("v_accvgpr_read_b32 v%c0, a[%c1]" ::"i"(o.d), "i"(o.s));
  else if constexpr (o.k == 1) asm volatile("v_mul_f32_e32 v%c0, s94, v%c0" ::"i"(o.d));
  else if constexpr (o.k == 2) asm volatile("v_cvt_pk_bf16_f32 v%c0, v%c1, v%c2" ::"i"(o.d), "i"(o.s), "i"(o.s + 1));
  else {
    constexpr int m = o.s >> 2;
    if constexpr ((o.s & 3) == 0)
      asm volatile("s_mov_b32 s93, %0" ::"i"(MODE == 2 ? (m >> 1) * 16384 + (m & 1) * 4096 : MODE == 1 ? m * 8192 : (m < 4 ? 2 * m : 15 - 2 * m) * 8192));
    asm volatile("buffer_store_dwordx4 v[%c0:%c1], v%c2, " SW ", s93 offen offset:%c3 sc1" ::"i"(o.d), "i"(o.d + 3), "i"(MODE ? 84 : 95), "i"((o.s & 3) * 1024) : "memory");
  }
}
template <int MODE, int B, int... Is> DI void e_ops_(iseq<Is...>) { (e_op<MODE, B + Is>(), ...); }
template <int MODE, int Gg> DI void e_ops() { e_ops_<MODE, ES[MODE].first[Gg]>(make_iseq<ES[MODE].first[Gg + 1] - ES[MODE].first[Gg]>{}); }
template <int P, int KS, int I> DI void g_wread(const GM& g) {
  constexpr int nf = I >> 1, hf = I & 1, r = 128 + 64 * P + 8 * nf + 4 * KS + 2 * hf;
  asm volatile("ds_read_b64_tr_b16 a[%c1:%c2], %0 offset:%c3" ::"v"(g.aB[hf][nf]), "i"(r), "i"(r + 1), "i"(P * 32768 + KS * 16384));
}
template <int P, int M, int KS> DI void g_aread(const GM& g) {
  constexpr int r = 96 + 8 * (M & 1) + 4 * KS;
  asm volatile("ds_read_b128 v[%c1:%c2], %0 offset:%c3" ::"v"(g.aA[KS]), "i"(r), "i"(r + 3), "i"(P * 32768 + M * 2048));
}
template <bool W, int V, int J> DI void g_dma() {
  if constexpr (W) {
    if constexpr (J == 0) asm volatile("buffer_load_dwordx4 v%c0, s[68:71], 0 offen" GSC " lds" ::"i"(V) : "memory");
    else asm volatile("buffer_load_dwordx4 v%c0, s[68:71], s%c1 offen" GSC " lds" ::"i"(V), "i"(79 + J) : "memory");
  } else {
    if constexpr (J == 0) asm volatile("buffer_load_dwordx4 v%c0, s[64:67], 0 offen" GSC " lds" ::"i"(V) : "memory");
    else asm volatile("buffer_load_dwordx4 v%c0, s[64:67], s%c1 offen" GSC " lds" ::"i"(V), "i"(71 + J) : "memory");
  }
}
template <int Q> DI void att_item() {
  if constexpr (Q == 0) sld<83, B_ST, 1>();
  if constexpr (Q == 1) asm volatile("s_mov_b32 s85, %0" ::"i"(B_PM));
  if constexpr (Q == 2) asm volatile("s_add_u32 s84, s83, 0x8000\n\ts_mov_b32 s86, %0" ::"i"(B_ACT) SCC1);
  if constexpr (Q >= 3 && Q < 6) asm volatile("v_mov_b64 v[%c0:%c1], 0" ::"i"(108 + 2 * Q), "i"(109 + 2 * Q));
  if constexpr (Q == 6) asm volatile("v_mov_b32 v120, 0x45800000");
}
template <int I> DI void g_pro1();
template <int K, int I> DI void g_part() {
  constexpr bool W = K < 2;
  asm volatile("s_add_u32 m0, s%c0, %1\n\ts_nop 0" ::"i"(96 + K), "i"(W ? I * 4096 : (I & 1) * 4096 + (I >> 1) * 16384) : "scc");
  g_dma<W, 115 + K, W ? I : (I & 1) + (I >> 1) * 4>();
}
template <int Y, int I> DI void g_tag() {
  if constexpr (I == 0) { if constexpr (Y) sld<88, C_QT, 2>(); else { sld<88, A_TAB>(); sld<89, A_TAB + 1>(); } }
  if constexpr (I == 1 && !Y) asm volatile("s_lshr_b64 s[88:89], s[88:89], 12" SCC3);
  if constexpr (I == 3) asm volatile("v_readfirstlane_b32 s60, v120");
  if constexpr (I == 5) asm volatile("v_readfirstlane_b32 s61, v121");
  if constexpr (I == 9) asm volatile("s_mul_i32 s61, s61, 0x4c957f2d\n\ts_mul_i32 s63, s60, 0x5851f42d");
  if constexpr (I == 11) asm volatile("s_add_u32 s61, s61, s63\n\ts_mul_hi_u32 s63, s60, 0x4c957f2d" SCC3);
  if constexpr (I == 13) asm volatile("s_add_u32 s61, s61, s63\n\ts_mul_i32 s60, s60, 0x4c957f2d" SCC3);
  if constexpr (I == 15) asm volatile("s_add_u32 s60, s60, 0xf767814f\n\ts_addc_u32 s61, s61, 0x14057b7e" SCC3);
  if constexpr (I == 17) asm volatile("v_mov_b64 v[82:83], s[60:61]");
}
template <bool TR, int G> DI void g_last_setup(int wid) {
  if constexpr (G == 0) { sld<72, A_SX>(); sld<76, A_MX>(); }
  if constexpr (G >= 1 && G < 4) sld<72 + G, A_SX + G>();
  if constexpr (G == 3) sld<63, A_VOO>();
  if constexpr (G >= 5 && G < 8) sld<72 + G, A_MX + G - 4>();
  if constexpr (G == 6) asm volatile("v_add_u32 v84, s63, v89");
  if constexpr (G == 7) asm volatile("s_lshl_b32 s63, %0, 10\n\ts_lshl_b32 s95, %0, 12" ::"s"(wid) SCC1);
  if constexpr (G == 8) asm volatile("v_add_u32 v85, %0, v84\n\tv_add_u32 v86, 0x10000, v89" ::"i"(TR ? 4096 : 8192));
  if constexpr (G == 9) asm volatile("v_add_u32 v87, s63, v86\n\tv_add_u32 v95, s95, v86");
  if constexpr (G == 10) asm volatile("s_lshl_b32 s80, %0, 13\n\ts_bitset1_b32 s80, 16" ::"s"(wid) SCC1);
  if constexpr (G == 11) asm volatile("s_add_u32 s81, s80, 0x8000\n\ts_mov_b32 s90, 0x8000" SCC3);
  if constexpr (G == 13) { sld<62, A_GF>(); asm volatile("s_mov_b32 s91, 0xc000"); }
  if constexpr (G == 14) { att_item<0>(); att_item<1>(); }
  if constexpr (G == 15) { att_item<2>(); sld<92, A_GH>(); }
  if constexpr (G == 12) { sld<87, B_MS, 1>(); asm volatile("s_add_u32 s82, s73, 2" SCC3); }
}
template <bool TR, int P, int H, int G, int MD, int PT> DI void g_gap(const GM& g, int wid, const PA& pa) {
  constexpr bool FIRST = MD == 1 || MD == 2, LAST = MD == 4;
  constexpr int m = 4 * H + (G >> 4), q = G & 15, n = (SNK & 8) && MD < 2 && (m & 1) ? 7 - (q >> 1) : q >> 1, ks = (SNK & 4 ? q ^ (q >> 1) : q) & 1;
  constexpr int a = 96 + 8 * (m & 1) + 4 * ks, b = 128 + 64 * P + 8 * n + 4 * ks, d0 = TR ? 64 * (m >> 1) + 8 * n + 4 * (m & 1) : 32 * m + 4 * n, d = m < 4 ? 128 + d0 : d0 - 128;
  constexpr bool ST0 = MD == 1 && H == 0;
  constexpr int J = LAST ? -1 : ST0 ? (G >= 48 && G % 4 == 0 ? G / 4 - 8 : -1) : g_j(G);
  constexpr int gj = (J & 3) ^ ((J & 3) >> 1);
  constexpr int EM = LAST ? 1 + TR : 0, eg = (MD == 2 ? 128 : 0) + 64 * H + G, L = LAST ? e_load(eg) : -1;
  if constexpr (MD == 1 && G == 32) asm volatile("s_waitcnt vmcnt(%0) lgkmcnt(%1)\n\ts_barrier" ::"i"(H ? 12 + (PT >= 0 ? PS.vm[eg] : 0) : 19), "i"(8 * H) : "memory");
  else if constexpr (q == 0 && G > 0) asm volatile("s_waitcnt lgkmcnt(%0)" ::"i"(LAST ? 0 : MD == 1 ? 8 * H : 4));
  if constexpr (J == 0) asm volatile("s_add_u32 m0, s79, %0" ::"i"((P ^ H ^ 1) * 32768 + (1 - H) * 8192) SCC1);
  else if constexpr (J == 4) asm volatile("s_add_u32 m0, s87, %0" ::"i"((P ^ H) * 32768 + (1 - H) * 16384) SCC1);
  else if constexpr (J > 0 && (J & 3) == 1) asm volatile("s_bitset1_b32 m0, 12");
  else if constexpr (J > 0 && (J & 3) == 2) asm volatile("s_bitset1_b32 m0, %0" ::"i"(J < 4 ? 14 : 13));
  else if constexpr (J > 0) asm volatile("s_bitset0_b32 m0, 12");
  if constexpr (L >= 0 && (L & 3) == 0) asm volatile("s_mov_b32 m0, s%c0" ::"i"(L < 16 ? 76 + (L >> 2) : 87));
  constexpr int QR = LAST ? e_qrd(eg) : -1;
  if constexpr (QR == 0) asm volatile("s_waitcnt vmcnt(%0)" ::"i"(8 + e_nld(0, eg) + ES[EM].st[eg]) : "memory");
  if constexpr (QR == 8) asm volatile("s_waitcnt vmcnt(%0)" ::"i"(e_nld(64, eg) + ES[EM].st[eg] - ES[EM].st[60]) : "memory");
#define GMF(D, X, Y, C) asm volatile("v_mfma_f32_16x16x32_bf16 " D "[%c0:%c1], " X ", " Y ", " C ::"i"(d), "i"(d + 3), "i"(a), "i"(a + 3), "i"(b), "i"(b + 3))
#define GMF2(D, C) do { if constexpr (TR) GMF(D, "v[%c2:%c3]", "a[%c4:%c5]", C); else GMF(D, "a[%c4:%c5]", "v[%c2:%c3]", C); } while (0)
  if constexpr (FIRST && !(q & 1)) { if constexpr (m < 4) GMF2("v", "0"); else GMF2("a", "0"); }
  else if constexpr (m < 4) GMF2("v", "v[%c0:%c1]");
  else GMF2("a", "a[%c0:%c1]");
#undef GMF2
#undef GMF
  if constexpr (J >= 0 && J < 4) g_dma<false, 84, (1 - H) * 2 + (gj & 1) + (gj >> 1) * 4>();
  else if constexpr (J >= 4) g_dma<true, 85, (1 - H) * 4 + gj>();
  if constexpr (L >= 0)
    asm volatile("buffer_load_dwordx4 v89, s[%c0:%c1], s%c2 offen offset:%c3 sc1 lds" ::"i"(L >= 4 && L < 8 || L >= 16 ? 68 : 56), "i"(L >= 4 && L < 8 || L >= 16 ? 71 : 59),
                 "i"(L < 16 ? 72 + (L >> 2) : 82), "i"((L & 3) * 1024) : "memory");
  if constexpr (QR >= 0)
    asm volatile("ds_read_b128 a[%c1:%c2], v%c0 offset:%c3" ::"i"(QR < 12 ? 87 : 95), "i"(128 + 4 * QR), "i"(131 + 4 * QR), "i"(QR < 12 ? QR * 4096 : 49152 + (QR - 12) * 1024));
  if constexpr ((q == 1 || q == 2) && !(LAST && m == 7)) g_aread<(m < 7 ? P : P ^ 1), (m + 1) & 7, q - 1>(g);
  if constexpr (!LAST) {
    if constexpr (MD == 1 && H == 1 && q >= 3 && q < 11) g_wread<P ^ 1, (G >> 5), ((G >> 4) & 1) * 8 + q - 3>(g);
    if constexpr (MD != 1 && q >= 3 && q < 11 && (q & 1)) g_wread<P ^ 1, H, (G >> 4) * 4 + (q - 3) / 2>(g);
    if constexpr (MD == 1 && eg >= 62 && eg < 80) g_tag<P, eg - 62>();
    if constexpr (H == 1 && G == 0) asm volatile("s_mov_b32 s90, s91");
    if constexpr (H == 1 && G == 4) asm volatile("v_lshl_add_u32 v84, s90, 7, v86\n\ts_bfe_u32 s91, s88, 0x40000" SCC3);
    if constexpr (H == 1 && G == 6) asm volatile("s_lshr_b64 s[88:89], s[88:89], 4\n\ts_lshl_b32 s92, s91, 19" SCC3);
    if constexpr (H == 1 && G == 10) asm volatile("v_add_u32 v85, s92, v87");
  } else if constexpr (eg < 16) g_last_setup<TR, eg>(wid);
  if constexpr (ST0 && G == 23) asm volatile("v_lshl_add_u32 v85, s91, 19, v87");
  if constexpr (ST0 && G < 36 && G % 3 == 0) g_part<G < 12 ? 2 : G < 24 ? 1 : 3, (G / 3) & 3>();
  if constexpr (ST0 && G >= 36 && G < 48 && G % 3 == 0) g_pro1<16 + G / 3>();
  if constexpr (MD == 3 && eg == 0) sld<63, A_VOQ>();
  if constexpr (MD == 3 && eg == 4) asm volatile("v_add_u32 v95, s63, v89\n\ts_mov_b32 s94, %0" ::"i"(__builtin_bit_cast(unsigned, CS)));
  if constexpr (MD >= 2) e_ops<EM, eg>();
  constexpr int pg = 128 * PT + 64 * H + G;
  if constexpr (PT == 2 && eg == 0) sld<93, A_ZO>();
  if constexpr (PT >= 0) p_ops<pg>(pa);
  if constexpr (G == 61 && !LAST) {
    constexpr int ns = MD >= 2 ? ES[0].st[eg + 1] - ES[0].st[eg >= GD13 ? eg - GD13 : 0] : PT >= 0 ? PS.vm[pg + 1] - PS.vm[pg >= GD13 ? pg - GD13 : 0] : 0;
    asm volatile("s_waitcnt vmcnt(%0) lgkmcnt(0)\n\ts_barrier" ::"i"(12 + ns) : "memory");
  } else if constexpr (G == 61) asm volatile("s_waitcnt lgkmcnt(0)\n\ts_barrier" ::: "memory");
  GAP();
}
template <bool TR, int P, int H, int MD, int PT, int... Gs> DI void g_half_(const GM& g, int wid, const PA& pa, iseq<Gs...>) { (g_gap<TR, P, H, Gs, MD, PT>(g, wid, pa), ...); }
template <bool TR, int P, int H, int MD> DI void g_half(const GM& g, int wid = 0) { g_half_<TR, P, H, MD, -1>(g, wid, PA{}, make_iseq<64>{}); }
template <int T, int H> DI void q_half(const GM& g, const PA& pa) { g_half_<false, (T & 1) ^ 1, H, T == 0, T>(g, 0, pa, make_iseq<64>{}); }
typedef __attribute__((ext_vector_type(16))) unsigned u32x16;
struct RSV { u32x16 a, b, c; };
DI void rsv_begin(RSV& r) { asm volatile("rsv_begin:" : "={v[32:47]}"(r.a), "={v[48:63]}"(r.b), "={v[64:79]}"(r.c)); }
DI void rsv_end(const RSV& r) { asm volatile("rsv_end:" ::"{v[32:47]}"(r.a), "{v[48:63]}"(r.b), "{v[64:79]}"(r.c)); }
template <int P, int... Is> DI void g_read0(const GM& g, iseq<Is...>) {
  (g_wread<P, 0, Is>(g), ...); (g_wread<P, 1, Is>(g), ...);
  g_aread<P, 0, 0>(g); g_aread<P, 0, 1>(g);
}
template <int I> DI void g_pro1() {
  constexpr int grp = I >> 2, i = I & 3, t = grp < 4 ? 0 : grp < 7 ? 1 : 2;
  constexpr bool isA = grp == 0 || grp == 3 || grp == 5;
  constexpr int jj = isA ? (grp == 3 ? 2 : 0) + (i & 1) + (i >> 1) * 4 : (grp == 2 || grp == 6 ? 4 : 0) + i;
  asm volatile("s_add_u32 m0, s%c0, %1\n\ts_nop 0" ::"i"(t == 1 ? 92 : 90), "i"((isA ? 0 : 65536) + jj * 4096) : "scc");
  g_dma<!isA, (isA ? 84 : 85) + (t ? 0 : 2), jj>();
}
template <int B, int... Is> DI void g_pro(iseq<Is...>) { (g_pro1<B + Is>(), ...); }
template <bool TR, bool SCL, class HO, class HE> DI void g_loop(const GM& g, HO&& odd, HE&& even) {
  constexpr int nkt = SCL ? 7 : 16, P0 = SCL ? 1 : 0;
#pragma unroll 1
  for (int t = 1;; t += 2) {
    g_half<TR, P0 ^ 1, 0, 0>(g);
    odd(t);
    GAP();
    g_half<TR, P0 ^ 1, 1, 0>(g);
    if (SCL && t + 2 >= nkt) break;
    g_half<TR, P0, 0, 0>(g);
    even(t + 1);
    GAP();
    g_half<TR, P0, 1, 0>(g);
    if (!SCL && t + 3 >= nkt) break;
  }
}

typedef __attribute__((ext_vector_type(4))) unsigned u32x4;
typedef __attribute__((ext_vector_type(4))) float f32x4;

constexpr int R_S = 128, R_P = 192, R_CI = 224, R_E = 96;
constexpr int R_TS = 112, R_RS = 114, R_M = 116;
constexpr int R_PD = 118, R_THR = 120, R_TMP = 121;
struct AC {
  u64 redo;
  unsigned rare;
};
template <int T, int D, int K> DI void d_op() {
  constexpr int t = T + 8 * (D & 1);
  if constexpr (K < 8) asm volatile("v_accvgpr_read_b32 v%c0, a[%c1]" ::"i"(t + K), "i"(8 * D + K));
  else if constexpr (K < 12) asm volatile("v_cvt_pk_bf16_f32 v%c0, v%c1, v%c2" ::"i"(t + K - 8), "i"(t + 2 * (K - 8)), "i"(t + 2 * (K - 8) + 1));
  else
    asm volatile("buffer_store_dwordx4 v[%c0:%c1], v%c2, " SW ", s%c3 offen offset:%c4 sc1" ::"i"(t), "i"(t + 3), "i"(84 + ((D >> 2) & 1)), "i"(90 + (D >> 3)),
                 "i"((D & 3) * 1024) : "memory");
}
template <int T, int O0, int... Os> DI void d_ops(iseq<Os...>) { (d_op<T, (O0 + Os) / 13, (O0 + Os) % 13>(), ...); }
template <int D> DI void d_store() { d_ops<160, 13 * D>(make_iseq<13>{}); }
template <int X, int F> DI void kread() {
  constexpr int r = 192 + 4 * (F & 15);
  asm volatile("ds_read_b128 a[%c0:%c1], v89 offset:%c2" ::"i"(r), "i"(r + 3), "i"(X * 32768 + F * 1024));
}
template <int X, int F> DI void vread() {
  constexpr int r = 192 + 4 * (F & 15);
  asm volatile("ds_read_b128 a[%c0:%c1], v86 offset:%c2" ::"i"(r), "i"(r + 3), "i"(X * 32768 + (F & 1) * 16384 + (F >> 1) * 1024));
}
template <int X, int... Fs> DI void kreads(iseq<Fs...>) { (kread<X, Fs>(), ...); }
template <int X, int... Fs> DI void vreads(iseq<Fs...>) { (vread<X, Fs>(), ...); }
template <int X, int J> DI void dma_m0() {
  if constexpr (J == 0) asm volatile("s_mov_b32 m0, s%c0" ::"i"(80 + X));
  else if constexpr (J == 4) asm volatile("s_or_b32 m0, s%c0, 0x1000" ::"i"(80 + X) SCC1);
  else if constexpr (J == 8) asm volatile("s_bitset0_b32 m0, 16");
  else if constexpr (J == 12) asm volatile("s_bitset0_b32 m0, 12");
}
template <int J> DI void dma_at() {
  asm volatile("buffer_load_dwordx4 v%c0, " SW ", s%c1 offen offset:%c2 sc1 lds" ::"i"(J >= 4 && J < 12 ? 91 : 90), "i"(J < 8 ? 82 : 84), "i"((J & 3) * 1024) : "memory");
}
template <int V, int S> DI void dma4() {
  asm volatile("buffer_load_dwordx4 v%c0, " SW ", s%c1 offen sc1 lds\n\tbuffer_load_dwordx4 v%c0, " SW ", s%c1 offen offset:1024 sc1 lds\n\t"
               "buffer_load_dwordx4 v%c0, " SW ", s%c1 offen offset:2048 sc1 lds\n\tbuffer_load_dwordx4 v%c0, " SW ", s%c1 offen offset:3072 sc1 lds"
               ::"i"(V), "i"(S) : "memory");
}
template <int Q> DI void k01_dma() {
  asm volatile("s_add_u32 m0, s80, %0\n\ts_nop 0" ::"i"((Q >> 1) * 32768 + (Q & 1) * 4096 - 65536) SCC1);
  dma4<90 + (Q & 1), 83 + (Q >> 1)>();
}
template <int R, int I, int ST = 4, int V = 89> DI void f_load() {
  asm volatile("buffer_load_dwordx4 a[%c0:%c1], v%c3, " SW ", s72 offen offset:%c2 sc1" ::"i"(R + ST * I), "i"(R + ST * I + 3), "i"((I & 3) * 1024), "i"(V) : "memory");
}
DI void f_next() { asm volatile("s_add_u32 s72, s72, 0x1000" SCC3); }
constexpr int o_wreg(int o, int f) { return o == 0 ? 128 + 4 * f : o == 3 ? 192 + 4 * f : 8 * f + 4 * (o - 1); }
template <int F, int LASTF = 3> DI void w_load() {
  f_load<o_wreg(F >> 4, 0), F & 15, o_wreg(F >> 4, 1) - o_wreg(F >> 4, 0), (F & 1) ? 84 : 89>();
  if constexpr ((F & 3) >= LASTF) asm volatile("s_add_u32 s72, s72, %0" ::"i"(F < 4 ? 0x1000 : F < 8 ? 0xf000 : F & 4 ? 0x7000 : 0x1000) SCC1);
}
#define NWF 40
constexpr int CV_KP = 5120 - 32, CV_KS = 10304 - 32;
struct CV { unsigned pa[4], pb, lw, vzo; };
constexpr int cv_J(int q) { int J = 7; while (q > J) { q -= J + 1; --J; } return J; }
constexpr int cv_i(int q) { int J = 7; while (q > J) { q -= J + 1; --J; } return q; }
template <int r_, int N> DI void cv_rd(const CV& c) {
  constexpr int R = (r_ + 1) & 7;
  if constexpr (N < 16) {
    constexpr int i = N >> 1, o = 16 * R + 128 * (i & 1) + 2 * (N & 1), r = 192 + 32 * (r_ & 1) + 2 * N;
    asm volatile("ds_read2_b32 a[%c1:%c2], %0 offset0:%c3 offset1:%c4" ::"v"(c.pa[i >> 1]), "i"(r), "i"(r + 1), "i"(o), "i"(o + 1));
  } else {
    constexpr int m = N - 16, r = 160 + 32 * (r_ & 1) + 4 * m;
    asm volatile("ds_read_b128 v[%c1:%c2], %0 offset:%c3" ::"v"(c.pb), "i"(r), "i"(r + 3), "i"(64 * R + 512 * (7 - m)));
  }
}
template <int O> DI void cv_z(const CV& c) {
  constexpr int pr = O / 7, o = O % 7, w = 128 + 4 * (7 - 2 * pr), y = w + 2 * (o & 1) - 4 * (o >> 1);
  if constexpr (o == 4) asm volatile("s_add_u32 s63, s65, %2\n\tv_permlane16_swap_b32_e32 v%c0, v%c1" ::"i"(w), "i"(w + 2), "i"((6 - 2 * pr) * 8 * 3584) SCC1);
  else if constexpr (o == 5) asm volatile("v_permlane16_swap_b32_e32 v%c0, v%c1" ::"i"(w + 1), "i"(w + 3));
  else if constexpr (o == 6) asm volatile("buffer_store_dwordx4 v[%c0:%c1], %2, " SW ", s63 offen sc1" ::"i"(w), "i"(w + 3), "v"(c.vzo) : "memory");
  else asm volatile("v_cvt_pk_bf16_f32 v%c0, v%c1, v%c2" ::"i"(w + o), "i"(y), "i"(y + 1));
}
#define CVS 6
#define CVF 150
#define CVP 206
#define CVK 250
constexpr int CVQ = 16 * CVS + 2;
constexpr int cv_vm(int T) {
  return T < 16 * CVS && T % CVS == CVS - 1 ? 1 : T >= CVQ && T < CVQ + 96 && (T - CVQ) % 6 == 0 ? 2 : T == CVF ? 4 : T == CVP ? 5 : T > CVK && T <= CVK + 48 && (T - CVK) % 3 == 1 ? 7 : 0;
}
constexpr int cv_cnt(int T0, int T1, int kind = 0) { int n = 0; for (int T = T0; T < T1; ++T) n += kind ? cv_vm(T) == kind : cv_vm(T) != 0; return n; }
static_assert(16 * CVS + 4 <= 108 && cv_vm(CVF) == 4 && cv_vm(CVP) == 5 && CVQ + 96 < CVK);
constexpr int cv_z0(int T) { return T < 271 ? 0 : T < 278 ? T - 271 : T < 281 ? 7 : T - 274; }
template <int I> DI void k01_dma1() {
  if constexpr ((I & 3) == 0) asm volatile("s_add_u32 m0, s80, %0\n\ts_nop 0" ::"i"((I >> 3) * 32768 + ((I >> 2) & 1) * 4096 - 65536) SCC1);
  asm volatile("buffer_load_dwordx4 v%c0, " SW ", s%c1 offen offset:%c2 sc1 lds" ::"i"(90 + ((I >> 2) & 1)), "i"(83 + (I >> 3)), "i"((I & 3) * 1024) : "memory");
}
template <int T> DI void cv_slot(const CV& c) {
  constexpr int r = T / 36, Q = T % 36, i = cv_i(Q), J = cv_J(Q), d = 128 + 4 * J, a = 192 + 32 * (r & 1) + 4 * i, b = 160 + 32 * (r & 1) + 4 * (J - i), vm = cv_vm(T);
  if constexpr (Q == 0) asm volatile("s_waitcnt lgkmcnt(0)");
  if constexpr (r == 0 && i == 0)
    asm volatile("v_mfma_f32_16x16x32_bf16 v[%c0:%c1], a[%c2:%c3], v[%c4:%c5], 0" ::"i"(d), "i"(d + 3), "i"(a), "i"(a + 3), "i"(b), "i"(b + 3));
  else
    asm volatile("v_mfma_f32_16x16x32_bf16 v[%c0:%c1], a[%c2:%c3], v[%c4:%c5], v[%c0:%c1]" ::"i"(d), "i"(d + 3), "i"(a), "i"(a + 3), "i"(b), "i"(b + 3));
  if constexpr (r == 7 && i == 0) asm volatile("v_mfma_f32_16x16x32_bf16 v[%c0:%c1], v[96:99], v[%c2:%c3], v[%c0:%c1]" ::"i"(d), "i"(d + 3), "i"(b), "i"(b + 3));
  if constexpr (Q < 24 && r < 7) cv_rd<r + 1, Q>(c);
  if constexpr (T == 32) asm volatile("ds_read_u16 v121, %0 offset:5120" ::"v"(c.lw));
  if constexpr (T == 33) asm volatile("ds_write_b128 %0, v[114:117] offset:%c1\n\tds_write_b128 %0, v[114:117] offset:%c2" ::"v"(c.lw), "i"(CV_KS), "i"(CV_KS + 16));
  if constexpr (T == 62) asm volatile("ds_write_b16 %0, v121 offset:%c1" ::"v"(c.lw), "i"(CV_KS + 30));
  if constexpr (T == 34) sld<72, A_OQ>();
  if constexpr (T == 35) sld<65, A_ZO>();
  if constexpr (T == CVQ - 2) { asm volatile("s_waitcnt vmcnt(16)" ::: "memory"); fl_check(); }
  if constexpr (T < 16 * CVS) d_ops<224, 13 * (T / CVS) + (T % CVS * 13) / CVS>(make_iseq<((T % CVS + 1) * 13) / CVS - (T % CVS * 13) / CVS>{});
  if constexpr (vm == 2) { f_load<128, (T - CVQ) / 6>(); if constexpr ((T - CVQ) / 6 % 4 == 3) f_next(); }
  if constexpr (vm == 4) {
    asm volatile("s_waitcnt vmcnt(%0)\n\ts_barrier" ::"i"(cv_cnt(16 * CVS, T)) : "memory");
    set_flag<A_FG>();
  }
  if constexpr (T == CVP - 3) fl_mask<A_N01, -1>();
  if constexpr (vm == 5) fl_issue();
  if constexpr (T == CVK) { asm volatile("s_waitcnt vmcnt(%0)" ::"i"(cv_cnt(CVP + 1, T)) : "memory"); fl_check(); }
  if constexpr (vm == 7) k01_dma1<cv_cnt(CVK, T, 7)>();
  if constexpr (cv_z0(T + 1) > cv_z0(T)) cv_z<cv_z0(T)>(c);
  GAP();
}
template <int... T> DI void cv_slots(const CV& c, iseq<T...>) { (cv_slot<T>(c), ...); }
template <int... N> DI void cv_rd0(const CV& c, iseq<N...>) { (cv_rd<0, N>(c), ...); }
template <int O> DI void cv_tail(const CV& c) {
  constexpr int k = cv_cnt(CVK, 288, 7) + O / 6;
  if constexpr (O % 6 == 0 && k < 16) k01_dma1<k>();
  cv_z<cv_z0(288) + O>(c);
}
template <int... O> DI void cv_tails(const CV& c, iseq<O...>) { (cv_tail<O>(c), ...); }
static_assert(cv_cnt(CVK, 288, 7) >= 8 && cv_cnt(CVK, 288, 7) + 3 >= 16);
constexpr int cv_end() {
  int T = CVK; while (cv_cnt(CVK, T + 1, 7) < 8) ++T;
  int n = 8; for (int o = cv_z0(T); o < 28; ++o) n += o % 7 == 6;
  return n;
}

template <int Q> DI void acc_zero() {
  asm volatile("v_mfma_f32_32x32x16_bf16 a[%c0:%c1], v[208:211], v[208:211], 0" ::"i"(16 * Q), "i"(16 * Q + 15));
}
struct SMS { int ex[128], op0[128], nop[128], last; };
constexpr SMS sm_sched() {
  SMS z{};
  int et[32] = {}, e = 0;
  for (int t = 0; t < 128; ++t) {
    const int g = t & 15;
    const bool x = t < 48 ? (g >= 8 && (g & 1)) : (g >= 8 && g != 10 && g != 13 && g != 15);
    z.ex[t] = x && e < 32 ? e : -1;
    if (z.ex[t] >= 0) et[e++] = t;
  }
  int o = 0;
  for (int t = 0; t < 128; ++t) {
    const int g = t & 15;
    int cap = g < 8 ? (g == 0 ? 0 : 1) : (z.ex[t] >= 0 || t < 48 ? 0 : 2);
    z.op0[t] = o; z.nop[t] = 0;
    while (cap > 0 && o < 48 && et[2 * (o / 3) + (o % 3 == 0 ? 0 : 1)] + 2 <= t) {
      const int el = 2 * (o / 3) + o % 3;
      if (!(o % 3 < 2 && (el == 0 || el == 4))) --cap;
      ++o; ++z.nop[t]; z.last = t;
    }
  }
  return z;
}
constexpr SMS SM = sm_sched();
static_assert(SM.last <= 115);
constexpr int p_reg(int Y, int k) { return R_P + 16 * Y + 4 * ((k >> 3) * 2 + ((k >> 1) & 1)) + ((k >> 2) & 1) * 2 + (k & 1); }
template <int Y, int O> DI void sm_op() {
  constexpr int k = O / 3, e = 2 * k + O % 3, ts = R_TS + ((e >> 2) & 1);
  if constexpr (O % 3 == 2) asm volatile("v_cvt_pk_bf16_f32 v%c0, v%c1, v%c2" ::"i"(p_reg(Y, k)), "i"(R_E + ((2 * k) & 15)), "i"(R_E + ((2 * k + 1) & 15)));
  else if constexpr (e == 1 || e == 5) asm volatile("v_add_f32_e32 v%c0, v%c1, v%c2" ::"i"(ts), "i"(R_E + e - 1), "i"(R_E + e));
  else if constexpr (e != 0 && e != 4) asm volatile("v_add_f32_e32 v%c0, v%c0, v%c1" ::"i"(ts), "i"(R_E + (e & 15)));
}
template <int Y, int T> DI void sm_gap(u64& redo) {
  if constexpr (SM.ex[T] >= 0) asm volatile("v_exp_f32 v%c0, v%c1" ::"i"(R_E + (SM.ex[T] & 15)), "i"(R_S + 32 * Y + SM.ex[T]));
  if constexpr (SM.nop[T] > 0) sm_op<Y, SM.op0[T]>();
  if constexpr (SM.nop[T] > 1) sm_op<Y, SM.op0[T] + 1>();
  if constexpr (SM.nop[T] > 2) sm_op<Y, SM.op0[T] + 2>();
  if constexpr (T == SM.last + 1) asm volatile("v_add_f32_e32 v%c0, v%c1, v%c2" ::"i"(R_TMP), "i"(R_TS), "i"(R_TS + 1));
  if constexpr (T == SM.last + 2) asm volatile("v_cmp_nle_f32_e64 %0, v%c1, v%c2" : "=s"(redo) : "i"(R_TMP), "i"(R_THR));
}
#define EVG 35
template <int X, int KB, int QB, int S_, bool FIRST, bool HEAD = S_ == 0> DI void qk_mfma() {
  constexpr int r = 192 + 4 * ((KB & 1) * 8 + S_), q = 128 + 4 * (QB * 8 + S_);
  constexpr int d = R_S + 32 * X + 4 * (KB * 2 + QB), ci = HEAD ? R_CI + 4 * (KB * 2 + QB) : d;
  if constexpr (FIRST && HEAD)
    asm volatile("v_mfma_f32_16x16x32_bf16 v[%c0:%c1], a[%c2:%c3], a[%c4:%c5], 0" ::"i"(d), "i"(d + 3), "i"(r), "i"(r + 3), "i"(q), "i"(q + 3));
  else
    asm volatile("v_mfma_f32_16x16x32_bf16 v[%c0:%c1], a[%c2:%c3], a[%c4:%c5], v[%c6:%c7]" ::"i"(d), "i"(d + 3), "i"(r), "i"(r + 3), "i"(q), "i"(q + 3),
                 "i"(ci), "i"(ci + 3));
}
template <int X, int FIRST, bool NOQK, int G> DI void gapA(AC& c) {
  constexpr int Xp = X ^ 1, kb = G >> 4, g = G & 15;
  if constexpr (NOQK && G < 48 && G % 3 == 0) { f_load<128, G / 3>(); if constexpr (G % 12 == 9) f_next(); }
  if constexpr (G == 48) asm volatile("s_waitcnt vmcnt(%0) lgkmcnt(0)\n\ts_barrier" ::"i"(NOQK ? 16 : FIRST == 2 ? 63 : FIRST == 1 ? 9 : FIRST == 3 ? 8 : 0) : "memory");
  else if constexpr (g == 0 && !NOQK) asm volatile("s_waitcnt lgkmcnt(0)");
  if constexpr (!NOQK) {
    if constexpr (SNK & 1) qk_mfma<X, kb, (g >> 3) ^ (kb & 1), g < 8 ? g : 15 - g, FIRST != 0, !(g & 7)>();
    else qk_mfma<X, kb, (g >> 3), g & 7, FIRST != 0>();
    if constexpr (g < 8 && kb < 3) kread<X, (kb + 1) * 8 + g>();
    if constexpr (G == 54) asm volatile("s_add_u32 s82, s83, 0x1000000" SCC3);
    if constexpr (G == 55) dma_m0<X, 0>();
    if constexpr (G >= 56 && !(G & 1) && FIRST != 2) dma_at<(G - 56) / 2>();
    if constexpr (G == EVG) asm volatile("v_readlane_b32 s87, v93, s86");
    if constexpr (G == EVG + 2) asm volatile("s_add_u32 s86, s86, 1" SCC3);
    if constexpr (G == 57) asm volatile("s_mov_b32 s83, s84\n\ts_andn2_b32 s84, s87, 0x3ff" SCC3);
    if constexpr (G == 59 && FIRST) asm volatile("s_and_b32 %0, s87, 0x3ff" : "=s"(c.rare) SCC2);
    if constexpr (G == 59 && !FIRST) asm volatile("s_and_b32 s63, s87, 0x3ff\n\ts_cmp_lg_u64 %0, 0" ::"s"(c.redo) SCC1);
    if constexpr (G == 61 && !FIRST) asm volatile("s_addc_u32 %0, s63, 0" : "=s"(c.rare) SCC2);
  } else if constexpr (SM.nop[64 + G] > 0) asm volatile("s_nop 0");
  if constexpr (!FIRST && g < 8 && kb == 3) vread<Xp, g>();
  if constexpr (FIRST == 2) d_ops<160, 13 * (G / 8) + (G % 8 * 13) / 8>(make_iseq<((G % 8 + 1) * 13) / 8 - (G % 8 * 13) / 8>{});
  if constexpr (FIRST == 2 && G == 34)
    asm volatile("v_and_b32 v124, 0xf0, v89\n\ts_waitcnt vmcnt(8)\n\t" A_CHECK("v[122:123]", "s[96:97]", "s72")
                 "buffer_load_dwordx2 v[122:123], v124, " SW ", s73 offen offset:1024 sc1" ::: "vcc", "scc", "memory");
  if constexpr (FIRST == 1 && G >= 8 && G < 40 && G % 4 == 1)
    asm volatile("s_mov_b32 m0, s75\n\ts_add_u32 s75, s75, 0x1000\n\tbuffer_load_dwordx4 v84, s[64:67], s94 offen lds\n\ts_add_u32 s94, s94, 0x3800" ::: SCC_ "memory");
  if constexpr (FIRST == 1 && G == 42) asm volatile("s_mov_b32 m0, 0x1f800\n\ts_mov_b64 s[64:65], s[88:89]\n\ts_mov_b32 s66, 0x1c0000\n\tbuffer_load_dword v121, s[64:67], 0 offen lds" ::: "memory");
  if constexpr (FIRST == 3) {
    constexpr int n = (G >> 4) * 4 + (G & 3), f = G < 32 ? (G - 17) / 4 : 4 + (G - 33) / 3;
    if constexpr (G == 2) { asm volatile("v_xor_b32 v84, 32, v85"); sld<94, B_WD, 1>(); }
    if constexpr (G < 32 && (G & 12) == 8)
      asm volatile("ds_read_b64_tr_b16 v[%c0:%c1], v%c4 offset:%c5\n\tds_read_b64_tr_b16 v[%c2:%c3], v%c4 offset:%c6" ::"i"(224 + 4 * n), "i"(225 + 4 * n), "i"(226 + 4 * n),
                   "i"(227 + 4 * n), "i"(n & 1 ? 84 : 85), "i"((n >> 1) * 8192), "i"((n >> 1) * 8192 + 4096));
    if constexpr (G == 32) asm volatile("s_add_u32 s94, s94, 0x1000" SCC3);
    if constexpr (G >= 17 && G < 45 && (G < 32 ? G % 4 == 1 : G % 3 == 0 && G > 32))
      asm volatile("buffer_store_dwordx4 v[%c0:%c1], v89, " SW ", s94 offen offset:%c2 sc1" ::"i"(224 + 4 * f), "i"(227 + 4 * f), "i"((f & 3) * 1024) : "memory");
  }
  if constexpr (FIRST == 3 && G == 42) sld<63, B_FSET, 1>();
  if constexpr (FIRST == 3 && G == 48) asm volatile(A_FLAG ::: "memory");
  if constexpr (FIRST && G == 44) asm volatile("v_mov_b64 v[208:209], 0\n\tv_mov_b64 v[210:211], 0");
  if constexpr (!FIRST) sm_gap<Xp, 64 + G>(c.redo);
  GAP();
}
template <int R> DI void rescale1(float alpha) {
  float t;
  asm volatile("v_accvgpr_read_b32 %0, a[%c2]\n\tv_mul_f32 %0, %0, %1\n\tv_accvgpr_write_b32 a[%c2], %0" : "=&v"(t) : "v"(alpha), "i"(R));
}
template <int Q0, int R> DI void rescale_q(const float (&alpha)[2]) { if constexpr (((R >> 2) & 1) >= Q0) rescale1<R>(alpha[(R >> 2) & 1]); }
template <int Q0, int... Rs> DI void rescale_all(const float (&alpha)[2], iseq<Rs...>) { (rescale_q<Q0, Rs>(alpha), ...); }
template <bool MAX> DI float row_reduce(float v) {
  auto p = __builtin_amdgcn_permlane32_swap(__float_as_uint(v), __float_as_uint(v), false, false);
  v = MAX ? fmaxf(__uint_as_float(p[0]), __uint_as_float(p[1])) : __uint_as_float(p[0]) + __uint_as_float(p[1]);
  p = __builtin_amdgcn_permlane16_swap(__float_as_uint(v), __float_as_uint(v), false, false);
  return MAX ? fmaxf(__uint_as_float(p[0]), __uint_as_float(p[1])) : __uint_as_float(p[0]) + __uint_as_float(p[1]);
}
template <int Y, int QB> DI float row_max() {
  constexpr int r = R_S + 32 * Y + 4 * QB;
  float mx;
  asm volatile("v_max_f32 %0, v%c1, v%c2\n\tv_max3_f32 %0, %0, v%c3, v%c4" : "=&v"(mx) : "i"(r), "i"(r + 1), "i"(r + 2), "i"(r + 3));
  static_for([&](auto K) {
    constexpr int q = r + 8 * (K + 1);
    float t = mx;
    asm volatile("v_max3_f32 %0, %0, v%c1, v%c2\n\tv_max3_f32 %0, %0, v%c3, v%c4" : "+v"(t) : "i"(q), "i"(q + 1), "i"(q + 2), "i"(q + 3));
    mx = t;
  }, make_iseq<3>{});
  return row_reduce<true>(mx);
}
template <int R> DI void v_sub(float d) { asm volatile("v_sub_f32 v%c0, v%c0, %1" ::"i"(R), "v"(d)); }
template <int B> DI void s_shift(const float (&d)[2]) { static_for([&](auto E) { v_sub<B + E>(d[(E >> 2) & 1]); }, make_iseq<32>{}); }
DI void m_shift(const float (&d)[2]) {
  s_shift<R_CI>(d);
  v_sub<R_M>(-d[0]); v_sub<R_M + 1>(-d[1]);
}
#define A_MASK(R0, R1, R2, R3, K0, K1, K2, K3) \
    "v_cmp_gt_i32_e64 s[64:65], " K0 ", v95\n\tv_cmp_gt_i32_e64 s[66:67], " K1 ", v95\n\tv_cmp_gt_i32_e64 s[68:69], " K2 ", v95\n\tv_cmp_gt_i32_e64 s[70:71], " K3 ", v95\n\t" \
    "v_cndmask_b32_e64 " R0 ", -v116, v127, s[64:65]\n\tv_cndmask_b32_e64 " R1 ", -v116, v127, s[66:67]\n\t" \
    "v_cndmask_b32_e64 " R2 ", -v116, v127, s[68:69]\n\tv_cndmask_b32_e64 " R3 ", -v116, v127, s[70:71]\n\t"
template <int K, int T> DI void mask_cmp() {
  asm volatile("v_cmp_gt_i32_e64 s[64:65], %c0, v%c4\n\tv_cmp_gt_i32_e64 s[66:67], %c1, v%c4\n\tv_cmp_gt_i32_e64 s[68:69], %c2, v%c4\n\tv_cmp_gt_i32_e64 s[70:71], %c3, v%c4"
               ::"i"(K), "i"(K + 1), "i"(K + 2), "i"(K + 3), "i"(T));
}
template <int R, int S> DI void mask_sel() {
  if constexpr (S < 0)
    asm volatile("v_cndmask_b32_e64 v%c0, v%c0, v127, s[64:65]\n\tv_cndmask_b32_e64 v%c1, v%c1, v127, s[66:67]\n\t"
                 "v_cndmask_b32_e64 v%c2, v%c2, v127, s[68:69]\n\tv_cndmask_b32_e64 v%c3, v%c3, v127, s[70:71]" ::"i"(R), "i"(R + 1), "i"(R + 2), "i"(R + 3));
  else
    asm volatile("v_cndmask_b32_e64 v%c0, -v%c4, v127, s[64:65]\n\tv_cndmask_b32_e64 v%c1, -v%c4, v127, s[66:67]\n\t"
                 "v_cndmask_b32_e64 v%c2, -v%c4, v127, s[68:69]\n\tv_cndmask_b32_e64 v%c3, -v%c4, v127, s[70:71]" ::"i"(R), "i"(R + 1), "i"(R + 2), "i"(R + 3), "i"(S));
}
template <int X, int Q0 = 0> DI void sm_redo() {
  constexpr int Xp = X ^ 1;
  float d[2] = {0.f, 0.f}, dt[2] = {0.f, 0.f}, alpha[2] = {1.f, 1.f}, ts[2] = {0.f, 0.f};
  asm volatile("s_nop 7");
  static_for([&](auto Q) {
    constexpr int qb = Q + Q0;
    const float pd = vget<R_PD + qb>();
    d[qb] = fmaxf(row_max<Xp, qb>() - pd, 0.f);
    dt[qb] = d[qb] + pd;
    vset<R_PD + qb>(d[qb]);
    alpha[qb] = __builtin_amdgcn_exp2f(-d[qb]);
    vset<R_RS + qb>(vget<R_RS + qb>() * alpha[qb]);
  }, make_iseq<2 - Q0>{});
  vset<R_THR>(d[0] > 0.f || d[1] > 0.f ? -1.f : 4096.f);
  m_shift(d);
  static_for([&](auto K) {
    constexpr int k = K, qb = (k >> 1) & 1;
    if constexpr (qb >= Q0) {
      const float e0 = __builtin_amdgcn_exp2f(vget<R_S + 32 * Xp + 2 * k>() - dt[qb]), e1 = __builtin_amdgcn_exp2f(vget<R_S + 32 * Xp + 2 * k + 1>() - dt[qb]);
      ts[qb] += e0; ts[qb] += e1;
      vset<p_reg(Xp, k)>(__uint_as_float(pk2(e0, e1)));
    }
  }, make_iseq<16>{});
  vset<R_TS>(ts[0]); vset<R_TS + 1>(ts[1]);
  rescale_all<Q0>(alpha, make_iseq<128>{});
  asm volatile("s_nop 1" ::: "memory");
  GAP();
}
template <int X, int FIRST, bool NOQK, int... Gs> DI void phaseA_(AC& c, iseq<Gs...>) {
  (gapA<X, FIRST, NOQK, Gs>(c), ...);
}
template <int X, int FIRST, bool NOQK> DI void phaseA(AC& c) { phaseA_<X, FIRST, NOQK>(c, make_iseq<64>{}); }

template <int Y, int F, int QB> DI void pv_mfma() {
  constexpr int r = 192 + 4 * (F & 15), o = 4 * ((F >> 1) * 2 + QB), p = R_P + 16 * Y + 4 * ((F & 1) * 2 + QB);
  asm volatile("v_mfma_f32_16x16x32_bf16 a[%c0:%c1], a[%c2:%c3], v[%c4:%c5], a[%c0:%c1]" ::"i"(o), "i"(o + 3), "i"(r), "i"(r + 3), "i"(p), "i"(p + 3));
}
template <int X, bool SMX, int G> DI void gapB(AC& c) {
  constexpr int Xp = X ^ 1, blk = G >> 4, g = G & 15;
  if constexpr (g == 0) asm volatile("s_waitcnt lgkmcnt(0)");
  if constexpr (SMX && g == 7 && blk < 3) dma_m0<X, blk * 4 + 4>();
  if constexpr (SNK & 2) pv_mfma<Xp, (G >> 2) * 2 + ((g ^ (g >> 1)) & 1), ((g >> 1) ^ (g >> 2)) & 1>();
  else pv_mfma<Xp, (G >> 2) * 2 + (g & 1), (g >> 1) & 1>();
  if constexpr (SMX && (G == 1 || G == 2)) asm volatile("v_add_f32_e32 v%c0, v%c0, v%c1" ::"i"(R_RS + G - 1), "i"(R_TS + G - 1));
  if constexpr (g < 8) {
    if constexpr (blk < 3) vread<Xp, (blk + 1) * 8 + g>();
    else if constexpr (SMX) kread<Xp, g>();
  }
  if constexpr (SMX) {
    if constexpr (g >= 8 && !(g & 1) && blk < 3) dma_at<blk * 4 + 4 + (g - 8) / 2>();
    sm_gap<X, G>(c.redo);
    GAP();
  }
}
template <int X, int... Gs> DI void phaseB_(AC& c, iseq<Gs...>) { (gapB<X, true, Gs>(c), ...); }
template <int X> DI void phaseB(AC& c) { phaseB_<X>(c, make_iseq<64>{}); }
constexpr int sm_nex() { int n = 0; for (int t = 0; t < 64; ++t) n += SM.ex[t] >= 0; return n; }
template <bool ST> DI void phaseB0(bool diag0, unsigned vml) {
  float d[2];
  dma_m0<0, 4>();
  if constexpr (ST) {
    asm volatile("s_waitcnt vmcnt(4)" ::: "memory");
    dma_at<4>(); d_store<8>();
    d[0] = vget<122>(); d[1] = vget<123>();
    dma_at<5>();
  } else {
    kreads<1>(make_iseq<8>{});
    dma_at<4>();
    asm volatile("s_nop 3");
    if (__builtin_expect(diag0, 0))
      static_for([&](auto I) { constexpr int i = I; mask_cmp<16 * i, 95>(); mask_sel<R_S + 8 * i, -1>(); }, make_iseq<4>{});
    d[0] = row_max<0, 0>(); GAP(); dma_at<5>();
    d[1] = row_max<0, 1>(); GAP(); dma_at<6>();
#pragma unroll
    for (int qb = 0; qb < 2; ++qb) if (!(d[qb] > -3.0e38f)) d[qb] = 0.f;
    asm volatile("buffer_store_dwordx2 %0, %1, " SW ", s73 offen offset:1024 sc1" ::"v"((f32x2c){d[0], d[1]}), "v"(vml) : "memory");
  }
  vset<R_M>(d[0]); vset<R_M + 1>(d[1]);
  const f32x2c nd[2] = {{-d[0], -d[0]}, {-d[1], -d[1]}};
  static_for([&](auto E) {
    constexpr int e = E;
    const f32x2c v = nd[(e >> 1) & 1];
    asm volatile("v_pk_add_f32 v[%c0:%c1], v[%c0:%c1], %4\n\tv_mov_b64 v[%c2:%c3], %4" ::"i"(R_S + 2 * e), "i"(R_S + 2 * e + 1), "i"(R_CI + 2 * e), "i"(R_CI + 2 * e + 1), "v"(v));
    if constexpr (ST && e == 0) d_store<9>();
    if constexpr (ST && e == 2) dma_at<6>();
    if constexpr (e == (ST ? 5 : 3)) dma_at<7>();
    if constexpr (e == 6) dma_m0<0, 8>();
    if constexpr (e == 7 || e == 11 || e == 15) dma_at<8 + (e - 7) / 4>();
    if constexpr (ST && (e == 4 || e == 9)) d_store<10 + e / 8>();
  }, make_iseq<16>{});
  static_assert(sm_nex() >= 16 && SM.op0[64] >= 3 * ((sm_nex() - 15) / 2) && SM.op0[64] <= 24);
  static_for([&](auto E) {
    constexpr int e = E;
    asm volatile("v_exp_f32 v%c0, v%c1" ::"i"(R_E + e), "i"(R_S + e));
    if constexpr (e % 3 == 0 && e < 15) acc_zero<e / 3>();
    if constexpr (e == 3) dma_at<11>();
    if constexpr (e == 6) dma_m0<0, 12>();
    if constexpr (e == 7 || e == 11 || e == 15) dma_at<12 + (e - 7) / 4>();
    if constexpr (ST && (e == 1 || e == 9)) d_store<12 + e / 8>();
  }, make_iseq<16>{});
  if constexpr (ST) {
    d_store<14>();
    asm volatile("s_barrier" ::: "memory");
    kreads<1>(make_iseq<8>{});
  }
  static_for([&](auto O) { sm_op<0, O>(); if constexpr (O % 7 == 1 && O < (ST ? 14 : 21)) acc_zero<5 + O / 7>(); }, make_iseq<SM.op0[64]>{});
  dma_at<15>();
  if constexpr (ST) { d_store<15>(); acc_zero<7>(); }
  static_for([&](auto E) { asm volatile("v_exp_f32 v%c0, v%c1" ::"i"(R_E + (int)E), "i"(R_S + 16 + (int)E)); }, make_iseq<sm_nex() - 16>{});
  GAP();
}
struct EP { float fa[2], fb[2]; };
template <int I, int E, int MERGE, int TB = 96> DI void epi_pair(const EP& p) {
  constexpr int qb = I & 1, r = 16 * (I >> 1) + 4 * qb + (E >> 1) * 8 + (E & 1) * 2, t = TB + 2 * E, w = 128 + 4 * I + E;
  if constexpr (MERGE == 2) asm volatile("v_accvgpr_read_b32 v%c0, a[%c2]\n\tv_accvgpr_read_b32 v%c1, a[%c3]" ::"i"(t), "i"(t + 1), "i"(r), "i"(r + 1));
  else
    asm volatile("v_accvgpr_read_b32 v%c0, a[%c2]\n\tv_accvgpr_read_b32 v%c1, a[%c3]\n\tv_mul_f32 v%c0, v%c0, %4\n\tv_mul_f32 v%c1, v%c1, %4"
                 ::"i"(t), "i"(t + 1), "i"(r), "i"(r + 1), "v"(p.fa[qb]));
  if constexpr (MERGE == 1)
    asm volatile("v_lshlrev_b32 v104, 16, v%c2\n\tv_and_b32 v105, 0xffff0000, v%c2\n\tv_fmac_f32 v%c0, v104, %3\n\tv_fmac_f32 v%c1, v105, %3"
                 ::"i"(t), "i"(t + 1), "i"(w), "v"(p.fb[qb]));
  asm volatile("v_cvt_pk_bf16_f32 v%c0, v%c1, v%c2" ::"i"(w), "i"(t), "i"(t + 1));
}
template <int I> DI void ctx_store() {
  asm volatile("buffer_store_dwordx4 v[%c0:%c1], v85, " SW ", s76 offen offset:%c2 sc1\n\tds_write_b128 v85, v[%c0:%c1] offset:%c3"
               ::"i"(128 + 4 * I), "i"(131 + 4 * I), "i"((I & 2) * 1024), "i"((I & 1) * 32768 + (I >> 1) * 2048) : "memory");
  if constexpr (I & 2) asm volatile("s_add_u32 s76, s76, %0" ::"i"(I == 14 ? 0x5000 : 0x1000) SCC1);
}
constexpr int mg_reg(int L, int I) { return I >= 8 ? 8 + 4 * I : I >= 4 ? R_CI + 8 * (I - 4) : R_S + 32 * L + 8 * I; }
template <int L, bool MG, int G> DI void gapAD(AC& c) {
  constexpr int kb = G >> 3, s = G & 7, db = ((G >> 1) + 8) & 15, o = 4 * (db * 2 + (G & 1)), a = 72 + 2 * (db & 1), b = mg_reg(L, (db >> 1) * 2 + (G & 1));
  if constexpr (G == 0) asm volatile("s_waitcnt lgkmcnt(0)");
  else if constexpr (G >= 8 && G < 28 && !(G & 1)) asm volatile("s_waitcnt lgkmcnt(%0)" ::"i"(G <= 24 ? 6 : 4));
  else if constexpr (G == 28) asm volatile("s_waitcnt vmcnt(16) lgkmcnt(0)\n\ts_barrier" ::: "memory");
  qk_mfma<L, kb, 1, s, false>();
  if constexpr (MG && G < 8) asm volatile("ds_read_b128 v[%c0:%c1], v85 offset:%c2" ::"i"(mg_reg(L, G)), "i"(mg_reg(L, G) + 3), "i"(G * 1024));
  if constexpr (G < 24) kread<L, 8 + G>();
  if constexpr (G < 24 && s == 1) mask_cmp<16 * (kb + 1), 126>();
  if constexpr (G < 24 && s == 3) mask_sel<R_CI + 8 * (kb + 1) + 4, R_M + 1>();
  if constexpr (G >= 4 && G < 28 && G % 3 == 2) w_load<(G - 4) / 3>();
  if constexpr (G == 25) asm volatile("s_add_u32 s82, s83, 0x1000000" SCC3);
  if constexpr (G == 27) dma_m0<L, 0>();
  if constexpr (G >= 28) { vread<L ^ 1, 2 * (G - 28)>(); vread<L ^ 1, 2 * (G - 28) + 1>(); dma_at<G - 28>(); }
  sm_gap<L ^ 1, 64 + 2 * G>(c.redo);
  if constexpr (MG) asm volatile("v_mfma_f32_16x16x32_bf16 a[%c0:%c1], v[%c2:%c3], v[%c4:%c5], a[%c0:%c1]" ::"i"(o), "i"(o + 3), "i"(a), "i"(a + 3), "i"(b), "i"(b + 3));
  sm_gap<L ^ 1, 65 + 2 * G>(c.redo);
  GAP();
}
template <int L, bool MG, int... Gs> DI void phaseAD(AC& c, iseq<Gs...>) {
  sld<72, B_NXT, 1>();
  mask_cmp<0, 126>(); mask_sel<R_CI + 4, R_M + 1>();
  (gapAD<L, MG, Gs>(c), ...);
}
#define BDX 8
constexpr int bd_w(bool EPI, int G) { return EPI && G >= 19 && G % 4 == 3 && (G - 19) / 4 < BDX ? 16 + (G - 19) / 4 : -1; }
template <int L, bool EPI, int G> DI void gapBD(AC& c, const EP& p) {
  constexpr int g = G & 15, e = (G >> 4) * 8 + (g - 8), o = e - 3;
  if constexpr (G == 1) dma_m0<L, 4>();
  if constexpr (G == 8) asm volatile("s_waitcnt vmcnt(14)" ::: "memory");
  if constexpr (G == 16) asm volatile("s_waitcnt lgkmcnt(0)\n\ts_barrier" ::: "memory");
  gapB<L, false, G>(c);
  if constexpr (G >= 8 && G < 12)
    asm volatile("ds_read_b128 a[%c0:%c1], v90 offset:%c4\n\tds_read_b128 a[%c2:%c3], v90 offset:%c5" ::"i"(96 + 8 * G), "i"(99 + 8 * G), "i"(100 + 8 * G), "i"(103 + 8 * G),
                 "i"((L ^ 1) * 32768 + (G - 8) * 2048), "i"((L ^ 1) * 32768 + (G - 8) * 2048 + 1024));
  if constexpr (G == 1 || G == 2) asm volatile("v_add_f32_e32 v%c0, v%c0, v%c1" ::"i"(R_RS + G - 1), "i"(R_TS + G - 1));
  if constexpr (G >= 3 && G < 19 && G % 4 == 3) dma_at<4 + (G - 3) / 4>();
  if constexpr (!EPI && G == 17)
    asm volatile("s_cmp_eq_u32 s97, 0\n\ts_cbranch_scc1 1f\n\ts_add_u32 s64, s100, 4 * 800\n\ts_addc_u32 s65, s101, 0\n\ts_swappc_b64 s[98:99], s[64:65]\n1:" ::: SCC_ "memory");
  if constexpr (bd_w(EPI, G) >= 0) w_load<bd_w(EPI, G)>();
  if constexpr (g >= 8 && e < 16) asm volatile("v_exp_f32 v%c0, v%c1" ::"i"(R_E + e), "i"(R_S + 32 * L + 4 + 8 * (e >> 2) + (e & 3)));
  if constexpr (g >= 8 && o >= 1 && o < 16) {
    if constexpr (o == 1) asm volatile("v_add_f32_e32 v%c0, v%c1, v%c2" ::"i"(R_TS + 1), "i"(R_E), "i"(R_E + 1));
    else asm volatile("v_add_f32_e32 v%c0, v%c0, v%c1" ::"i"(R_TS + 1), "i"(R_E + o));
    if constexpr (o & 1) asm volatile("v_cvt_pk_bf16_f32 v%c0, v%c1, v%c2" ::"i"(p_reg(L, (o >> 2) * 4 + 2 + ((o >> 1) & 1))), "i"(R_E + o - 1), "i"(R_E + o));
  }
  if constexpr (G == 45) asm volatile("v_cmp_nle_f32_e64 %0, v%c1, v%c2" : "=s"(c.redo) : "i"(R_TS + 1), "i"(R_THR));
  if constexpr (EPI && G >= 10 && G < 58 && !(G & 1)) epi_pair<((G - 10) >> 3) * 2, ((G - 10) >> 1) & 3, 0, 224>(p);
  if constexpr (EPI && G >= 17 && G < 58 && (G & 7) == 1) ctx_store<((G - 17) >> 3) * 2>();
  GAP();
}
template <int L, bool EPI, int... Gs> DI void phaseBD(AC& c, const EP& p, iseq<Gs...>) {
  if constexpr (!EPI) { sld<96, B_LM, 1>(); sld<97, B_LM + 1, 1>(); asm volatile("s_nop 0"); fl_check(); }
  (gapBD<L, EPI, Gs>(c, p), ...);
}

#define ACLOB_ALL "a0","a1","a2","a3","a4","a5","a6","a7","a8","a9","a10","a11","a12","a13","a14","a15","a16","a17","a18","a19","a20","a21","a22","a23","a24","a25","a26","a27","a28","a29","a30","a31","a32","a33","a34","a35","a36","a37","a38","a39","a40","a41","a42","a43","a44","a45","a46","a47","a48","a49","a50","a51","a52","a53","a54","a55","a56","a57","a58","a59","a60","a61","a62","a63","a64","a65","a66","a67","a68","a69","a70","a71","a72","a73","a74","a75","a76","a77","a78","a79","a80","a81","a82","a83","a84","a85","a86","a87","a88","a89","a90","a91","a92","a93","a94","a95","a96","a97","a98","a99","a100","a101","a102","a103","a104","a105","a106","a107","a108","a109","a110","a111","a112","a113","a114","a115","a116","a117","a118","a119","a120","a121","a122","a123","a124","a125","a126","a127","a128","a129","a130","a131","a132","a133","a134","a135","a136","a137","a138","a139","a140","a141","a142","a143","a144","a145","a146","a147","a148","a149","a150","a151","a152","a153","a154","a155","a156","a157","a158","a159","a160","a161","a162","a163","a164","a165","a166","a167","a168","a169","a170","a171","a172","a173","a174","a175","a176","a177","a178","a179","a180","a181","a182","a183","a184","a185","a186","a187","a188","a189","a190","a191","a192","a193","a194","a195","a196","a197","a198","a199","a200","a201","a202","a203","a204","a205","a206","a207","a208","a209","a210","a211","a212","a213","a214","a215","a216","a217","a218","a219","a220","a221","a222","a223","a224","a225","a226","a227","a228","a229","a230","a231","a232","a233","a234","a235","a236","a237","a238","a239","a240","a241","a242","a243","a244","a245","a246","a247","a248","a249","a250","a251","a252","a253","a254","a255"

template <int I> DI void epi_store() {
  asm volatile("buffer_store_dwordx4 v[%c0:%c1], v90, " SW ", s%c2 offen offset:%c3 sc1" ::"i"(128 + 4 * I), "i"(131 + 4 * I), "i"(76 + (I >> 2)), "i"((I & 3) * 1024) : "memory");
}
template <int HI> DI void epi_init() {
  asm volatile("v_readlane_b32 s76, v93, %c0\n\ts_nop 0\n\ts_add_u32 s77, s76, 0x1000\n\ts_add_u32 s78, s76, %1\n\ts_add_u32 s79, s78, 0x1000" ::"i"(B_SO), "i"(HI) SCC1);
}
template <int I> DI void epi_pread() {
  asm volatile("ds_read_b128 v[%c0:%c1], v107 offset:%c2" ::"i"(128 + 4 * I), "i"(131 + 4 * I), "i"(I * 1024));
}
template <int... Is> DI void epi_preads(iseq<Is...>) { (epi_pread<Is>(), ...); }
template <int G> DI void gapF(AC& c, const EP& p) {
  if constexpr (G < 64) gapB<ODD, false, G>(c);
  if constexpr (G == 1) asm volatile("v_mov_b32 v93, v94\n\ts_brev_b32 s73, 1");
  if constexpr (G >= 3 && G < 10) att_item<G - 3>();
  if constexpr (G >= 10) epi_pair<(G - 10) / 4, (G - 10) & 3, 2>(p);
  if constexpr (G >= 14 && ((G - 14) & 3) == 0) epi_store<(G - 14) / 4>();
  if constexpr (G == 73) epi_store<15>();
  GAP();
}
template <int... Gs> DI void phaseF(AC& c, const EP& p, iseq<Gs...>) { (gapF<Gs>(c, p), ...); }
constexpr int fd_w(bool EPI, int G) {
  const int f0 = EPI ? 16 + BDX : 16, n = NWF - f0;
  for (int i = 0; i < n; ++i) if (G == (EPI ? 2 + (i * 37) / n : i < 16 ? 2 + 2 * i : 4 + 2 * i)) return f0 + i;
  return -1;
}
#define FDA 50
#define FDE 20
constexpr int fd_cnt(bool EPI, int G0, int G1) { int n = 0; for (int G = G0; G < G1; ++G) n += (fd_w(EPI, G) >= 0) + ((G & 3) == 3); return n; }
constexpr bool fd_ok() {
  for (int G = 0; G < 64; ++G) {
    const int a = fd_w(0, G), b = fd_w(1, G);
    if ((a >= 16 && G < 2 * (a & 15) + (a >= 32 ? 34 : 2)) || (b >= 28 && G < (b >= 32 ? 2 * (b - 32) + 10 : 2 * (b - 28) + 2))) return false;
  }
  return true;
}
static_assert(fd_ok() && NWF == 40 && BDX <= 12);
template <int L, bool EPI, int G> DI void gapFD(const EP& p) {
  constexpr int I = EPI ? (G < 8 ? 12 + (G >> 2) * 2 : ((G - 8) >> 2) * 2 + 1) : G < 32 ? (G >> 2) * 2 : ((G - 32) >> 2) * 2 + 1;
  constexpr int u = EPI ? G : G - 2, uf = SNK & 2 ? u ^ ((u >> 1) & 1) : u;
  if constexpr (u >= 16 && u < 32 && (u & 3) == 0) asm volatile("s_waitcnt lgkmcnt(%0)" ::"i"(28 - u));
  if constexpr (u >= 0 && u < 32) pv_mfma<L, uf, 1>();
  if constexpr (u >= 0 && u < 16) vread<L, uf + 16>();
  constexpr int F = fd_w(EPI, G);
  if constexpr (F >= 0) w_load<F>();
  epi_pair<I, G & 3, !EPI>(p);
  if constexpr ((G & 3) == 3) ctx_store<I>();
  if constexpr (G == (EPI ? FDE : FDA)) {
    asm volatile("s_waitcnt vmcnt(%0)\n\ts_barrier" ::"i"(fd_cnt(EPI, EPI ? 8 : 32, G + 1)) : "memory");
    set_flag<A_FC>();
  }
  GAP();
}
template <int L, bool EPI, int... Gs> DI void phaseFD(const EP& p, iseq<Gs...>) { (gapFD<L, EPI, Gs>(p), ...); }
#define A_CT2 "v_readlane_b32 s63, v93, 3\n\ts_sub_u32 s64, s100, at_event - at_tail\n\ts_subb_u32 s65, s101, 0\n\ts_mov_b32 s66, at_event - at_tail\n\ts_mov_b32 s67, s59\n\t" \
    "v_lshl_add_u32 v78, v89, 9, s63\n\ts_mov_b64 exec, 0x1f\n\tbuffer_load_dword v84, v78, s[64:67], 0 offen\n\ts_mov_b64 exec, -1\n\t"
#define A_CT4 "s_mov_b64 s[64:65], s[100:101]\n\ts_movk_i32 s66, 0xf00\n\ts_mov_b32 s67, s59\n\tv_lshlrev_b32 v78, 3, v89\n\tbuffer_load_dword v84, v78, s[64:67], 0 offen\n\t"
#define A_RET "s_setpc_b64 s[98:99]\n\t"
#define A_EFLAG(L) "v_readlane_b32 s63, v93, " #L "\n\ts_nop 3\n\t" A_FLAG
#define A_EPOLL "v_readlane_b32 s96, v93, s85\n\ts_add_u32 s85, s85, 1\n\tv_readlane_b32 s97, v93, s85\n\ts_add_u32 s85, s85, 1\n\t" A_POLL("v[122:123]", "s[96:97]", "s62")
#define A_Q1 "buffer_load_dwordx4 v87, s[64:67], s95 offen lds\n\ts_add_u32 m0, m0, 0x400\n\ts_add_u32 s95, s95, 0x1c00\n\t"
#define A_EPOLL1 "s_mov_b32 exec_lo, 0xffff0000\n\ts_mov_b32 exec_hi, 0xffff\n\tbuffer_load_dwordx2 v[124:125], v88, " SW ", s92 offen sc1\n\ts_mov_b64 exec, -1\n\t" A_CT2
#define A_Z1(K, O) "v_xor_b32 v49, " K ", v48\n\tbuffer_load_dwordx4 v49, " SW ", s63 offen offset:" O " sc1 lds\n\t"
#define A_EQ "v_readlane_b32 s68, v92, 30\n\tv_readlane_b32 s95, v92, 28\n\ts_mov_b64 s[64:65], s[88:89]\n\ts_mov_b32 s66, 0x1c0000\n\ts_mov_b32 s67, s59\n\t" \
    "s_add_u32 m0, s68, 0x1000\n\ts_nop 1\n\t" A_Q1 A_Q1 A_Q1 A_Q1
#define A_ELOAD "v_readlane_b32 s68, v92, 30\n\tv_readlane_b32 s63, v92, 29\n\tv_lshrrev_b32 v48, 5, v89\n\ts_mov_b32 s96, 0\n\ts_mov_b32 s97, 0x3fff0000\n\t" \
    "v_and_b32 v48, 16, v48\n\t" A_CHECK("v[124:125]", "s[96:97]", "s92") "v_xor_b32 v48, v48, v89\n\ts_mov_b32 m0, s68\n\t" \
    A_Z1("0", "0") A_Z1("32", "1024") A_Z1("64", "2048") "s_mov_b32 exec_hi, 0\n\t" A_Z1("0x60", "3072") "s_mov_b32 exec_hi, -1\n\t"
#define A_PZ2(Z0, Z1, O) "ds_read_u16_d16_hi " Z0 ", v46 offset:" O "\n\tds_read_u16_d16_hi " Z1 ", v47 offset:" O "\n\t"
#define A_PRD(X, Q, O, Z0, Z1, Z2, Z3, Z4, Z5, Z6, Z7) "v_xor_b32 v46, " X ", v44\n\tds_read_b128 " Q ", v45 offset:" O "\n\tv_xor_b32 v47, 64, v46\n\t" \
    A_PZ2(Z0, Z1, "0") A_PZ2(Z2, Z3, "128") A_PZ2(Z4, Z5, "256") A_PZ2(Z6, Z7, "384")
#define A_PM2(Q, Z0, Z1) "v_lshlrev_b32 v40, 16, " Q "\n\tv_and_b32 v41, 0xffff0000, " Q "\n\tv_mul_f32 v40, v40, " Z0 "\n\tv_mul_f32 v41, v41, " Z1 "\n\tv_cvt_pk_bf16_f32 " Q ", v40, v41\n\t"
#define A_PST(Q) "buffer_store_dwordx4 " Q ", v43, s[64:67], s63 offen\n\ts_add_u32 s63, s63, 0x3800\n\t"
#define A_PA "v64", "v65", "v66", "v67", "v68", "v69", "v70", "v71"
#define A_PB "v72", "v73", "v74", "v75", "v76", "v77", "v78", "v79"
#define A_PRD_(...) A_PRD(__VA_ARGS__)
#define A_EPOST "v_readlane_b32 s68, v92, 30\n\tv_lshrrev_b32 v40, 4, v89\n\tv_readlane_b32 s64, v92, 0\n\tv_mul_u32_u24 v41, 37, v40\n\tv_readlane_b32 s65, v92, 1\n\t" \
    "v_lshrrev_b32 v41, 8, v41\n\tv_readlane_b32 s63, v94, 23\n\tv_mad_i32_i24 v42, v41, -7, v40\n\ts_movk_i32 s66, 0x380\n\ts_movk_i32 s67, 0x210\n\t" \
    "v_mad_u32_u24 v43, v41, s66, v87\n\tv_lshl_add_u32 v44, v41, 1, s68\n\tv_add_u32 v45, s68, v89\n\tv_mad_u32_u24 v44, v42, s67, v44\n\t" \
    "v_mov_b64 v[64:65], 0\n\tv_mov_b64 v[66:67], 0\n\tv_mov_b64 v[68:69], 0\n\tv_mov_b64 v[70:71], 0\n\t" \
    "v_mov_b64 v[72:73], 0\n\tv_mov_b64 v[74:75], 0\n\tv_mov_b64 v[76:77], 0\n\tv_mov_b64 v[78:79], 0\n\ts_mov_b32 s66, 0x380000\n\ts_mov_b32 s67, s59\n\t" \
    A_PRD_("0", "v[48:51]", "4096", A_PA) A_PRD_("16", "v[52:55]", "5120", A_PB) "s_waitcnt lgkmcnt(9)\n\t" \
    A_PM2("v48", "v64", "v65") A_PM2("v49", "v66", "v67") A_PM2("v50", "v68", "v69") A_PM2("v51", "v70", "v71") A_PST("v[48:51]") \
    A_PRD_("32", "v[56:59]", "6144", A_PA) "s_waitcnt lgkmcnt(9)\n\t" \
    A_PM2("v52", "v72", "v73") A_PM2("v53", "v74", "v75") A_PM2("v54", "v76", "v77") A_PM2("v55", "v78", "v79") A_PST("v[52:55]") \
    A_PRD_("48", "v[60:63]", "7168", A_PB) "s_waitcnt lgkmcnt(9)\n\t" \
    A_PM2("v56", "v64", "v65") A_PM2("v57", "v66", "v67") A_PM2("v58", "v68", "v69") A_PM2("v59", "v70", "v71") A_PST("v[56:59]") "s_waitcnt lgkmcnt(0)\n\t" \
    A_PM2("v60", "v72", "v73") A_PM2("v61", "v74", "v75") A_PM2("v62", "v76", "v77") A_PM2("v63", "v78", "v79") A_PST("v[60:63]")
#define A_TR(R0, R1, R2, R3, O0, O1) "ds_read_b64_tr_b16 " R0 ", v85 offset:" O0 "\n\tds_read_b64_tr_b16 " R1 ", v85 offset:" O1 "\n\t" \
    "ds_read_b64_tr_b16 " R2 ", v84 offset:" O0 "\n\tds_read_b64_tr_b16 " R3 ", v84 offset:" O1 "\n\t"
#define A_WS(R, S, O) "buffer_store_dwordx4 " R ", v89, " SW ", " S " offen offset:" O " sc1\n\t"
#define A_PL1(O) "buffer_load_dwordx4 v89, " SW ", s63 offen offset:" O " sc1 lds\n\t"
#define A_PL2(R, O) "buffer_load_dwordx4 " R ", v89, " SW ", s95 offen offset:" O " sc1\n\t"
#define A_EPLOAD "v_readlane_b32 s68, v92, 30\n\tv_readlane_b32 s63, v93, 21\n\ts_mov_b32 m0, s68\n\ts_add_u32 s95, s63, 0x8000\n\ts_nop 2\n\t" \
    A_PL1("0") A_PL1("1024") A_PL1("2048") A_PL1("3072") "s_bitset1_b32 m0, 12\n\ts_add_u32 s63, s63, 0x1000\n\t" A_PL1("0") A_PL1("1024") A_PL1("2048") A_PL1("3072") \
    A_PL2("v[40:43]", "0") A_PL2("v[44:47]", "1024") A_PL2("v[48:51]", "2048") A_PL2("v[52:55]", "3072") "s_add_u32 s95, s95, 0x1000\n\t" \
    A_PL2("v[56:59]", "0") A_PL2("v[60:63]", "1024") A_PL2("v[64:67]", "2048") A_PL2("v[68:71]", "3072") \
    "v_and_b32 v78, 0xf0, v89\n\tbuffer_load_dwordx4 v[72:75], v78, " SW ", s73 offen sc1\n\t"
#define A_EPOLLZ "s_mov_b32 exec_lo, 0\n\ts_mov_b32 exec_hi, 0x3fff0000\n\tbuffer_load_dwordx2 v[124:125], v88, " SW ", s92 offen sc1\n\ts_mov_b64 exec, -1\n\t"
#define A_ECHK1 "s_mov_b32 s70, 0xffff0000\n\ts_mov_b32 s71, 0xffff\n\t" A_CHECK("v[124:125]", "s[70:71]", "s92")
#define A_AT(N) ".fill (at_event + 4 * " #N " - .) / 4, 4, 0xbf800000\n\t"
#define A_EVENT \
    ".p2align 6\nat_event:\n\t" A_RET \
    A_AT(16) "v_cmp_eq_u64 vcc, s[60:61], v[122:123]\n\ts_and_b64 vcc, vcc, s[96:97]\n\ts_cmp_eq_u64 vcc, s[96:97]\n\ts_cbranch_scc0 7f\n\t" A_RET \
    A_AT(32) A_EFLAG(5) A_CT4 A_RET \
    A_AT(48) A_EFLAG(5) A_AT(56) A_EPOLL A_RET \
    A_AT(80) A_EFLAG(4) A_RET \
    A_AT(96) A_EFLAG(5) A_CT4 A_EPOLL A_RET \
    A_AT(128) "v_xor_b32 v84, 32, v85\n\tv_readlane_b32 s63, v92, 27\n\t" \
    A_TR("v[48:49]", "v[50:51]", "v[52:53]", "v[54:55]", "0", "4096") A_TR("v[56:57]", "v[58:59]", "v[60:61]", "v[62:63]", "8192", "12288") \
    A_TR("v[64:65]", "v[66:67]", "v[68:69]", "v[70:71]", "16384", "20480") A_TR("v[72:73]", "v[74:75]", "v[76:77]", "v[78:79]", "24576", "28672") \
    "s_add_u32 s95, s63, 0x1000\n\ts_waitcnt lgkmcnt(8)\n\t" \
    A_WS("v[48:51]", "s63", "0") A_WS("v[52:55]", "s63", "1024") A_WS("v[56:59]", "s63", "2048") A_WS("v[60:63]", "s63", "3072") "s_waitcnt lgkmcnt(0)\n\t" \
    A_WS("v[64:67]", "s95", "0") A_WS("v[68:71]", "s95", "1024") A_WS("v[72:75]", "s95", "2048") A_WS("v[76:79]", "s95", "3072") A_RET \
    A_AT(192) A_EPOLL1 A_RET \
    A_AT(228) A_EPOLL A_AT(240) A_EPOLL1 \
    A_MASK("v224", "v225", "v226", "v227", "0", "1", "2", "3") \
    A_MASK("v232", "v233", "v234", "v235", "16", "17", "18", "19") \
    A_MASK("v240", "v241", "v242", "v243", "32", "33", "34", "35") \
    A_MASK("v248", "v249", "v250", "v251", "48", "49", "50", "51") A_RET \
    "7:\n\t" A_SPIN("v[122:123]", "s[96:97]", "s62") A_RET \
    A_AT(344) A_EFLAG(4) A_EPOLLZ A_EQ A_RET \
    A_AT(400) A_ELOAD A_RET \
    A_AT(448) A_EFLAG(5) A_CT4 A_EPOLLZ A_EQ A_RET \
    A_AT(512) A_EPOST A_RET \
    A_AT(768) "5:\n\t" A_CHECK("v[122:123]", "s[96:97]", "s62") "s_branch 6f\n\t" A_AT(800) "6:\n\t" A_EPLOAD A_RET \
    A_AT(872) A_ECHK1 A_RET \
    A_AT(900) A_ECHK1 "s_branch 5b\n\t"
static_assert(B_CT == 3 && B_FSET == 5 && B_FW == 4 && A_M0F == 30 && A_PQ == 28 && A_PZ == 29 && A_WDST == 27 && C_PO == 23 && B_LP == 21);
DI void event() {
  asm volatile("s_and_b32 s63, s87, 0x3ff\n\ts_lshl2_add_u32 s64, s63, s100\n\ts_addc_u32 s65, s101, 0\n\ts_swappc_b64 s[98:99], s[64:65]" ::: "vcc", SCC_ "memory",
               "v78", "v79");
  GAP();
}

#define OFU 20
static_assert(OFU >= 4 && OFU < 28 && OFU % 2 == 0);
#define OGB 14
constexpr int o_qo(int P) { return O_SEQ[P] & 3; }
constexpr int o_o(int P) { return O_SEQ[P] >> 4; }
constexpr int o_ks(int k) { return k % 7 < 4 ? 4 + k % 7 : k % 7 - 4; }
constexpr int o_sq(int P) {
  for (int q = P; q < 16; ++q) if (P < 1 || o_qo(q) == o_qo(P - 1)) return -1;
  return o_qo(P - 1);
}
constexpr int o_last() { int l = 0; for (int P = 0; P < 16; ++P) if (o_sq(P) >= 0) l = P; return l; }
constexpr bool o_first(int P) { for (int q = 0; q < P; ++q) if (o_qo(q) == o_qo(P)) return false; return true; }
template <int P, int MB, int KK> DI void o_read() {
  constexpr int r = 128 + 4 * (8 * MB + KK), sl = o_k(P) < 0 ? o_qo(P) : o_ks(o_k(P));
  asm volatile("ds_read_b128 v[%c0:%c1], v%c2 offset:%c3" ::"i"(r), "i"(r + 3), "i"(sl < 4 ? 96 + sl : 86), "i"((sl < 4 ? 0 : (sl & 3) * 16384) + (2 * KK + MB) * 1024));
}
template <int... Is> DI void o_read0(iseq<Is...>) { (o_read<0, (Is >> 3), Is & 7>(), ...); }
constexpr int o_gb(int NF) { return NF == 2 ? OGB : 0; }
#define OTAB {40, 44, 48, -1}, {52, -1, 0, 1}, {56, -1, 2, 60}, {-1, -1, 3, 4}, {-1, -1, 5, 6}, {-1, -1, 7, -1}, {-1, -1, 8, -1}, {-1, -1, 9, -1}, {-1, -1, 10, -1}, {-1, -1, 11, -1}
constexpr int O_TAB[][4] = {OTAB};
constexpr int o_op(int P, int U, int NF) {
  const int c = U / 8 == 1 && o_sq(P) >= 0 ? 99 : P < (int)(sizeof(O_TAB) / 16) ? O_TAB[P][U / 8] : -1, i = (U & 7) / 2;
  if (c < 0 || (U & 1)) return 0;
  if (c < 32) return 200 + 4 * c + i;
  if (c == 99) return i == 3 || (i == 1 && NF == 2) ? 300 + i : 0;
  return NF == 1 && (i & 1) ? 0 : 100 + c + i;
}
constexpr int o_vm(int P, int NF) {
  int n = 0;
  for (int i = 0; i <= P; ++i) {
    for (int U = 0; U < (i < P ? 32 : o_gb(NF)); ++U) {
      const int op = o_op(i, U, NF);
      if (op) ++n;
      if (op >= 200 && op < 300 && (op - 200) / 4 == o_k(P + 1)) n = 0;
      if (op >= 100 && op < 200 && (op - 100) / 16 == o_o(P + 1)) n = 0;
    }
  }
  return n;
}
constexpr bool o_ok() {
  for (int P = 1; P < 16; ++P) {
    for (int i = P - 1; i < 16; ++i) for (int U = 0; U < 32; ++U) {
      const int op = o_op(i, U, 2);
      if (i > P - 1 || U >= 0) {
        if (op >= 200 && op < 300 && (op - 200) / 4 == o_k(P)) return false;
        if (op >= 100 && op < 200 && (op - 100) / 16 == o_o(P)) return false;
      }
    }
  }
  for (int i = 0; i < 16; ++i) for (int U = 0; U < 32; ++U) {
    const int op = o_op(i, U, 2);
    if (op >= 200 && op < 300) {
      const int k = (op - 200) / 4, sl = o_ks(k);
      int prev = -1;
      for (int P = 0; P < 16; ++P) if ((o_k(P) < 0 ? o_qo(P) : o_ks(o_k(P))) == sl && (o_k(P) < k)) prev = P;
      if (prev >= 0 && !(i > prev + 1 || (i == prev + 1 && U >= 16))) return false;
    }
  }
  return true;
}
static_assert(NWF == 40 && o_ok());
template <int K> DI void o_m0() {
  constexpr int sl = o_ks(K);
  if constexpr (sl < 4) asm volatile("s_add_u32 m0, s80, s%c0" ::"i"(64 + sl) SCC1);
  else asm volatile("s_add_u32 m0, s80, %0" ::"i"(sl * 16384) SCC1);
}
template <int NF, int SR, int C> DI void o_pack() {
  if constexpr (NF == 2 || !(C & 2))
    asm volatile("v_cvt_pk_bf16_f32 v%c0, v%c1, v%c2" ::"i"(NF == 2 ? SR + C : SR + (C >> 2) * 2 + (C & 1)), "i"(SR + 2 * C), "i"(SR + 2 * C + 1));
}
template <int SR, int F> DI void o_sst() {
  asm volatile("buffer_store_dwordx4 v[%c0:%c1], v89, " SW ", s77 offen offset:%c2 sc1" ::"i"(SR + 4 * F), "i"(SR + 4 * F + 3), "i"(F * 1024) : "memory");
}
template <int I, int V> DI void o_slab() {
  asm volatile("buffer_load_dwordx4 v[%c0:%c1], v%c4, " SW ", s%c2 offen offset:%c3 sc1" ::"i"(192 + 4 * I), "i"(195 + 4 * I), "i"(64 + I / 2), "i"((I & 1) * 1024), "i"(V) : "memory");
}
template <int NF, int P, int G> DI void o_gap() {
  constexpr int NG = 16 * NF, ch = G >> 3, mb = ch / NF, nfi = (SNK & 16) && NF == 2 ? (ch ^ (ch >> 1)) & 1 : ch % NF, kk = (SNK & 16) && NF == 2 && (ch & 1) ? 7 - (G & 7) : G & 7, sq = o_sq(P), sr = 192 + 16 * sq;
  constexpr int w = o_wreg(o_o(P), 2 * kk + nfi), b = 128 + 4 * (8 * mb + kk), d = 192 + 16 * o_qo(P) + 4 * (2 * mb + nfi);
  constexpr bool nxt = P != 15;
  constexpr int U = NF == 2 ? G : 2 * G, op = o_op(P, U, NF), opn = U < 24 ? o_op(P, U + 8, NF) : nxt ? o_op(P + 1, U - 24, NF) : 0;
  if constexpr (G == 0) asm volatile("s_waitcnt lgkmcnt(8)" ::: "memory");
  if constexpr (G == NG / 2) asm volatile("s_waitcnt lgkmcnt(%0)" ::"i"(nxt && NF == 1 ? 8 : 0) : "memory");
  if constexpr (nxt && G == o_gb(NF) && P >= 1 && (o_k(nxt ? P + 1 : P) >= 0 || P < 3)) asm volatile("s_waitcnt vmcnt(%0)\n\ts_barrier" ::"i"(o_vm(P, NF)) : "memory");
  if constexpr (P == 1 && G == o_gb(NF))
    asm volatile("v_readlane_b32 s63, v92, %c0\n\ts_nop 0\n\ts_add_u32 s63, s63, 32\n\t" A_FLAG ::"i"(A_FC) : SCC_ "memory");
  if constexpr (P == o_last() + 1 && U == OFU) {
    asm volatile("s_waitcnt vmcnt(0)" ::: "memory");
    set_flag<A_FS>();
  }
  if constexpr (op >= 200 && op < 300 && (op & 3) == 0) o_m0<(op - 200) / 4>();
  if constexpr (o_first(P) && !(G & 7))
    asm volatile("v_mfma_f32_16x16x32_bf16 v[%c0:%c1], a[%c2:%c3], v[%c4:%c5], 0" ::"i"(d), "i"(d + 3), "i"(w), "i"(w + 3), "i"(b), "i"(b + 3));
  else
    asm volatile("v_mfma_f32_16x16x32_bf16 v[%c0:%c1], a[%c2:%c3], v[%c4:%c5], v[%c0:%c1]" ::"i"(d), "i"(d + 3), "i"(w), "i"(w + 3), "i"(b), "i"(b + 3));
  if constexpr (nxt && G >= NG - 16) o_read<P + 1, (G >> 3) & 1, kk>();
  if constexpr (op >= 100 && op < 200) w_load<op - 100, 1 + NF>();
  if constexpr (op >= 200 && op < 300)
    asm volatile("buffer_load_dwordx4 v89, " SW ", s%c0 offen offset:%c1 sc1 lds" ::"i"(73 + U / 8), "i"((op & 3) * 1024) : "memory");
  if constexpr (sq >= 0) {
    if constexpr (G == 0) sld<77, A_SS + sq>();
    if constexpr (NF == 2 && G >= 6 && G < 14) o_pack<2, sr, G - 6>();
    if constexpr (NF == 1 && (G == 4 || G == 5)) { o_pack<1, sr, 4 * (G - 4)>(); o_pack<1, sr, 4 * (G - 4) + 1>(); }
    if constexpr (op >= 300) o_sst<sr, NF == 2 ? (op - 300) / 2 : 0>();
  }
  if constexpr (opn >= 200 && opn < 300 && (opn & 3) == 0) sld<73 + ((U + 8) & 31) / 8, A_PC + (opn - 200) / 4>();
  if constexpr (P == 12 && G == NG - 4) { sld<62, A_FSW>(); fl_mask<A_FSM, -1>(); }
  if constexpr (P == 13 && G == 1) fl_issue();
  if constexpr (P == 13 && G >= 2 && G < 5) sld<62 + G, A_SL + G - 2>();
  if constexpr (P == 13 && G == NG - 2)
    asm volatile("v_cmp_eq_u64 vcc, s[60:61], v[122:123]\n\ts_and_b64 vcc, vcc, s[96:97]\n\ts_cmp_eq_u64 vcc, s[96:97]\n\ts_cselect_b32 s67, 0, 0x80000000\n\t"
                 "v_or_b32 v100, s67, v89" ::: "vcc", SCC_ "memory");
  if constexpr (P == 14 && G % NF == 0 && G >= 2 * NF && G < 14 * NF && !((G / NF) & 1) && (NF == 2 || !((G / NF) & 2) == false)) o_slab<G / NF / 2 - 1, 100>();
  GAP();
}
template <int NF, int P, int... Gs> DI void o_interval_(iseq<Gs...>) {
  (o_gap<NF, P, Gs>(), ...);
}
template <int NF, int P> DI void o_interval() { o_interval_<NF, P>(make_iseq<16 * NF>{}); }
template <int NF> DI void o_slabs() {
  unsigned late;
  asm volatile("s_mov_b32 %0, s67" : "=s"(late));
  if (late) {
    fl_wait();
    static_for([&](auto I) { if constexpr (NF == 2 || !(I & 1)) o_slab<I, 89>(); }, make_iseq<6>{});
  }
  GAP();
}
template <int NF> DI void o_sum() {
  static_for([&](auto I) {
    constexpr int i = I, q = i >> 2, f = i & 3, b = 192 + 8 * q + (NF == 2 ? 4 * (f >> 1) : 0), a = 72 + 2 * (NF == 2 ? f & 1 : f >> 1);
    if constexpr (f == 0) asm volatile("s_waitcnt vmcnt(%0)" ::"i"((2 - q) * NF) : "memory");
    if constexpr (NF == 2 || !(f & 1))
      asm volatile("v_mfma_f32_16x16x32_bf16 v[%c0:%c1], v[%c2:%c3], v[%c4:%c5], v[%c0:%c1]" ::"i"(240 + 4 * f), "i"(243 + 4 * f), "i"(a), "i"(a + 3), "i"(b), "i"(b + 3));
  }, make_iseq<12>{});
  asm volatile("s_nop 7");
}

#define NSG 64
#define KWAIT "s_waitcnt lgkmcnt(0)\n\t"
#define TOUCH0 0x20
#define TOUCH1 0x20
__global__ __launch_bounds__(256, 1) __attribute__((amdgpu_num_vgpr(82), amdgpu_num_sgpr(NSG)))
void fused_kernel(const bf16* a0, const bf16* a1, const bf16* a2, const bf16* a3, const bf16* a4, char* ws, const bf16* __restrict__ ak, const bf16* __restrict__ av,
                  const bf16* __restrict__ ash, const bf16* __restrict__ assmk, const bf16* __restrict__ assmD, const bf16* __restrict__ wo,
                  const bf16* __restrict__ aq, bf16* __restrict__ out) {
  int b = blockIdx.x;
  asm volatile("s_load_dwordx8 s[92:99], %1, 0\n\ts_load_dwordx2 s[60:61], %1, 32" : "+s"(b) : "s"(__builtin_amdgcn_kernarg_segment_ptr()));
  const bf16* ckv;
  const int bp = b >> 3;
  extern __shared__ __attribute__((aligned(16))) char lds[];
  asm volatile("at_code_begin:" ::: ACLOB_ALL, "v255", "s101");
  KST(0, 0);
  const int tid = threadIdx.x;
  int lane = tid & 63;
  const int wid = __builtin_amdgcn_readfirstlane(tid >> 6);
  const int j = b & 7, r = b >> 3, rr = r & 15, pj = rr >> 1, kv = pj & 1;
  const bool isY = r >= 16, doA = !isY && pj != NCV;
  const int cr = rr - (pj > NCV ? 2 : 0);
  const bf16* wkv;
  const int dq = Q44 ? (j & 1) - ((rr >> 1) & 1) : 0;
  unsigned vq, vw, tb0, vbA2, vD;
  const char* tp;
  {
    GM g;
    {
      static_assert(Q44 && K2(1) == 3);
#define CTP 0
#define P_CT "s_getpc_b64 s[76:77]\n\tv_mbcnt_lo_u32_b32 v99, -1, 0\n\ts_lshr_b32 s88, %4, 3\n\ts_and_b32 s76, s76, 0xffffff80\n\ts_lshl_b32 s88, s88, 9\n\ts_and_b32 s77, s77, 0xffff\n\t" \
             "s_lshl1_add_u32 s88, s90, s88\n\ts_mov_b32 s78, pf_code_end - at_code_begin\n\ts_mov_b32 s79, 0x20000\n\tv_lshl_add_u32 v99, v99, 14, s88\n\ts_mov_b32 m0, %5\n\t" \
             "s_mov_b64 exec, 0xff\n\tbuffer_load_dword v99, s[76:79], 0 offen lds\n\ts_mov_b64 exec, -1\n\t"
#define P_CTL(R0, R1, R2, R3, RR) "s_getpc_b64 s[" R0 ":" R1 "]\n\tv_mbcnt_lo_u32_b32 v100, -1, 0\n\tv_readfirstlane_b32 s89, %3\n\ts_lshr_b32 s88, %4, 3\n\ts_and_b32 s" R0 ", s" R0 ", 0xffffff80\n\t" \
             "s_lshl_b32 s88, s88, 9\n\ts_and_b32 s" R1 ", s" R1 ", 0xffff\n\ts_lshl1_add_u32 s88, s89, s88\n\ts_mov_b32 s" R2 ", pf_code_end - at_code_begin\n\ts_mov_b32 s" R3 ", 0x20000\n\t" \
             "v_lshl_add_u32 v100, v100, 14, s88\n\ts_mov_b32 m0, %5\n\ts_mov_b64 exec, 0xff\n\tbuffer_load_dword v100, " RR ", 0 offen lds\n\ts_mov_b64 exec, -1\n\t"
#define P_CT0 P_CT
#define P_CTX1
#define P_CTX2
#define P_LA(SO) "buffer_load_dwordx4 v86, s[64:67], " SO " offen" GSC " lds\n\t"
#define P_LW(SO) "buffer_load_dwordx4 v87, s[68:71], " SO " offen" GSC " lds\n\t"
      asm volatile("v_readfirstlane_b32 s90, %3\n\tv_lshrrev_b32 v96, 4, %3\n\t" P_CT0 "v_lshrrev_b32 v97, 3, %3\n\ts_and_b32 s63, %2, 1\n\tv_xor_b32 v98, v96, %3\n\ts_lshl_b32 s88, s63, 2\n\t"
                   "s_bfe_u32 s89, %2, 0x20005\n\tv_and_b32 v98, 7, v98\n\ts_movk_i32 s62, 0x800\n\ts_bitcmp1_b32 %2, 7\n\t"
                   "s_cselect_b32 s88, s88, 0\n\ts_cmovk_i32 s62, 0x380\n\ts_add_u32 s89, s89, s88\n\ts_lshl_b32 s89, s89, 8\n\tv_lshlrev_b32 v98, 4, v98\n\t"
                   "v_add_u32 v97, s89, v97\n\ts_lshl_b32 s90, s90, 4\n\ts_and_b32 s88, %2, 0x80\n\ts_lshl_b32 s88, s88, 8\n\tv_mad_u32_u24 v86, v97, s62, v98\n\t"
                   "s_or_b32 s90, s90, s88\n\ts_lshl_b32 s66, s62, 11\n\ts_mov_b32 s67, 0x20000\n\ts_mov_b32 m0, s90\n\ts_lshl_b32 s72, s62, 5\n\ts_lshl_b32 s75, s62, 7\n\t"
                   "s_add_u32 s76, s75, s72\n\ts_bitcmp1_b32 %2, 7\n\t" KWAIT "s_cselect_b64 s[64:65], s[94:95], s[92:93]\n\t"
                   P_LA("0")
                   "s_add_u32 m0, s90, 0x1000\n\tv_and_b32 v99, 3, v96\n\t" P_LA("s72")
                   "s_add_u32 m0, s90, 0x4000\n\tv_lshrrev_b32 v97, 6, %3\n\t" P_LA("s75")
                   "s_add_u32 m0, s90, 0x5000\n\tv_lshl_or_b32 v99, v99, 2, v97\n\t" P_LA("s76")
                   P_CTX1 "s_bfe_u32 s88, %2, 0x10004\n\tv_xor_b32 v99, v99, %3\n\ts_sub_u32 s63, s63, s88\n\tv_and_b32 v99, 15, v99\n\ts_and_b32 s88, %2, 15\n\t"
                   "v_lshlrev_b32 v99, 4, v99\n\ts_bitcmp1_b32 %2, 7\n\ts_cselect_b32 s63, s63, 0\n\tv_lshl_add_u32 v99, v96, 13, v99\n\ts_sub_u32 s88, s88, s63\n\t"
                   "s_lshl_b32 s88, s88, 9\n\ts_bitcmp1_b32 %2, 4\n\ts_cselect_b64 %1, s[98:99], s[96:97]\n\tv_add_u32 v87, s88, v99\n\ts_bitcmp1_b32 %2, 7\n\t"
                   "s_cselect_b64 s[68:69], s[60:61], %1\n\ts_lshl_b32 s70, s62, 12\n\ts_add_u32 m0, s90, 0x10000\n\ts_mov_b32 s71, 0x20000\n\t"
                   P_LW("0")
                   "s_add_u32 m0, s90, 0x11000\n\ts_mov_b32 s80, 0x20000\n\t" P_LW("s80")
                   "s_add_u32 m0, s90, 0x12000\n\ts_movk_i32 s81, 0x100\n\t" P_LW("s81")
                   "s_add_u32 m0, s90, 0x13000\n\ts_mov_b32 s82, 0x20100\n\t" P_LW("s82")
                   "s_add_u32 m0, s90, 0x14000\n\ts_mov_b32 s83, 0x40000\n\t" P_LW("s83")
                   "s_add_u32 m0, s90, 0x15000\n\ts_mov_b32 s84, 0x60000\n\t" P_LW("s84")
                   "s_add_u32 m0, s90, 0x16000\n\ts_mov_b32 s85, 0x40100\n\t" P_LW("s85")
                   "s_add_u32 m0, s90, 0x17000\n\ts_mov_b32 s86, 0x60100\n\t" P_LW("s86")
                   P_CTX2 "s_mov_b64 %0, s[92:93]\n\ts_lshl_b32 s73, s72, 1\n\ts_add_u32 s74, s73, s72\n\ts_add_u32 s77, s75, s73\n\ts_add_u32 s78, s75, s74\n\t"
                   "s_and_b32 s79, s90, 0xfff\n\ts_add_u32 s87, s79, 0x10000\n\tv_add_u32 v84, 0x80, v86\n\tv_add_u32 v85, 0x80000, v87\n\t"
                   "s_mov_b32 s58, 0x6400000\n\ts_mov_b32 s59, 0x20000\n\ts_xor_b32 s92, s90, 0x8000\n\t"
                   "s_bfe_u32 s88, %2, 0x40003\n\ts_bfe_u32 s89, %2, 0x20005\n\ts_add_u32 s88, s88, s89\n\ts_and_b32 s88, s88, 3\n\ts_bitcmp1_b32 %2, 7\n\t"
                   "s_cselect_b32 s88, 0, s88\n\ts_add_u32 s91, s88, 2"
                   : "=&s"(ckv), "=&s"(wkv) : "s"(b), "v"(tid), "s"(blockIdx.x), "i"(L_SP + 16384) : "scc", "memory");
      GAP();
      asm volatile("" : "+s"(ws), "+s"(ak), "+s"(av), "+s"(ash), "+s"(assmk), "+s"(assmD), "+s"(wo), "+s"(aq), "+s"(out));
    }
    auto addrs = [&]() __attribute__((always_inline)) {
      asm volatile("" : "+v"(lane));
      const int fr = lane & 15, fq = lane >> 4, q = (lane & 15) >> 2, p = lane & 3;
#pragma unroll
      for (int k2 = 0; k2 < 2; ++k2)
        g.aA[k2] = (wid >> 1) * 16384 + (fr >> 3) * 1024 + (fr & 7) * 128 + (((4 * k2 + fq) ^ (fr >> 1)) * 16);
#pragma unroll
      for (int hf = 0; hf < 2; ++hf) {
        const unsigned a0 = 65536 + (wid & 1) * 8192 + off_b(8 * fq + 4 * hf + q, p >> 1) + 8 * (p & 1);
#pragma unroll
        for (int nf = 0; nf < 8; ++nf) { g.aB[hf][nf] = a0 ^ (nf * 32); asm volatile("" : "+v"(g.aB[hf][nf])); }
      }
      GAP();
    };
    auto consts2 = [&]() __attribute__((always_inline)) {
      const int tl = (lane * 37) >> 8, ck = lane - tl * 7;
      vq = lane < 56 ? (tl * DA + ck * 8) * 2 : OOB;
      asm volatile("" : "+v"(vq));
    };
    auto consts = [&](auto Yc) __attribute__((always_inline)) {
      const int fr = lane & 15, fq = lane >> 4;
      if constexpr (Yc) {
        vbA2 = ((((rr >> 2) + 4) * 256 + wid * 8 + (lane >> 3)) * DC + ((lane & 7) ^ (((wid & 1) * 4 + fq) & 7)) * 8) * 2;
        asm volatile("" : "+v"(vbA2));
      } else {
        consts2();
        vw = (4 * wid + fq) * DA * 2 + 16 * (fr ^ ((fq << 2) | wid));
        tb0 = L_SP + off_b(4 * fq + (fr >> 2), wid * 4 + ((lane & 3) >> 1)) + 8 * (lane & 1);
        tp = (lane < 32 && doA ? (const char*)assmk + (j * 56 + cr * 4 + wid) * 4096 : (const char*)ckv + 0x200000 + ((b & 127) * 4 + wid) * 4096) + (lane & 31) * 128;
        asm volatile("" : "+v"(vw), "+v"(tb0), "+v"(tp));
      }
      GAP();
    };
    addrs();
    PA pa;
    const int pu = p_unit(j, rr);
    if (isY) {
      const int rp = tid >> 3, c = tid & 7, ch = tid >> 4, q = tid & 15;
      pa.vo = pu < 0 ? OOB : (((pu & 15) * 128 + rp * 4) * DA + (pu >> 4) * 64 + c * 8) * 2;
      pa.lw = L_SP + 20480 + wid * 1024 + c * 64;
      pa.lx = L_SP + c * 2048 + (((31 - rp) * 8) ^ (c << 4));
      pa.lr = L_SP + ch * 256 + 16 * (q ^ (ch >> 3));
      pa.vs = ch * (SEQ * 2) + q * 16;
      asm volatile("" : "+v"(pa.vo), "+v"(pa.lw), "+v"(pa.lx), "+v"(pa.lr), "+v"(pa.vs));
      consts(std::true_type{});
      asm volatile("" : "=v"(vq), "=v"(vw), "=v"(tb0), "=v"(tp));
    } else {
      consts(std::false_type{});
      asm volatile("" : "=v"(pa.vo), "=v"(pa.lw), "=v"(pa.lx), "=v"(pa.lr), "=v"(pa.vs), "=v"(vbA2));
    }
    {
      const int fr = lane & 15, fq = lane >> 4;
      vseti<88>(fq * 4352 + fr * 8); vseti<89>(lane * 16); vseti<90>(wid * 8192 + lane * 16); vseti<91>(wid * 8192 + lane * 16 + 4096);
      asm volatile("s_getpc_b64 s[100:101]\n\ts_add_u32 s100, s100, at_event@rel32@lo+4\n\ts_addc_u32 s101, s101, at_event@rel32@hi+12" ::: "scc");
      asm volatile("s_lshl_b32 s63, %0, 18\n\tv_add_u32 v115, s63, v85\n\ts_xor_b32 s63, s63, 0x40000\n\tv_add_u32 v116, s63, v85\n\t"
                   "s_mul_i32 s63, %0, s73\n\tv_add_u32 v117, s63, v84\n\ts_sub_u32 s63, s73, s63\n\tv_add_u32 v118, s63, v84\n\t"
                   "s_lshl_b32 s63, %0, 13\n\ts_add_u32 s98, s92, s63\n\ts_xor_b32 s99, s98, 0x2000\n\t"
                   "s_add_u32 s96, s98, s63\n\ts_bitset1_b32 s96, 16\n\ts_xor_b32 s97, s96, 0x4000" ::"s"((rr + (rr >> 2)) & 1) : "scc");
      g_pro<12>(make_iseq<4>{});
      g_part<0, 0>(); g_part<0, 1>(); g_part<0, 2>(); g_part<0, 3>();
      {
        const char* xp;
        asm volatile("s_mov_b64 %0, s[94:95]" : "=s"(xp));
        asm volatile("s_mov_b32 m0, %1\n\ts_mov_b64 exec, 0x3fff\n\tglobal_load_lds_dword %0, off\n\ts_mov_b64 exec, -1"
                     ::"v"(xp + ((b * 4 + wid) * 14 + lane) * 128), "i"(L_SP + 16384) : "memory");
      }
      asm volatile("s_mov_b64 s[56:57], %0\n\ts_mov_b32 s63, %2\n\t"
                   "global_load_dwordx3 v[92:94], %1, off\n\tglobal_load_ushort v95, %3, off\n\tbuffer_load_dwordx2 v[120:121], off, " SW ", s63 sc1"
                   ::"s"(ws), "v"(&PTAB[j].v[r][0][tid][0]), "i"(fwo(6144)), "v"(assmD + j * 56 + (doA ? cr * 4 + wid : 0)) : "memory");
      pa.sk = __builtin_amdgcn_make_buffer_rsrc((void*)ak, (short)0, SEQ * DA * 2, 0x00020000);
      pa.sv = __builtin_amdgcn_make_buffer_rsrc((void*)av, (short)0, SEQ * DA * 2, 0x00020000);
      pa.sw = __builtin_amdgcn_make_buffer_rsrc((void*)(ash + (pu >> 4) * 256), (short)0, pu < 0 ? 0 : 512, 0x00020000);
      asm volatile("" : "+s"(pa.sk), "+s"(pa.sv), "+s"(pa.sw));
    }
    auto start = [&](auto Pc) __attribute__((always_inline)) {
      constexpr int P = Pc;
      asm volatile("s_waitcnt vmcnt(%0) lgkmcnt(0)\n\ts_barrier" ::"i"(CTP == 2 ? 13 : 12) : "memory");
      g_read0<P>(g, make_iseq<16>{});
      asm volatile("s_waitcnt lgkmcnt(0)\n\ts_barrier" ::: "memory");
      GAP();
    };
    if (isY) {
      start(std::integral_constant<int, 1>{});
      RSV rsv;
      rsv_begin(rsv);
      static_for([&](auto Tc) __attribute__((always_inline)) {
        constexpr int T = Tc;
        q_half<T, 0>(g, pa);
        if constexpr (T == 4) {
          const bf16* w_ = wkv; const int d_ = dq * 512;
          sld<88, A_TAB>(); sld<89, A_TAB + 1>();
          asm volatile("s_mov_b64 s[68:69], %0\n\ts_mov_b32 s70, 0x800000\n\tv_add_u32 v87, %1, v87" ::"s"(w_), "s"(d_));
          code_touch<0>();
        }
        if constexpr (T == 5) {
          const bf16* c_ = ckv; const unsigned v_ = vbA2;
          asm volatile("s_mov_b64 s[64:65], %0\n\ts_mov_b32 s66, 0x400000\n\tv_mov_b32 v86, %1\n\ts_mov_b32 s72, 0x10000\n\ts_mov_b32 s73, 0x20000\n\t"
                       "s_mov_b32 s74, 0x30000\n\ts_mov_b32 s75, 0x40000\n\ts_mov_b32 s76, 0x50000\n\ts_mov_b32 s77, 0x60000\n\ts_mov_b32 s78, 0x70000"
                       ::"s"(c_), "v"(v_));
        }
        GAP();
        q_half<T, 1>(g, pa);
      }, make_iseq<6>{});
      rsv_end(rsv);
      asm volatile("v_lshrrev_b32 %0, 4, v89" : "=v"(lane));
      consts2();
      asm volatile("" : "=v"(vw), "=v"(tb0), "=v"(tp), "=v"(vD));
      GAP();
      STT(0);
      g_half<false, 1, 0, 3>(g);
      g_half<false, 1, 1, 3>(g);
      set_flag<A_FPREP>();
      KST(0, 3);
    }
    unsigned om = 0x7000u | (isY ? 4u : (wid & 1) ? TOUCH1 : TOUCH0);
    asm volatile("" : "+s"(om));
    auto pollG = [&]() __attribute__((always_inline)) {
      sld<62, A_NPB>();
      fl_mask<A_NP, A_NP + 1>();
      asm volatile("s_nop 1");
      fl_issue();
    };
    auto oddG = [&](int t) __attribute__((always_inline)) {
      if (__builtin_expect((om >> t) & 1, 0)) {
        if (t != 13) {
          asm volatile("s_mov_b32 m0, %1\n\ts_nop 0\n\tglobal_load_lds_dword %0, off" ::"v"(tp), "i"(L_SP) : "memory");
        } else {
          fl_check();
          sld<63, A_VBB>(); sld<87, A_MB2>();
          asm volatile("s_mov_b64 s[68:69], %0\n\ts_mov_b32 s70, %1\n\ts_mov_b64 s[88:89], 0\n\tv_add_u32 v87, s63, v89\n\t"
                       "s_movk_i32 s80, 0x400\n\ts_movk_i32 s81, 0x800\n\ts_movk_i32 s82, 0xc00\n\ts_movk_i32 s83, 0x1000\n\t"
                       "s_movk_i32 s84, 0x1400\n\ts_movk_i32 s85, 0x1800\n\ts_movk_i32 s86, 0x1c00"
                       ::"s"(isY ? (const void*)ws : (const void*)assmk), "s"(isY ? 0x6400000 : DA * SEQ * 2));
        }
      }
    };
    auto evenG = [&](int t) __attribute__((always_inline)) {
      if (__builtin_expect((om >> t) & 1, 0)) {
        if (t == 2) set_flag<A_FT2>();
        else if (t == 12) pollG();
        else {
          sld<63, A_VBA>(); sld<79, A_MA2>();
          asm volatile("s_mov_b64 s[64:65], s[56:57]\n\ts_mov_b32 s66, s58\n\tv_add_u32 v87, 0x2000, v87\n\tv_add_u32 v86, s63, v89\n\t"
                       "s_movk_i32 s72, 0x1000\n\ts_movk_i32 s75, 0x4000\n\ts_movk_i32 s76, 0x5000");
          code_touch<1>();
        }
      }
    };
    auto tileG = [&](auto TRc) __attribute__((always_inline)) {
      constexpr bool TR = TRc;
      if (isY) {
        STT(5);
        g_half<TR, 0, 0, 2>(g);
        g_half<TR, 0, 1, 2>(g);
      } else {
        start(std::integral_constant<int, 0>{});
        g_half<TR, 0, 0, 1>(g);
        g_half<TR, 0, 1, 1>(g);
        vD = __float_as_uint(vget<95>());
      }
      g_loop<TR, false>(g, oddG, evenG);
      STT(10); STT(11);
      g_half<TR, 1, 0, 4>(g, wid);
      g_half<TR, 1, 1, 4>(g, wid);
      GAP();
    };
    if (kv) tileG(std::true_type{});
    else tileG(std::false_type{});
    STT(16);
  }
  att_item<3>(); att_item<4>(); att_item<5>(); att_item<6>();
  vseti<95>(wid * 16 + (lane & 15) - 4 * (lane >> 4)); vseti<126>(48 - wid * 16 + (lane & 15) - 4 * (lane >> 4)); vseti<127>(0xff800000u);
  asm volatile("s_mov_b64 s[88:89], %0\n\tv_mov_b32 v87, %1\n\ts_mov_b64 s[96:97], %2\n\tv_writelane_b32 v92, s96, 0\n\tv_writelane_b32 v92, s97, 1" ::"s"(aq), "v"(vq), "s"(out));
  KST(0, 4);
  if (isY) {
    sld<72, A_FCW>(); sld<96, B_MM, 1>(); sld<73, B_OP, 1>();
    asm volatile("s_mov_b32 s97, 0\n\ts_add_u32 m0, s78, 0x2000\n\ts_add_u32 s74, s74, 0x2000\n\t" A_POLL("v[122:123]", "s[96:97]", "s72") ::: "scc", "memory");
    dma4<89, 74>();
  } else if (doA) {
    const int l16 = lane & 15, fq = lane >> 4;
    CV c;
    c.lw = 65536 + wid * 15488;
    asm volatile("ds_write_b128 %0, v[114:117] offset:4096" ::"v"(c.lw + lane * 16));
    fl_mask<-1, A_NQ>();
#pragma unroll
    for (int i = 0; i < 4; ++i) c.pa[i] = c.lw + (((l16 + 1) & 1) ? CV_KS : CV_KP) + 4 * ((8 * fq + l16 + 1) >> 1) + 1024 * i;
    c.pb = c.lw + (2032 - 16 * l16 + 8 * fq) * 2 - 512 * 7;
    KST(0, 5);
    cv_rd0(c, make_iseq<24>{});
    static_for([&](auto I) { vseti<96 + I>(15 - l16 >> 1 == 4 * fq + I ? vD << (l16 & 1 ? 0 : 16) : 0u); }, make_iseq<4>{});
    fl_issue();
    c.vzo = (l16 >> 1) * 3584 + (l16 & 1) * 32 + (fq >> 1) * 16 + (1 - (fq & 1)) * 8 * 3584;
    GAP();
    cv_slots(c, make_iseq<144>{});
    static_for([&](auto T) { cv_slot<144 + T>(c); }, make_iseq<144>{});
    cv_tails(c, make_iseq<28 - cv_z0(288)>{});
    KST(0, 6);
    asm volatile("s_waitcnt vmcnt(%0) lgkmcnt(0)\n\ts_barrier" ::"i"(cv_end()) : "memory");
  } else {
    d_ops<160, 0>(make_iseq<208>{});
    fl_mask<-1, A_NQ>();
    sld<72, A_OQ>();
    fl_wait();
    asm volatile("s_waitcnt lgkmcnt(0)\n\ts_barrier" ::: "memory");
    set_flag<A_FG>();
    static_for([&](auto I) { f_load<128, I>(); if constexpr ((I & 3) == 3) f_next(); }, make_iseq<16>{});
    KST(0, 5);
    fl_mask<A_N01, -1>();
    fl_wait();
    k01_dma<0>(); k01_dma<1>(); k01_dma<2>(); k01_dma<3>();
    KST(0, 6);
    asm volatile("s_waitcnt vmcnt(8)\n\ts_barrier" ::: "memory");
  }
  if (!isY) {
    sld<94, A_WSRC>(); sld<73, B_OP, 1>();
    asm volatile("s_mov_b64 s[64:65], %0\n\ts_mov_b32 s66, 0x380000\n\ts_mov_b32 s67, s59\n\ts_mov_b32 s75, %1\n\t"
                 "v_mov_b32 v84, %2\n\tv_mov_b32 v85, %3\n\tv_mov_b32 v121, %4"
                 ::"s"(wo), "s"(L_SP + wid * 1024), "v"(vw), "v"(tb0), "v"(lane < 28 ? ((b * 4 + wid) * 28 + lane) * 128 : OOB));
  }
  u32x16 rs0, rs1; u32x4 rs2; u32x2 rs3;
#define STA(k)
  {
    AC c;
    asm volatile("rsv2_begin:" : "={v[40:55]}"(rs0), "={v[56:71]}"(rs1), "={v[72:75]}"(rs2), "={v[76:77]}"(rs3));
    const unsigned vml = (lane & 15) * 16;
#pragma unroll 1
    for (int it = r >> 4;; ++it) {
      if (it == r >> 4) KST(0, 8); else KST(0, 11);
      kreads<0>(make_iseq<8>{});
      const int n = prm<B_N, 1>();
      GAP();
      STA(0);
      if (isY) {
        phaseA<0, 2, false>(c);
        STA(1); STA(2);
        phaseB0<true>(false, vml);
      } else {
        if (it) phaseA<0, 3, false>(c);
        else phaseA<0, 1, false>(c);
        STA(1);
        if (c.rare) event();
        STA(2);
        phaseB0<false>(it && n == 2, vml);
      }
      STA(3);
      const int nl = n - it;
#pragma unroll 1
      for (int i = 1; i != nl; i += 2) {
        phaseA<1, 0, false>(c);
        STA(9 + i);
        if (__builtin_expect(c.rare != 0, 0)) { if (c.redo) sm_redo<1>(); event(); }
        phaseB<1>(c);
        if (i + 1 == nl) break;
        phaseA<0, 0, false>(c);
        STA(10 + i);
        if (__builtin_expect(c.rare != 0, 0)) { if (c.redo) sm_redo<0>(); event(); }
        phaseB<0>(c);
      }
      STA(4);
      if (it) break;
      sld<72, B_NXT, 1>(); sld<73, B_OP, 1>();
      epi_init<32768>();
      phaseA<ODD, 0, true>(c);
      if (__builtin_expect(c.redo != 0, 0)) sm_redo<ODD>();
      KST(0, 9);
      const float l[2] = {row_reduce<false>(vget<R_RS>() + vget<R_TS>()), row_reduce<false>(vget<R_RS + 1>() + vget<R_TS + 1>())};
      const f32x4 ml = {vget<R_M>(), l[0], vget<R_M + 1>(), l[1]};
      asm volatile("buffer_store_dwordx4 %0, %1, " SW ", s73 offen sc1" ::"v"(ml), "v"(vml) : "memory");
      phaseF(c, EP{}, make_iseq<74>{});
      asm volatile("s_waitcnt vmcnt(16)" ::: "memory");
      KST(0, 10);
    }
    asm volatile("at_tail:");
    vseti<84>(wid == 3 ? OOB : lane * 16);
    auto sel = [&]() __attribute__((always_inline)) {
      const unsigned one = (lane >> 4) == ((lane & 15) >> 2) ? 0x3f80u << ((lane & 1) * 16) : 0u, s0 = lane & 2 ? 0u : one, s1 = lane & 2 ? one : 0u;
      asm volatile("v_mov_b32 v72, %0\n\tv_mov_b32 v73, %1\n\tv_mov_b64 v[74:75], 0\n\tv_mov_b32 v76, %0\n\tv_mov_b32 v77, %1" ::"v"(s0), "v"(s1));
    };
    auto fin = [&](auto Lc, auto Mc) __attribute__((always_inline)) {
      constexpr int L = Lc, M = Mc;
      EP p;
      phaseAD<L, M == 1>(c, make_iseq<32>{});
      if (__builtin_expect(c.redo != 0, 0)) sm_redo<L>();
      STA(5);
      sld<76, B_SO, 1>();
      vseti<85>((wid >> 1) * 16384 + (wid & 1) * 1024 + lane * 16);
      if constexpr (M < 2) p.fa[0] = __builtin_amdgcn_rcpf(row_reduce<false>(vget<R_RS>() + vget<R_TS>()));
      GAP();
      phaseBD<L, M < 2>(c, p, make_iseq<64>{});
      if (__builtin_expect(c.redo != 0, 0)) sm_redo<L ^ 1, 1>();
      STA(6);
      KST(0, 12);
      asm volatile("s_waitcnt vmcnt(%0)\n\ts_barrier" ::"i"(M < 2 ? 6 + BDX : 0) : "memory");
      if constexpr (M == 2) { asm volatile("v_add_u32 v107, %0, v90" ::"i"(L_SP)); epi_preads(make_iseq<8>{}); }
      vreads<L>(make_iseq<16>{});
      const float l1 = row_reduce<false>(vget<R_RS + 1>() + vget<R_TS + 1>());
      if constexpr (M < 2) {
        p.fa[1] = __builtin_amdgcn_rcpf(l1);
        asm volatile("s_waitcnt lgkmcnt(0)" ::: "memory");
        phaseFD<L, true>(p, make_iseq<40>{});
      } else {
        const float l[2] = {row_reduce<false>(vget<R_RS>()), l1}, cm[2] = {vget<R_M>(), vget<R_M + 1>()};
        const f32x4 ml = {vget<72>(), vget<73>(), vget<74>(), vget<75>()};
#pragma unroll
        for (int e = 0; e < 2; ++e) {
          const float mm = fmaxf(cm[e], ml[2 * e]);
          const float wa = __builtin_amdgcn_exp2f(cm[e] - mm), wb = __builtin_amdgcn_exp2f(ml[2 * e] - mm);
          const float inv = __builtin_amdgcn_rcpf(l[e] * wa + ml[2 * e + 1] * wb);
          p.fa[e] = wa * inv; p.fb[e] = wb * inv;
        }
        sel();
        static_for([&](auto I) { asm volatile("v_mov_b32 v%c0, v%c1" ::"i"(160 + (int)I), "i"(40 + (int)I)); }, make_iseq<32>{});
        asm volatile("s_waitcnt lgkmcnt(0)" ::: "memory");
        phaseFD<L, false>(p, make_iseq<64>{});
      }
    };
    using I0 = std::integral_constant<int, 0>; using I1 = std::integral_constant<int, 1>; using I2 = std::integral_constant<int, 2>;
    using LY = std::integral_constant<int, ODD ? 0 : 1>;
    if (isY) {
      const unsigned late = prm<B_LM + 1, 1>();
      asm volatile("s_waitcnt vmcnt(12)" ::: "memory");
      const float cm[2] = {vget<R_M>(), vget<R_M + 1>()};
      const f32x4 ml = {vget<72>(), vget<73>(), vget<74>(), vget<75>()};
      if (__builtin_expect(!late && __builtin_amdgcn_ballot_w64(cm[0] != ml[0] || cm[1] != ml[2]) == 0, 1)) {
        vset<R_RS>(vget<R_RS>() + 0.25f * ml[1]); vset<R_RS + 1>(vget<R_RS + 1>() + 0.25f * ml[3]);
        sel();
        asm volatile("v_add_u32 v85, %0, v90" ::"i"(L_SP));
        fin(LY{}, I1{});
      } else fin(LY{}, I2{});
    } else { sel(); fin(I1{}, I0{}); }
    KST(0, 13);
  }
  {
    sld<64, A_OWN>(); sld<65, A_OWN + 1>(); sld<66, A_OWN + 2>(); sld<67, A_OWN + 3>();
    asm volatile("s_lshl_b32 s80, %0, 12\n\tv_add_u32 v96, s64, v89\n\tv_add_u32 v97, s65, v89\n\tv_add_u32 v98, s66, v89\n\tv_add_u32 v99, s67, v89\n\t"
                 "s_waitcnt lgkmcnt(0)\n\ts_barrier" ::"s"(wid) : "scc", "memory");
    o_read0(make_iseq<16>{});
    {
      const int fr = lane & 15, fq = lane >> 4;
      const unsigned vo = wid < 3 || !(fq & 1) ? prm<A_OUT>() + (fr * OUTW + (fq & 1) * 16 + (fq >> 1) * 8) * 2 : OOB;
      asm volatile("v_mov_b32 v101, %0\n\tv_add_u32 v102, %1, %0\n\ts_mov_b64 s[88:89], %2\n\ts_mov_b32 s90, 0x380000\n\ts_mov_b32 s91, s59"
                   ::"v"(vo), "s"((j < 4 ? 16 : -16) * OUTW * 2), "s"(out));
      GAP();
    }
    if (wid < 3) {
      static_for([&](auto P) {
        o_interval<2, P>();
        if constexpr (P == 1) KST(0, 27);
        if constexpr (P == 3) KST(0, 28);
      }, make_iseq<15>{});
      o_interval<2, 15>();
      o_slabs<2>();
      o_sum<2>();
    } else {
      sld<62, A_FCW>();
      fl_mask<A_FCM, -1>();
      GAP();
      static_for([&](auto Pc) {
        constexpr int P = Pc;
        o_interval<1, P>();
        if constexpr (P == 0 || P == 2) { fl_wait(); asm volatile("s_lshl_b32 s96, s96, 4" ::: "scc"); }
      }, make_iseq<15>{});
      o_interval<1, 15>();
      o_slabs<1>();
      o_sum<1>();
    }
    asm volatile("rsv2_end:" ::"{v[40:55]}"(rs0), "{v[56:71]}"(rs1), "{v[72:75]}"(rs2), "{v[76:77]}"(rs3));
    KST(0, 29);
#define A_CV(D, S0, S1) "v_cvt_pk_bf16_f32 v" #D ", v" #S0 ", v" #S1 "\n\t"
    asm volatile(A_CV(224, 240, 241) A_CV(226, 244, 245) A_CV(225, 242, 243) A_CV(227, 246, 247) A_CV(228, 248, 249) A_CV(230, 252, 253) A_CV(229, 250, 251) A_CV(231, 254, 255)
                 "v_permlane16_swap_b32_e32 v224, v226\n\tv_permlane16_swap_b32_e32 v225, v227\n\tv_permlane16_swap_b32_e32 v228, v230\n\tv_permlane16_swap_b32_e32 v229, v231\n\t"
                 "buffer_store_dwordx4 v[224:227], v101, s[88:91], 0 offen\n\tbuffer_store_dwordx4 v[228:231], v102, s[88:91], 0 offen" ::: "memory");
    set_flag<A_FE>();
  }
  KST(0, 1);
  asm volatile("s_endpgm\n" A_EVENT "\npf_code_end:" ::: "memory");
}

extern "C" void kernel_launch(void* const* d_in, const int* in_sizes, int n_in, void* d_out, int out_size, void* d_ws,
                              size_t ws_size, hipStream_t stream) {
  const bf16* Aq = (const bf16*)d_in[0];
  const bf16* Ak = (const bf16*)d_in[1];
  const bf16* Av = (const bf16*)d_in[2];
  const bf16* Ash = (const bf16*)d_in[3];
  const bf16* Assmk = (const bf16*)d_in[4];
  const bf16* AssmD = (const bf16*)d_in[5];
  const bf16* Bx = (const bf16*)d_in[6];
  const bf16* Bckv = (const bf16*)d_in[7];
  const bf16* Bwq = (const bf16*)d_in[8];
  const bf16* Bwuk = (const bf16*)d_in[9];
  const bf16* Bwuv = (const bf16*)d_in[10];
  const bf16* Bwo = (const bf16*)d_in[11];
  bf16* out = (bf16*)d_out;
  (void)hipFuncSetAttribute((const void*)fused_kernel, hipFuncAttributeMaxDynamicSharedMemorySize, 163840);
  fused_kernel<<<256, 256, 163840, stream>>>(Bckv, Bx, Bwuk, Bwuv, Bwq, (char*)d_ws + ((ws_size - 0x8000000) & ~(size_t)0x1fffff), Ak, Av, Ash, Assmk, AssmD, Bwo, Aq, out);
}
```

```cpp
#include <hip/hip_runtime.h>
#include <hip/hip_bf16.h>
#include <stdint.h>
#include <utility>
#include <type_traits>

typedef __hip_bfloat16 bf16;
typedef __attribute__((ext_vector_type(4))) short bf16x4;
typedef __attribute__((ext_vector_type(2))) unsigned u32x2;

constexpr int SEQ = 2048;
constexpr int DA = 448;
constexpr int NH = 16;
constexpr int HD = 256;
constexpr int DC = 1024;
constexpr int OUTW = 896;

#define KST(kid, which)

__device__ __forceinline__ float bf2f(unsigned short u) { return __uint_as_float(((unsigned)u) << 16); }
__device__ __forceinline__ unsigned short f2bf(float f) { return __bfloat16_as_ushort(__float2bfloat16(f)); }
__device__ __forceinline__ float rbf(float f) { return bf2f(f2bf(f)); }
typedef __attribute__((ext_vector_type(2))) __bf16 bf16x2n;
typedef __attribute__((ext_vector_type(2))) float f32x2n;
__device__ __forceinline__ unsigned pk2(float a, float b) {
  return __builtin_bit_cast(unsigned, __builtin_convertvector((f32x2n){a, b}, bf16x2n));
}

__device__ __forceinline__ int off_b(int row, int ch) { return 256 * row + 16 * (ch ^ (((row & 3) << 2) | ((row >> 2) & 3))); }

#define DI __device__ __forceinline__
#define SCC_
#define SCC1
#define SCC2
#define SCC3
#define GAP() __builtin_amdgcn_sched_barrier(0)
template <int R> DI float vget() { float t; asm volatile("v_mov_b32 %0, v%c1" : "=v"(t) : "i"(R)); return t; }
template <int R> DI void vset(float x) { asm volatile("v_mov_b32 v%c0, %1" ::"i"(R), "v"(x)); }
template <int R> DI void vseti(unsigned x) { asm volatile("v_mov_b32 v%c0, %1" ::"i"(R), "v"(x)); }
template <int R> DI float acc_read1() { float t; asm volatile("v_accvgpr_read_b32 %0, a[%c1]" : "=v"(t) : "i"(R)); return t; }
template <class F, int... I> DI void static_for(F&& f, std::integer_sequence<int, I...>) {
  (f(std::integral_constant<int, I>{}), ...);
}
template <int... Is> using iseq = std::integer_sequence<int, Is...>;
template <int N> using make_iseq = std::make_integer_sequence<int, N>;
typedef unsigned long long u64;

constexpr unsigned O_K = 1u << 24, O_V = 2u << 24, O_C = 3u << 24, O_P = 4u << 24, O_F = O_P + 128 * 67584, O_S = O_F + 400 * 4352,
                   O_X = O_S + (1u << 24), O_Z = O_X + DA * SEQ * 2, O_W = O_Z + DA * SEQ * 2;
static_assert(O_W + 16 * 262144 <= 0x6400000);
constexpr unsigned fwo(int w) { return O_F + (w >> 4) * 4352 + (w & 15) * 8; }
constexpr unsigned OOB = 0x80000000u, M0X = 0x28000;
constexpr unsigned L_SP = 131072;
#define CMAX 10
#define NYA 15
#define NYB 15
#define MAP4 2
#define TSEQ 1
constexpr int O_SEQ[16] = {0x00, 0x01, 0x02, 0x10, 0x20, 0x30, 0x11, 0x21, 0x31, 0x12, 0x22, 0x32, 0x13, 0x23, 0x33, 0x03};
constexpr int o_k(int P) { int k = 0; for (int q = 0; q < P; ++q) k += O_SEQ[q] >= 16; return O_SEQ[P] >= 16 ? k : -1; }
#define Q44 1
#define KTYV 0x76543210fedcba98ull
#define LATEP 0
#define PKS 3
#define PPOS 99, 10, 9, 9, 9, 8
#define TABX 0xfedcb9875a463210ull, 0xefadc6982b574310ull, 0xfebad7693c285410ull, 0xefcba8764d392510ull
#define K2(c) (2 + (c))
#define POSTM 7
#define NCV 0
#define WP0 8
constexpr int ODD = NYA & 1;
static_assert((NYB & 1) == ODD);
constexpr int NYS[8] = {NYA, NYA, NYA, NYA, NYB, NYB, NYB, NYB};
enum { A_TAB = 0, A_FPREP = 2, A_FT2, A_NP, A_NPB = 6, A_VBB, A_MB2, A_VBA, A_MA2, A_VOO, A_SX, A_MX = 16, A_VOQ = 20, A_FG, A_NQ, A_N01, A_ZO, A_OQ,
       A_WSRC, A_WDST, A_PQ, A_PZ, A_M0F, A_GH, A_PC = 32, A_WLD = 44, A_FC, A_FCW, A_FCM, A_SS, A_FS = 51, A_FSW, A_FSM, A_SL, A_OUT = 57, A_OWN, A_FE = 62, A_GF };
enum { B_N = 0, B_ST, B_MS, B_CT, B_FW, B_FSET, B_NXT, B_SO, B_OP, B_MM, B_PM, C_FCM = 16, C_QT, B_LM, B_LP = 21, B_WD, C_PO, B_ACT = 32 };
enum { E_CHK = 1, E_PACK = 4, E_POLL = 8, E_POLL1 = 16, E_MASK = 32, E_FLAG = 64, E_WFLAG = 128, E_POLLZ = 256, E_LOAD = 512, E_POST = 1024, E_PLOAD = 2048, E_CHK1 = 4096 };
constexpr unsigned ev_code(unsigned a) {
  return a == 0 ? 0 : a == E_CHK ? 16 : a == E_FLAG ? 32 : a == (E_FLAG | E_POLL) ? 96 : a == E_POLL ? 56 : a == (E_WFLAG | E_POLLZ) ? 344 : a == E_WFLAG ? 80 : a == (E_FLAG | E_POLLZ) ? 448 : a == E_LOAD ? 400 : a == E_POST ? 512 : a == E_PLOAD ? 768 : a == (E_PLOAD | E_CHK1) ? 900 : a == E_CHK1 ? 872 :
         a == E_PACK ? 128 : a == E_POLL1 ? 192 : a == (E_POLL | E_POLL1 | E_MASK) ? 228 : a == (E_POLL1 | E_MASK) ? 240 : 1u << 31;
}
struct PTab { unsigned v[32][4][64][3]; bool bad; };
constexpr int p_unit(int j, int rr) { return (j & 1) && rr < 4 ? -1 : (j >> 1) * 28 + (j & 1) * 12 + rr; }
constexpr int o_head(int ks, int p) { return 2 * ks + (p >> 1) + 8 * (p & 1); }
constexpr PTab make_tab(int j) {
  PTab t{};
  for (int r = 0; r < 32; ++r) for (int wid = 0; wid < 4; ++wid) {
    auto& e = t.v[r][wid];
    const int rr = r & 15, hsg = rr & 1, pjg = rr >> 1, kv = pjg & 1, gw = j * 128;
    const bool isY = r >= 16;
    const int ks = j >> 1, p = MAP4 ? rr & 3 : (j & 1) * 2 + hsg, h = o_head(ks, p), hsel = h >> 3, ga = (h & 7) * 128;
    const int pj = MAP4 == 1 ? (rr >> 2) * 2 + (j & 1) : MAP4 == 2 ? (rr >> 2) + 4 * (j & 1) : pjg;
    const int mt = (rr >> 2) + (isY ? 4 : 0), cr = rr - (pjg > NCV ? 2 : 0), cch = j * 56 + cr * 4 + wid, NY = NYS[pj];
    const bool doA = !isY && pjg != NCV;
    const unsigned oG = (unsigned)(j + 8 * hsg) << 20;
    const int nx0 = 32 - 2 * pj - NY, cx = nx0 < CMAX ? nx0 : CMAX, ex = nx0 - cx, dg = 30 - 2 * pj;
    int seq[2][32] = {}, ns = 0;
    auto run = [&](int it, int t0, int t1) { for (int q = t0; q < t1; ++q) seq[it][ns++] = q; };
    const int x1 = TSEQ ? dg - ex : 16, c4 = 32 - 8 - NY < CMAX ? 32 - 8 - NY : CMAX;
    if (!isY) {
      run(0, 0, cx); run(0, x1, x1 + ex);
      ns = 0;
      const int s1 = TSEQ >= 2 && nx0 + TSEQ - 2 + 2 <= 2 * pj ? (nx0 < 4 ? 2 : nx0 + TSEQ - 2) : 0;
      run(1, s1, 2 * pj); run(1, 0, s1); run(1, 2 * pj, 2 * pj + 2);
    } else if (ex || !TSEQ) { run(0, cx, x1); run(0, x1 + ex, dg + 2); }
    else { run(0, c4 > cx ? c4 : cx, 16); run(0, cx, c4); run(0, 16, dg + 2); }
    const u64 own = h == j + 8 * hsg && isY ? 1ull << (hsel * 16 + kv * 8 + mt) : 0;
    u64 needB = 0;
    int c1 = 99;
    for (int i = (isY ? NY : nx0) - 1; i >= 0; --i) if (seq[0][i] >= 16) { needB |= 0x101ull << (hsel * 16 + (seq[0][i] >> 2)); c1 = i - 2; }
    needB &= ~own;
    const u64 needQ = (1ull << (32 + hsel * 8 + (pj >> 1))) | (1ull << (32 + hsel * 8 + ((15 - pj) >> 1)));
    const u64 needA = 0x0f0full << (hsel * 16);
    const unsigned oQ = (unsigned)h << 20, oK = oQ + O_K, oP = O_P + (h * 8 + pj) * 67584;
    const unsigned oQ0 = oQ + ((15 - pj) * 128 + wid * 32) * 512, oK0 = oK + seq[0][0] * 32768, lowb = ((wid & 1) + (wid >> 1) * 4) * 4096;
    auto w0 = [&](unsigned x) { return wid == 0 ? x : OOB; };
    const int cls = (rr + (rr >> 2)) & 3;
    const u64 tabX[4] = {TABX};
    const u64 tab = isY ? KTYV : tabX[cls];
    if ((tabX[cls] & 0xfff) != 0x10u + K2(cls) * 0x100) t.bad = true;
    e[C_QT][2] = 0x6543;
    e[A_TAB][0] = (unsigned)tab; e[A_TAB + 1][0] = (unsigned)(tab >> 32);
    const int pu = p_unit(j, rr);
    e[A_FPREP][0] = pu < 0 ? OOB : w0(fwo(6160 + pu));
    const int hq = Q44 ? o_head(ks, rr & 3) : j + 8 * hsg, tq = Q44 ? (rr >> 2) + 4 * (j & 1) : pjg;
    e[A_FT2][0] = isY ? w0(fwo((hq & 7) * 128 + 32 + (hq >> 3) * 8 + tq)) : OOB;
    const u64 np = isY ? needA | needQ : 0xffffull;
    e[A_NP][0] = (unsigned)np; e[A_NP + 1][0] = (unsigned)(np >> 32); e[A_NPB][0] = fwo(isY ? ga : 6160 + (cch >> 6) * 16);
    e[A_VBB][0] = oQ0; e[A_MB2][0] = isY ? 65536 + wid * 1024 : M0X;
    e[A_VBA][0] = oK0 + wid * 1024; e[A_MA2][0] = isY ? wid * 1024 : M0X;
    e[A_VOO][0] = oG + (kv ? O_V : O_K) + mt * 131072 + (wid >> 1) * 65536 + (wid & 1) * (kv ? 8192 : 4096);
    e[A_SX][0] = isY ? oK0 + lowb + 8192 : O_X + cch * 4096; e[A_SX + 1][0] = isY ? oQ0 + 12288 : cch * 4096;
    e[A_SX + 2][0] = oK0 + 32768 + lowb; e[A_SX + 3][0] = oK0 + O_K + wid * 8192;
    e[A_MX][0] = isY ? lowb + 8192 : doA ? 65536 + wid * 15488 : M0X; e[A_MX + 1][0] = isY ? 114688 + wid * 4096 : e[A_MX][0] + 5120;
    e[A_MX + 2][0] = isY ? 32768 + lowb : M0X; e[A_MX + 3][0] = isY ? 65536 + wid * 8192 : M0X;
    e[A_VOQ][0] = ((unsigned)hq << 20) + tq * 131072 + (wid >> 1) * 65536 + (wid & 1) * 4096;
    e[A_FG][0] = w0(fwo(gw + hsg * 16 + kv * 8 + mt));
    e[A_NQ][0] = (unsigned)(needQ >> 32); e[A_N01][0] = 0x0101u << (hsel * 16);
    e[A_ZO][0] = isY ? (pu < 0 ? OOB : O_X + (pu >> 4) * (64 * SEQ * 2) + (15 - (pu & 15)) * 256) : O_Z + j * (56 * SEQ * 2) + (cr * 4 + wid) * 64; e[A_OQ][0] = oQ0;
    const int qbl = isY ? 15 - pj : pj, u = 2 * pj, tile = qbl * 4 + p;
    e[A_WSRC][0] = ((o_head(ks, p ^ (u >> 2)) * 256 + (u & 3) * 64) * DA + p * 112) * 2;
    e[A_WLD][0] = O_W + h * 262144 + wid * 8192;
    e[A_WDST][0] = e[A_WLD][0] + u * 16384;
    e[A_PQ][0] = ((rr * 4 + wid) * 32 * DA + j * 56) * 2; e[A_PZ][0] = O_Z + (j * 64 + rr * 4 + wid) * 3584;
    e[A_M0F][0] = L_SP + wid * 8192; e[C_PO][2] = ((rr * 4 + wid) * 32 * OUTW + j * 56) * 2;
    e[A_GH][0] = fwo(gw + 64);
    auto qo = [&](int i) { return i == 3 ? ks : i + (i >= ks); };
    auto ctxo = [&](int o, int qb, int q) { return O_C + ((unsigned)o_head(ks, p ^ o) << 20) + qb * 65536 + q * 16384 + wid * 4096; };
    auto slab = [&](int tl, int q, int src) { return O_S + tl * 262144 + (q * 8 + src) * 8192 + wid * 2048; };
    for (int q = 0; q < 16; ++q) if (o_k(q) >= 0) e[A_PC + o_k(q)][0] = ctxo(O_SEQ[q] >> 4, qbl, qo(O_SEQ[q] & 3));
    e[A_FC][0] = w0(fwo(1024 + (qbl * 4 + ks) * 16 + p)); e[A_FCW][0] = fwo(1024 + (qbl * 4 + ks) * 16);
    e[A_FCM][0] = 15 & ~(1 << p);
    for (int q = 0; q < 3; ++q) { e[A_SS + q][0] = slab(tile, qo(q), 2 * ks); e[A_SL + q][0] = slab(tile, ks, 2 * qo(q)); }
    e[A_FS][0] = fwo(2048 + (tile * 4 + wid) * 16 + 2 * ks); e[A_FSW][0] = fwo(2048 + (tile * 4 + wid) * 16); e[A_FSM][0] = 0x55 & ~(1 << (2 * ks));
    e[A_OUT][0] = ((qbl * 128 + (ks < 2 ? 2 * ks : 11 - 2 * ks) * 16) * OUTW + DA + p * 112 + wid * 32) * 2;
    for (int q = 0; q < 4; ++q) e[A_OWN + q][0] = qo(q) * 16384;
    e[A_FE][0] = r == 28 && j == 4 ? w0(fwo(6144)) : OOB;
    e[A_GF][0] = fwo(ga);
    int wstep = isY || pj >= PKS ? 99 : 4, wpos = 0;
    for (int it = 0; it < 2; ++it) {
      const int R = 1 + it, n = isY ? NY : it ? 2 * pj + 2 : nx0, qb = isY || !it ? 15 - pj : pj;
      const bool last = isY || it;
      unsigned act[32] = {};
      int np_ = 0;
      auto poll = [&](int i, u64 m) {
        const int l = B_PM + 2 * np_++;
        e[l][R] = (unsigned)m; e[l + 1][R] = (unsigned)(m >> 32);
        if (act[i] & E_POLL) t.bad = true;
        act[i] |= E_POLL; act[i + 1] |= E_CHK;
      };
      if (!it || isY) act[1] |= E_FLAG;
      const bool late = isY && pj < LATEP;
      if (isY) {
        const int pa[8] = {99, 99, PPOS};
        int s_ = late ? n - 2 : pa[pj] + 15 - NY < n - 3 ? pa[pj] + 15 - NY : n - 3;
        int q = 1;
        if ((POSTM >> pj) & 1) {
          act[1] |= E_POLLZ; act[2] = E_LOAD;
          q = c1 == 4 ? 5 : 3;
          act[q] = E_POST;
        }
        while (s_ <= q || (s_ + 1 >= c1 - 1 && s_ <= c1)) ++s_;
        if (c1 < 4 || s_ > n - 3 + late) t.bad = true;
        if (c1 < s_) poll(c1 - 1, needB);
        poll(s_, 1ull << (48 + hsel * 8 + pj));
        act[s_ + 1] = late ? 0 : E_PLOAD;
        if (c1 > s_) poll(c1 - 1, needB);
      }
      else if (!it) { if (n > 2) poll(1, needA); if (c1 < 99) poll(c1 - 1, needB); }
      else if (nx0 <= 2) poll(1, needA);
      if (late) e[B_LM + 1][R] = 1u << (16 + hsel * 8 + pj);
      e[B_LP][R] = oP + wid * 8192;
      const int* sq = seq[isY ? 0 : it];
      if (sq[1] != sq[0] + 1 || (!isY && seq[1][0] + 1 >= 4 && nx0 < 4)) t.bad = true;
      if (last && n > 2) act[n - 3] |= E_POLL1 | E_MASK;
      if (!last && pj == 0) act[n - 2] |= E_POLL1;
      if (last) act[n - 2] |= E_CHK1;
      if (it && !isY && pj >= PKS) wstep = 5, wpos = WP0;
      const int wend = (POSTM >> pj) & 1 ? 6 : 8;
      const int ws_[4] = {E_PACK, wend == 6 ? E_WFLAG : E_WFLAG | E_POLLZ, E_LOAD, E_POST};
      for (int i = 0; i < (last ? n - 3 : n) && !isY; ++i, ++wpos)
        if (wstep < wend && wpos >= WP0 && !act[i] && i) act[i] = ws_[wstep++ - 4];
      if (it && wstep < wend) t.bad = true;
      e[B_WD][R] = pj >= PKS ? e[A_WDST][0] : OOB;
      e[B_FW][R] = wid < 2 ? fwo((j ^ wid) * 128 + 80 + wid * 16 + rr) : OOB;
      e[B_MS][R] = doA ? e[A_MX][0] + 10304 : M0X; e[B_CT][R] = (rr * 4 + wid) * 128;
      for (int i = 0; i < n; ++i) {
        const unsigned sk = i + 2 < n ? oK + sq[i + 2] * 32768 : last ? O_W + h * 262144 + 32768 : oK + (seq[1][0] + (i & 1)) * 32768;
        e[B_ACT + i][R] = sk | ev_code(act[i]);
        if (ev_code(act[i]) > 1023 || (sk & 1023)) t.bad = true;
      }
      e[B_N][R] = n; e[B_ST][R] = oK + sq[0] * 32768;
      e[B_FSET][R] = isY ? e[A_FG][0] : it ? w0(fwo(ga + 48 + hsel * 8 + pj)) : wid == 1 ? fwo(1024 + (qb * 4 + ks) * 16 + 8 + p) : doA ? w0(fwo(gw + 112 + cr)) : OOB;
      e[B_MM][R] = 256u << p;
      e[B_NXT][R] = last ? e[A_WLD][0] : oQ + (pj * 128 + wid * 32) * 512;
      e[B_SO][R] = last ? oQ + O_C + qb * 65536 : oP;
      e[B_OP][R] = oP + 65536 + wid * 256;
    }
  }
  return t;
}
constexpr PTab PT0 = make_tab(0), PT1 = make_tab(1), PT2 = make_tab(2), PT3 = make_tab(3), PT4 = make_tab(4), PT5 = make_tab(5), PT6 = make_tab(6), PT7 = make_tab(7);
static_assert(!PT0.bad && !PT7.bad);
static __device__ const PTab PTAB[8] = {PT0, PT1, PT2, PT3, PT4, PT5, PT6, PT7};

#define SPINB "0x100000"
#define SW "s[56:59]"
template <int S, int L, int ROW = 0> DI void sld() { asm volatile("v_readlane_b32 s%c0, v%c1, %c2" ::"i"(S), "i"(92 + ROW), "i"(L)); }
template <int L, int ROW = 0> DI unsigned prm() { unsigned x; asm volatile("v_readlane_b32 %0, v%c1, %c2\n\ts_nop 4" : "=s"(x) : "i"(92 + ROW), "i"(L)); return x; }
#define A_FLAG "s_mov_b64 exec, 1\n\tbuffer_store_dwordx2 v[82:83], off, " SW ", s63 sc1\n\ts_mov_b64 exec, -1\n\t"
#define A_POLL(V, M, B) "s_mov_b64 exec, " M "\n\tbuffer_load_dwordx2 " V ", v88, " SW ", " B " offen sc1\n\ts_mov_b64 exec, -1\n\t"
#define A_SPIN(V, M, B) "s_mov_b32 s95, " SPINB "\n\ts_mov_b64 exec, " M "\n" \
    "8:\n\tbuffer_load_dwordx2 " V ", v88, " SW ", " B " offen sc1\n\ts_waitcnt vmcnt(0)\n\tv_cmp_eq_u64 vcc, s[60:61], " V "\n\t" \
    "s_cmp_eq_u64 vcc, " M "\n\ts_cbranch_scc1 9f\n\ts_sleep 4\n\ts_sub_u32 s95, s95, 1\n\ts_cbranch_scc0 8b\n9:\n\ts_mov_b64 exec, -1\n\t"
#define A_CHECK(V, M, B) "v_cmp_eq_u64 vcc, s[60:61], " V "\n\ts_and_b64 vcc, vcc, " M "\n\ts_cmp_eq_u64 vcc, " M "\n\ts_cbranch_scc1 9f\n\t" A_SPIN(V, M, B)
#define A_CTOUCH(B, E, M) "v_readlane_b32 s63, v93, 3\n\ts_sub_u32 s96, s100, at_event - " B "\n\ts_subb_u32 s97, s101, 0\n\t" \
    "s_mov_b32 s98, " E " - " B "\n\ts_mov_b32 s99, s59\n\tv_lshl_add_u32 %0, v89, 9, s63\n\t" \
    "s_mov_b32 m0, 0x27f00\n\ts_mov_b64 exec, " M "\n\tbuffer_load_dword %0, s[96:99], 0 offen lds\n\ts_mov_b64 exec, -1\n\t"
template <int K> DI void code_touch() {
  unsigned t;
  if constexpr (K) asm volatile(A_CTOUCH("rsv_end", "at_tail", "0xff") : "=&v"(t) :: "scc", "memory");
  else asm volatile(A_CTOUCH("rsv_end", "rsv2_begin", "0x1f") : "=&v"(t) :: "scc", "memory");
}
template <int L, int ROW = 0> DI void set_flag() {
  asm volatile("v_readlane_b32 s63, v%c0, %c1\n\ts_nop 3\n\t" A_FLAG ::"i"(92 + ROW), "i"(L) : "memory");
}
template <int LO, int HI> DI void fl_mask() {
  if constexpr (LO >= 0) sld<96, LO>(); else asm volatile("s_mov_b32 s96, 0");
  if constexpr (HI >= 0) sld<97, HI>(); else asm volatile("s_mov_b32 s97, 0");
}
DI void fl_issue() { asm volatile(A_POLL("v[122:123]", "s[96:97]", "s62") ::: "memory"); }
DI void fl_check() { asm volatile(A_CHECK("v[122:123]", "s[96:97]", "s62") ::: "vcc", "scc", "memory"); }
DI void fl_wait() { asm volatile("s_nop 4\n\t" A_SPIN("v[122:123]", "s[96:97]", "s62") ::: "vcc", "scc", "memory"); }

typedef __attribute__((ext_vector_type(4))) unsigned u32x4c;
typedef __attribute__((ext_vector_type(4))) float f32x4c;
typedef __attribute__((ext_vector_type(2))) float f32x2c;
__device__ __forceinline__ float bfl(unsigned u) { return __uint_as_float(u << 16); }
__device__ __forceinline__ float bfh(unsigned u) { return __uint_as_float(u & 0xffff0000u); }

#define CODE_TOUCH(LB, LE, wx, NL, m0v) do { unsigned blo_, bhi_, elo_; \
    asm volatile("s_getpc_b64 s[100:101]\n\ts_add_u32 s100, s100, " LB "@rel32@lo+4\n\ts_addc_u32 s101, s101, " LB "@rel32@hi+12\n\t" \
                 "s_mov_b32 %0, s100\n\ts_mov_b32 %1, s101\n\t" \
                 "s_getpc_b64 s[100:101]\n\ts_add_u32 s100, s100, " LE "@rel32@lo+4\n\ts_mov_b32 %2, s100" \
                 : "=s"(blo_), "=s"(bhi_), "=s"(elo_) :: "scc"); \
    const unsigned n_ = elo_ - (blo_ & ~127u); \
    const __amdgpu_buffer_rsrc_t srd_ = __builtin_amdgcn_make_buffer_rsrc((void*)(((unsigned long long)bhi_ << 32) | (blo_ & ~127u)), (short)0, n_ < 262144u ? n_ : 0u, 0x00020000); \
    const unsigned l_ = threadIdx.x & 63, vo_ = l_ < (NL) ? ((wx) + l_ * 128u) * 128u : 0x80000000u; \
    asm volatile("s_mov_b32 m0, %0\n\ts_nop 4\n\tbuffer_load_dword %1, %2, 0 offen lds" ::"s"(m0v), "v"(vo_), "s"(srd_) : "memory"); } while (0)

#define STT(k)
#define SNK 31
#define GSC " sc1"
#define PL0 66
#define PLS 7
#define PCAP 0x55555555
#define PST 272
#define ECAP 3
#define EPACE_K 7
#define EPACE_V 6
typedef __attribute__((ext_vector_type(4))) float f32x4g;
typedef __attribute__((ext_vector_type(4))) unsigned u32x4g;
DI u32x4g pair16(f32x4g x, f32x4g y) {
  const unsigned a0 = pk2(x[0], x[1]), a1 = pk2(x[2], x[3]), b0 = pk2(y[0], y[1]), b1 = pk2(y[2], y[3]);
  auto r0 = __builtin_amdgcn_permlane16_swap(a0, b0, false, false), r1 = __builtin_amdgcn_permlane16_swap(a1, b1, false, false);
  return (u32x4g){r0[0], r1[0], r0[1], r1[1]};
}
constexpr int LDW = NH * HD;
struct GM { unsigned aA[2], aB[2][8]; };
#define GDP 8, 12, 24, 28, 40, 44, 56, 60
constexpr int GDPOS[8] = {GDP};
constexpr int g_j(int G) { for (int i = 0; i < 8; ++i) if (GDPOS[i] == (G & 63)) return i; return -1; }
constexpr int GD13 = 125 - GDPOS[3];
struct PA { unsigned vo, lw, lx, lr, vs; __amdgpu_buffer_rsrc_t sk, sv, sw; };
struct POp { int k, d, a, b; };
struct PSch { POp op[760]; int first[770], vm[770], n; };
constexpr PSch p_sched() {
  PSch z{};
  POp l[760] = {};
  int n = 0, early[760] = {};
  auto put = [&](int k, int d, int a, int b, int e = 0) { l[n] = POp{k, d, a, b}; early[n] = e; ++n; };
  for (int q = 0; q < 10; ++q) put(10, q < 6 ? q : q + 2, 0, 0, PL0 + PLS * q);
  for (int c = 0; c < 8; ++c) {
    const int I = c >> 1, u = c & 1;
    for (int t = 0; t < 4; ++t) put(t & 1, 112 + t, 76 + 2 * u + (t >> 1), 0, c ? 0 : PST);
    if (u && I < 3) put(11, I + 1, 0, 0);
    for (int q = 0; q < 7; ++q) put(u, 116 + q, 32 + 4 * q + I, 0);
    for (int e = 2; e >= 0; e -= 2) {
      put(2, 125, 117 + e, 115); put(2, 123, 116 + e, 115);
      for (int t = 1; t < 4; ++t) { put(4, 125, 117 + e + t, 115 - t); put(4, 123, 116 + e + t, 115 - t); }
      const int T = e ? 126 : 127;
      put(u, 124, 64 + 4 * e + I, 0); put(u, T, 60 + 4 * e + I, 0);
      put(2, 125, 125, 124); put(2, 123, 123, T);
      put(5, T, 125, 123);
    }
    put(12, c, 0, 0, (PST + 2) / 64 * 64 + 62);
  }
  const int nm = n;
  for (int q = 0; q < 4; ++q) put(13, q, 0, 0);
  for (int q = 0; q < 4; ++q) put(14, q, 0, 0);
  int g = 0, i = 0, hold = 0;
  for (; g < 768; ++g) {
    const int q = g & 15;
    int cap = g_j(g) >= 0 ? 0 : (PCAP >> (2 * q)) & 3;
    z.first[g] = z.n; z.vm[g + 1] = z.vm[g];
    if (g == PST - 16) z.op[z.n++] = POp{11, 0, 0, 0};
    if (g == PST - 20) z.op[z.n++] = POp{15, 0, 0, 0};
    if (g >= PST - 16 && g < PST - 13) for (int r = (g - PST + 16) * 7 / 3; r < (g - PST + 17) * 7 / 3; ++r) z.op[z.n++] = POp{16, r, 0, 0};
    while (cap > 0 && i < n && early[i] <= g && hold <= g) {
      const int k = l[i].k;
      if ((k == 11 || k == 13) && q > 2) { if (k == 13) break; }
      z.op[z.n++] = l[i];
      if (k == 10 || k == 14) { ++z.vm[g + 1]; cap = 0; hold = g + 3; }
      if (i == nm - 1) hold = (g + 3) / 64 * 64 + 64;
      if (i == nm + 3) hold = g / 16 * 16 + 16;
      --cap; ++i;
    }
  }
  z.first[768] = z.n;
  if (i < n) z.n = -1;
  return z;
}
constexpr PSch PS = p_sched();
constexpr int p_last() { int g = 0; while (PS.first[g] < PS.n) ++g; return g; }
static_assert(PS.n > 0 && PS.vm[(PST - 16) / 64 * 64 - 3 - GD13] == 10 && PST % 16 == 0);
template <int I> DI void p_op(const PA& p) {
  constexpr POp o = PS.op[I];
  if constexpr (o.k == 0) asm volatile("v_lshlrev_b32_e32 v%c0, 16, v%c1" ::"i"(o.d), "i"(o.a));
  else if constexpr (o.k == 1) asm volatile("v_and_b32_e32 v%c0, s94, v%c1" ::"i"(o.d), "i"(o.a));
  else if constexpr (o.k == 2) asm volatile("v_mul_f32_e32 v%c0, v%c1, v%c2" ::"i"(o.d), "i"(o.a), "i"(o.b));
  else if constexpr (o.k == 4) asm volatile("v_fmac_f32_e32 v%c0, v%c1, v%c2" ::"i"(o.d), "i"(o.a), "i"(o.b));
  else if constexpr (o.k == 5) asm volatile("v_cvt_pk_bf16_f32 v%c0, v%c1, v%c2" ::"i"(o.d), "i"(o.a), "i"(o.b));
  else if constexpr (o.k == 10 && o.d == 0)
    asm volatile("s_mov_b32 s93, m0\n\ts_add_u32 m0, s79, %1\n\ts_mov_b32 s94, 0xffff0000\n\tbuffer_load_dwordx4 v89, %0, 0 offen lds\n\ts_mov_b32 m0, s93"
                 ::"s"(p.sw), "i"(L_SP + 20480) : SCC_ "memory");
  else if constexpr (o.k == 10 && o.d < 6) {
    if constexpr (o.d == 1) asm volatile("v_and_b32 v113, 0x380, v89\n\tv_mul_u32_u24 v114, 21, v113\n\tv_sub_u32 v114, %0, v114\n\ts_mul_i32 s95, s79, 5" ::"v"(p.vo));
    asm volatile("v_add_u32 v112, %1, v114\n\ts_mov_b32 s93, m0\n\ts_add_u32 m0, s95, %2\n\ts_mov_b32 exec_lo, %3\n\ts_mov_b32 exec_hi, %4\n\t"
                 "buffer_load_dwordx4 v112, %0, 0 offen lds\n\ts_mov_b64 exec, -1\n\ts_mov_b32 m0, s93"
                 ::"s"(p.sk), "i"((o.d - 2) * 8 * DA * 2), "i"(L_SP + (o.d - 1) * 1024), "i"(o.d == 1 ? 0 : -1), "i"(o.d == 1 ? -256 : -1) : SCC_ "memory");
  } else if constexpr (o.k == 10)
    asm volatile("buffer_load_dwordx4 v[%c0:%c1], %2, %3, 0 offen offset:%c4" ::"i"(28 + 4 * o.d), "i"(31 + 4 * o.d), "v"(p.vo), "s"(p.sv), "i"((o.d & 3) * DA * 2) : "memory");
  else if constexpr (o.k == 15) asm volatile("v_mad_u32_u24 v113, v113, 3, v89\n\tv_add_u32 v113, s95, v113\n\tv_add_u32 v113, %0, v113" ::"i"(L_SP + 640));
  else if constexpr (o.k == 16) asm volatile("ds_read_b128 v[%c0:%c1], v113 offset:%c2" ::"i"(32 + 4 * o.d), "i"(35 + 4 * o.d), "i"(o.d * 128));
  else if constexpr (o.k == 11) asm volatile("ds_read_b128 v[76:79], %0 offset:%c1" ::"v"(p.lw), "i"(o.d * 16));
  else if constexpr (o.k == 12) asm volatile("ds_write_b64 %0, v[126:127] offset:%c1" ::"v"(p.lx), "i"(o.d * 256));
  else if constexpr (o.k == 13)
    asm volatile("v_xor_b32 v112, %3, %2\n\tds_read_b128 v[%c0:%c1], v112 offset:%c4" ::"i"(32 + 4 * o.d), "i"(35 + 4 * o.d), "v"(p.lr), "i"(o.d * 32), "i"(o.d * 4096));
  else
    asm volatile("buffer_store_dwordx4 v[%c0:%c1], %2, " SW ", s93 offen sc1\n\ts_add_u32 s93, s93, 0x10000" ::"i"(32 + 4 * o.d), "i"(35 + 4 * o.d), "v"(p.vs) : SCC_ "memory");
}
template <int B, int... Is> DI void p_ops_(const PA& p, iseq<Is...>) { (p_op<B + Is>(p), ...); }
template <int Gg> DI void p_ops(const PA& p) { p_ops_<PS.first[Gg]>(p, make_iseq<PS.first[Gg + 1] - PS.first[Gg]>{}); }
constexpr float CS = 0.0625f * 1.4426950408889634f;
struct EOp { int k, d, s, g; };
struct ESch { EOp op[640]; int first[260], st[260], n; bool ok; };
constexpr int e_load(int g) { return g >= 64 ? ((g & 7) == 4 ? g >> 3 : -1) : g % 5 != 4 || g > 59 ? -1 : g < 40 ? g / 5 : g / 5 + 8; }
constexpr int e_nld(int g0, int g1) { int n = 0; for (int g = g0; g < g1; ++g) n += e_load(g) >= 0; return n; }
constexpr int e_qrd(int g) { return g >= 32 && g < 48 && (g & 1) ? (g - 32) / 2 : g >= 104 && g < 120 && (g & 1) ? 8 + (g - 104) / 2 : -1; }
constexpr ESch e_sched(int MODE) {
  ESch t{}, z{};
  int cap[260] = {}, used[260] = {}, dead[640] = {};
  const int NG = MODE == 0 ? 256 : 128;
  for (int g = 0; g < NG; ++g) {
    const int q = g & 15;
    if (MODE == 0) cap[g] = g_j(g) >= 0 ? 1 : (q == 1 || q == 2 || q == 3 || q == 5 || q == 7 || q == 9) ? 2 : q == 14 ? 4 : 3;
    else cap[g] = g < 16 ? 1 : (q == 1 || q == 2 || e_load(g) >= 0 || e_qrd(g) >= 0) ? 1 : 2;
  }
  cap[NG] = 1000;
  int n = 0, g = 0;
  auto put = [&](int k, int d, int s_, int early, int dl) {
    if (g < early) g = early < NG ? early : NG;
    while (used[g] >= cap[g]) ++g;
    ++used[g]; if (k == 3) ++used[g];
    t.op[n] = EOp{k, d, s_, g}; dead[n] = dl; ++n;
  };
  auto av = [](int m, int nf) { return 16 * m + 2 * nf + 10; };
  for (int j = 0; j < (MODE == 0 ? 32 : 16); ++j) {
    const int m0 = MODE == 2 ? (j >> 3) * 2 : j >> 2, n0 = MODE == 2 ? j & 7 : (j & 3) * 2;
    const int m1 = MODE == 2 ? m0 + 1 : m0, n1 = MODE == 2 ? n0 : n0 + 1;
    const int T = j < 16 ? 128 + 8 * j : 112 + 8 * (j & 1), dl0 = 128 + 32 * (j >> 3) + 2 * (j & 7) - 1, dl1 = j < 16 ? dl0 : 999;
    const int pace = MODE == 0 ? 0 : (MODE == 1 ? 14 : 26) + j * (MODE == 1 ? EPACE_K : EPACE_V);
    if (j >= 16) for (int k = 0; k < 8; ++k) put(0, T + k, 8 * (j - 16) + k, k < 4 ? av(m0, n0) : av(m1, n1), dl0);
    for (int h = 0; h < 2; ++h) {
      const int e = h ? av(m1, n1) : av(m0, n0);
      if (MODE == 0) for (int k = 0; k < 4; ++k) put(1, T + 4 * h + k, 0, e, dl1);
      for (int k = 0; k < 2; ++k) put(2, T + 2 * h + k, T + 4 * h + 2 * k, e, dl1);
    }
    put(3, T, j, j < 16 || MODE ? pace : 0, dl1);
  }
  z.ok = true;
  for (int q = 0; q <= NG; ++q) {
    z.first[q] = z.n; z.st[q + 1] = z.st[q];
    for (int i = 0; i < n; ++i) if (t.op[i].g == q) {
      z.op[z.n++] = t.op[i];
      if (t.op[i].k == 3) ++z.st[q + 1];
      if (MODE == 0 && q > dead[i]) z.ok = false;
    }
  }
  z.first[NG + 1] = z.n;
  return z;
}
constexpr ESch ES[3] = {e_sched(0), e_sched(1), e_sched(2)};
static_assert(ES[0].ok && ES[0].first[256] == ES[0].n && ES[1].st[126] == 16 && ES[2].st[126] == 16);
template <int MODE, int I> DI void e_op() {
  constexpr EOp o = ES[MODE].op[I];
  if constexpr (o.k == 0) asm volatile("v_accvgpr_read_b32 v%c0, a[%c1]" ::"i"(o.d), "i"(o.s));
  else if constexpr (o.k == 1) asm volatile("v_mul_f32_e32 v%c0, s94, v%c0" ::"i"(o.d));
  else if constexpr (o.k == 2) asm volatile("v_cvt_pk_bf16_f32 v%c0, v%c1, v%c2" ::"i"(o.d), "i"(o.s), "i"(o.s + 1));
  else {
    constexpr int m = o.s >> 2;
    if constexpr ((o.s & 3) == 0)
      asm volatile("s_mov_b32 s93, %0" ::"i"(MODE == 2 ? (m >> 1) * 16384 + (m & 1) * 4096 : MODE == 1 ? m * 8192 : (m < 4 ? 2 * m : 15 - 2 * m) * 8192));
    asm volatile("buffer_store_dwordx4 v[%c0:%c1], v%c2, " SW ", s93 offen offset:%c3 sc1" ::"i"(o.d), "i"(o.d + 3), "i"(MODE ? 84 : 95), "i"((o.s & 3) * 1024) : "memory");
  }
}
template <int MODE, int B, int... Is> DI void e_ops_(iseq<Is...>) { (e_op<MODE, B + Is>(), ...); }
template <int MODE, int Gg> DI void e_ops() { e_ops_<MODE, ES[MODE].first[Gg]>(make_iseq<ES[MODE].first[Gg + 1] - ES[MODE].first[Gg]>{}); }
template <int P, int KS, int I> DI void g_wread(const GM& g) {
  constexpr int nf = I >> 1, hf = I & 1, r = 128 + 64 * P + 8 * nf + 4 * KS + 2 * hf;
  asm volatile("ds_read_b64_tr_b16 a[%c1:%c2], %0 offset:%c3" ::"v"(g.aB[hf][nf]), "i"(r), "i"(r + 1), "i"(P * 32768 + KS * 16384));
}
template <int P, int M, int KS> DI void g_aread(const GM& g) {
  constexpr int r = 96 + 8 * (M & 1) + 4 * KS;
  asm volatile("ds_read_b128 v[%c1:%c2], %0 offset:%c3" ::"v"(g.aA[KS]), "i"(r), "i"(r + 3), "i"(P * 32768 + M * 2048));
}
template <bool W, int V, int J> DI void g_dma() {
  if constexpr (W) {
    if constexpr (J == 0) asm volatile("buffer_load_dwordx4 v%c0, s[68:71], 0 offen" GSC " lds" ::"i"(V) : "memory");
    else asm volatile("buffer_load_dwordx4 v%c0, s[68:71], s%c1 offen" GSC " lds" ::"i"(V), "i"(79 + J) : "memory");
  } else {
    if constexpr (J == 0) asm volatile("buffer_load_dwordx4 v%c0, s[64:67], 0 offen" GSC " lds" ::"i"(V) : "memory");
    else asm volatile("buffer_load_dwordx4 v%c0, s[64:67], s%c1 offen" GSC " lds" ::"i"(V), "i"(71 + J) : "memory");
  }
}
template <int Q> DI void att_item() {
  if constexpr (Q == 0) sld<83, B_ST, 1>();
  if constexpr (Q == 1) asm volatile("s_mov_b32 s85, %0" ::"i"(B_PM));
  if constexpr (Q == 2) asm volatile("s_add_u32 s84, s83, 0x8000\n\ts_mov_b32 s86, %0" ::"i"(B_ACT) SCC1);
  if constexpr (Q >= 3 && Q < 6) asm volatile("v_mov_b64 v[%c0:%c1], 0" ::"i"(108 + 2 * Q), "i"(109 + 2 * Q));
  if constexpr (Q == 6) asm volatile("v_mov_b32 v120, 0x45800000");
}
template <int I> DI void g_pro1();
template <int K, int I> DI void g_part() {
  constexpr bool W = K < 2;
  asm volatile("s_add_u32 m0, s%c0, %1\n\ts_nop 0" ::"i"(96 + K), "i"(W ? I * 4096 : (I & 1) * 4096 + (I >> 1) * 16384) : "scc");
  g_dma<W, 115 + K, W ? I : (I & 1) + (I >> 1) * 4>();
}
template <int Y, int I> DI void g_tag() {
  if constexpr (I == 0) { if constexpr (Y) sld<88, C_QT, 2>(); else { sld<88, A_TAB>(); sld<89, A_TAB + 1>(); } }
  if constexpr (I == 1 && !Y) asm volatile("s_lshr_b64 s[88:89], s[88:89], 12" SCC3);
  if constexpr (I == 3) asm volatile("v_readfirstlane_b32 s60, v120");
  if constexpr (I == 5) asm volatile("v_readfirstlane_b32 s61, v121");
  if constexpr (I == 9) asm volatile("s_mul_i32 s61, s61, 0x4c957f2d\n\ts_mul_i32 s63, s60, 0x5851f42d");
  if constexpr (I == 11) asm volatile("s_add_u32 s61, s61, s63\n\ts_mul_hi_u32 s63, s60, 0x4c957f2d" SCC3);
  if constexpr (I == 13) asm volatile("s_add_u32 s61, s61, s63\n\ts_mul_i32 s60, s60, 0x4c957f2d" SCC3);
  if constexpr (I == 15) asm volatile("s_add_u32 s60, s60, 0xf767814f\n\ts_addc_u32 s61, s61, 0x14057b7e" SCC3);
  if constexpr (I == 17) asm volatile("v_mov_b64 v[82:83], s[60:61]");
}
template <bool TR, int G> DI void g_last_setup(int wid) {
  if constexpr (G == 0) { sld<72, A_SX>(); sld<76, A_MX>(); }
  if constexpr (G >= 1 && G < 4) sld<72 + G, A_SX + G>();
  if constexpr (G == 3) sld<63, A_VOO>();
  if constexpr (G >= 5 && G < 8) sld<72 + G, A_MX + G - 4>();
  if constexpr (G == 6) asm volatile("v_add_u32 v84, s63, v89");
  if constexpr (G == 7) asm volatile("s_lshl_b32 s63, %0, 10\n\ts_lshl_b32 s95, %0, 12" ::"s"(wid) SCC1);
  if constexpr (G == 8) asm volatile("v_add_u32 v85, %0, v84\n\tv_add_u32 v86, 0x10000, v89" ::"i"(TR ? 4096 : 8192));
  if constexpr (G == 9) asm volatile("v_add_u32 v87, s63, v86\n\tv_add_u32 v95, s95, v86");
  if constexpr (G == 10) asm volatile("s_lshl_b32 s80, %0, 13\n\ts_bitset1_b32 s80, 16" ::"s"(wid) SCC1);
  if constexpr (G == 11) asm volatile("s_add_u32 s81, s80, 0x8000\n\ts_mov_b32 s90, 0x8000" SCC3);
  if constexpr (G == 13) { sld<62, A_GF>(); asm volatile("s_mov_b32 s91, 0xc000"); }
  if constexpr (G == 14) { att_item<0>(); att_item<1>(); }
  if constexpr (G == 15) { att_item<2>(); sld<92, A_GH>(); }
  if constexpr (G == 12) { sld<87, B_MS, 1>(); asm volatile("s_add_u32 s82, s73, 2" SCC3); }
}
template <bool TR, int P, int H, int G, int MD, int PT> DI void g_gap(const GM& g, int wid, const PA& pa) {
  constexpr bool FIRST = MD == 1 || MD == 2, LAST = MD == 4;
  constexpr int m = 4 * H + (G >> 4), q = G & 15, n = (SNK & 8) && MD < 2 && (m & 1) ? 7 - (q >> 1) : q >> 1, ks = (SNK & 4 ? q ^ (q >> 1) : q) & 1;
  constexpr int a = 96 + 8 * (m & 1) + 4 * ks, b = 128 + 64 * P + 8 * n + 4 * ks, d0 = TR ? 64 * (m >> 1) + 8 * n + 4 * (m & 1) : 32 * m + 4 * n, d = m < 4 ? 128 + d0 : d0 - 128;
  constexpr bool ST0 = MD == 1 && H == 0;
  constexpr int J = LAST ? -1 : ST0 ? (G >= 48 && G % 4 == 0 ? G / 4 - 8 : -1) : g_j(G);
  constexpr int gj = (J & 3) ^ ((J & 3) >> 1);
  constexpr int EM = LAST ? 1 + TR : 0, eg = (MD == 2 ? 128 : 0) + 64 * H + G, L = LAST ? e_load(eg) : -1;
  if constexpr (MD == 1 && G == 32) asm volatile("s_waitcnt vmcnt(%0) lgkmcnt(%1)\n\ts_barrier" ::"i"(H ? 12 + (PT >= 0 ? PS.vm[eg] : 0) : 19), "i"(8 * H) : "memory");
  else if constexpr (q == 0 && G > 0) asm volatile("s_waitcnt lgkmcnt(%0)" ::"i"(LAST ? 0 : MD == 1 ? 8 * H : 4));
  if constexpr (J == 0) asm volatile("s_add_u32 m0, s79, %0" ::"i"((P ^ H ^ 1) * 32768 + (1 - H) * 8192) SCC1);
  else if constexpr (J == 4) asm volatile("s_add_u32 m0, s87, %0" ::"i"((P ^ H) * 32768 + (1 - H) * 16384) SCC1);
  else if constexpr (J > 0 && (J & 3) == 1) asm volatile("s_bitset1_b32 m0, 12");
  else if constexpr (J > 0 && (J & 3) == 2) asm volatile("s_bitset1_b32 m0, %0" ::"i"(J < 4 ? 14 : 13));
  else if constexpr (J > 0) asm volatile("s_bitset0_b32 m0, 12");
  if constexpr (L >= 0 && (L & 3) == 0) asm volatile("s_mov_b32 m0, s%c0" ::"i"(L < 16 ? 76 + (L >> 2) : 87));
  constexpr int QR = LAST ? e_qrd(eg) : -1;
  if constexpr (QR == 0) asm volatile("s_waitcnt vmcnt(%0)" ::"i"(8 + e_nld(0, eg) + ES[EM].st[eg]) : "memory");
  if constexpr (QR == 8) asm volatile("s_waitcnt vmcnt(%0)" ::"i"(e_nld(64, eg) + ES[EM].st[eg] - ES[EM].st[60]) : "memory");
#define GMF(D, X, Y, C) asm volatile("v_mfma_f32_16x16x32_bf16 " D "[%c0:%c1], " X ", " Y ", " C ::"i"(d), "i"(d + 3), "i"(a), "i"(a + 3), "i"(b), "i"(b + 3))
#define GMF2(D, C) do { if constexpr (TR) GMF(D, "v[%c2:%c3]", "a[%c4:%c5]", C); else GMF(D, "a[%c4:%c5]", "v[%c2:%c3]", C); } while (0)
  if constexpr (FIRST && !(q & 1)) { if constexpr (m < 4) GMF2("v", "0"); else GMF2("a", "0"); }
  else if constexpr (m < 4) GMF2("v", "v[%c0:%c1]");
  else GMF2("a", "a[%c0:%c1]");
#undef GMF2
#undef GMF
  if constexpr (J >= 0 && J < 4) g_dma<false, 84, (1 - H) * 2 + (gj & 1) + (gj >> 1) * 4>();
  else if constexpr (J >= 4) g_dma<true, 85, (1 - H) * 4 + gj>();
  if constexpr (L >= 0)
    asm volatile("buffer_load_dwordx4 v89, s[%c0:%c1], s%c2 offen offset:%c3 sc1 lds" ::"i"(L >= 4 && L < 8 || L >= 16 ? 68 : 56), "i"(L >= 4 && L < 8 || L >= 16 ? 71 : 59),
                 "i"(L < 16 ? 72 + (L >> 2) : 82), "i"((L & 3) * 1024) : "memory");
  if constexpr (QR >= 0)
    asm volatile("ds_read_b128 a[%c1:%c2], v%c0 offset:%c3" ::"i"(QR < 12 ? 87 : 95), "i"(128 + 4 * QR), "i"(131 + 4 * QR), "i"(QR < 12 ? QR * 4096 : 49152 + (QR - 12) * 1024));
  if constexpr ((q == 1 || q == 2) && !(LAST && m == 7)) g_aread<(m < 7 ? P : P ^ 1), (m + 1) & 7, q - 1>(g);
  if constexpr (!LAST) {
    if constexpr (MD == 1 && H == 1 && q >= 3 && q < 11) g_wread<P ^ 1, (G >> 5), ((G >> 4) & 1) * 8 + q - 3>(g);
    if constexpr (MD != 1 && q >= 3 && q < 11 && (q & 1)) g_wread<P ^ 1, H, (G >> 4) * 4 + (q - 3) / 2>(g);
    if constexpr (MD == 1 && eg >= 62 && eg < 80) g_tag<P, eg - 62>();
    if constexpr (H == 1 && G == 0) asm volatile("s_mov_b32 s90, s91");
    if constexpr (H == 1 && G == 4) asm volatile("v_lshl_add_u32 v84, s90, 7, v86\n\ts_bfe_u32 s91, s88, 0x40000" SCC3);
    if constexpr (H == 1 && G == 6) asm volatile("s_lshr_b64 s[88:89], s[88:89], 4\n\ts_lshl_b32 s92, s91, 19" SCC3);
    if constexpr (H == 1 && G == 10) asm volatile("v_add_u32 v85, s92, v87");
  } else if constexpr (eg < 16) g_last_setup<TR, eg>(wid);
  if constexpr (ST0 && G == 23) asm volatile("v_lshl_add_u32 v85, s91, 19, v87");
  if constexpr (ST0 && G < 36 && G % 3 == 0) g_part<G < 12 ? 2 : G < 24 ? 1 : 3, (G / 3) & 3>();
  if constexpr (ST0 && G >= 36 && G < 48 && G % 3 == 0) g_pro1<16 + G / 3>();
  if constexpr (MD == 3 && eg == 0) sld<63, A_VOQ>();
  if constexpr (MD == 3 && eg == 4) asm volatile("v_add_u32 v95, s63, v89\n\ts_mov_b32 s94, %0" ::"i"(__builtin_bit_cast(unsigned, CS)));
  if constexpr (MD >= 2) e_ops<EM, eg>();
  constexpr int pg = 128 * PT + 64 * H + G;
  if constexpr (PT == 2 && eg == 0) sld<93, A_ZO>();
  if constexpr (PT >= 0) p_ops<pg>(pa);
  if constexpr (G == 61 && !LAST) {
    constexpr int ns = MD >= 2 ? ES[0].st[eg + 1] - ES[0].st[eg >= GD13 ? eg - GD13 : 0] : PT >= 0 ? PS.vm[pg + 1] - PS.vm[pg >= GD13 ? pg - GD13 : 0] : 0;
    asm volatile("s_waitcnt vmcnt(%0) lgkmcnt(0)\n\ts_barrier" ::"i"(12 + ns) : "memory");
  } else if constexpr (G == 61) asm volatile("s_waitcnt lgkmcnt(0)\n\ts_barrier" ::: "memory");
  GAP();
}
template <bool TR, int P, int H, int MD, int PT, int... Gs> DI void g_half_(const GM& g, int wid, const PA& pa, iseq<Gs...>) { (g_gap<TR, P, H, Gs, MD, PT>(g, wid, pa), ...); }
template <bool TR, int P, int H, int MD> DI void g_half(const GM& g, int wid = 0) { g_half_<TR, P, H, MD, -1>(g, wid, PA{}, make_iseq<64>{}); }
template <int T, int H> DI void q_half(const GM& g, const PA& pa) { g_half_<false, (T & 1) ^ 1, H, T == 0, T>(g, 0, pa, make_iseq<64>{}); }
typedef __attribute__((ext_vector_type(16))) unsigned u32x16;
struct RSV { u32x16 a, b, c; };
DI void rsv_begin(RSV& r) { asm volatile("rsv_begin:" : "={v[32:47]}"(r.a), "={v[48:63]}"(r.b), "={v[64:79]}"(r.c)); }
DI void rsv_end(const RSV& r) { asm volatile("rsv_end:" ::"{v[32:47]}"(r.a), "{v[48:63]}"(r.b), "{v[64:79]}"(r.c)); }
template <int P, int... Is> DI void g_read0(const GM& g, iseq<Is...>) {
  (g_wread<P, 0, Is>(g), ...); (g_wread<P, 1, Is>(g), ...);
  g_aread<P, 0, 0>(g); g_aread<P, 0, 1>(g);
}
template <int I> DI void g_pro1() {
  constexpr int grp = I >> 2, i = I & 3, t = grp < 4 ? 0 : grp < 7 ? 1 : 2;
  constexpr bool isA = grp == 0 || grp == 3 || grp == 5;
  constexpr int jj = isA ? (grp == 3 ? 2 : 0) + (i & 1) + (i >> 1) * 4 : (grp == 2 || grp == 6 ? 4 : 0) + i;
  asm volatile("s_add_u32 m0, s%c0, %1\n\ts_nop 0" ::"i"(t == 1 ? 92 : 90), "i"((isA ? 0 : 65536) + jj * 4096) : "scc");
  g_dma<!isA, (isA ? 84 : 85) + (t ? 0 : 2), jj>();
}
template <int B, int... Is> DI void g_pro(iseq<Is...>) { (g_pro1<B + Is>(), ...); }
template <bool TR, bool SCL, class HO, class HE> DI void g_loop(const GM& g, HO&& odd, HE&& even) {
  constexpr int nkt = SCL ? 7 : 16, P0 = SCL ? 1 : 0;
#pragma unroll 1
  for (int t = 1;; t += 2) {
    g_half<TR, P0 ^ 1, 0, 0>(g);
    odd(t);
    GAP();
    g_half<TR, P0 ^ 1, 1, 0>(g);
    if (SCL && t + 2 >= nkt) break;
    g_half<TR, P0, 0, 0>(g);
    even(t + 1);
    GAP();
    g_half<TR, P0, 1, 0>(g);
    if (!SCL && t + 3 >= nkt) break;
  }
}

typedef __attribute__((ext_vector_type(4))) unsigned u32x4;
typedef __attribute__((ext_vector_type(4))) float f32x4;

constexpr int R_S = 128, R_P = 192, R_CI = 224, R_E = 96;
constexpr int R_TS = 112, R_RS = 114, R_M = 116;
constexpr int R_PD = 118, R_THR = 120, R_TMP = 121;
struct AC {
  u64 redo;
  unsigned rare;
};
template <int T, int D, int K> DI void d_op() {
  constexpr int t = T + 8 * (D & 1);
  if constexpr (K < 8) asm volatile("v_accvgpr_read_b32 v%c0, a[%c1]" ::"i"(t + K), "i"(8 * D + K));
  else if constexpr (K < 12) asm volatile("v_cvt_pk_bf16_f32 v%c0, v%c1, v%c2" ::"i"(t + K - 8), "i"(t + 2 * (K - 8)), "i"(t + 2 * (K - 8) + 1));
  else
    asm volatile("buffer_store_dwordx4 v[%c0:%c1], v%c2, " SW ", s%c3 offen offset:%c4 sc1" ::"i"(t), "i"(t + 3), "i"(84 + ((D >> 2) & 1)), "i"(90 + (D >> 3)),
                 "i"((D & 3) * 1024) : "memory");
}
template <int T, int O0, int... Os> DI void d_ops(iseq<Os...>) { (d_op<T, (O0 + Os) / 13, (O0 + Os) % 13>(), ...); }
template <int D> DI void d_store() { d_ops<160, 13 * D>(make_iseq<13>{}); }
template <int X, int F> DI void kread() {
  constexpr int r = 192 + 4 * (F & 15);
  asm volatile("ds_read_b128 a[%c0:%c1], v89 offset:%c2" ::"i"(r), "i"(r + 3), "i"(X * 32768 + F * 1024));
}
template <int X, int F> DI void vread() {
  constexpr int r = 192 + 4 * (F & 15);
  asm volatile("ds_read_b128 a[%c0:%c1], v86 offset:%c2" ::"i"(r), "i"(r + 3), "i"(X * 32768 + (F & 1) * 16384 + (F >> 1) * 1024));
}
template <int X, int... Fs> DI void kreads(iseq<Fs...>) { (kread<X, Fs>(), ...); }
template <int X, int... Fs> DI void vreads(iseq<Fs...>) { (vread<X, Fs>(), ...); }
template <int X, int J> DI void dma_m0() {
  if constexpr (J == 0) asm volatile("s_mov_b32 m0, s%c0" ::"i"(80 + X));
  else if constexpr (J == 4) asm volatile("s_or_b32 m0, s%c0, 0x1000" ::"i"(80 + X) SCC1);
  else if constexpr (J == 8) asm volatile("s_bitset0_b32 m0, 16");
  else if constexpr (J == 12) asm volatile("s_bitset0_b32 m0, 12");
}
template <int J> DI void dma_at() {
  asm volatile("buffer_load_dwordx4 v%c0, " SW ", s%c1 offen offset:%c2 sc1 lds" ::"i"(J >= 4 && J < 12 ? 91 : 90), "i"(J < 8 ? 82 : 84), "i"((J & 3) * 1024) : "memory");
}
template <int V, int S> DI void dma4() {
  asm volatile("buffer_load_dwordx4 v%c0, " SW ", s%c1 offen sc1 lds\n\tbuffer_load_dwordx4 v%c0, " SW ", s%c1 offen offset:1024 sc1 lds\n\t"
               "buffer_load_dwordx4 v%c0, " SW ", s%c1 offen offset:2048 sc1 lds\n\tbuffer_load_dwordx4 v%c0, " SW ", s%c1 offen offset:3072 sc1 lds"
               ::"i"(V), "i"(S) : "memory");
}
template <int Q> DI void k01_dma() {
  asm volatile("s_add_u32 m0, s80, %0\n\ts_nop 0" ::"i"((Q >> 1) * 32768 + (Q & 1) * 4096 - 65536) SCC1);
  dma4<90 + (Q & 1), 83 + (Q >> 1)>();
}
template <int R, int I, int ST = 4, int V = 89> DI void f_load() {
  asm volatile("buffer_load_dwordx4 a[%c0:%c1], v%c3, " SW ", s72 offen offset:%c2 sc1" ::"i"(R + ST * I), "i"(R + ST * I + 3), "i"((I & 3) * 1024), "i"(V) : "memory");
}
DI void f_next() { asm volatile("s_add_u32 s72, s72, 0x1000" SCC3); }
constexpr int o_wreg(int o, int f) { return o == 0 ? 128 + 4 * f : o == 3 ? 192 + 4 * f : 8 * f + 4 * (o - 1); }
template <int F, int LASTF = 3> DI void w_load() {
  f_load<o_wreg(F >> 4, 0), F & 15, o_wreg(F >> 4, 1) - o_wreg(F >> 4, 0), (F & 1) ? 84 : 89>();
  if constexpr ((F & 3) >= LASTF) asm volatile("s_add_u32 s72, s72, %0" ::"i"(F < 4 ? 0x1000 : F < 8 ? 0xf000 : F & 4 ? 0x7000 : 0x1000) SCC1);
}
#define NWF 40
constexpr int CV_KP = 5120 - 32, CV_KS = 10304 - 32;
struct CV { unsigned pa[4], pb, lw, vzo; };
constexpr int cv_J(int q) { int J = 7; while (q > J) { q -= J + 1; --J; } return J; }
constexpr int cv_i(int q) { int J = 7; while (q > J) { q -= J + 1; --J; } return q; }
template <int r_, int N> DI void cv_rd(const CV& c) {
  constexpr int R = (r_ + 1) & 7;
  if constexpr (N < 16) {
    constexpr int i = N >> 1, o = 16 * R + 128 * (i & 1) + 2 * (N & 1), r = 192 + 32 * (r_ & 1) + 2 * N;
    asm volatile("ds_read2_b32 a[%c1:%c2], %0 offset0:%c3 offset1:%c4" ::"v"(c.pa[i >> 1]), "i"(r), "i"(r + 1), "i"(o), "i"(o + 1));
  } else {
    constexpr int m = N - 16, r = 160 + 32 * (r_ & 1) + 4 * m;
    asm volatile("ds_read_b128 v[%c1:%c2], %0 offset:%c3" ::"v"(c.pb), "i"(r), "i"(r + 3), "i"(64 * R + 512 * (7 - m)));
  }
}
template <int O> DI void cv_z(const CV& c) {
  constexpr int pr = O / 7, o = O % 7, w = 128 + 4 * (7 - 2 * pr), y = w + 2 * (o & 1) - 4 * (o >> 1);
  if constexpr (o == 4) asm volatile("s_add_u32 s63, s65, %2\n\tv_permlane16_swap_b32_e32 v%c0, v%c1" ::"i"(w), "i"(w + 2), "i"((6 - 2 * pr) * 8 * 3584) SCC1);
  else if constexpr (o == 5) asm volatile("v_permlane16_swap_b32_e32 v%c0, v%c1" ::"i"(w + 1), "i"(w + 3));
  else if constexpr (o == 6) asm volatile("buffer_store_dwordx4 v[%c0:%c1], %2, " SW ", s63 offen sc1" ::"i"(w), "i"(w + 3), "v"(c.vzo) : "memory");
  else asm volatile("v_cvt_pk_bf16_f32 v%c0, v%c1, v%c2" ::"i"(w + o), "i"(y), "i"(y + 1));
}
#define CVS 6
#define CVF 150
#define CVP 206
#define CVK 250
constexpr int CVQ = 16 * CVS + 2;
constexpr int cv_vm(int T) {
  return T < 16 * CVS && T % CVS == CVS - 1 ? 1 : T >= CVQ && T < CVQ + 96 && (T - CVQ) % 6 == 0 ? 2 : T == CVF ? 4 : T == CVP ? 5 : T > CVK && T <= CVK + 48 && (T - CVK) % 3 == 1 ? 7 : 0;
}
constexpr int cv_cnt(int T0, int T1, int kind = 0) { int n = 0; for (int T = T0; T < T1; ++T) n += kind ? cv_vm(T) == kind : cv_vm(T) != 0; return n; }
static_assert(16 * CVS + 4 <= 108 && cv_vm(CVF) == 4 && cv_vm(CVP) == 5 && CVQ + 96 < CVK);
constexpr int cv_z0(int T) { return T < 271 ? 0 : T < 278 ? T - 271 : T < 281 ? 7 : T - 274; }
template <int I> DI void k01_dma1() {
  if constexpr ((I & 3) == 0) asm volatile("s_add_u32 m0, s80, %0\n\ts_nop 0" ::"i"((I >> 3) * 32768 + ((I >> 2) & 1) * 4096 - 65536) SCC1);
  asm volatile("buffer_load_dwordx4 v%c0, " SW ", s%c1 offen offset:%c2 sc1 lds" ::"i"(90 + ((I >> 2) & 1)), "i"(83 + (I >> 3)), "i"((I & 3) * 1024) : "memory");
}
template <int T> DI void cv_slot(const CV& c) {
  constexpr int r = T / 36, Q = T % 36, i = cv_i(Q), J = cv_J(Q), d = 128 + 4 * J, a = 192 + 32 * (r & 1) + 4 * i, b = 160 + 32 * (r & 1) + 4 * (J - i), vm = cv_vm(T);
  if constexpr (Q == 0) asm volatile("s_waitcnt lgkmcnt(0)");
  if constexpr (r == 0 && i == 0)
    asm volatile("v_mfma_f32_16x16x32_bf16 v[%c0:%c1], a[%c2:%c3], v[%c4:%c5], 0" ::"i"(d), "i"(d + 3), "i"(a), "i"(a + 3), "i"(b), "i"(b + 3));
  else
    asm volatile("v_mfma_f32_16x16x32_bf16 v[%c0:%c1], a[%c2:%c3], v[%c4:%c5], v[%c0:%c1]" ::"i"(d), "i"(d + 3), "i"(a), "i"(a + 3), "i"(b), "i"(b + 3));
  if constexpr (r == 7 && i == 0) asm volatile("v_mfma_f32_16x16x32_bf16 v[%c0:%c1], v[96:99], v[%c2:%c3], v[%c0:%c1]" ::"i"(d), "i"(d + 3), "i"(b), "i"(b + 3));
  if constexpr (Q < 24 && r < 7) cv_rd<r + 1, Q>(c);
  if constexpr (T == 32) asm volatile("ds_read_u16 v121, %0 offset:5120" ::"v"(c.lw));
  if constexpr (T == 33) asm volatile("ds_write_b128 %0, v[114:117] offset:%c1\n\tds_write_b128 %0, v[114:117] offset:%c2" ::"v"(c.lw), "i"(CV_KS), "i"(CV_KS + 16));
  if constexpr (T == 62) asm volatile("ds_write_b16 %0, v121 offset:%c1" ::"v"(c.lw), "i"(CV_KS + 30));
  if constexpr (T == 34) sld<72, A_OQ>();
  if constexpr (T == 35) sld<65, A_ZO>();
  if constexpr (T == CVQ - 2) { asm volatile("s_waitcnt vmcnt(16)" ::: "memory"); fl_check(); }
  if constexpr (T < 16 * CVS) d_ops<224, 13 * (T / CVS) + (T % CVS * 13) / CVS>(make_iseq<((T % CVS + 1) * 13) / CVS - (T % CVS * 13) / CVS>{});
  if constexpr (vm == 2) { f_load<128, (T - CVQ) / 6>(); if constexpr ((T - CVQ) / 6 % 4 == 3) f_next(); }
  if constexpr (vm == 4) {
    asm volatile("s_waitcnt vmcnt(%0)\n\ts_barrier" ::"i"(cv_cnt(16 * CVS, T)) : "memory");
    set_flag<A_FG>();
  }
  if constexpr (T == CVP - 3) fl_mask<A_N01, -1>();
  if constexpr (vm == 5) fl_issue();
  if constexpr (T == CVK) { asm volatile("s_waitcnt vmcnt(%0)" ::"i"(cv_cnt(CVP + 1, T)) : "memory"); fl_check(); }
  if constexpr (vm == 7) k01_dma1<cv_cnt(CVK, T, 7)>();
  if constexpr (cv_z0(T + 1) > cv_z0(T)) cv_z<cv_z0(T)>(c);
  GAP();
}
template <int... T> DI void cv_slots(const CV& c, iseq<T...>) { (cv_slot<T>(c), ...); }
template <int... N> DI void cv_rd0(const CV& c, iseq<N...>) { (cv_rd<0, N>(c), ...); }
template <int O> DI void cv_tail(const CV& c) {
  constexpr int k = cv_cnt(CVK, 288, 7) + O / 6;
  if constexpr (O % 6 == 0 && k < 16) k01_dma1<k>();
  cv_z<cv_z0(288) + O>(c);
}
template <int... O> DI void cv_tails(const CV& c, iseq<O...>) { (cv_tail<O>(c), ...); }
static_assert(cv_cnt(CVK, 288, 7) >= 8 && cv_cnt(CVK, 288, 7) + 3 >= 16);
constexpr int cv_end() {
  int T = CVK; while (cv_cnt(CVK, T + 1, 7) < 8) ++T;
  int n = 8; for (int o = cv_z0(T); o < 28; ++o) n += o % 7 == 6;
  return n;
}

template <int Q> DI void acc_zero() {
  asm volatile("v_mfma_f32_32x32x16_bf16 a[%c0:%c1], v[208:211], v[208:211], 0" ::"i"(16 * Q), "i"(16 * Q + 15));
}
struct SMS { int ex[128], op0[128], nop[128], last; };
constexpr SMS sm_sched() {
  SMS z{};
  int et[32] = {}, e = 0;
  for (int t = 0; t < 128; ++t) {
    const int g = t & 15;
    const bool x = t < 48 ? (g >= 8 && (g & 1)) : (g >= 8 && g != 10 && g != 13 && g != 15);
    z.ex[t] = x && e < 32 ? e : -1;
    if (z.ex[t] >= 0) et[e++] = t;
  }
  int o = 0;
  for (int t = 0; t < 128; ++t) {
    const int g = t & 15;
    int cap = g < 8 ? (g == 0 ? 0 : 1) : (z.ex[t] >= 0 || t < 48 ? 0 : 2);
    z.op0[t] = o; z.nop[t] = 0;
    while (cap > 0 && o < 48 && et[2 * (o / 3) + (o % 3 == 0 ? 0 : 1)] + 2 <= t) {
      const int el = 2 * (o / 3) + o % 3;
      if (!(o % 3 < 2 && (el == 0 || el == 4))) --cap;
      ++o; ++z.nop[t]; z.last = t;
    }
  }
  return z;
}
constexpr SMS SM = sm_sched();
static_assert(SM.last <= 115);
constexpr int p_reg(int Y, int k) { return R_P + 16 * Y + 4 * ((k >> 3) * 2 + ((k >> 1) & 1)) + ((k >> 2) & 1) * 2 + (k & 1); }
template <int Y, int O> DI void sm_op() {
  constexpr int k = O / 3, e = 2 * k + O % 3, ts = R_TS + ((e >> 2) & 1);
  if constexpr (O % 3 == 2) asm volatile("v_cvt_pk_bf16_f32 v%c0, v%c1, v%c2" ::"i"(p_reg(Y, k)), "i"(R_E + ((2 * k) & 15)), "i"(R_E + ((2 * k + 1) & 15)));
  else if constexpr (e == 1 || e == 5) asm volatile("v_add_f32_e32 v%c0, v%c1, v%c2" ::"i"(ts), "i"(R_E + e - 1), "i"(R_E + e));
  else if constexpr (e != 0 && e != 4) asm volatile("v_add_f32_e32 v%c0, v%c0, v%c1" ::"i"(ts), "i"(R_E + (e & 15)));
}
template <int Y, int T> DI void sm_gap(u64& redo) {
  if constexpr (SM.ex[T] >= 0) asm volatile("v_exp_f32 v%c0, v%c1" ::"i"(R_E + (SM.ex[T] & 15)), "i"(R_S + 32 * Y + SM.ex[T]));
  if constexpr (SM.nop[T] > 0) sm_op<Y, SM.op0[T]>();
  if constexpr (SM.nop[T] > 1) sm_op<Y, SM.op0[T] + 1>();
  if constexpr (SM.nop[T] > 2) sm_op<Y, SM.op0[T] + 2>();
  if constexpr (T == SM.last + 1) asm volatile("v_add_f32_e32 v%c0, v%c1, v%c2" ::"i"(R_TMP), "i"(R_TS), "i"(R_TS + 1));
  if constexpr (T == SM.last + 2) asm volatile("v_cmp_nle_f32_e64 %0, v%c1, v%c2" : "=s"(redo) : "i"(R_TMP), "i"(R_THR));
}
#define EVG 35
template <int X, int KB, int QB, int S_, bool FIRST, bool HEAD = S_ == 0> DI void qk_mfma() {
  constexpr int r = 192 + 4 * ((KB & 1) * 8 + S_), q = 128 + 4 * (QB * 8 + S_);
  constexpr int d = R_S + 32 * X + 4 * (KB * 2 + QB), ci = HEAD ? R_CI + 4 * (KB * 2 + QB) : d;
  if constexpr (FIRST && HEAD)
    asm volatile("v_mfma_f32_16x16x32_bf16 v[%c0:%c1], a[%c2:%c3], a[%c4:%c5], 0" ::"i"(d), "i"(d + 3), "i"(r), "i"(r + 3), "i"(q), "i"(q + 3));
  else
    asm volatile("v_mfma_f32_16x16x32_bf16 v[%c0:%c1], a[%c2:%c3], a[%c4:%c5], v[%c6:%c7]" ::"i"(d), "i"(d + 3), "i"(r), "i"(r + 3), "i"(q), "i"(q + 3),
                 "i"(ci), "i"(ci + 3));
}
template <int X, int FIRST, bool NOQK, int G> DI void gapA(AC& c) {
  constexpr int Xp = X ^ 1, kb = G >> 4, g = G & 15;
  if constexpr (NOQK && G < 48 && G % 3 == 0) { f_load<128, G / 3>(); if constexpr (G % 12 == 9) f_next(); }
  if constexpr (G == 48) asm volatile("s_waitcnt vmcnt(%0) lgkmcnt(0)\n\ts_barrier" ::"i"(NOQK ? 16 : FIRST == 2 ? 63 : FIRST == 1 ? 9 : FIRST == 3 ? 8 : 0) : "memory");
  else if constexpr (g == 0 && !NOQK) asm volatile("s_waitcnt lgkmcnt(0)");
  if constexpr (!NOQK) {
    if constexpr (SNK & 1) qk_mfma<X, kb, (g >> 3) ^ (kb & 1), g < 8 ? g : 15 - g, FIRST != 0, !(g & 7)>();
    else qk_mfma<X, kb, (g >> 3), g & 7, FIRST != 0>();
    if constexpr (g < 8 && kb < 3) kread<X, (kb + 1) * 8 + g>();
    if constexpr (G == 54) asm volatile("s_add_u32 s82, s83, 0x1000000" SCC3);
    if constexpr (G == 55) dma_m0<X, 0>();
    if constexpr (G >= 56 && !(G & 1) && FIRST != 2) dma_at<(G - 56) / 2>();
    if constexpr (G == EVG) asm volatile("v_readlane_b32 s87, v93, s86");
    if constexpr (G == EVG + 2) asm volatile("s_add_u32 s86, s86, 1" SCC3);
    if constexpr (G == 57) asm volatile("s_mov_b32 s83, s84\n\ts_andn2_b32 s84, s87, 0x3ff" SCC3);
    if constexpr (G == 59 && FIRST) asm volatile("s_and_b32 %0, s87, 0x3ff" : "=s"(c.rare) SCC2);
    if constexpr (G == 59 && !FIRST) asm volatile("s_and_b32 s63, s87, 0x3ff\n\ts_cmp_lg_u64 %0, 0" ::"s"(c.redo) SCC1);
    if constexpr (G == 61 && !FIRST) asm volatile("s_addc_u32 %0, s63, 0" : "=s"(c.rare) SCC2);
  } else if constexpr (SM.nop[64 + G] > 0) asm volatile("s_nop 0");
  if constexpr (!FIRST && g < 8 && kb == 3) vread<Xp, g>();
  if constexpr (FIRST == 2) d_ops<160, 13 * (G / 8) + (G % 8 * 13) / 8>(make_iseq<((G % 8 + 1) * 13) / 8 - (G % 8 * 13) / 8>{});
  if constexpr (FIRST == 2 && G == 34)
    asm volatile("v_and_b32 v124, 0xf0, v89\n\ts_waitcnt vmcnt(8)\n\t" A_CHECK("v[122:123]", "s[96:97]", "s72")
                 "buffer_load_dwordx2 v[122:123], v124, " SW ", s73 offen offset:1024 sc1" ::: "vcc", "scc", "memory");
  if constexpr (FIRST == 1 && G >= 8 && G < 40 && G % 4 == 1)
    asm volatile("s_mov_b32 m0, s75\n\ts_add_u32 s75, s75, 0x1000\n\tbuffer_load_dwordx4 v84, s[64:67], s94 offen lds\n\ts_add_u32 s94, s94, 0x3800" ::: SCC_ "memory");
  if constexpr (FIRST == 1 && G == 42) asm volatile("s_mov_b32 m0, 0x1f800\n\ts_mov_b64 s[64:65], s[88:89]\n\ts_mov_b32 s66, 0x1c0000\n\tbuffer_load_dword v121, s[64:67], 0 offen lds" ::: "memory");
  if constexpr (FIRST == 3) {
    constexpr int n = (G >> 4) * 4 + (G & 3), f = G < 32 ? (G - 17) / 4 : 4 + (G - 33) / 3;
    if constexpr (G == 2) { asm volatile("v_xor_b32 v84, 32, v85"); sld<94, B_WD, 1>(); }
    if constexpr (G < 32 && (G & 12) == 8)
      asm volatile("ds_read_b64_tr_b16 v[%c0:%c1], v%c4 offset:%c5\n\tds_read_b64_tr_b16 v[%c2:%c3], v%c4 offset:%c6" ::"i"(224 + 4 * n), "i"(225 + 4 * n), "i"(226 + 4 * n),
                   "i"(227 + 4 * n), "i"(n & 1 ? 84 : 85), "i"((n >> 1) * 8192), "i"((n >> 1) * 8192 + 4096));
    if constexpr (G == 32) asm volatile("s_add_u32 s94, s94, 0x1000" SCC3);
    if constexpr (G >= 17 && G < 45 && (G < 32 ? G % 4 == 1 : G % 3 == 0 && G > 32))
      asm volatile("buffer_store_dwordx4 v[%c0:%c1], v89, " SW ", s94 offen offset:%c2 sc1" ::"i"(224 + 4 * f), "i"(227 + 4 * f), "i"((f & 3) * 1024) : "memory");
  }
  if constexpr (FIRST == 3 && G == 42) sld<63, B_FSET, 1>();
  if constexpr (FIRST == 3 && G == 48) asm volatile(A_FLAG ::: "memory");
  if constexpr (FIRST && G == 44) asm volatile("v_mov_b64 v[208:209], 0\n\tv_mov_b64 v[210:211], 0");
  if constexpr (!FIRST) sm_gap<Xp, 64 + G>(c.redo);
  GAP();
}
template <int R> DI void rescale1(float alpha) {
  float t;
  asm volatile("v_accvgpr_read_b32 %0, a[%c2]\n\tv_mul_f32 %0, %0, %1\n\tv_accvgpr_write_b32 a[%c2], %0" : "=&v"(t) : "v"(alpha), "i"(R));
}
template <int Q0, int R> DI void rescale_q(const float (&alpha)[2]) { if constexpr (((R >> 2) & 1) >= Q0) rescale1<R>(alpha[(R >> 2) & 1]); }
template <int Q0, int... Rs> DI void rescale_all(const float (&alpha)[2], iseq<Rs...>) { (rescale_q<Q0, Rs>(alpha), ...); }
template <bool MAX> DI float row_reduce(float v) {
  auto p = __builtin_amdgcn_permlane32_swap(__float_as_uint(v), __float_as_uint(v), false, false);
  v = MAX ? fmaxf(__uint_as_float(p[0]), __uint_as_float(p[1])) : __uint_as_float(p[0]) + __uint_as_float(p[1]);
  p = __builtin_amdgcn_permlane16_swap(__float_as_uint(v), __float_as_uint(v), false, false);
  return MAX ? fmaxf(__uint_as_float(p[0]), __uint_as_float(p[1])) : __uint_as_float(p[0]) + __uint_as_float(p[1]);
}
template <int Y, int QB> DI float row_max() {
  constexpr int r = R_S + 32 * Y + 4 * QB;
  float mx;
  asm volatile("v_max_f32 %0, v%c1, v%c2\n\tv_max3_f32 %0, %0, v%c3, v%c4" : "=&v"(mx) : "i"(r), "i"(r + 1), "i"(r + 2), "i"(r + 3));
  static_for([&](auto K) {
    constexpr int q = r + 8 * (K + 1);
    float t = mx;
    asm volatile("v_max3_f32 %0, %0, v%c1, v%c2\n\tv_max3_f32 %0, %0, v%c3, v%c4" : "+v"(t) : "i"(q), "i"(q + 1), "i"(q + 2), "i"(q + 3));
    mx = t;
  }, make_iseq<3>{});
  return row_reduce<true>(mx);
}
template <int R> DI void v_sub(float d) { asm volatile("v_sub_f32 v%c0, v%c0, %1" ::"i"(R), "v"(d)); }
template <int B> DI void s_shift(const float (&d)[2]) { static_for([&](auto E) { v_sub<B + E>(d[(E >> 2) & 1]); }, make_iseq<32>{}); }
DI void m_shift(const float (&d)[2]) {
  s_shift<R_CI>(d);
  v_sub<R_M>(-d[0]); v_sub<R_M + 1>(-d[1]);
}
#define A_MASK(R0, R1, R2, R3, K0, K1, K2, K3) \
    "v_cmp_gt_i32_e64 s[64:65], " K0 ", v95\n\tv_cmp_gt_i32_e64 s[66:67], " K1 ", v95\n\tv_cmp_gt_i32_e64 s[68:69], " K2 ", v95\n\tv_cmp_gt_i32_e64 s[70:71], " K3 ", v95\n\t" \
    "v_cndmask_b32_e64 " R0 ", -v116, v127, s[64:65]\n\tv_cndmask_b32_e64 " R1 ", -v116, v127, s[66:67]\n\t" \
    "v_cndmask_b32_e64 " R2 ", -v116, v127, s[68:69]\n\tv_cndmask_b32_e64 " R3 ", -v116, v127, s[70:71]\n\t"
template <int K, int T> DI void mask_cmp() {
  asm volatile("v_cmp_gt_i32_e64 s[64:65], %c0, v%c4\n\tv_cmp_gt_i32_e64 s[66:67], %c1, v%c4\n\tv_cmp_gt_i32_e64 s[68:69], %c2, v%c4\n\tv_cmp_gt_i32_e64 s[70:71], %c3, v%c4"
               ::"i"(K), "i"(K + 1), "i"(K + 2), "i"(K + 3), "i"(T));
}
template <int R, int S> DI void mask_sel() {
  if constexpr (S < 0)
    asm volatile("v_cndmask_b32_e64 v%c0, v%c0, v127, s[64:65]\n\tv_cndmask_b32_e64 v%c1, v%c1, v127, s[66:67]\n\t"
                 "v_cndmask_b32_e64 v%c2, v%c2, v127, s[68:69]\n\tv_cndmask_b32_e64 v%c3, v%c3, v127, s[70:71]" ::"i"(R), "i"(R + 1), "i"(R + 2), "i"(R + 3));
  else
    asm volatile("v_cndmask_b32_e64 v%c0, -v%c4, v127, s[64:65]\n\tv_cndmask_b32_e64 v%c1, -v%c4, v127, s[66:67]\n\t"
                 "v_cndmask_b32_e64 v%c2, -v%c4, v127, s[68:69]\n\tv_cndmask_b32_e64 v%c3, -v%c4, v127, s[70:71]" ::"i"(R), "i"(R + 1), "i"(R + 2), "i"(R + 3), "i"(S));
}
template <int X, int Q0 = 0> DI void sm_redo() {
  constexpr int Xp = X ^ 1;
  float d[2] = {0.f, 0.f}, dt[2] = {0.f, 0.f}, alpha[2] = {1.f, 1.f}, ts[2] = {0.f, 0.f};
  asm volatile("s_nop 7");
  static_for([&](auto Q) {
    constexpr int qb = Q + Q0;
    const float pd = vget<R_PD + qb>();
    d[qb] = fmaxf(row_max<Xp, qb>() - pd, 0.f);
    dt[qb] = d[qb] + pd;
    vset<R_PD + qb>(d[qb]);
    alpha[qb] = __builtin_amdgcn_exp2f(-d[qb]);
    vset<R_RS + qb>(vget<R_RS + qb>() * alpha[qb]);
  }, make_iseq<2 - Q0>{});
  vset<R_THR>(d[0] > 0.f || d[1] > 0.f ? -1.f : 4096.f);
  m_shift(d);
  static_for([&](auto K) {
    constexpr int k = K, qb = (k >> 1) & 1;
    if constexpr (qb >= Q0) {
      const float e0 = __builtin_amdgcn_exp2f(vget<R_S + 32 * Xp + 2 * k>() - dt[qb]), e1 = __builtin_amdgcn_exp2f(vget<R_S + 32 * Xp + 2 * k + 1>() - dt[qb]);
      ts[qb] += e0; ts[qb] += e1;
      vset<p_reg(Xp, k)>(__uint_as_float(pk2(e0, e1)));
    }
  }, make_iseq<16>{});
  vset<R_TS>(ts[0]); vset<R_TS + 1>(ts[1]);
  rescale_all<Q0>(alpha, make_iseq<128>{});
  asm volatile("s_nop 1" ::: "memory");
  GAP();
}
template <int X, int FIRST, bool NOQK, int... Gs> DI void phaseA_(AC& c, iseq<Gs...>) {
  (gapA<X, FIRST, NOQK, Gs>(c), ...);
}
template <int X, int FIRST, bool NOQK> DI void phaseA(AC& c) { phaseA_<X, FIRST, NOQK>(c, make_iseq<64>{}); }

template <int Y, int F, int QB> DI void pv_mfma() {
  constexpr int r = 192 + 4 * (F & 15), o = 4 * ((F >> 1) * 2 + QB), p = R_P + 16 * Y + 4 * ((F & 1) * 2 + QB);
  asm volatile("v_mfma_f32_16x16x32_bf16 a[%c0:%c1], a[%c2:%c3], v[%c4:%c5], a[%c0:%c1]" ::"i"(o), "i"(o + 3), "i"(r), "i"(r + 3), "i"(p), "i"(p + 3));
}
template <int X, bool SMX, int G> DI void gapB(AC& c) {
  constexpr int Xp = X ^ 1, blk = G >> 4, g = G & 15;
  if constexpr (g == 0) asm volatile("s_waitcnt lgkmcnt(0)");
  if constexpr (SMX && g == 7 && blk < 3) dma_m0<X, blk * 4 + 4>();
  if constexpr (SNK & 2) pv_mfma<Xp, (G >> 2) * 2 + ((g ^ (g >> 1)) & 1), ((g >> 1) ^ (g >> 2)) & 1>();
  else pv_mfma<Xp, (G >> 2) * 2 + (g & 1), (g >> 1) & 1>();
  if constexpr (SMX && (G == 1 || G == 2)) asm volatile("v_add_f32_e32 v%c0, v%c0, v%c1" ::"i"(R_RS + G - 1), "i"(R_TS + G - 1));
  if constexpr (g < 8) {
    if constexpr (blk < 3) vread<Xp, (blk + 1) * 8 + g>();
    else if constexpr (SMX) kread<Xp, g>();
  }
  if constexpr (SMX) {
    if constexpr (g >= 8 && !(g & 1) && blk < 3) dma_at<blk * 4 + 4 + (g - 8) / 2>();
    sm_gap<X, G>(c.redo);
    GAP();
  }
}
template <int X, int... Gs> DI void phaseB_(AC& c, iseq<Gs...>) { (gapB<X, true, Gs>(c), ...); }
template <int X> DI void phaseB(AC& c) { phaseB_<X>(c, make_iseq<64>{}); }
constexpr int sm_nex() { int n = 0; for (int t = 0; t < 64; ++t) n += SM.ex[t] >= 0; return n; }
template <bool ST> DI void phaseB0(bool diag0, unsigned vml) {
  float d[2];
  dma_m0<0, 4>();
  if constexpr (ST) {
    asm volatile("s_waitcnt vmcnt(4)" ::: "memory");
    dma_at<4>(); d_store<8>();
    d[0] = vget<122>(); d[1] = vget<123>();
    dma_at<5>();
  } else {
    kreads<1>(make_iseq<8>{});
    dma_at<4>();
    asm volatile("s_nop 3");
    if (__builtin_expect(diag0, 0))
      static_for([&](auto I) { constexpr int i = I; mask_cmp<16 * i, 95>(); mask_sel<R_S + 8 * i, -1>(); }, make_iseq<4>{});
    d[0] = row_max<0, 0>(); GAP(); dma_at<5>();
    d[1] = row_max<0, 1>(); GAP(); dma_at<6>();
#pragma unroll
    for (int qb = 0; qb < 2; ++qb) if (!(d[qb] > -3.0e38f)) d[qb] = 0.f;
    asm volatile("buffer_store_dwordx2 %0, %1, " SW ", s73 offen offset:1024 sc1" ::"v"((f32x2c){d[0], d[1]}), "v"(vml) : "memory");
  }
  vset<R_M>(d[0]); vset<R_M + 1>(d[1]);
  const f32x2c nd[2] = {{-d[0], -d[0]}, {-d[1], -d[1]}};
  static_for([&](auto E) {
    constexpr int e = E;
    const f32x2c v = nd[(e >> 1) & 1];
    asm volatile("v_pk_add_f32 v[%c0:%c1], v[%c0:%c1], %4\n\tv_mov_b64 v[%c2:%c3], %4" ::"i"(R_S + 2 * e), "i"(R_S + 2 * e + 1), "i"(R_CI + 2 * e), "i"(R_CI + 2 * e + 1), "v"(v));
    if constexpr (ST && e == 0) d_store<9>();
    if constexpr (ST && e == 2) dma_at<6>();
    if constexpr (e == (ST ? 5 : 3)) dma_at<7>();
    if constexpr (e == 6) dma_m0<0, 8>();
    if constexpr (e == 7 || e == 11 || e == 15) dma_at<8 + (e - 7) / 4>();
    if constexpr (ST && (e == 4 || e == 9)) d_store<10 + e / 8>();
  }, make_iseq<16>{});
  static_assert(sm_nex() >= 16 && SM.op0[64] >= 3 * ((sm_nex() - 15) / 2) && SM.op0[64] <= 24);
  static_for([&](auto E) {
    constexpr int e = E;
    asm volatile("v_exp_f32 v%c0, v%c1" ::"i"(R_E + e), "i"(R_S + e));
    if constexpr (e % 3 == 0 && e < 15) acc_zero<e / 3>();
    if constexpr (e == 3) dma_at<11>();
    if constexpr (e == 6) dma_m0<0, 12>();
    if constexpr (e == 7 || e == 11 || e == 15) dma_at<12 + (e - 7) / 4>();
    if constexpr (ST && (e == 1 || e == 9)) d_store<12 + e / 8>();
  }, make_iseq<16>{});
  if constexpr (ST) {
    d_store<14>();
    asm volatile("s_barrier" ::: "memory");
    kreads<1>(make_iseq<8>{});
  }
  static_for([&](auto O) { sm_op<0, O>(); if constexpr (O % 7 == 1 && O < (ST ? 14 : 21)) acc_zero<5 + O / 7>(); }, make_iseq<SM.op0[64]>{});
  dma_at<15>();
  if constexpr (ST) { d_store<15>(); acc_zero<7>(); }
  static_for([&](auto E) { asm volatile("v_exp_f32 v%c0, v%c1" ::"i"(R_E + (int)E), "i"(R_S + 16 + (int)E)); }, make_iseq<sm_nex() - 16>{});
  GAP();
}
struct EP { float fa[2], fb[2]; };
template <int I, int E, int MERGE, int TB = 96> DI void epi_pair(const EP& p) {
  constexpr int qb = I & 1, r = 16 * (I >> 1) + 4 * qb + (E >> 1) * 8 + (E & 1) * 2, t = TB + 2 * E, w = 128 + 4 * I + E;
  if constexpr (MERGE == 2) asm volatile("v_accvgpr_read_b32 v%c0, a[%c2]\n\tv_accvgpr_read_b32 v%c1, a[%c3]" ::"i"(t), "i"(t + 1), "i"(r), "i"(r + 1));
  else
    asm volatile("v_accvgpr_read_b32 v%c0, a[%c2]\n\tv_accvgpr_read_b32 v%c1, a[%c3]\n\tv_mul_f32 v%c0, v%c0, %4\n\tv_mul_f32 v%c1, v%c1, %4"
                 ::"i"(t), "i"(t + 1), "i"(r), "i"(r + 1), "v"(p.fa[qb]));
  if constexpr (MERGE == 1)
    asm volatile("v_lshlrev_b32 v104, 16, v%c2\n\tv_and_b32 v105, 0xffff0000, v%c2\n\tv_fmac_f32 v%c0, v104, %3\n\tv_fmac_f32 v%c1, v105, %3"
                 ::"i"(t), "i"(t + 1), "i"(w), "v"(p.fb[qb]));
  asm volatile("v_cvt_pk_bf16_f32 v%c0, v%c1, v%c2" ::"i"(w), "i"(t), "i"(t + 1));
}
template <int I> DI void ctx_store() {
  asm volatile("buffer_store_dwordx4 v[%c0:%c1], v85, " SW ", s76 offen offset:%c2 sc1\n\tds_write_b128 v85, v[%c0:%c1] offset:%c3"
               ::"i"(128 + 4 * I), "i"(131 + 4 * I), "i"((I & 2) * 1024), "i"((I & 1) * 32768 + (I >> 1) * 2048) : "memory");
  if constexpr (I & 2) asm volatile("s_add_u32 s76, s76, %0" ::"i"(I == 14 ? 0x5000 : 0x1000) SCC1);
}
constexpr int mg_reg(int L, int I) { return I >= 8 ? 8 + 4 * I : I >= 4 ? R_CI + 8 * (I - 4) : R_S + 32 * L + 8 * I; }
template <int L, bool MG, int G> DI void gapAD(AC& c) {
  constexpr int kb = G >> 3, s = G & 7, db = ((G >> 1) + 8) & 15, o = 4 * (db * 2 + (G & 1)), a = 72 + 2 * (db & 1), b = mg_reg(L, (db >> 1) * 2 + (G & 1));
  if constexpr (G == 0) asm volatile("s_waitcnt lgkmcnt(0)");
  else if constexpr (G >= 8 && G < 28 && !(G & 1)) asm volatile("s_waitcnt lgkmcnt(%0)" ::"i"(G <= 24 ? 6 : 4));
  else if constexpr (G == 28) asm volatile("s_waitcnt vmcnt(16) lgkmcnt(0)\n\ts_barrier" ::: "memory");
  qk_mfma<L, kb, 1, s, false>();
  if constexpr (MG && G < 8) asm volatile("ds_read_b128 v[%c0:%c1], v85 offset:%c2" ::"i"(mg_reg(L, G)), "i"(mg_reg(L, G) + 3), "i"(G * 1024));
  if constexpr (G < 24) kread<L, 8 + G>();
  if constexpr (G < 24 && s == 1) mask_cmp<16 * (kb + 1), 126>();
  if constexpr (G < 24 && s == 3) mask_sel<R_CI + 8 * (kb + 1) + 4, R_M + 1>();
  if constexpr (G >= 4 && G < 28 && G % 3 == 2) w_load<(G - 4) / 3>();
  if constexpr (G == 25) asm volatile("s_add_u32 s82, s83, 0x1000000" SCC3);
  if constexpr (G == 27) dma_m0<L, 0>();
  if constexpr (G >= 28) { vread<L ^ 1, 2 * (G - 28)>(); vread<L ^ 1, 2 * (G - 28) + 1>(); dma_at<G - 28>(); }
  sm_gap<L ^ 1, 64 + 2 * G>(c.redo);
  if constexpr (MG) asm volatile("v_mfma_f32_16x16x32_bf16 a[%c0:%c1], v[%c2:%c3], v[%c4:%c5], a[%c0:%c1]" ::"i"(o), "i"(o + 3), "i"(a), "i"(a + 3), "i"(b), "i"(b + 3));
  sm_gap<L ^ 1, 65 + 2 * G>(c.redo);
  GAP();
}
template <int L, bool MG, int... Gs> DI void phaseAD(AC& c, iseq<Gs...>) {
  sld<72, B_NXT, 1>();
  mask_cmp<0, 126>(); mask_sel<R_CI + 4, R_M + 1>();
  (gapAD<L, MG, Gs>(c), ...);
}
#define BDX 8
constexpr int bd_w(bool EPI, int G) { return EPI && G >= 19 && G % 4 == 3 && (G - 19) / 4 < BDX ? 16 + (G - 19) / 4 : -1; }
template <int L, bool EPI, int G> DI void gapBD(AC& c, const EP& p) {
  constexpr int g = G & 15, e = (G >> 4) * 8 + (g - 8), o = e - 3;
  if constexpr (G == 1) dma_m0<L, 4>();
  if constexpr (G == 8) asm volatile("s_waitcnt vmcnt(14)" ::: "memory");
  if constexpr (G == 16) asm volatile("s_waitcnt lgkmcnt(0)\n\ts_barrier" ::: "memory");
  gapB<L, false, G>(c);
  if constexpr (G >= 8 && G < 12)
    asm volatile("ds_read_b128 a[%c0:%c1], v90 offset:%c4\n\tds_read_b128 a[%c2:%c3], v90 offset:%c5" ::"i"(96 + 8 * G), "i"(99 + 8 * G), "i"(100 + 8 * G), "i"(103 + 8 * G),
                 "i"((L ^ 1) * 32768 + (G - 8) * 2048), "i"((L ^ 1) * 32768 + (G - 8) * 2048 + 1024));
  if constexpr (G == 1 || G == 2) asm volatile("v_add_f32_e32 v%c0, v%c0, v%c1" ::"i"(R_RS + G - 1), "i"(R_TS + G - 1));
  if constexpr (G >= 3 && G < 19 && G % 4 == 3) dma_at<4 + (G - 3) / 4>();
  if constexpr (!EPI && G == 17)
    asm volatile("s_cmp_eq_u32 s97, 0\n\ts_cbranch_scc1 1f\n\ts_add_u32 s64, s100, 4 * 800\n\ts_addc_u32 s65, s101, 0\n\ts_swappc_b64 s[98:99], s[64:65]\n1:" ::: SCC_ "memory");
  if constexpr (bd_w(EPI, G) >= 0) w_load<bd_w(EPI, G)>();
  if constexpr (g >= 8 && e < 16) asm volatile("v_exp_f32 v%c0, v%c1" ::"i"(R_E + e), "i"(R_S + 32 * L + 4 + 8 * (e >> 2) + (e & 3)));
  if constexpr (g >= 8 && o >= 1 && o < 16) {
    if constexpr (o == 1) asm volatile("v_add_f32_e32 v%c0, v%c1, v%c2" ::"i"(R_TS + 1), "i"(R_E), "i"(R_E + 1));
    else asm volatile("v_add_f32_e32 v%c0, v%c0, v%c1" ::"i"(R_TS + 1), "i"(R_E + o));
    if constexpr (o & 1) asm volatile("v_cvt_pk_bf16_f32 v%c0, v%c1, v%c2" ::"i"(p_reg(L, (o >> 2) * 4 + 2 + ((o >> 1) & 1))), "i"(R_E + o - 1), "i"(R_E + o));
  }
  if constexpr (G == 45) asm volatile("v_cmp_nle_f32_e64 %0, v%c1, v%c2" : "=s"(c.redo) : "i"(R_TS + 1), "i"(R_THR));
  if constexpr (EPI && G >= 10 && G < 58 && !(G & 1)) epi_pair<((G - 10) >> 3) * 2, ((G - 10) >> 1) & 3, 0, 224>(p);
  if constexpr (EPI && G >= 17 && G < 58 && (G & 7) == 1) ctx_store<((G - 17) >> 3) * 2>();
  GAP();
}
template <int L, bool EPI, int... Gs> DI void phaseBD(AC& c, const EP& p, iseq<Gs...>) {
  if constexpr (!EPI) { sld<96, B_LM, 1>(); sld<97, B_LM + 1, 1>(); asm volatile("s_nop 0"); fl_check(); }
  (gapBD<L, EPI, Gs>(c, p), ...);
}

#define ACLOB_ALL "a0","a1","a2","a3","a4","a5","a6","a7","a8","a9","a10","a11","a12","a13","a14","a15","a16","a17","a18","a19","a20","a21","a22","a23","a24","a25","a26","a27","a28","a29","a30","a31","a32","a33","a34","a35","a36","a37","a38","a39","a40","a41","a42","a43","a44","a45","a46","a47","a48","a49","a50","a51","a52","a53","a54","a55","a56","a57","a58","a59","a60","a61","a62","a63","a64","a65","a66","a67","a68","a69","a70","a71","a72","a73","a74","a75","a76","a77","a78","a79","a80","a81","a82","a83","a84","a85","a86","a87","a88","a89","a90","a91","a92","a93","a94","a95","a96","a97","a98","a99","a100","a101","a102","a103","a104","a105","a106","a107","a108","a109","a110","a111","a112","a113","a114","a115","a116","a117","a118","a119","a120","a121","a122","a123","a124","a125","a126","a127","a128","a129","a130","a131","a132","a133","a134","a135","a136","a137","a138","a139","a140","a141","a142","a143","a144","a145","a146","a147","a148","a149","a150","a151","a152","a153","a154","a155","a156","a157","a158","a159","a160","a161","a162","a163","a164","a165","a166","a167","a168","a169","a170","a171","a172","a173","a174","a175","a176","a177","a178","a179","a180","a181","a182","a183","a184","a185","a186","a187","a188","a189","a190","a191","a192","a193","a194","a195","a196","a197","a198","a199","a200","a201","a202","a203","a204","a205","a206","a207","a208","a209","a210","a211","a212","a213","a214","a215","a216","a217","a218","a219","a220","a221","a222","a223","a224","a225","a226","a227","a228","a229","a230","a231","a232","a233","a234","a235","a236","a237","a238","a239","a240","a241","a242","a243","a244","a245","a246","a247","a248","a249","a250","a251","a252","a253","a254","a255"

template <int I> DI void epi_store() {
  asm volatile("buffer_store_dwordx4 v[%c0:%c1], v90, " SW ", s%c2 offen offset:%c3 sc1" ::"i"(128 + 4 * I), "i"(131 + 4 * I), "i"(76 + (I >> 2)), "i"((I & 3) * 1024) : "memory");
}
template <int HI> DI void epi_init() {
  asm volatile("v_readlane_b32 s76, v93, %c0\n\ts_nop 0\n\ts_add_u32 s77, s76, 0x1000\n\ts_add_u32 s78, s76, %1\n\ts_add_u32 s79, s78, 0x1000" ::"i"(B_SO), "i"(HI) SCC1);
}
template <int I> DI void epi_pread() {
  asm volatile("ds_read_b128 v[%c0:%c1], v107 offset:%c2" ::"i"(128 + 4 * I), "i"(131 + 4 * I), "i"(I * 1024));
}
template <int... Is> DI void epi_preads(iseq<Is...>) { (epi_pread<Is>(), ...); }
template <int G> DI void gapF(AC& c, const EP& p) {
  if constexpr (G < 64) gapB<ODD, false, G>(c);
  if constexpr (G == 1) asm volatile("v_mov_b32 v93, v94\n\ts_brev_b32 s73, 1");
  if constexpr (G >= 3 && G < 10) att_item<G - 3>();
  if constexpr (G >= 10) epi_pair<(G - 10) / 4, (G - 10) & 3, 2>(p);
  if constexpr (G >= 14 && ((G - 14) & 3) == 0) epi_store<(G - 14) / 4>();
  if constexpr (G == 73) epi_store<15>();
  GAP();
}
template <int... Gs> DI void phaseF(AC& c, const EP& p, iseq<Gs...>) { (gapF<Gs>(c, p), ...); }
constexpr int fd_w(bool EPI, int G) {
  const int f0 = EPI ? 16 + BDX : 16, n = NWF - f0;
  for (int i = 0; i < n; ++i) if (G == (EPI ? 2 + (i * 37) / n : i < 16 ? 2 + 2 * i : 4 + 2 * i)) return f0 + i;
  return -1;
}
#define FDA 50
#define FDE 20
constexpr int fd_cnt(bool EPI, int G0, int G1) { int n = 0; for (int G = G0; G < G1; ++G) n += (fd_w(EPI, G) >= 0) + ((G & 3) == 3); return n; }
constexpr bool fd_ok() {
  for (int G = 0; G < 64; ++G) {
    const int a = fd_w(0, G), b = fd_w(1, G);
    if ((a >= 16 && G < 2 * (a & 15) + (a >= 32 ? 34 : 2)) || (b >= 28 && G < (b >= 32 ? 2 * (b - 32) + 10 : 2 * (b - 28) + 2))) return false;
  }
  return true;
}
static_assert(fd_ok() && NWF == 40 && BDX <= 12);
template <int L, bool EPI, int G> DI void gapFD(const EP& p) {
  constexpr int I = EPI ? (G < 8 ? 12 + (G >> 2) * 2 : ((G - 8) >> 2) * 2 + 1) : G < 32 ? (G >> 2) * 2 : ((G - 32) >> 2) * 2 + 1;
  constexpr int u = EPI ? G : G - 2, uf = SNK & 2 ? u ^ ((u >> 1) & 1) : u;
  if constexpr (u >= 16 && u < 32 && (u & 3) == 0) asm volatile("s_waitcnt lgkmcnt(%0)" ::"i"(28 - u));
  if constexpr (u >= 0 && u < 32) pv_mfma<L, uf, 1>();
  if constexpr (u >= 0 && u < 16) vread<L, uf + 16>();
  constexpr int F = fd_w(EPI, G);
  if constexpr (F >= 0) w_load<F>();
  epi_pair<I, G & 3, !EPI>(p);
  if constexpr ((G & 3) == 3) ctx_store<I>();
  if constexpr (G == (EPI ? FDE : FDA)) {
    asm volatile("s_waitcnt vmcnt(%0)\n\ts_barrier" ::"i"(fd_cnt(EPI, EPI ? 8 : 32, G + 1)) : "memory");
    set_flag<A_FC>();
  }
  GAP();
}
template <int L, bool EPI, int... Gs> DI void phaseFD(const EP& p, iseq<Gs...>) { (gapFD<L, EPI, Gs>(p), ...); }
#define A_CT2 "v_readlane_b32 s63, v93, 3\n\ts_sub_u32 s64, s100, at_event - at_tail\n\ts_subb_u32 s65, s101, 0\n\ts_mov_b32 s66, at_event - at_tail\n\ts_mov_b32 s67, s59\n\t" \
    "v_lshl_add_u32 v78, v89, 9, s63\n\ts_mov_b64 exec, 0x1f\n\tbuffer_load_dword v84, v78, s[64:67], 0 offen\n\ts_mov_b64 exec, -1\n\t"
#define A_CT4 "s_mov_b64 s[64:65], s[100:101]\n\ts_movk_i32 s66, 0xf00\n\ts_mov_b32 s67, s59\n\tv_lshlrev_b32 v78, 3, v89\n\tbuffer_load_dword v84, v78, s[64:67], 0 offen\n\t"
#define A_RET "s_setpc_b64 s[98:99]\n\t"
#define A_EFLAG(L) "v_readlane_b32 s63, v93, " #L "\n\ts_nop 3\n\t" A_FLAG
#define A_EPOLL "v_readlane_b32 s96, v93, s85\n\ts_add_u32 s85, s85, 1\n\tv_readlane_b32 s97, v93, s85\n\ts_add_u32 s85, s85, 1\n\t" A_POLL("v[122:123]", "s[96:97]", "s62")
#define A_Q1 "buffer_load_dwordx4 v87, s[64:67], s95 offen lds\n\ts_add_u32 m0, m0, 0x400\n\ts_add_u32 s95, s95, 0x1c00\n\t"
#define A_EPOLL1 "s_mov_b32 exec_lo, 0xffff0000\n\ts_mov_b32 exec_hi, 0xffff\n\tbuffer_load_dwordx2 v[124:125], v88, " SW ", s92 offen sc1\n\ts_mov_b64 exec, -1\n\t" A_CT2
#define A_Z1(K, O) "v_xor_b32 v49, " K ", v48\n\tbuffer_load_dwordx4 v49, " SW ", s63 offen offset:" O " sc1 lds\n\t"
#define A_EQ "v_readlane_b32 s68, v92, 30\n\tv_readlane_b32 s95, v92, 28\n\ts_mov_b64 s[64:65], s[88:89]\n\ts_mov_b32 s66, 0x1c0000\n\ts_mov_b32 s67, s59\n\t" \
    "s_add_u32 m0, s68, 0x1000\n\ts_nop 1\n\t" A_Q1 A_Q1 A_Q1 A_Q1
#define A_ELOAD "v_readlane_b32 s68, v92, 30\n\tv_readlane_b32 s63, v92, 29\n\tv_lshrrev_b32 v48, 5, v89\n\ts_mov_b32 s96, 0\n\ts_mov_b32 s97, 0x3fff0000\n\t" \
    "v_and_b32 v48, 16, v48\n\t" A_CHECK("v[124:125]", "s[96:97]", "s92") "v_xor_b32 v48, v48, v89\n\ts_mov_b32 m0, s68\n\t" \
    A_Z1("0", "0") A_Z1("32", "1024") A_Z1("64", "2048") "s_mov_b32 exec_hi, 0\n\t" A_Z1("0x60", "3072") "s_mov_b32 exec_hi, -1\n\t"
#define A_PZ2(Z0, Z1, O) "ds_read_u16_d16_hi " Z0 ", v46 offset:" O "\n\tds_read_u16_d16_hi " Z1 ", v47 offset:" O "\n\t"
#define A_PRD(X, Q, O, Z0, Z1, Z2, Z3, Z4, Z5, Z6, Z7) "v_xor_b32 v46, " X ", v44\n\tds_read_b128 " Q ", v45 offset:" O "\n\tv_xor_b32 v47, 64, v46\n\t" \
    A_PZ2(Z0, Z1, "0") A_PZ2(Z2, Z3, "128") A_PZ2(Z4, Z5, "256") A_PZ2(Z6, Z7, "384")
#define A_PM2(Q, Z0, Z1) "v_lshlrev_b32 v40, 16, " Q "\n\tv_and_b32 v41, 0xffff0000, " Q "\n\tv_mul_f32 v40, v40, " Z0 "\n\tv_mul_f32 v41, v41, " Z1 "\n\tv_cvt_pk_bf16_f32 " Q ", v40, v41\n\t"
#define A_PST(Q) "buffer_store_dwordx4 " Q ", v43, s[64:67], s63 offen\n\ts_add_u32 s63, s63, 0x3800\n\t"
#define A_PA "v64", "v65", "v66", "v67", "v68", "v69", "v70", "v71"
#define A_PB "v72", "v73", "v74", "v75", "v76", "v77", "v78", "v79"
#define A_PRD_(...) A_PRD(__VA_ARGS__)
#define A_EPOST "v_readlane_b32 s68, v92, 30\n\tv_lshrrev_b32 v40, 4, v89\n\tv_readlane_b32 s64, v92, 0\n\tv_mul_u32_u24 v41, 37, v40\n\tv_readlane_b32 s65, v92, 1\n\t" \
    "v_lshrrev_b32 v41, 8, v41\n\tv_readlane_b32 s63, v94, 23\n\tv_mad_i32_i24 v42, v41, -7, v40\n\ts_movk_i32 s66, 0x380\n\ts_movk_i32 s67, 0x210\n\t" \
    "v_mad_u32_u24 v43, v41, s66, v87\n\tv_lshl_add_u32 v44, v41, 1, s68\n\tv_add_u32 v45, s68, v89\n\tv_mad_u32_u24 v44, v42, s67, v44\n\t" \
    "v_mov_b64 v[64:65], 0\n\tv_mov_b64 v[66:67], 0\n\tv_mov_b64 v[68:69], 0\n\tv_mov_b64 v[70:71], 0\n\t" \
    "v_mov_b64 v[72:73], 0\n\tv_mov_b64 v[74:75], 0\n\tv_mov_b64 v[76:77], 0\n\tv_mov_b64 v[78:79], 0\n\ts_mov_b32 s66, 0x380000\n\ts_mov_b32 s67, s59\n\t" \
    A_PRD_("0", "v[48:51]", "4096", A_PA) A_PRD_("16", "v[52:55]", "5120", A_PB) "s_waitcnt lgkmcnt(9)\n\t" \
    A_PM2("v48", "v64", "v65") A_PM2("v49", "v66", "v67") A_PM2("v50", "v68", "v69") A_PM2("v51", "v70", "v71") A_PST("v[48:51]") \
    A_PRD_("32", "v[56:59]", "6144", A_PA) "s_waitcnt lgkmcnt(9)\n\t" \
    A_PM2("v52", "v72", "v73") A_PM2("v53", "v74", "v75") A_PM2("v54", "v76", "v77") A_PM2("v55", "v78", "v79") A_PST("v[52:55]") \
    A_PRD_("48", "v[60:63]", "7168", A_PB) "s_waitcnt lgkmcnt(9)\n\t" \
    A_PM2("v56", "v64", "v65") A_PM2("v57", "v66", "v67") A_PM2("v58", "v68", "v69") A_PM2("v59", "v70", "v71") A_PST("v[56:59]") "s_waitcnt lgkmcnt(0)\n\t" \
    A_PM2("v60", "v72", "v73") A_PM2("v61", "v74", "v75") A_PM2("v62", "v76", "v77") A_PM2("v63", "v78", "v79") A_PST("v[60:63]")
#define A_TR(R0, R1, R2, R3, O0, O1) "ds_read_b64_tr_b16 " R0 ", v85 offset:" O0 "\n\tds_read_b64_tr_b16 " R1 ", v85 offset:" O1 "\n\t" \
    "ds_read_b64_tr_b16 " R2 ", v84 offset:" O0 "\n\tds_read_b64_tr_b16 " R3 ", v84 offset:" O1 "\n\t"
#define A_WS(R, S, O) "buffer_store_dwordx4 " R ", v89, " SW ", " S " offen offset:" O " sc1\n\t"
#define A_PL1(O) "buffer_load_dwordx4 v89, " SW ", s63 offen offset:" O " sc1 lds\n\t"
#define A_PL2(R, O) "buffer_load_dwordx4 " R ", v89, " SW ", s95 offen offset:" O " sc1\n\t"
#define A_EPLOAD "v_readlane_b32 s68, v92, 30\n\tv_readlane_b32 s63, v93, 21\n\ts_mov_b32 m0, s68\n\ts_add_u32 s95, s63, 0x8000\n\ts_nop 2\n\t" \
    A_PL1("0") A_PL1("1024") A_PL1("2048") A_PL1("3072") "s_bitset1_b32 m0, 12\n\ts_add_u32 s63, s63, 0x1000\n\t" A_PL1("0") A_PL1("1024") A_PL1("2048") A_PL1("3072") \
    A_PL2("v[40:43]", "0") A_PL2("v[44:47]", "1024") A_PL2("v[48:51]", "2048") A_PL2("v[52:55]", "3072") "s_add_u32 s95, s95, 0x1000\n\t" \
    A_PL2("v[56:59]", "0") A_PL2("v[60:63]", "1024") A_PL2("v[64:67]", "2048") A_PL2("v[68:71]", "3072") \
    "v_and_b32 v78, 0xf0, v89\n\tbuffer_load_dwordx4 v[72:75], v78, " SW ", s73 offen sc1\n\t"
#define A_EPOLLZ "s_mov_b32 exec_lo, 0\n\ts_mov_b32 exec_hi, 0x3fff0000\n\tbuffer_load_dwordx2 v[124:125], v88, " SW ", s92 offen sc1\n\ts_mov_b64 exec, -1\n\t"
#define A_ECHK1 "s_mov_b32 s70, 0xffff0000\n\ts_mov_b32 s71, 0xffff\n\t" A_CHECK("v[124:125]", "s[70:71]", "s92")
#define A_AT(N) ".fill (at_event + 4 * " #N " - .) / 4, 4, 0xbf800000\n\t"
#define A_EVENT \
    ".p2align 6\nat_event:\n\t" A_RET \
    A_AT(16) "v_cmp_eq_u64 vcc, s[60:61], v[122:123]\n\ts_and_b64 vcc, vcc, s[96:97]\n\ts_cmp_eq_u64 vcc, s[96:97]\n\ts_cbranch_scc0 7f\n\t" A_RET \
    A_AT(32) A_EFLAG(5) A_CT4 A_RET \
    A_AT(48) A_EFLAG(5) A_AT(56) A_EPOLL A_RET \
    A_AT(80) A_EFLAG(4) A_RET \
    A_AT(96) A_EFLAG(5) A_CT4 A_EPOLL A_RET \
    A_AT(128) "v_xor_b32 v84, 32, v85\n\tv_readlane_b32 s63, v92, 27\n\t" \
    A_TR("v[48:49]", "v[50:51]", "v[52:53]", "v[54:55]", "0", "4096") A_TR("v[56:57]", "v[58:59]", "v[60:61]", "v[62:63]", "8192", "12288") \
    A_TR("v[64:65]", "v[66:67]", "v[68:69]", "v[70:71]", "16384", "20480") A_TR("v[72:73]", "v[74:75]", "v[76:77]", "v[78:79]", "24576", "28672") \
    "s_add_u32 s95, s63, 0x1000\n\ts_waitcnt lgkmcnt(8)\n\t" \
    A_WS("v[48:51]", "s63", "0") A_WS("v[52:55]", "s63", "1024") A_WS("v[56:59]", "s63", "2048") A_WS("v[60:63]", "s63", "3072") "s_waitcnt lgkmcnt(0)\n\t" \
    A_WS("v[64:67]", "s95", "0") A_WS("v[68:71]", "s95", "1024") A_WS("v[72:75]", "s95", "2048") A_WS("v[76:79]", "s95", "3072") A_RET \
    A_AT(192) A_EPOLL1 A_RET \
    A_AT(228) A_EPOLL A_AT(240) A_EPOLL1 \
    A_MASK("v224", "v225", "v226", "v227", "0", "1", "2", "3") \
    A_MASK("v232", "v233", "v234", "v235", "16", "17", "18", "19") \
    A_MASK("v240", "v241", "v242", "v243", "32", "33", "34", "35") \
    A_MASK("v248", "v249", "v250", "v251", "48", "49", "50", "51") A_RET \
    "7:\n\t" A_SPIN("v[122:123]", "s[96:97]", "s62") A_RET \
    A_AT(344) A_EFLAG(4) A_EPOLLZ A_EQ A_RET \
    A_AT(400) A_ELOAD A_RET \
    A_AT(448) A_EFLAG(5) A_CT4 A_EPOLLZ A_EQ A_RET \
    A_AT(512) A_EPOST A_RET \
    A_AT(768) "5:\n\t" A_CHECK("v[122:123]", "s[96:97]", "s62") "s_branch 6f\n\t" A_AT(800) "6:\n\t" A_EPLOAD A_RET \
    A_AT(872) A_ECHK1 A_RET \
    A_AT(900) A_ECHK1 "s_branch 5b\n\t"
static_assert(B_CT == 3 && B_FSET == 5 && B_FW == 4 && A_M0F == 30 && A_PQ == 28 && A_PZ == 29 && A_WDST == 27 && C_PO == 23 && B_LP == 21);
DI void event() {
  asm volatile("s_and_b32 s63, s87, 0x3ff\n\ts_lshl2_add_u32 s64, s63, s100\n\ts_addc_u32 s65, s101, 0\n\ts_swappc_b64 s[98:99], s[64:65]" ::: "vcc", SCC_ "memory",
               "v78", "v79");
  GAP();
}

#define OFU 20
static_assert(OFU >= 4 && OFU < 28 && OFU % 2 == 0);
#define OGB 14
constexpr int o_qo(int P) { return O_SEQ[P] & 3; }
constexpr int o_o(int P) { return O_SEQ[P] >> 4; }
constexpr int o_ks(int k) { return k % 7 < 4 ? 4 + k % 7 : k % 7 - 4; }
constexpr int o_sq(int P) {
  for (int q = P; q < 16; ++q) if (P < 1 || o_qo(q) == o_qo(P - 1)) return -1;
  return o_qo(P - 1);
}
constexpr int o_last() { int l = 0; for (int P = 0; P < 16; ++P) if (o_sq(P) >= 0) l = P; return l; }
constexpr bool o_first(int P) { for (int q = 0; q < P; ++q) if (o_qo(q) == o_qo(P)) return false; return true; }
template <int P, int MB, int KK> DI void o_read() {
  constexpr int r = 128 + 4 * (8 * MB + KK), sl = o_k(P) < 0 ? o_qo(P) : o_ks(o_k(P));
  asm volatile("ds_read_b128 v[%c0:%c1], v%c2 offset:%c3" ::"i"(r), "i"(r + 3), "i"(sl < 4 ? 96 + sl : 86), "i"((sl < 4 ? 0 : (sl & 3) * 16384) + (2 * KK + MB) * 1024));
}
template <int... Is> DI void o_read0(iseq<Is...>) { (o_read<0, (Is >> 3), Is & 7>(), ...); }
constexpr int o_gb(int NF) { return NF == 2 ? OGB : 0; }
#define OTAB {40, 44, 48, -1}, {52, -1, 0, 1}, {56, -1, 2, 60}, {-1, -1, 3, 4}, {-1, -1, 5, 6}, {-1, -1, 7, -1}, {-1, -1, 8, -1}, {-1, -1, 9, -1}, {-1, -1, 10, -1}, {-1, -1, 11, -1}
constexpr int O_TAB[][4] = {OTAB};
constexpr int o_op(int P, int U, int NF) {
  const int c = U / 8 == 1 && o_sq(P) >= 0 ? 99 : P < (int)(sizeof(O_TAB) / 16) ? O_TAB[P][U / 8] : -1, i = (U & 7) / 2;
  if (c < 0 || (U & 1)) return 0;
  if (c < 32) return 200 + 4 * c + i;
  if (c == 99) return i == 3 || (i == 1 && NF == 2) ? 300 + i : 0;
  return NF == 1 && (i & 1) ? 0 : 100 + c + i;
}
constexpr int o_vm(int P, int NF) {
  int n = 0;
  for (int i = 0; i <= P; ++i) {
    for (int U = 0; U < (i < P ? 32 : o_gb(NF)); ++U) {
      const int op = o_op(i, U, NF);
      if (op) ++n;
      if (op >= 200 && op < 300 && (op - 200) / 4 == o_k(P + 1)) n = 0;
      if (op >= 100 && op < 200 && (op - 100) / 16 == o_o(P + 1)) n = 0;
    }
  }
  return n;
}
constexpr bool o_ok() {
  for (int P = 1; P < 16; ++P) {
    for (int i = P - 1; i < 16; ++i) for (int U = 0; U < 32; ++U) {
      const int op = o_op(i, U, 2);
      if (i > P - 1 || U >= 0) {
        if (op >= 200 && op < 300 && (op - 200) / 4 == o_k(P)) return false;
        if (op >= 100 && op < 200 && (op - 100) / 16 == o_o(P)) return false;
      }
    }
  }
  for (int i = 0; i < 16; ++i) for (int U = 0; U < 32; ++U) {
    const int op = o_op(i, U, 2);
    if (op >= 200 && op < 300) {
      const int k = (op - 200) / 4, sl = o_ks(k);
      int prev = -1;
      for (int P = 0; P < 16; ++P) if ((o_k(P) < 0 ? o_qo(P) : o_ks(o_k(P))) == sl && (o_k(P) < k)) prev = P;
      if (prev >= 0 && !(i > prev + 1 || (i == prev + 1 && U >= 16))) return false;
    }
  }
  return true;
}
static_assert(NWF == 40 && o_ok());
template <int K> DI void o_m0() {
  constexpr int sl = o_ks(K);
  if constexpr (sl < 4) asm volatile("s_add_u32 m0, s80, s%c0" ::"i"(64 + sl) SCC1);
  else asm volatile("s_add_u32 m0, s80, %0" ::"i"(sl * 16384) SCC1);
}
template <int NF, int SR, int C> DI void o_pack() {
  if constexpr (NF == 2 || !(C & 2))
    asm volatile("v_cvt_pk_bf16_f32 v%c0, v%c1, v%c2" ::"i"(NF == 2 ? SR + C : SR + (C >> 2) * 2 + (C & 1)), "i"(SR + 2 * C), "i"(SR + 2 * C + 1));
}
template <int SR, int F> DI void o_sst() {
  asm volatile("buffer_store_dwordx4 v[%c0:%c1], v89, " SW ", s77 offen offset:%c2 sc1" ::"i"(SR + 4 * F), "i"(SR + 4 * F + 3), "i"(F * 1024) : "memory");
}
template <int I, int V> DI void o_slab() {
  asm volatile("buffer_load_dwordx4 v[%c0:%c1], v%c4, " SW ", s%c2 offen offset:%c3 sc1" ::"i"(192 + 4 * I), "i"(195 + 4 * I), "i"(64 + I / 2), "i"((I & 1) * 1024), "i"(V) : "memory");
}
template <int NF, int P, int G> DI void o_gap() {
  constexpr int NG = 16 * NF, ch = G >> 3, mb = ch / NF, nfi = (SNK & 16) && NF == 2 ? (ch ^ (ch >> 1)) & 1 : ch % NF, kk = (SNK & 16) && NF == 2 && (ch & 1) ? 7 - (G & 7) : G & 7, sq = o_sq(P), sr = 192 + 16 * sq;
  constexpr int w = o_wreg(o_o(P), 2 * kk + nfi), b = 128 + 4 * (8 * mb + kk), d = 192 + 16 * o_qo(P) + 4 * (2 * mb + nfi);
  constexpr bool nxt = P != 15;
  constexpr int U = NF == 2 ? G : 2 * G, op = o_op(P, U, NF), opn = U < 24 ? o_op(P, U + 8, NF) : nxt ? o_op(P + 1, U - 24, NF) : 0;
  if constexpr (G == 0) asm volatile("s_waitcnt lgkmcnt(8)" ::: "memory");
  if constexpr (G == NG / 2) asm volatile("s_waitcnt lgkmcnt(%0)" ::"i"(nxt && NF == 1 ? 8 : 0) : "memory");
  if constexpr (nxt && G == o_gb(NF) && P >= 1 && (o_k(nxt ? P + 1 : P) >= 0 || P < 3)) asm volatile("s_waitcnt vmcnt(%0)\n\ts_barrier" ::"i"(o_vm(P, NF)) : "memory");
  if constexpr (P == 1 && G == o_gb(NF))
    asm volatile("v_readlane_b32 s63, v92, %c0\n\ts_nop 0\n\ts_add_u32 s63, s63, 32\n\t" A_FLAG ::"i"(A_FC) : SCC_ "memory");
  if constexpr (P == o_last() + 1 && U == OFU) {
    asm volatile("s_waitcnt vmcnt(0)" ::: "memory");
    set_flag<A_FS>();
  }
  if constexpr (op >= 200 && op < 300 && (op & 3) == 0) o_m0<(op - 200) / 4>();
  if constexpr (o_first(P) && !(G & 7))
    asm volatile("v_mfma_f32_16x16x32_bf16 v[%c0:%c1], a[%c2:%c3], v[%c4:%c5], 0" ::"i"(d), "i"(d + 3), "i"(w), "i"(w + 3), "i"(b), "i"(b + 3));
  else
    asm volatile("v_mfma_f32_16x16x32_bf16 v[%c0:%c1], a[%c2:%c3], v[%c4:%c5], v[%c0:%c1]" ::"i"(d), "i"(d + 3), "i"(w), "i"(w + 3), "i"(b), "i"(b + 3));
  if constexpr (nxt && G >= NG - 16) o_read<P + 1, (G >> 3) & 1, kk>();
  if constexpr (op >= 100 && op < 200) w_load<op - 100, 1 + NF>();
  if constexpr (op >= 200 && op < 300)
    asm volatile("buffer_load_dwordx4 v89, " SW ", s%c0 offen offset:%c1 sc1 lds" ::"i"(73 + U / 8), "i"((op & 3) * 1024) : "memory");
  if constexpr (sq >= 0) {
    if constexpr (G == 0) sld<77, A_SS + sq>();
    if constexpr (NF == 2 && G >= 6 && G < 14) o_pack<2, sr, G - 6>();
    if constexpr (NF == 1 && (G == 4 || G == 5)) { o_pack<1, sr, 4 * (G - 4)>(); o_pack<1, sr, 4 * (G - 4) + 1>(); }
    if constexpr (op >= 300) o_sst<sr, NF == 2 ? (op - 300) / 2 : 0>();
  }
  if constexpr (opn >= 200 && opn < 300 && (opn & 3) == 0) sld<73 + ((U + 8) & 31) / 8, A_PC + (opn - 200) / 4>();
  if constexpr (P == 12 && G == NG - 4) { sld<62, A_FSW>(); fl_mask<A_FSM, -1>(); }
  if constexpr (P == 13 && G == 1) fl_issue();
  if constexpr (P == 13 && G >= 2 && G < 5) sld<62 + G, A_SL + G - 2>();
  if constexpr (P == 13 && G == NG - 2)
    asm volatile("v_cmp_eq_u64 vcc, s[60:61], v[122:123]\n\ts_and_b64 vcc, vcc, s[96:97]\n\ts_cmp_eq_u64 vcc, s[96:97]\n\ts_cselect_b32 s67, 0, 0x80000000\n\t"
                 "v_or_b32 v100, s67, v89" ::: "vcc", SCC_ "memory");
  if constexpr (P == 14 && G % NF == 0 && G >= 2 * NF && G < 14 * NF && !((G / NF) & 1) && (NF == 2 || !((G / NF) & 2) == false)) o_slab<G / NF / 2 - 1, 100>();
  GAP();
}
template <int NF, int P, int... Gs> DI void o_interval_(iseq<Gs...>) {
  (o_gap<NF, P, Gs>(), ...);
}
template <int NF, int P> DI void o_interval() { o_interval_<NF, P>(make_iseq<16 * NF>{}); }
template <int NF> DI void o_slabs() {
  unsigned late;
  asm volatile("s_mov_b32 %0, s67" : "=s"(late));
  if (late) {
    fl_wait();
    static_for([&](auto I) { if constexpr (NF == 2 || !(I & 1)) o_slab<I, 89>(); }, make_iseq<6>{});
  }
  GAP();
}
template <int NF> DI void o_sum() {
  static_for([&](auto I) {
    constexpr int i = I, q = i >> 2, f = i & 3, b = 192 + 8 * q + (NF == 2 ? 4 * (f >> 1) : 0), a = 72 + 2 * (NF == 2 ? f & 1 : f >> 1);
    if constexpr (f == 0) asm volatile("s_waitcnt vmcnt(%0)" ::"i"((2 - q) * NF) : "memory");
    if constexpr (NF == 2 || !(f & 1))
      asm volatile("v_mfma_f32_16x16x32_bf16 v[%c0:%c1], v[%c2:%c3], v[%c4:%c5], v[%c0:%c1]" ::"i"(240 + 4 * f), "i"(243 + 4 * f), "i"(a), "i"(a + 3), "i"(b), "i"(b + 3));
  }, make_iseq<12>{});
  asm volatile("s_nop 7");
}

#define NSG 64
#define KWAIT "s_waitcnt lgkmcnt(0)\n\t"
#define TOUCH0 0x20
#define TOUCH1 0x20
__global__ __launch_bounds__(256, 1) __attribute__((amdgpu_num_vgpr(82), amdgpu_num_sgpr(NSG)))
void fused_kernel(const bf16* a0, const bf16* a1, const bf16* a2, const bf16* a3, const bf16* a4, char* ws, const bf16* __restrict__ ak, const bf16* __restrict__ av,
                  const bf16* __restrict__ ash, const bf16* __restrict__ assmk, const bf16* __restrict__ assmD, const bf16* __restrict__ wo,
                  const bf16* __restrict__ aq, bf16* __restrict__ out) {
  int b = blockIdx.x;
  asm volatile("s_load_dwordx8 s[92:99], %1, 0\n\ts_load_dwordx2 s[60:61], %1, 32" : "+s"(b) : "s"(__builtin_amdgcn_kernarg_segment_ptr()));
  const bf16* ckv;
  const int bp = b >> 3;
  extern __shared__ __attribute__((aligned(16))) char lds[];
  asm volatile("at_code_begin:" ::: ACLOB_ALL, "v255", "s101");
  KST(0, 0);
  const int tid = threadIdx.x;
  int lane = tid & 63;
  const int wid = __builtin_amdgcn_readfirstlane(tid >> 6);
  const int j = b & 7, r = b >> 3, rr = r & 15, pj = rr >> 1, kv = pj & 1;
  const bool isY = r >= 16, doA = !isY && pj != NCV;
  const int cr = rr - (pj > NCV ? 2 : 0);
  const bf16* wkv;
  const int dq = Q44 ? (j & 1) - ((rr >> 1) & 1) : 0;
  unsigned vq, vw, tb0, vbA2, vD;
  const char* tp;
  {
    GM g;
    {
      static_assert(Q44 && K2(1) == 3);
#define CTP 0
#define P_TM "s_memrealtime s[52:53]\n\ts_waitcnt lgkmcnt(0)\n\t"
#define P_T40
#define P_T41
#define P_T42
#define P_CT "s_getpc_b64 s[76:77]\n\tv_mbcnt_lo_u32_b32 v99, -1, 0\n\ts_lshr_b32 s88, %4, 3\n\ts_and_b32 s76, s76, 0xffffff80\n\ts_lshl_b32 s88, s88, 9\n\ts_and_b32 s77, s77, 0xffff\n\t" \
             "s_lshl1_add_u32 s88, s90, s88\n\ts_mov_b32 s78, pf_code_end - at_code_begin\n\ts_mov_b32 s79, 0x20000\n\tv_lshl_add_u32 v99, v99, 14, s88\n\ts_mov_b32 m0, %5\n\t" \
             "s_mov_b64 exec, 0xff\n\tbuffer_load_dword v99, s[76:79], 0 offen lds\n\ts_mov_b64 exec, -1\n\t"
#define P_CTL(R0, R1, R2, R3, RR) "s_getpc_b64 s[" R0 ":" R1 "]\n\tv_mbcnt_lo_u32_b32 v100, -1, 0\n\tv_readfirstlane_b32 s89, %3\n\ts_lshr_b32 s88, %4, 3\n\ts_and_b32 s" R0 ", s" R0 ", 0xffffff80\n\t" \
             "s_lshl_b32 s88, s88, 9\n\ts_and_b32 s" R1 ", s" R1 ", 0xffff\n\ts_lshl1_add_u32 s88, s89, s88\n\ts_mov_b32 s" R2 ", pf_code_end - at_code_begin\n\ts_mov_b32 s" R3 ", 0x20000\n\t" \
             "v_lshl_add_u32 v100, v100, 14, s88\n\ts_mov_b32 m0, %5\n\ts_mov_b64 exec, 0xff\n\tbuffer_load_dword v100, " RR ", 0 offen lds\n\ts_mov_b64 exec, -1\n\t"
#define P_CT0 P_CT
#define P_CTX1
#define P_CTX2
#define P_LA(SO) "buffer_load_dwordx4 v86, s[64:67], " SO " offen" GSC " lds\n\t"
#define P_LW(SO) "buffer_load_dwordx4 v87, s[68:71], " SO " offen" GSC " lds\n\t"
      asm volatile("v_readfirstlane_b32 s90, %3\n\tv_lshrrev_b32 v96, 4, %3\n\t" P_CT0 "v_lshrrev_b32 v97, 3, %3\n\ts_and_b32 s63, %2, 1\n\tv_xor_b32 v98, v96, %3\n\ts_lshl_b32 s88, s63, 2\n\t"
                   "s_bfe_u32 s89, %2, 0x20005\n\tv_and_b32 v98, 7, v98\n\ts_movk_i32 s62, 0x800\n\ts_bitcmp1_b32 %2, 7\n\t"
                   "s_cselect_b32 s88, s88, 0\n\ts_cmovk_i32 s62, 0x380\n\ts_add_u32 s89, s89, s88\n\ts_lshl_b32 s89, s89, 8\n\tv_lshlrev_b32 v98, 4, v98\n\t"
                   "v_add_u32 v97, s89, v97\n\ts_lshl_b32 s90, s90, 4\n\ts_and_b32 s88, %2, 0x80\n\ts_lshl_b32 s88, s88, 8\n\tv_mad_u32_u24 v86, v97, s62, v98\n\t"
                   "s_or_b32 s90, s90, s88\n\ts_lshl_b32 s66, s62, 11\n\ts_mov_b32 s67, 0x20000\n\ts_mov_b32 m0, s90\n\ts_lshl_b32 s72, s62, 5\n\ts_lshl_b32 s75, s62, 7\n\t"
                   "s_add_u32 s76, s75, s72\n\t" P_T40 "s_bitcmp1_b32 %2, 7\n\t" KWAIT "s_cselect_b64 s[64:65], s[94:95], s[92:93]\n\t"
                   P_LA("0") P_T41
                   "s_add_u32 m0, s90, 0x1000\n\tv_and_b32 v99, 3, v96\n\t" P_LA("s72")
                   "s_add_u32 m0, s90, 0x4000\n\tv_lshrrev_b32 v97, 6, %3\n\t" P_LA("s75")
                   "s_add_u32 m0, s90, 0x5000\n\tv_lshl_or_b32 v99, v99, 2, v97\n\t" P_LA("s76")
                   P_T42 P_CTX1 "s_bfe_u32 s88, %2, 0x10004\n\tv_xor_b32 v99, v99, %3\n\ts_sub_u32 s63, s63, s88\n\tv_and_b32 v99, 15, v99\n\ts_and_b32 s88, %2, 15\n\t"
                   "v_lshlrev_b32 v99, 4, v99\n\ts_bitcmp1_b32 %2, 7\n\ts_cselect_b32 s63, s63, 0\n\tv_lshl_add_u32 v99, v96, 13, v99\n\ts_sub_u32 s88, s88, s63\n\t"
                   "s_lshl_b32 s88, s88, 9\n\ts_bitcmp1_b32 %2, 4\n\ts_cselect_b64 %1, s[98:99], s[96:97]\n\tv_add_u32 v87, s88, v99\n\ts_bitcmp1_b32 %2, 7\n\t"
                   "s_cselect_b64 s[68:69], s[60:61], %1\n\ts_lshl_b32 s70, s62, 12\n\ts_add_u32 m0, s90, 0x10000\n\ts_mov_b32 s71, 0x20000\n\t"
                   P_LW("0")
                   "s_add_u32 m0, s90, 0x11000\n\ts_mov_b32 s80, 0x20000\n\t" P_LW("s80")
                   "s_add_u32 m0, s90, 0x12000\n\ts_movk_i32 s81, 0x100\n\t" P_LW("s81")
                   "s_add_u32 m0, s90, 0x13000\n\ts_mov_b32 s82, 0x20100\n\t" P_LW("s82")
                   "s_add_u32 m0, s90, 0x14000\n\ts_mov_b32 s83, 0x40000\n\t" P_LW("s83")
                   "s_add_u32 m0, s90, 0x15000\n\ts_mov_b32 s84, 0x60000\n\t" P_LW("s84")
                   "s_add_u32 m0, s90, 0x16000\n\ts_mov_b32 s85, 0x40100\n\t" P_LW("s85")
                   "s_add_u32 m0, s90, 0x17000\n\ts_mov_b32 s86, 0x60100\n\t" P_LW("s86")
                   P_CTX2 "s_mov_b64 %0, s[92:93]\n\ts_lshl_b32 s73, s72, 1\n\ts_add_u32 s74, s73, s72\n\ts_add_u32 s77, s75, s73\n\ts_add_u32 s78, s75, s74\n\t"
                   "s_and_b32 s79, s90, 0xfff\n\ts_add_u32 s87, s79, 0x10000\n\tv_add_u32 v84, 0x80, v86\n\tv_add_u32 v85, 0x80000, v87\n\t"
                   "s_mov_b32 s58, 0x6400000\n\ts_mov_b32 s59, 0x20000\n\ts_xor_b32 s92, s90, 0x8000\n\t"
                   "s_bfe_u32 s88, %2, 0x40003\n\ts_bfe_u32 s89, %2, 0x20005\n\ts_add_u32 s88, s88, s89\n\ts_and_b32 s88, s88, 3\n\ts_bitcmp1_b32 %2, 7\n\t"
                   "s_cselect_b32 s88, 0, s88\n\ts_add_u32 s91, s88, 2"
                   : "=&s"(ckv), "=&s"(wkv) : "s"(b), "v"(tid), "s"(blockIdx.x), "i"(L_SP + 16384) : "scc", "memory");
      GAP();
      asm volatile("" : "+s"(ws), "+s"(ak), "+s"(av), "+s"(ash), "+s"(assmk), "+s"(assmD), "+s"(wo), "+s"(aq), "+s"(out));
    }
    auto addrs = [&]() __attribute__((always_inline)) {
      asm volatile("" : "+v"(lane));
      const int fr = lane & 15, fq = lane >> 4, q = (lane & 15) >> 2, p = lane & 3;
#pragma unroll
      for (int k2 = 0; k2 < 2; ++k2)
        g.aA[k2] = (wid >> 1) * 16384 + (fr >> 3) * 1024 + (fr & 7) * 128 + (((4 * k2 + fq) ^ (fr >> 1)) * 16);
#pragma unroll
      for (int hf = 0; hf < 2; ++hf) {
        const unsigned a0 = 65536 + (wid & 1) * 8192 + off_b(8 * fq + 4 * hf + q, p >> 1) + 8 * (p & 1);
#pragma unroll
        for (int nf = 0; nf < 8; ++nf) { g.aB[hf][nf] = a0 ^ (nf * 32); asm volatile("" : "+v"(g.aB[hf][nf])); }
      }
      GAP();
    };
    auto consts2 = [&]() __attribute__((always_inline)) {
      const int tl = (lane * 37) >> 8, ck = lane - tl * 7;
      vq = lane < 56 ? (tl * DA + ck * 8) * 2 : OOB;
      asm volatile("" : "+v"(vq));
    };
    auto consts = [&](auto Yc) __attribute__((always_inline)) {
      const int fr = lane & 15, fq = lane >> 4;
      if constexpr (Yc) {
        vbA2 = ((((rr >> 2) + 4) * 256 + wid * 8 + (lane >> 3)) * DC + ((lane & 7) ^ (((wid & 1) * 4 + fq) & 7)) * 8) * 2;
        asm volatile("" : "+v"(vbA2));
      } else {
        consts2();
        vw = (4 * wid + fq) * DA * 2 + 16 * (fr ^ ((fq << 2) | wid));
        tb0 = L_SP + off_b(4 * fq + (fr >> 2), wid * 4 + ((lane & 3) >> 1)) + 8 * (lane & 1);
        tp = (lane < 32 && doA ? (const char*)assmk + (j * 56 + cr * 4 + wid) * 4096 : (const char*)ckv + 0x200000 + ((b & 127) * 4 + wid) * 4096) + (lane & 31) * 128;
        asm volatile("" : "+v"(vw), "+v"(tb0), "+v"(tp));
      }
      GAP();
    };
    addrs();
    PA pa;
    const int pu = p_unit(j, rr);
    if (isY) {
      const int rp = tid >> 3, c = tid & 7, ch = tid >> 4, q = tid & 15;
      pa.vo = pu < 0 ? OOB : (((pu & 15) * 128 + rp * 4) * DA + (pu >> 4) * 64 + c * 8) * 2;
      pa.lw = L_SP + 20480 + wid * 1024 + c * 64;
      pa.lx = L_SP + c * 2048 + (((31 - rp) * 8) ^ (c << 4));
      pa.lr = L_SP + ch * 256 + 16 * (q ^ (ch >> 3));
      pa.vs = ch * (SEQ * 2) + q * 16;
      asm volatile("" : "+v"(pa.vo), "+v"(pa.lw), "+v"(pa.lx), "+v"(pa.lr), "+v"(pa.vs));
      consts(std::true_type{});
      asm volatile("" : "=v"(vq), "=v"(vw), "=v"(tb0), "=v"(tp));
    } else {
      consts(std::false_type{});
      asm volatile("" : "=v"(pa.vo), "=v"(pa.lw), "=v"(pa.lx), "=v"(pa.lr), "=v"(pa.vs), "=v"(vbA2));
    }
    {
      const int fr = lane & 15, fq = lane >> 4;
      vseti<88>(fq * 4352 + fr * 8); vseti<89>(lane * 16); vseti<90>(wid * 8192 + lane * 16); vseti<91>(wid * 8192 + lane * 16 + 4096);
      asm volatile("s_getpc_b64 s[100:101]\n\ts_add_u32 s100, s100, at_event@rel32@lo+4\n\ts_addc_u32 s101, s101, at_event@rel32@hi+12" ::: "scc");
      asm volatile("s_lshl_b32 s63, %0, 18\n\tv_add_u32 v115, s63, v85\n\ts_xor_b32 s63, s63, 0x40000\n\tv_add_u32 v116, s63, v85\n\t"
                   "s_mul_i32 s63, %0, s73\n\tv_add_u32 v117, s63, v84\n\ts_sub_u32 s63, s73, s63\n\tv_add_u32 v118, s63, v84\n\t"
                   "s_lshl_b32 s63, %0, 13\n\ts_add_u32 s98, s92, s63\n\ts_xor_b32 s99, s98, 0x2000\n\t"
                   "s_add_u32 s96, s98, s63\n\ts_bitset1_b32 s96, 16\n\ts_xor_b32 s97, s96, 0x4000" ::"s"((rr + (rr >> 2)) & 1) : "scc");
      g_pro<12>(make_iseq<4>{});
      g_part<0, 0>(); g_part<0, 1>(); g_part<0, 2>(); g_part<0, 3>();
      {
        const char* xp;
        asm volatile("s_mov_b64 %0, s[94:95]" : "=s"(xp));
        asm volatile("s_mov_b32 m0, %1\n\ts_mov_b64 exec, 0x3fff\n\tglobal_load_lds_dword %0, off\n\ts_mov_b64 exec, -1"
                     ::"v"(xp + ((b * 4 + wid) * 14 + lane) * 128), "i"(L_SP + 16384) : "memory");
      }
      asm volatile("s_mov_b64 s[56:57], %0\n\ts_mov_b32 s63, %2\n\t"
                   "global_load_dwordx3 v[92:94], %1, off\n\tglobal_load_ushort v95, %3, off\n\tbuffer_load_dwordx2 v[120:121], off, " SW ", s63 sc1"
                   ::"s"(ws), "v"(&PTAB[j].v[r][0][tid][0]), "i"(fwo(6144)), "v"(assmD + j * 56 + (doA ? cr * 4 + wid : 0)) : "memory");
      pa.sk = __builtin_amdgcn_make_buffer_rsrc((void*)ak, (short)0, SEQ * DA * 2, 0x00020000);
      pa.sv = __builtin_amdgcn_make_buffer_rsrc((void*)av, (short)0, SEQ * DA * 2, 0x00020000);
      pa.sw = __builtin_amdgcn_make_buffer_rsrc((void*)(ash + (pu >> 4) * 256), (short)0, pu < 0 ? 0 : 512, 0x00020000);
      asm volatile("" : "+s"(pa.sk), "+s"(pa.sv), "+s"(pa.sw));
    }
    auto start = [&](auto Pc) __attribute__((always_inline)) {
      constexpr int P = Pc;
      asm volatile("s_waitcnt vmcnt(%0) lgkmcnt(0)\n\ts_barrier" ::"i"(CTP == 2 ? 13 : 12) : "memory");
      g_read0<P>(g, make_iseq<16>{});
      asm volatile("s_waitcnt lgkmcnt(0)\n\ts_barrier" ::: "memory");
      GAP();
    };
    if (isY) {
      start(std::integral_constant<int, 1>{});
      RSV rsv;
      rsv_begin(rsv);
      static_for([&](auto Tc) __attribute__((always_inline)) {
        constexpr int T = Tc;
        q_half<T, 0>(g, pa);
        if constexpr (T == 4) {
          const bf16* w_ = wkv; const int d_ = dq * 512;
          sld<88, A_TAB>(); sld<89, A_TAB + 1>();
          asm volatile("s_mov_b64 s[68:69], %0\n\ts_mov_b32 s70, 0x800000\n\tv_add_u32 v87, %1, v87" ::"s"(w_), "s"(d_));
          code_touch<0>();
        }
        if constexpr (T == 5) {
          const bf16* c_ = ckv; const unsigned v_ = vbA2;
          asm volatile("s_mov_b64 s[64:65], %0\n\ts_mov_b32 s66, 0x400000\n\tv_mov_b32 v86, %1\n\ts_mov_b32 s72, 0x10000\n\ts_mov_b32 s73, 0x20000\n\t"
                       "s_mov_b32 s74, 0x30000\n\ts_mov_b32 s75, 0x40000\n\ts_mov_b32 s76, 0x50000\n\ts_mov_b32 s77, 0x60000\n\ts_mov_b32 s78, 0x70000"
                       ::"s"(c_), "v"(v_));
        }
        GAP();
        q_half<T, 1>(g, pa);
      }, make_iseq<6>{});
      rsv_end(rsv);
      asm volatile("v_lshrrev_b32 %0, 4, v89" : "=v"(lane));
      consts2();
      asm volatile("" : "=v"(vw), "=v"(tb0), "=v"(tp), "=v"(vD));
      GAP();
      STT(0);
      g_half<false, 1, 0, 3>(g);
      g_half<false, 1, 1, 3>(g);
      set_flag<A_FPREP>();
      KST(0, 3);
    }
    unsigned om = 0x7000u | (isY ? 4u : (wid & 1) ? TOUCH1 : TOUCH0);
    asm volatile("" : "+s"(om));
    auto pollG = [&]() __attribute__((always_inline)) {
      sld<62, A_NPB>();
      fl_mask<A_NP, A_NP + 1>();
      asm volatile("s_nop 1");
      fl_issue();
    };
    auto oddG = [&](int t) __attribute__((always_inline)) {
      if (__builtin_expect((om >> t) & 1, 0)) {
        if (t != 13) {
          asm volatile("s_mov_b32 m0, %1\n\ts_nop 0\n\tglobal_load_lds_dword %0, off" ::"v"(tp), "i"(L_SP) : "memory");
        } else {
          fl_check();
          sld<63, A_VBB>(); sld<87, A_MB2>();
          asm volatile("s_mov_b64 s[68:69], %0\n\ts_mov_b32 s70, %1\n\ts_mov_b64 s[88:89], 0\n\tv_add_u32 v87, s63, v89\n\t"
                       "s_movk_i32 s80, 0x400\n\ts_movk_i32 s81, 0x800\n\ts_movk_i32 s82, 0xc00\n\ts_movk_i32 s83, 0x1000\n\t"
                       "s_movk_i32 s84, 0x1400\n\ts_movk_i32 s85, 0x1800\n\ts_movk_i32 s86, 0x1c00"
                       ::"s"(isY ? (const void*)ws : (const void*)assmk), "s"(isY ? 0x6400000 : DA * SEQ * 2));
        }
      }
    };
    auto evenG = [&](int t) __attribute__((always_inline)) {
      if (__builtin_expect((om >> t) & 1, 0)) {
        if (t == 2) set_flag<A_FT2>();
        else if (t == 12) pollG();
        else {
          sld<63, A_VBA>(); sld<79, A_MA2>();
          asm volatile("s_mov_b64 s[64:65], s[56:57]\n\ts_mov_b32 s66, s58\n\tv_add_u32 v87, 0x2000, v87\n\tv_add_u32 v86, s63, v89\n\t"
                       "s_movk_i32 s72, 0x1000\n\ts_movk_i32 s75, 0x4000\n\ts_movk_i32 s76, 0x5000");
          code_touch<1>();
        }
      }
    };
    auto tileG = [&](auto TRc) __attribute__((always_inline)) {
      constexpr bool TR = TRc;
      if (isY) {
        STT(5);
        g_half<TR, 0, 0, 2>(g);
        g_half<TR, 0, 1, 2>(g);
      } else {
        start(std::integral_constant<int, 0>{});
        g_half<TR, 0, 0, 1>(g);
        g_half<TR, 0, 1, 1>(g);
        vD = __float_as_uint(vget<95>());
      }
      g_loop<TR, false>(g, oddG, evenG);
      STT(10); STT(11);
      g_half<TR, 1, 0, 4>(g, wid);
      g_half<TR, 1, 1, 4>(g, wid);
      GAP();
    };
    if (kv) tileG(std::true_type{});
    else tileG(std::false_type{});
    STT(16);
  }
  att_item<3>(); att_item<4>(); att_item<5>(); att_item<6>();
  vseti<95>(wid * 16 + (lane & 15) - 4 * (lane >> 4)); vseti<126>(48 - wid * 16 + (lane & 15) - 4 * (lane >> 4)); vseti<127>(0xff800000u);
  asm volatile("s_mov_b64 s[88:89], %0\n\tv_mov_b32 v87, %1\n\ts_mov_b64 s[96:97], %2\n\tv_writelane_b32 v92, s96, 0\n\tv_writelane_b32 v92, s97, 1" ::"s"(aq), "v"(vq), "s"(out));
  KST(0, 4);
  if (isY) {
    sld<72, A_FCW>(); sld<96, B_MM, 1>(); sld<73, B_OP, 1>();
    asm volatile("s_mov_b32 s97, 0\n\ts_add_u32 m0, s78, 0x2000\n\ts_add_u32 s74, s74, 0x2000\n\t" A_POLL("v[122:123]", "s[96:97]", "s72") ::: "scc", "memory");
    dma4<89, 74>();
  } else if (doA) {
    const int l16 = lane & 15, fq = lane >> 4;
    CV c;
    c.lw = 65536 + wid * 15488;
    asm volatile("ds_write_b128 %0, v[114:117] offset:4096" ::"v"(c.lw + lane * 16));
    fl_mask<-1, A_NQ>();
#pragma unroll
    for (int i = 0; i < 4; ++i) c.pa[i] = c.lw + (((l16 + 1) & 1) ? CV_KS : CV_KP) + 4 * ((8 * fq + l16 + 1) >> 1) + 1024 * i;
    c.pb = c.lw + (2032 - 16 * l16 + 8 * fq) * 2 - 512 * 7;
    KST(0, 5);
    cv_rd0(c, make_iseq<24>{});
    static_for([&](auto I) { vseti<96 + I>(15 - l16 >> 1 == 4 * fq + I ? vD << (l16 & 1 ? 0 : 16) : 0u); }, make_iseq<4>{});
    fl_issue();
    c.vzo = (l16 >> 1) * 3584 + (l16 & 1) * 32 + (fq >> 1) * 16 + (1 - (fq & 1)) * 8 * 3584;
    GAP();
    cv_slots(c, make_iseq<144>{});
    static_for([&](auto T) { cv_slot<144 + T>(c); }, make_iseq<144>{});
    cv_tails(c, make_iseq<28 - cv_z0(288)>{});
    KST(0, 6);
    asm volatile("s_waitcnt vmcnt(%0) lgkmcnt(0)\n\ts_barrier" ::"i"(cv_end()) : "memory");
  } else {
    d_ops<160, 0>(make_iseq<208>{});
    fl_mask<-1, A_NQ>();
    sld<72, A_OQ>();
    fl_wait();
    asm volatile("s_waitcnt lgkmcnt(0)\n\ts_barrier" ::: "memory");
    set_flag<A_FG>();
    static_for([&](auto I) { f_load<128, I>(); if constexpr ((I & 3) == 3) f_next(); }, make_iseq<16>{});
    KST(0, 5);
    fl_mask<A_N01, -1>();
    fl_wait();
    k01_dma<0>(); k01_dma<1>(); k01_dma<2>(); k01_dma<3>();
    KST(0, 6);
    asm volatile("s_waitcnt vmcnt(8)\n\ts_barrier" ::: "memory");
  }
  if (!isY) {
    sld<94, A_WSRC>(); sld<73, B_OP, 1>();
    asm volatile("s_mov_b64 s[64:65], %0\n\ts_mov_b32 s66, 0x380000\n\ts_mov_b32 s67, s59\n\ts_mov_b32 s75, %1\n\t"
                 "v_mov_b32 v84, %2\n\tv_mov_b32 v85, %3\n\tv_mov_b32 v121, %4"
                 ::"s"(wo), "s"(L_SP + wid * 1024), "v"(vw), "v"(tb0), "v"(lane < 28 ? ((b * 4 + wid) * 28 + lane) * 128 : OOB));
  }
  u32x16 rs0, rs1; u32x4 rs2; u32x2 rs3;
#define STA(k)
  {
    AC c;
    asm volatile("rsv2_begin:" : "={v[40:55]}"(rs0), "={v[56:71]}"(rs1), "={v[72:75]}"(rs2), "={v[76:77]}"(rs3));
    const unsigned vml = (lane & 15) * 16;
#pragma unroll 1
    for (int it = r >> 4;; ++it) {
      if (it == r >> 4) KST(0, 8); else KST(0, 11);
      kreads<0>(make_iseq<8>{});
      const int n = prm<B_N, 1>();
      GAP();
      STA(0);
      if (isY) {
        phaseA<0, 2, false>(c);
        STA(1); STA(2);
        phaseB0<true>(false, vml);
      } else {
        if (it) phaseA<0, 3, false>(c);
        else phaseA<0, 1, false>(c);
        STA(1);
        if (c.rare) event();
        STA(2);
        phaseB0<false>(it && n == 2, vml);
      }
      STA(3);
      const int nl = n - it;
#pragma unroll 1
      for (int i = 1; i != nl; i += 2) {
        phaseA<1, 0, false>(c);
        STA(9 + i);
        if (__builtin_expect(c.rare != 0, 0)) { if (c.redo) sm_redo<1>(); event(); }
        phaseB<1>(c);
        if (i + 1 == nl) break;
        phaseA<0, 0, false>(c);
        STA(10 + i);
        if (__builtin_expect(c.rare != 0, 0)) { if (c.redo) sm_redo<0>(); event(); }
        phaseB<0>(c);
      }
      STA(4);
      if (it) break;
      sld<72, B_NXT, 1>(); sld<73, B_OP, 1>();
      epi_init<32768>();
      phaseA<ODD, 0, true>(c);
      if (__builtin_expect(c.redo != 0, 0)) sm_redo<ODD>();
      KST(0, 9);
      const float l[2] = {row_reduce<false>(vget<R_RS>() + vget<R_TS>()), row_reduce<false>(vget<R_RS + 1>() + vget<R_TS + 1>())};
      const f32x4 ml = {vget<R_M>(), l[0], vget<R_M + 1>(), l[1]};
      asm volatile("buffer_store_dwordx4 %0, %1, " SW ", s73 offen sc1" ::"v"(ml), "v"(vml) : "memory");
      phaseF(c, EP{}, make_iseq<74>{});
      asm volatile("s_waitcnt vmcnt(16)" ::: "memory");
      KST(0, 10);
    }
    asm volatile("at_tail:");
    vseti<84>(wid == 3 ? OOB : lane * 16);
    auto sel = [&]() __attribute__((always_inline)) {
      const unsigned one = (lane >> 4) == ((lane & 15) >> 2) ? 0x3f80u << ((lane & 1) * 16) : 0u, s0 = lane & 2 ? 0u : one, s1 = lane & 2 ? one : 0u;
      asm volatile("v_mov_b32 v72, %0\n\tv_mov_b32 v73, %1\n\tv_mov_b64 v[74:75], 0\n\tv_mov_b32 v76, %0\n\tv_mov_b32 v77, %1" ::"v"(s0), "v"(s1));
    };
    auto fin = [&](auto Lc, auto Mc) __attribute__((always_inline)) {
      constexpr int L = Lc, M = Mc;
      EP p;
      phaseAD<L, M == 1>(c, make_iseq<32>{});
      if (__builtin_expect(c.redo != 0, 0)) sm_redo<L>();
      STA(5);
      sld<76, B_SO, 1>();
      vseti<85>((wid >> 1) * 16384 + (wid & 1) * 1024 + lane * 16);
      if constexpr (M < 2) p.fa[0] = __builtin_amdgcn_rcpf(row_reduce<false>(vget<R_RS>() + vget<R_TS>()));
      GAP();
      phaseBD<L, M < 2>(c, p, make_iseq<64>{});
      if (__builtin_expect(c.redo != 0, 0)) sm_redo<L ^ 1, 1>();
      STA(6);
      KST(0, 12);
      asm volatile("s_waitcnt vmcnt(%0)\n\ts_barrier" ::"i"(M < 2 ? 6 + BDX : 0) : "memory");
      if constexpr (M == 2) { asm volatile("v_add_u32 v107, %0, v90" ::"i"(L_SP)); epi_preads(make_iseq<8>{}); }
      vreads<L>(make_iseq<16>{});
      const float l1 = row_reduce<false>(vget<R_RS + 1>() + vget<R_TS + 1>());
      if constexpr (M < 2) {
        p.fa[1] = __builtin_amdgcn_rcpf(l1);
        asm volatile("s_waitcnt lgkmcnt(0)" ::: "memory");
        phaseFD<L, true>(p, make_iseq<40>{});
      } else {
        const float l[2] = {row_reduce<false>(vget<R_RS>()), l1}, cm[2] = {vget<R_M>(), vget<R_M + 1>()};
        const f32x4 ml = {vget<72>(), vget<73>(), vget<74>(), vget<75>()};
#pragma unroll
        for (int e = 0; e < 2; ++e) {
          const float mm = fmaxf(cm[e], ml[2 * e]);
          const float wa = __builtin_amdgcn_exp2f(cm[e] - mm), wb = __builtin_amdgcn_exp2f(ml[2 * e] - mm);
          const float inv = __builtin_amdgcn_rcpf(l[e] * wa + ml[2 * e + 1] * wb);
          p.fa[e] = wa * inv; p.fb[e] = wb * inv;
        }
        sel();
        static_for([&](auto I) { asm volatile("v_mov_b32 v%c0, v%c1" ::"i"(160 + (int)I), "i"(40 + (int)I)); }, make_iseq<32>{});
        asm volatile("s_waitcnt lgkmcnt(0)" ::: "memory");
        phaseFD<L, false>(p, make_iseq<64>{});
      }
    };
    using I0 = std::integral_constant<int, 0>; using I1 = std::integral_constant<int, 1>; using I2 = std::integral_constant<int, 2>;
    using LY = std::integral_constant<int, ODD ? 0 : 1>;
    if (isY) {
      const unsigned late = prm<B_LM + 1, 1>();
      asm volatile("s_waitcnt vmcnt(12)" ::: "memory");
      const float cm[2] = {vget<R_M>(), vget<R_M + 1>()};
      const f32x4 ml = {vget<72>(), vget<73>(), vget<74>(), vget<75>()};
      if (__builtin_expect(!late && __builtin_amdgcn_ballot_w64(cm[0] != ml[0] || cm[1] != ml[2]) == 0, 1)) {
        vset<R_RS>(vget<R_RS>() + 0.25f * ml[1]); vset<R_RS + 1>(vget<R_RS + 1>() + 0.25f * ml[3]);
        sel();
        asm volatile("v_add_u32 v85, %0, v90" ::"i"(L_SP));
        fin(LY{}, I1{});
      } else fin(LY{}, I2{});
    } else { sel(); fin(I1{}, I0{}); }
    KST(0, 13);
  }
  {
    sld<64, A_OWN>(); sld<65, A_OWN + 1>(); sld<66, A_OWN + 2>(); sld<67, A_OWN + 3>();
    asm volatile("s_lshl_b32 s80, %0, 12\n\tv_add_u32 v96, s64, v89\n\tv_add_u32 v97, s65, v89\n\tv_add_u32 v98, s66, v89\n\tv_add_u32 v99, s67, v89\n\t"
                 "s_waitcnt lgkmcnt(0)\n\ts_barrier" ::"s"(wid) : "scc", "memory");
    o_read0(make_iseq<16>{});
    {
      const int fr = lane & 15, fq = lane >> 4;
      const unsigned vo = wid < 3 || !(fq & 1) ? prm<A_OUT>() + (fr * OUTW + (fq & 1) * 16 + (fq >> 1) * 8) * 2 : OOB;
      asm volatile("v_mov_b32 v101, %0\n\tv_add_u32 v102, %1, %0\n\ts_mov_b64 s[88:89], %2\n\ts_mov_b32 s90, 0x380000\n\ts_mov_b32 s91, s59"
                   ::"v"(vo), "s"((j < 4 ? 16 : -16) * OUTW * 2), "s"(out));
      GAP();
    }
    if (wid < 3) {
      static_for([&](auto P) {
        o_interval<2, P>();
        if constexpr (P == 1) KST(0, 27);
        if constexpr (P == 3) KST(0, 28);
      }, make_iseq<15>{});
      o_interval<2, 15>();
      o_slabs<2>();
      o_sum<2>();
    } else {
      sld<62, A_FCW>();
      fl_mask<A_FCM, -1>();
      GAP();
      static_for([&](auto Pc) {
        constexpr int P = Pc;
        o_interval<1, P>();
        if constexpr (P == 0 || P == 2) { fl_wait(); asm volatile("s_lshl_b32 s96, s96, 4" ::: "scc"); }
      }, make_iseq<15>{});
      o_interval<1, 15>();
      o_slabs<1>();
      o_sum<1>();
    }
    asm volatile("rsv2_end:" ::"{v[40:55]}"(rs0), "{v[56:71]}"(rs1), "{v[72:75]}"(rs2), "{v[76:77]}"(rs3));
    KST(0, 29);
#define A_CV(D, S0, S1) "v_cvt_pk_bf16_f32 v" #D ", v" #S0 ", v" #S1 "\n\t"
    asm volatile(A_CV(224, 240, 241) A_CV(226, 244, 245) A_CV(225, 242, 243) A_CV(227, 246, 247) A_CV(228, 248, 249) A_CV(230, 252, 253) A_CV(229, 250, 251) A_CV(231, 254, 255)
                 "v_permlane16_swap_b32_e32 v224, v226\n\tv_permlane16_swap_b32_e32 v225, v227\n\tv_permlane16_swap_b32_e32 v228, v230\n\tv_permlane16_swap_b32_e32 v229, v231\n\t"
                 "buffer_store_dwordx4 v[224:227], v101, s[88:91], 0 offen\n\tbuffer_store_dwordx4 v[228:231], v102, s[88:91], 0 offen" ::: "memory");
    set_flag<A_FE>();
  }
  KST(0, 1);
  asm volatile("s_endpgm\n" A_EVENT "\npf_code_end:" ::: "memory");
}

extern "C" void kernel_launch(void* const* d_in, const int* in_sizes, int n_in, void* d_out, int out_size, void* d_ws,
                              size_t ws_size, hipStream_t stream) {
  const bf16* Aq = (const bf16*)d_in[0];
  const bf16* Ak = (const bf16*)d_in[1];
  const bf16* Av = (const bf16*)d_in[2];
  const bf16* Ash = (const bf16*)d_in[3];
  const bf16* Assmk = (const bf16*)d_in[4];
  const bf16* AssmD = (const bf16*)d_in[5];
  const bf16* Bx = (const bf16*)d_in[6];
  const bf16* Bckv = (const bf16*)d_in[7];
  const bf16* Bwq = (const bf16*)d_in[8];
  const bf16* Bwuk = (const bf16*)d_in[9];
  const bf16* Bwuv = (const bf16*)d_in[10];
  const bf16* Bwo = (const bf16*)d_in[11];
  bf16* out = (bf16*)d_out;
  (void)hipFuncSetAttribute((const void*)fused_kernel, hipFuncAttributeMaxDynamicSharedMemorySize, 163840);
  fused_kernel<<<256, 256, 163840, stream>>>(Bckv, Bx, Bwuk, Bwuv, Bwq, (char*)d_ws + ((ws_size - 0x8000000) & ~(size_t)0x1fffff), Ak, Av, Ash, Assmk, AssmD, Bwo, Aq, out);
}
```

```cpp
#include <hip/hip_runtime.h>
#include <hip/hip_bf16.h>
#include <stdint.h>
#include <utility>
#include <type_traits>

typedef __hip_bfloat16 bf16;
typedef __attribute__((ext_vector_type(4))) short bf16x4;
typedef __attribute__((ext_vector_type(2))) unsigned u32x2;

constexpr int SEQ = 2048;
constexpr int DA = 448;
constexpr int NH = 16;
constexpr int HD = 256;
constexpr int DC = 1024;
constexpr int OUTW = 896;

#define KST(kid, which)

__device__ __forceinline__ float bf2f(unsigned short u) { return __uint_as_float(((unsigned)u) << 16); }
__device__ __forceinline__ unsigned short f2bf(float f) { return __bfloat16_as_ushort(__float2bfloat16(f)); }
__device__ __forceinline__ float rbf(float f) { return bf2f(f2bf(f)); }
typedef __attribute__((ext_vector_type(2))) __bf16 bf16x2n;
typedef __attribute__((ext_vector_type(2))) float f32x2n;
__device__ __forceinline__ unsigned pk2(float a, float b) {
  return __builtin_bit_cast(unsigned, __builtin_convertvector((f32x2n){a, b}, bf16x2n));
}

__device__ __forceinline__ int off_b(int row, int ch) { return 256 * row + 16 * (ch ^ (((row & 3) << 2) | ((row >> 2) & 3))); }

#define DI __device__ __forceinline__
#define SCC_
#define SCC1
#define SCC2
#define SCC3
#define GAP() __builtin_amdgcn_sched_barrier(0)
template <int R> DI float vget() { float t; asm volatile("v_mov_b32 %0, v%c1" : "=v"(t) : "i"(R)); return t; }
template <int R> DI void vset(float x) { asm volatile("v_mov_b32 v%c0, %1" ::"i"(R), "v"(x)); }
template <int R> DI void vseti(unsigned x) { asm volatile("v_mov_b32 v%c0, %1" ::"i"(R), "v"(x)); }
template <int R> DI float acc_read1() { float t; asm volatile("v_accvgpr_read_b32 %0, a[%c1]" : "=v"(t) : "i"(R)); return t; }
template <class F, int... I> DI void static_for(F&& f, std::integer_sequence<int, I...>) {
  (f(std::integral_constant<int, I>{}), ...);
}
template <int... Is> using iseq = std::integer_sequence<int, Is...>;
template <int N> using make_iseq = std::make_integer_sequence<int, N>;
typedef unsigned long long u64;

constexpr unsigned O_K = 1u << 24, O_V = 2u << 24, O_C = 3u << 24, O_P = 4u << 24, O_F = O_P + 128 * 67584, O_S = O_F + 400 * 4352,
                   O_X = O_S + (1u << 24), O_Z = O_X + DA * SEQ * 2, O_W = O_Z + DA * SEQ * 2;
static_assert(O_W + 16 * 262144 <= 0x6400000);
constexpr unsigned fwo(int w) { return O_F + (w >> 4) * 4352 + (w & 15) * 8; }
constexpr unsigned OOB = 0x80000000u, M0X = 0x28000;
constexpr unsigned L_SP = 131072;
#define CMAX 10
#define NYA 15
#define NYB 15
#define MAP4 2
#define TSEQ 1
constexpr int O_SEQ[16] = {0x00, 0x01, 0x02, 0x10, 0x20, 0x30, 0x11, 0x21, 0x31, 0x12, 0x22, 0x32, 0x13, 0x23, 0x33, 0x03};
constexpr int o_k(int P) { int k = 0; for (int q = 0; q < P; ++q) k += O_SEQ[q] >= 16; return O_SEQ[P] >= 16 ? k : -1; }
#define Q44 1
#define KTYV 0x76543210fedcba98ull
#define LATEP 0
#define PKS 3
#define PPOS 99, 10, 9, 9, 9, 8
#define TABX 0xfedcb9875a463210ull, 0xefadc6982b574310ull, 0xfebad7693c285410ull, 0xefcba8764d392510ull
#define K2(c) (2 + (c))
#define POSTM 7
#define NCV 0
#define WP0 8
constexpr int ODD = NYA & 1;
static_assert((NYB & 1) == ODD);
constexpr int NYS[8] = {NYA, NYA, NYA, NYA, NYB, NYB, NYB, NYB};
enum { A_TAB = 0, A_FPREP = 2, A_FT2, A_NP, A_NPB = 6, A_VBB, A_MB2, A_VBA, A_MA2, A_VOO, A_SX, A_MX = 16, A_VOQ = 20, A_FG, A_NQ, A_N01, A_ZO, A_OQ,
       A_WSRC, A_WDST, A_PQ, A_PZ, A_M0F, A_GH, A_PC = 32, A_WLD = 44, A_FC, A_FCW, A_FCM, A_SS, A_FS = 51, A_FSW, A_FSM, A_SL, A_OUT = 57, A_OWN, A_FE = 62, A_GF };
enum { B_N = 0, B_ST, B_MS, B_CT, B_FW, B_FSET, B_NXT, B_SO, B_OP, B_MM, B_PM, C_FCM = 16, C_QT, B_LM, B_LP = 21, B_WD, C_PO, B_WT, B_ACT = 32 };
enum { E_CHK = 1, E_PACK = 4, E_POLL = 8, E_POLL1 = 16, E_MASK = 32, E_FLAG = 64, E_WFLAG = 128, E_POLLZ = 256, E_LOAD = 512, E_POST = 1024, E_PLOAD = 2048, E_CHK1 = 4096 };
constexpr unsigned ev_code(unsigned a) {
  return a == 0 ? 0 : a == E_CHK ? 16 : a == E_FLAG ? 32 : a == (E_FLAG | E_POLL) ? 96 : a == E_POLL ? 56 : a == (E_WFLAG | E_POLLZ) ? 344 : a == E_WFLAG ? 80 : a == (E_FLAG | E_POLLZ) ? 448 : a == E_LOAD ? 400 : a == E_POST ? 512 : a == E_PLOAD ? 768 : a == (E_PLOAD | E_CHK1) ? 912 : a == E_CHK1 ? 872 :
         a == E_PACK ? 128 : a == E_POLL1 ? 192 : a == (E_POLL | E_POLL1 | E_MASK) ? 228 : a == (E_POLL1 | E_MASK) ? 240 : 1u << 31;
}
struct PTab { unsigned v[32][4][64][3]; bool bad; };
constexpr int p_unit(int j, int rr) { return (j & 1) && rr < 4 ? -1 : (j >> 1) * 28 + (j & 1) * 12 + rr; }
constexpr int o_head(int ks, int p) { return 2 * ks + (p >> 1) + 8 * (p & 1); }
constexpr PTab make_tab(int j) {
  PTab t{};
  for (int r = 0; r < 32; ++r) for (int wid = 0; wid < 4; ++wid) {
    auto& e = t.v[r][wid];
    const int rr = r & 15, hsg = rr & 1, pjg = rr >> 1, kv = pjg & 1, gw = j * 128;
    const bool isY = r >= 16;
    const int ks = j >> 1, p = MAP4 ? rr & 3 : (j & 1) * 2 + hsg, h = o_head(ks, p), hsel = h >> 3, ga = (h & 7) * 128;
    const int pj = MAP4 == 1 ? (rr >> 2) * 2 + (j & 1) : MAP4 == 2 ? (rr >> 2) + 4 * (j & 1) : pjg;
    const int mt = (rr >> 2) + (isY ? 4 : 0), cr = rr - (pjg > NCV ? 2 : 0), cch = j * 56 + cr * 4 + wid, NY = NYS[pj];
    const bool doA = !isY && pjg != NCV;
    const unsigned oG = (unsigned)(j + 8 * hsg) << 20;
    const int nx0 = 32 - 2 * pj - NY, cx = nx0 < CMAX ? nx0 : CMAX, ex = nx0 - cx, dg = 30 - 2 * pj;
    int seq[2][32] = {}, ns = 0;
    auto run = [&](int it, int t0, int t1) { for (int q = t0; q < t1; ++q) seq[it][ns++] = q; };
    const int x1 = TSEQ ? dg - ex : 16, c4 = 32 - 8 - NY < CMAX ? 32 - 8 - NY : CMAX;
    if (!isY) {
      run(0, 0, cx); run(0, x1, x1 + ex);
      ns = 0;
      const int s1 = TSEQ >= 2 && nx0 + TSEQ - 2 + 2 <= 2 * pj ? (nx0 < 4 ? 2 : nx0 + TSEQ - 2) : 0;
      run(1, s1, 2 * pj); run(1, 0, s1); run(1, 2 * pj, 2 * pj + 2);
    } else if (ex || !TSEQ) { run(0, cx, x1); run(0, x1 + ex, dg + 2); }
    else { run(0, c4 > cx ? c4 : cx, 16); run(0, cx, c4); run(0, 16, dg + 2); }
    const u64 own = h == j + 8 * hsg && isY ? 1ull << (hsel * 16 + kv * 8 + mt) : 0;
    u64 needB = 0;
    int c1 = 99;
    for (int i = (isY ? NY : nx0) - 1; i >= 0; --i) if (seq[0][i] >= 16) { needB |= 0x101ull << (hsel * 16 + (seq[0][i] >> 2)); c1 = i - 2; }
    needB &= ~own;
    const u64 needQ = (1ull << (32 + hsel * 8 + (pj >> 1))) | (1ull << (32 + hsel * 8 + ((15 - pj) >> 1)));
    const u64 needA = 0x0f0full << (hsel * 16);
    const unsigned oQ = (unsigned)h << 20, oK = oQ + O_K, oP = O_P + (h * 8 + pj) * 67584;
    const unsigned oQ0 = oQ + ((15 - pj) * 128 + wid * 32) * 512, oK0 = oK + seq[0][0] * 32768, lowb = ((wid & 1) + (wid >> 1) * 4) * 4096;
    auto w0 = [&](unsigned x) { return wid == 0 ? x : OOB; };
    const int cls = (rr + (rr >> 2)) & 3;
    const u64 tabX[4] = {TABX};
    const u64 tab = isY ? KTYV : tabX[cls];
    if ((tabX[cls] & 0xfff) != 0x10u + K2(cls) * 0x100) t.bad = true;
    e[C_QT][2] = 0x6543;
    e[A_TAB][0] = (unsigned)tab; e[A_TAB + 1][0] = (unsigned)(tab >> 32);
    const int pu = p_unit(j, rr);
    e[A_FPREP][0] = pu < 0 ? OOB : w0(fwo(6160 + pu));
    const int hq = Q44 ? o_head(ks, rr & 3) : j + 8 * hsg, tq = Q44 ? (rr >> 2) + 4 * (j & 1) : pjg;
    e[A_FT2][0] = isY ? w0(fwo((hq & 7) * 128 + 32 + (hq >> 3) * 8 + tq)) : OOB;
    const u64 np = isY ? needA | needQ : 0xffffull;
    e[A_NP][0] = (unsigned)np; e[A_NP + 1][0] = (unsigned)(np >> 32); e[A_NPB][0] = fwo(isY ? ga : 6160 + (cch >> 6) * 16);
    e[A_VBB][0] = oQ0; e[A_MB2][0] = isY ? 65536 + wid * 1024 : M0X;
    e[A_VBA][0] = oK0 + wid * 1024; e[A_MA2][0] = isY ? wid * 1024 : M0X;
    e[A_VOO][0] = oG + (kv ? O_V : O_K) + mt * 131072 + (wid >> 1) * 65536 + (wid & 1) * (kv ? 8192 : 4096);
    e[A_SX][0] = isY ? oK0 + lowb + 8192 : O_X + cch * 4096; e[A_SX + 1][0] = isY ? oQ0 + 12288 : cch * 4096;
    e[A_SX + 2][0] = oK0 + 32768 + lowb; e[A_SX + 3][0] = oK0 + O_K + wid * 8192;
    e[A_MX][0] = isY ? lowb + 8192 : doA ? 65536 + wid * 15488 : M0X; e[A_MX + 1][0] = isY ? 114688 + wid * 4096 : e[A_MX][0] + 5120;
    e[A_MX + 2][0] = isY ? 32768 + lowb : M0X; e[A_MX + 3][0] = isY ? 65536 + wid * 8192 : M0X;
    e[A_VOQ][0] = ((unsigned)hq << 20) + tq * 131072 + (wid >> 1) * 65536 + (wid & 1) * 4096;
    e[A_FG][0] = w0(fwo(gw + hsg * 16 + kv * 8 + mt));
    e[A_NQ][0] = (unsigned)(needQ >> 32); e[A_N01][0] = 0x0101u << (hsel * 16);
    e[A_ZO][0] = isY ? (pu < 0 ? OOB : O_X + (pu >> 4) * (64 * SEQ * 2) + (15 - (pu & 15)) * 256) : O_Z + j * (56 * SEQ * 2) + (cr * 4 + wid) * 64; e[A_OQ][0] = oQ0;
    const int qbl = isY ? 15 - pj : pj, u = 2 * pj, tile = qbl * 4 + p;
    e[A_WSRC][0] = ((o_head(ks, p ^ (u >> 2)) * 256 + (u & 3) * 64) * DA + p * 112) * 2;
    e[A_WLD][0] = O_W + h * 262144 + wid * 8192;
    e[A_WDST][0] = e[A_WLD][0] + u * 16384;
    e[A_PQ][0] = ((rr * 4 + wid) * 32 * DA + j * 56) * 2; e[A_PZ][0] = O_Z + (j * 64 + rr * 4 + wid) * 3584;
    e[A_M0F][0] = L_SP + wid * 8192; e[C_PO][2] = ((rr * 4 + wid) * 32 * OUTW + j * 56) * 2;
    e[A_GH][0] = fwo(gw + 64);
    auto qo = [&](int i) { return i == 3 ? ks : i + (i >= ks); };
    auto ctxo = [&](int o, int qb, int q) { return O_C + ((unsigned)o_head(ks, p ^ o) << 20) + qb * 65536 + q * 16384 + wid * 4096; };
    auto slab = [&](int tl, int q, int src) { return O_S + tl * 262144 + (q * 8 + src) * 8192 + wid * 2048; };
    for (int q = 0; q < 16; ++q) if (o_k(q) >= 0) e[A_PC + o_k(q)][0] = ctxo(O_SEQ[q] >> 4, qbl, qo(O_SEQ[q] & 3));
    e[A_FC][0] = w0(fwo(1024 + (qbl * 4 + ks) * 16 + p)); e[A_FCW][0] = fwo(1024 + (qbl * 4 + ks) * 16);
    e[A_FCM][0] = 15 & ~(1 << p);
    for (int q = 0; q < 3; ++q) { e[A_SS + q][0] = slab(tile, qo(q), 2 * ks); e[A_SL + q][0] = slab(tile, ks, 2 * qo(q)); }
    e[A_FS][0] = fwo(2048 + (tile * 4 + wid) * 16 + 2 * ks); e[A_FSW][0] = fwo(2048 + (tile * 4 + wid) * 16); e[A_FSM][0] = 0x55 & ~(1 << (2 * ks));
    e[A_OUT][0] = ((qbl * 128 + (ks < 2 ? 2 * ks : 11 - 2 * ks) * 16) * OUTW + DA + p * 112 + wid * 32) * 2;
    for (int q = 0; q < 4; ++q) e[A_OWN + q][0] = qo(q) * 16384;
    e[A_FE][0] = r == 28 && j == 4 ? w0(fwo(6144)) : OOB;
    e[A_GF][0] = fwo(ga);
    int wstep = isY || pj >= PKS ? 99 : 4, wpos = 0;
    for (int it = 0; it < 2; ++it) {
      const int R = 1 + it, n = isY ? NY : it ? 2 * pj + 2 : nx0, qb = isY || !it ? 15 - pj : pj;
      const bool last = isY || it;
      unsigned act[32] = {};
      int np_ = 0;
      auto poll = [&](int i, u64 m) {
        const int l = B_PM + 2 * np_++;
        e[l][R] = (unsigned)m; e[l + 1][R] = (unsigned)(m >> 32);
        if (act[i] & E_POLL) t.bad = true;
        act[i] |= E_POLL; act[i + 1] |= E_CHK;
      };
      if (!it || isY) act[1] |= E_FLAG;
      const bool late = isY && pj < LATEP;
      if (isY) {
        const int pa[8] = {99, 99, PPOS};
        int s_ = late ? n - 2 : pa[pj] + 15 - NY < n - 3 ? pa[pj] + 15 - NY : n - 3;
        int q = 1;
        if ((POSTM >> pj) & 1) {
          act[1] |= E_POLLZ; act[2] = E_LOAD;
          q = c1 == 4 ? 5 : 3;
          act[q] = E_POST;
        }
        while (s_ <= q || (s_ + 1 >= c1 - 1 && s_ <= c1)) ++s_;
        if (c1 < 4 || s_ > n - 3 + late) t.bad = true;
        if (c1 < s_) poll(c1 - 1, needB);
        poll(s_, 1ull << (48 + hsel * 8 + pj));
        act[s_ + 1] = late ? 0 : E_PLOAD;
        if (c1 > s_) poll(c1 - 1, needB);
      }
      else if (!it) { if (n > 2) poll(1, needA); if (c1 < 99) poll(c1 - 1, needB); }
      else if (nx0 <= 2) poll(1, needA);
      if (late) e[B_LM + 1][R] = 1u << (16 + hsel * 8 + pj);
      e[B_LP][R] = oP + wid * 8192;
      const int* sq = seq[isY ? 0 : it];
      if (sq[1] != sq[0] + 1 || (!isY && seq[1][0] + 1 >= 4 && nx0 < 4)) t.bad = true;
      if (last && n > 2) act[n - 3] |= E_POLL1 | E_MASK;
      if (!last && pj == 0) act[n - 2] |= E_POLL1;
      if (last) act[n - 2] |= E_CHK1;
      if (it && !isY && pj >= PKS) wstep = 5, wpos = WP0;
      const int wend = (POSTM >> pj) & 1 ? 6 : 8;
      const int ws_[4] = {E_PACK, wend == 6 ? E_WFLAG : E_WFLAG | E_POLLZ, E_LOAD, E_POST};
      for (int i = 0; i < (last ? n - 3 : n) && !isY; ++i, ++wpos)
        if (wstep < wend && wpos >= WP0 && !act[i] && i) act[i] = ws_[wstep++ - 4];
      if (it && wstep < wend) t.bad = true;
      e[B_WD][R] = pj >= PKS ? e[A_WDST][0] : OOB;
      e[B_WT][R] = O_W + h * 262144 + ((isY ? 4 : 0) + (pj & 3)) * 32768 + wid * 8192;
      e[B_FW][R] = wid < 2 ? fwo((j ^ wid) * 128 + 80 + wid * 16 + rr) : OOB;
      e[B_MS][R] = doA ? e[A_MX][0] + 10304 : M0X; e[B_CT][R] = (rr * 4 + wid) * 128;
      for (int i = 0; i < n; ++i) {
        const unsigned sk = i + 2 < n ? oK + sq[i + 2] * 32768 : last ? O_W + h * 262144 + 32768 : oK + (seq[1][0] + (i & 1)) * 32768;
        e[B_ACT + i][R] = sk | ev_code(act[i]);
        if (ev_code(act[i]) > 1023 || (sk & 1023)) t.bad = true;
      }
      e[B_N][R] = n; e[B_ST][R] = oK + sq[0] * 32768;
      e[B_FSET][R] = isY ? e[A_FG][0] : it ? w0(fwo(ga + 48 + hsel * 8 + pj)) : wid == 1 ? fwo(1024 + (qb * 4 + ks) * 16 + 8 + p) : doA ? w0(fwo(gw + 112 + cr)) : OOB;
      e[B_MM][R] = 256u << p;
      e[B_NXT][R] = last ? e[A_WLD][0] : oQ + (pj * 128 + wid * 32) * 512;
      e[B_SO][R] = last ? oQ + O_C + qb * 65536 : oP;
      e[B_OP][R] = oP + 65536 + wid * 256;
    }
  }
  return t;
}
constexpr PTab PT0 = make_tab(0), PT1 = make_tab(1), PT2 = make_tab(2), PT3 = make_tab(3), PT4 = make_tab(4), PT5 = make_tab(5), PT6 = make_tab(6), PT7 = make_tab(7);
static_assert(!PT0.bad && !PT7.bad);
static __device__ const PTab PTAB[8] = {PT0, PT1, PT2, PT3, PT4, PT5, PT6, PT7};

#define SPINB "0x100000"
#define SW "s[56:59]"
template <int S, int L, int ROW = 0> DI void sld() { asm volatile("v_readlane_b32 s%c0, v%c1, %c2" ::"i"(S), "i"(92 + ROW), "i"(L)); }
template <int L, int ROW = 0> DI unsigned prm() { unsigned x; asm volatile("v_readlane_b32 %0, v%c1, %c2\n\ts_nop 4" : "=s"(x) : "i"(92 + ROW), "i"(L)); return x; }
#define A_FLAG "s_mov_b64 exec, 1\n\tbuffer_store_dwordx2 v[82:83], off, " SW ", s63 sc1\n\ts_mov_b64 exec, -1\n\t"
#define A_POLL(V, M, B) "s_mov_b64 exec, " M "\n\tbuffer_load_dwordx2 " V ", v88, " SW ", " B " offen sc1\n\ts_mov_b64 exec, -1\n\t"
#define A_SPIN(V, M, B) "s_mov_b32 s95, " SPINB "\n\ts_mov_b64 exec, " M "\n" \
    "8:\n\tbuffer_load_dwordx2 " V ", v88, " SW ", " B " offen sc1\n\ts_waitcnt vmcnt(0)\n\tv_cmp_eq_u64 vcc, s[60:61], " V "\n\t" \
    "s_cmp_eq_u64 vcc, " M "\n\ts_cbranch_scc1 9f\n\ts_sleep 4\n\ts_sub_u32 s95, s95, 1\n\ts_cbranch_scc0 8b\n9:\n\ts_mov_b64 exec, -1\n\t"
#define A_CHECK(V, M, B) "v_cmp_eq_u64 vcc, s[60:61], " V "\n\ts_and_b64 vcc, vcc, " M "\n\ts_cmp_eq_u64 vcc, " M "\n\ts_cbranch_scc1 9f\n\t" A_SPIN(V, M, B)
#define A_CTOUCH(B, E, M) "v_readlane_b32 s63, v93, 3\n\ts_sub_u32 s96, s100, at_event - " B "\n\ts_subb_u32 s97, s101, 0\n\t" \
    "s_mov_b32 s98, " E " - " B "\n\ts_mov_b32 s99, s59\n\tv_lshl_add_u32 %0, v89, 9, s63\n\t" \
    "s_mov_b32 m0, 0x27f00\n\ts_mov_b64 exec, " M "\n\tbuffer_load_dword %0, s[96:99], 0 offen lds\n\ts_mov_b64 exec, -1\n\t"
template <int K> DI void code_touch() {
  unsigned t;
  if constexpr (K) asm volatile(A_CTOUCH("rsv_end", "at_tail", "0xff") : "=&v"(t) :: "scc", "memory");
  else asm volatile(A_CTOUCH("rsv_end", "rsv2_begin", "0x1f") : "=&v"(t) :: "scc", "memory");
}
template <int L, int ROW = 0> DI void set_flag() {
  asm volatile("v_readlane_b32 s63, v%c0, %c1\n\ts_nop 3\n\t" A_FLAG ::"i"(92 + ROW), "i"(L) : "memory");
}
template <int LO, int HI> DI void fl_mask() {
  if constexpr (LO >= 0) sld<96, LO>(); else asm volatile("s_mov_b32 s96, 0");
  if constexpr (HI >= 0) sld<97, HI>(); else asm volatile("s_mov_b32 s97, 0");
}
DI void fl_issue() { asm volatile(A_POLL("v[122:123]", "s[96:97]", "s62") ::: "memory"); }
DI void fl_check() { asm volatile(A_CHECK("v[122:123]", "s[96:97]", "s62") ::: "vcc", "scc", "memory"); }
DI void fl_wait() { asm volatile("s_nop 4\n\t" A_SPIN("v[122:123]", "s[96:97]", "s62") ::: "vcc", "scc", "memory"); }

typedef __attribute__((ext_vector_type(4))) unsigned u32x4c;
typedef __attribute__((ext_vector_type(4))) float f32x4c;
typedef __attribute__((ext_vector_type(2))) float f32x2c;
__device__ __forceinline__ float bfl(unsigned u) { return __uint_as_float(u << 16); }
__device__ __forceinline__ float bfh(unsigned u) { return __uint_as_float(u & 0xffff0000u); }

#define CODE_TOUCH(LB, LE, wx, NL, m0v) do { unsigned blo_, bhi_, elo_; \
    asm volatile("s_getpc_b64 s[100:101]\n\ts_add_u32 s100, s100, " LB "@rel32@lo+4\n\ts_addc_u32 s101, s101, " LB "@rel32@hi+12\n\t" \
                 "s_mov_b32 %0, s100\n\ts_mov_b32 %1, s101\n\t" \
                 "s_getpc_b64 s[100:101]\n\ts_add_u32 s100, s100, " LE "@rel32@lo+4\n\ts_mov_b32 %2, s100" \
                 : "=s"(blo_), "=s"(bhi_), "=s"(elo_) :: "scc"); \
    const unsigned n_ = elo_ - (blo_ & ~127u); \
    const __amdgpu_buffer_rsrc_t srd_ = __builtin_amdgcn_make_buffer_rsrc((void*)(((unsigned long long)bhi_ << 32) | (blo_ & ~127u)), (short)0, n_ < 262144u ? n_ : 0u, 0x00020000); \
    const unsigned l_ = threadIdx.x & 63, vo_ = l_ < (NL) ? ((wx) + l_ * 128u) * 128u : 0x80000000u; \
    asm volatile("s_mov_b32 m0, %0\n\ts_nop 4\n\tbuffer_load_dword %1, %2, 0 offen lds" ::"s"(m0v), "v"(vo_), "s"(srd_) : "memory"); } while (0)

#define STT(k)
#define SNK 31
#define GSC " sc1"
#define PL0 66
#define PLS 7
#define PCAP 0x55555555
#define PST 272
#define ECAP 3
#define EPACE_K 7
#define EPACE_V 6
typedef __attribute__((ext_vector_type(4))) float f32x4g;
typedef __attribute__((ext_vector_type(4))) unsigned u32x4g;
DI u32x4g pair16(f32x4g x, f32x4g y) {
  const unsigned a0 = pk2(x[0], x[1]), a1 = pk2(x[2], x[3]), b0 = pk2(y[0], y[1]), b1 = pk2(y[2], y[3]);
  auto r0 = __builtin_amdgcn_permlane16_swap(a0, b0, false, false), r1 = __builtin_amdgcn_permlane16_swap(a1, b1, false, false);
  return (u32x4g){r0[0], r1[0], r0[1], r1[1]};
}
constexpr int LDW = NH * HD;
struct GM { unsigned aA[2], aB[2][8]; };
#define GDP 8, 12, 24, 28, 40, 44, 56, 60
constexpr int GDPOS[8] = {GDP};
constexpr int g_j(int G) { for (int i = 0; i < 8; ++i) if (GDPOS[i] == (G & 63)) return i; return -1; }
constexpr int GD13 = 125 - GDPOS[3];
struct PA { unsigned vo, lw, lx, lr, vs; __amdgpu_buffer_rsrc_t sk, sv, sw; };
struct POp { int k, d, a, b; };
struct PSch { POp op[760]; int first[770], vm[770], n; };
constexpr PSch p_sched() {
  PSch z{};
  POp l[760] = {};
  int n = 0, early[760] = {};
  auto put = [&](int k, int d, int a, int b, int e = 0) { l[n] = POp{k, d, a, b}; early[n] = e; ++n; };
  for (int q = 0; q < 10; ++q) put(10, q < 6 ? q : q + 2, 0, 0, PL0 + PLS * q);
  for (int c = 0; c < 8; ++c) {
    const int I = c >> 1, u = c & 1;
    for (int t = 0; t < 4; ++t) put(t & 1, 112 + t, 76 + 2 * u + (t >> 1), 0, c ? 0 : PST);
    if (u && I < 3) put(11, I + 1, 0, 0);
    for (int q = 0; q < 7; ++q) put(u, 116 + q, 32 + 4 * q + I, 0);
    for (int e = 2; e >= 0; e -= 2) {
      put(2, 125, 117 + e, 115); put(2, 123, 116 + e, 115);
      for (int t = 1; t < 4; ++t) { put(4, 125, 117 + e + t, 115 - t); put(4, 123, 116 + e + t, 115 - t); }
      const int T = e ? 126 : 127;
      put(u, 124, 64 + 4 * e + I, 0); put(u, T, 60 + 4 * e + I, 0);
      put(2, 125, 125, 124); put(2, 123, 123, T);
      put(5, T, 125, 123);
    }
    put(12, c, 0, 0, (PST + 2) / 64 * 64 + 62);
  }
  const int nm = n;
  for (int q = 0; q < 4; ++q) put(13, q, 0, 0);
  for (int q = 0; q < 4; ++q) put(14, q, 0, 0);
  int g = 0, i = 0, hold = 0;
  for (; g < 768; ++g) {
    const int q = g & 15;
    int cap = g_j(g) >= 0 ? 0 : (PCAP >> (2 * q)) & 3;
    z.first[g] = z.n; z.vm[g + 1] = z.vm[g];
    if (g == PST - 16) z.op[z.n++] = POp{11, 0, 0, 0};
    if (g == PST - 20) z.op[z.n++] = POp{15, 0, 0, 0};
    if (g >= PST - 16 && g < PST - 13) for (int r = (g - PST + 16) * 7 / 3; r < (g - PST + 17) * 7 / 3; ++r) z.op[z.n++] = POp{16, r, 0, 0};
    while (cap > 0 && i < n && early[i] <= g && hold <= g) {
      const int k = l[i].k;
      if ((k == 11 || k == 13) && q > 2) { if (k == 13) break; }
      z.op[z.n++] = l[i];
      if (k == 10 || k == 14) { ++z.vm[g + 1]; cap = 0; hold = g + 3; }
      if (i == nm - 1) hold = (g + 3) / 64 * 64 + 64;
      if (i == nm + 3) hold = g / 16 * 16 + 16;
      --cap; ++i;
    }
  }
  z.first[768] = z.n;
  if (i < n) z.n = -1;
  return z;
}
constexpr PSch PS = p_sched();
constexpr int p_last() { int g = 0; while (PS.first[g] < PS.n) ++g; return g; }
static_assert(PS.n > 0 && PS.vm[(PST - 16) / 64 * 64 - 3 - GD13] == 10 && PST % 16 == 0);
template <int I> DI void p_op(const PA& p) {
  constexpr POp o = PS.op[I];
  if constexpr (o.k == 0) asm volatile("v_lshlrev_b32_e32 v%c0, 16, v%c1" ::"i"(o.d), "i"(o.a));
  else if constexpr (o.k == 1) asm volatile("v_and_b32_e32 v%c0, s94, v%c1" ::"i"(o.d), "i"(o.a));
  else if constexpr (o.k == 2) asm volatile("v_mul_f32_e32 v%c0, v%c1, v%c2" ::"i"(o.d), "i"(o.a), "i"(o.b));
  else if constexpr (o.k == 4) asm volatile("v_fmac_f32_e32 v%c0, v%c1, v%c2" ::"i"(o.d), "i"(o.a), "i"(o.b));
  else if constexpr (o.k == 5) asm volatile("v_cvt_pk_bf16_f32 v%c0, v%c1, v%c2" ::"i"(o.d), "i"(o.a), "i"(o.b));
  else if constexpr (o.k == 10 && o.d == 0)
    asm volatile("s_mov_b32 s93, m0\n\ts_add_u32 m0, s79, %1\n\ts_mov_b32 s94, 0xffff0000\n\tbuffer_load_dwordx4 v89, %0, 0 offen lds\n\ts_mov_b32 m0, s93"
                 ::"s"(p.sw), "i"(L_SP + 20480) : SCC_ "memory");
  else if constexpr (o.k == 10 && o.d < 6) {
    if constexpr (o.d == 1) asm volatile("v_and_b32 v113, 0x380, v89\n\tv_mul_u32_u24 v114, 21, v113\n\tv_sub_u32 v114, %0, v114\n\ts_mul_i32 s95, s79, 5" ::"v"(p.vo));
    asm volatile("v_add_u32 v112, %1, v114\n\ts_mov_b32 s93, m0\n\ts_add_u32 m0, s95, %2\n\ts_mov_b32 exec_lo, %3\n\ts_mov_b32 exec_hi, %4\n\t"
                 "buffer_load_dwordx4 v112, %0, 0 offen lds\n\ts_mov_b64 exec, -1\n\ts_mov_b32 m0, s93"
                 ::"s"(p.sk), "i"((o.d - 2) * 8 * DA * 2), "i"(L_SP + (o.d - 1) * 1024), "i"(o.d == 1 ? 0 : -1), "i"(o.d == 1 ? -256 : -1) : SCC_ "memory");
  } else if constexpr (o.k == 10)
    asm volatile("buffer_load_dwordx4 v[%c0:%c1], %2, %3, 0 offen offset:%c4" ::"i"(28 + 4 * o.d), "i"(31 + 4 * o.d), "v"(p.vo), "s"(p.sv), "i"((o.d & 3) * DA * 2) : "memory");
  else if constexpr (o.k == 15) asm volatile("v_mad_u32_u24 v113, v113, 3, v89\n\tv_add_u32 v113, s95, v113\n\tv_add_u32 v113, %0, v113" ::"i"(L_SP + 640));
  else if constexpr (o.k == 16) asm volatile("ds_read_b128 v[%c0:%c1], v113 offset:%c2" ::"i"(32 + 4 * o.d), "i"(35 + 4 * o.d), "i"(o.d * 128));
  else if constexpr (o.k == 11) asm volatile("ds_read_b128 v[76:79], %0 offset:%c1" ::"v"(p.lw), "i"(o.d * 16));
  else if constexpr (o.k == 12) asm volatile("ds_write_b64 %0, v[126:127] offset:%c1" ::"v"(p.lx), "i"(o.d * 256));
  else if constexpr (o.k == 13)
    asm volatile("v_xor_b32 v112, %3, %2\n\tds_read_b128 v[%c0:%c1], v112 offset:%c4" ::"i"(32 + 4 * o.d), "i"(35 + 4 * o.d), "v"(p.lr), "i"(o.d * 32), "i"(o.d * 4096));
  else
    asm volatile("buffer_store_dwordx4 v[%c0:%c1], %2, " SW ", s93 offen sc1\n\ts_add_u32 s93, s93, 0x10000" ::"i"(32 + 4 * o.d), "i"(35 + 4 * o.d), "v"(p.vs) : SCC_ "memory");
}
template <int B, int... Is> DI void p_ops_(const PA& p, iseq<Is...>) { (p_op<B + Is>(p), ...); }
template <int Gg> DI void p_ops(const PA& p) { p_ops_<PS.first[Gg]>(p, make_iseq<PS.first[Gg + 1] - PS.first[Gg]>{}); }
constexpr float CS = 0.0625f * 1.4426950408889634f;
struct EOp { int k, d, s, g; };
struct ESch { EOp op[640]; int first[260], st[260], n; bool ok; };
constexpr int e_load(int g) { return g >= 64 ? ((g & 7) == 4 ? g >> 3 : -1) : g % 5 != 4 || g > 59 ? -1 : g < 40 ? g / 5 : g / 5 + 8; }
constexpr int e_nld(int g0, int g1) { int n = 0; for (int g = g0; g < g1; ++g) n += e_load(g) >= 0; return n; }
constexpr int e_qrd(int g) { return g >= 32 && g < 48 && (g & 1) ? (g - 32) / 2 : g >= 104 && g < 120 && (g & 1) ? 8 + (g - 104) / 2 : -1; }
constexpr ESch e_sched(int MODE) {
  ESch t{}, z{};
  int cap[260] = {}, used[260] = {}, dead[640] = {};
  const int NG = MODE == 0 ? 256 : 128;
  for (int g = 0; g < NG; ++g) {
    const int q = g & 15;
    if (MODE == 0) cap[g] = g_j(g) >= 0 ? 1 : (q == 1 || q == 2 || q == 3 || q == 5 || q == 7 || q == 9) ? 2 : q == 14 ? 4 : 3;
    else cap[g] = g < 16 ? 1 : (q == 1 || q == 2 || e_load(g) >= 0 || e_qrd(g) >= 0) ? 1 : 2;
  }
  cap[NG] = 1000;
  int n = 0, g = 0;
  auto put = [&](int k, int d, int s_, int early, int dl) {
    if (g < early) g = early < NG ? early : NG;
    while (used[g] >= cap[g]) ++g;
    ++used[g]; if (k == 3) ++used[g];
    t.op[n] = EOp{k, d, s_, g}; dead[n] = dl; ++n;
  };
  auto av = [](int m, int nf) { return 16 * m + 2 * nf + 10; };
  for (int j = 0; j < (MODE == 0 ? 32 : 16); ++j) {
    const int m0 = MODE == 2 ? (j >> 3) * 2 : j >> 2, n0 = MODE == 2 ? j & 7 : (j & 3) * 2;
    const int m1 = MODE == 2 ? m0 + 1 : m0, n1 = MODE == 2 ? n0 : n0 + 1;
    const int T = j < 16 ? 128 + 8 * j : 112 + 8 * (j & 1), dl0 = 128 + 32 * (j >> 3) + 2 * (j & 7) - 1, dl1 = j < 16 ? dl0 : 999;
    const int pace = MODE == 0 ? 0 : (MODE == 1 ? 14 : 26) + j * (MODE == 1 ? EPACE_K : EPACE_V);
    if (j >= 16) for (int k = 0; k < 8; ++k) put(0, T + k, 8 * (j - 16) + k, k < 4 ? av(m0, n0) : av(m1, n1), dl0);
    for (int h = 0; h < 2; ++h) {
      const int e = h ? av(m1, n1) : av(m0, n0);
      if (MODE == 0) for (int k = 0; k < 4; ++k) put(1, T + 4 * h + k, 0, e, dl1);
      for (int k = 0; k < 2; ++k) put(2, T + 2 * h + k, T + 4 * h + 2 * k, e, dl1);
    }
    put(3, T, j, j < 16 || MODE ? pace : 0, dl1);
  }
  z.ok = true;
  for (int q = 0; q <= NG; ++q) {
    z.first[q] = z.n; z.st[q + 1] = z.st[q];
    for (int i = 0; i < n; ++i) if (t.op[i].g == q) {
      z.op[z.n++] = t.op[i];
      if (t.op[i].k == 3) ++z.st[q + 1];
      if (MODE == 0 && q > dead[i]) z.ok = false;
    }
  }
  z.first[NG + 1] = z.n;
  return z;
}
constexpr ESch ES[3] = {e_sched(0), e_sched(1), e_sched(2)};
static_assert(ES[0].ok && ES[0].first[256] == ES[0].n && ES[1].st[126] == 16 && ES[2].st[126] == 16);
template <int MODE, int I> DI void e_op() {
  constexpr EOp o = ES[MODE].op[I];
  if constexpr (o.k == 0) asm volatile("v_accvgpr_read_b32 v%c0, a[%c1]" ::"i"(o.d), "i"(o.s));
  else if constexpr (o.k == 1) asm volatile("v_mul_f32_e32 v%c0, s94, v%c0" ::"i"(o.d));
  else if constexpr (o.k == 2) asm volatile("v_cvt_pk_bf16_f32 v%c0, v%c1, v%c2" ::"i"(o.d), "i"(o.s), "i"(o.s + 1));
  else {
    constexpr int m = o.s >> 2;
    if constexpr ((o.s & 3) == 0)
      asm volatile("s_mov_b32 s93, %0" ::"i"(MODE == 2 ? (m >> 1) * 16384 + (m & 1) * 4096 : MODE == 1 ? m * 8192 : (m < 4 ? 2 * m : 15 - 2 * m) * 8192));
    asm volatile("buffer_store_dwordx4 v[%c0:%c1], v%c2, " SW ", s93 offen offset:%c3 sc1" ::"i"(o.d), "i"(o.d + 3), "i"(MODE ? 84 : 95), "i"((o.s & 3) * 1024) : "memory");
  }
}
template <int MODE, int B, int... Is> DI void e_ops_(iseq<Is...>) { (e_op<MODE, B + Is>(), ...); }
template <int MODE, int Gg> DI void e_ops() { e_ops_<MODE, ES[MODE].first[Gg]>(make_iseq<ES[MODE].first[Gg + 1] - ES[MODE].first[Gg]>{}); }
template <int P, int KS, int I> DI void g_wread(const GM& g) {
  constexpr int nf = I >> 1, hf = I & 1, r = 128 + 64 * P + 8 * nf + 4 * KS + 2 * hf;
  asm volatile("ds_read_b64_tr_b16 a[%c1:%c2], %0 offset:%c3" ::"v"(g.aB[hf][nf]), "i"(r), "i"(r + 1), "i"(P * 32768 + KS * 16384));
}
template <int P, int M, int KS> DI void g_aread(const GM& g) {
  constexpr int r = 96 + 8 * (M & 1) + 4 * KS;
  asm volatile("ds_read_b128 v[%c1:%c2], %0 offset:%c3" ::"v"(g.aA[KS]), "i"(r), "i"(r + 3), "i"(P * 32768 + M * 2048));
}
template <bool W, int V, int J> DI void g_dma() {
  if constexpr (W) {
    if constexpr (J == 0) asm volatile("buffer_load_dwordx4 v%c0, s[68:71], 0 offen" GSC " lds" ::"i"(V) : "memory");
    else asm volatile("buffer_load_dwordx4 v%c0, s[68:71], s%c1 offen" GSC " lds" ::"i"(V), "i"(79 + J) : "memory");
  } else {
    if constexpr (J == 0) asm volatile("buffer_load_dwordx4 v%c0, s[64:67], 0 offen" GSC " lds" ::"i"(V) : "memory");
    else asm volatile("buffer_load_dwordx4 v%c0, s[64:67], s%c1 offen" GSC " lds" ::"i"(V), "i"(71 + J) : "memory");
  }
}
template <int Q> DI void att_item() {
  if constexpr (Q == 0) sld<83, B_ST, 1>();
  if constexpr (Q == 1) asm volatile("s_mov_b32 s85, %0" ::"i"(B_PM));
  if constexpr (Q == 2) asm volatile("s_add_u32 s84, s83, 0x8000\n\ts_mov_b32 s86, %0" ::"i"(B_ACT) SCC1);
  if constexpr (Q >= 3 && Q < 6) asm volatile("v_mov_b64 v[%c0:%c1], 0" ::"i"(108 + 2 * Q), "i"(109 + 2 * Q));
  if constexpr (Q == 6) asm volatile("v_mov_b32 v120, 0x45800000");
}
template <int I> DI void g_pro1();
template <int K, int I> DI void g_part() {
  constexpr bool W = K < 2;
  asm volatile("s_add_u32 m0, s%c0, %1\n\ts_nop 0" ::"i"(96 + K), "i"(W ? I * 4096 : (I & 1) * 4096 + (I >> 1) * 16384) : "scc");
  g_dma<W, 115 + K, W ? I : (I & 1) + (I >> 1) * 4>();
}
template <int Y, int I> DI void g_tag() {
  if constexpr (I == 0) { if constexpr (Y) sld<88, C_QT, 2>(); else { sld<88, A_TAB>(); sld<89, A_TAB + 1>(); } }
  if constexpr (I == 1 && !Y) asm volatile("s_lshr_b64 s[88:89], s[88:89], 12" SCC3);
  if constexpr (I == 3) asm volatile("v_readfirstlane_b32 s60, v120");
  if constexpr (I == 5) asm volatile("v_readfirstlane_b32 s61, v121");
  if constexpr (I == 9) asm volatile("s_mul_i32 s61, s61, 0x4c957f2d\n\ts_mul_i32 s63, s60, 0x5851f42d");
  if constexpr (I == 11) asm volatile("s_add_u32 s61, s61, s63\n\ts_mul_hi_u32 s63, s60, 0x4c957f2d" SCC3);
  if constexpr (I == 13) asm volatile("s_add_u32 s61, s61, s63\n\ts_mul_i32 s60, s60, 0x4c957f2d" SCC3);
  if constexpr (I == 15) asm volatile("s_add_u32 s60, s60, 0xf767814f\n\ts_addc_u32 s61, s61, 0x14057b7e" SCC3);
  if constexpr (I == 17) asm volatile("v_mov_b64 v[82:83], s[60:61]");
}
template <bool TR, int G> DI void g_last_setup(int wid) {
  if constexpr (G == 0) { sld<72, A_SX>(); sld<76, A_MX>(); }
  if constexpr (G >= 1 && G < 4) sld<72 + G, A_SX + G>();
  if constexpr (G == 3) sld<63, A_VOO>();
  if constexpr (G >= 5 && G < 8) sld<72 + G, A_MX + G - 4>();
  if constexpr (G == 6) asm volatile("v_add_u32 v84, s63, v89");
  if constexpr (G == 7) asm volatile("s_lshl_b32 s63, %0, 10\n\ts_lshl_b32 s95, %0, 12" ::"s"(wid) SCC1);
  if constexpr (G == 8) asm volatile("v_add_u32 v85, %0, v84\n\tv_add_u32 v86, 0x10000, v89" ::"i"(TR ? 4096 : 8192));
  if constexpr (G == 9) asm volatile("v_add_u32 v87, s63, v86\n\tv_add_u32 v95, s95, v86");
  if constexpr (G == 10) asm volatile("s_lshl_b32 s80, %0, 13\n\ts_bitset1_b32 s80, 16" ::"s"(wid) SCC1);
  if constexpr (G == 11) asm volatile("s_add_u32 s81, s80, 0x8000\n\ts_mov_b32 s90, 0x8000" SCC3);
  if constexpr (G == 13) { sld<62, A_GF>(); asm volatile("s_mov_b32 s91, 0xc000"); }
  if constexpr (G == 14) { att_item<0>(); att_item<1>(); }
  if constexpr (G == 15) { att_item<2>(); sld<92, A_GH>(); }
  if constexpr (G == 12) { sld<87, B_MS, 1>(); asm volatile("s_add_u32 s82, s73, 2" SCC3); }
}
template <bool TR, int P, int H, int G, int MD, int PT> DI void g_gap(const GM& g, int wid, const PA& pa) {
  constexpr bool FIRST = MD == 1 || MD == 2, LAST = MD == 4;
  constexpr int m = 4 * H + (G >> 4), q = G & 15, n = (SNK & 8) && MD < 2 && (m & 1) ? 7 - (q >> 1) : q >> 1, ks = (SNK & 4 ? q ^ (q >> 1) : q) & 1;
  constexpr int a = 96 + 8 * (m & 1) + 4 * ks, b = 128 + 64 * P + 8 * n + 4 * ks, d0 = TR ? 64 * (m >> 1) + 8 * n + 4 * (m & 1) : 32 * m + 4 * n, d = m < 4 ? 128 + d0 : d0 - 128;
  constexpr bool ST0 = MD == 1 && H == 0;
  constexpr int J = LAST ? -1 : ST0 ? (G >= 48 && G % 4 == 0 ? G / 4 - 8 : -1) : g_j(G);
  constexpr int gj = (J & 3) ^ ((J & 3) >> 1);
  constexpr int EM = LAST ? 1 + TR : 0, eg = (MD == 2 ? 128 : 0) + 64 * H + G, L = LAST ? e_load(eg) : -1;
  if constexpr (MD == 1 && G == 32) asm volatile("s_waitcnt vmcnt(%0) lgkmcnt(%1)\n\ts_barrier" ::"i"(H ? 12 + (PT >= 0 ? PS.vm[eg] : 0) : 19), "i"(8 * H) : "memory");
  else if constexpr (q == 0 && G > 0) asm volatile("s_waitcnt lgkmcnt(%0)" ::"i"(LAST ? 0 : MD == 1 ? 8 * H : 4));
  if constexpr (J == 0) asm volatile("s_add_u32 m0, s79, %0" ::"i"((P ^ H ^ 1) * 32768 + (1 - H) * 8192) SCC1);
  else if constexpr (J == 4) asm volatile("s_add_u32 m0, s87, %0" ::"i"((P ^ H) * 32768 + (1 - H) * 16384) SCC1);
  else if constexpr (J > 0 && (J & 3) == 1) asm volatile("s_bitset1_b32 m0, 12");
  else if constexpr (J > 0 && (J & 3) == 2) asm volatile("s_bitset1_b32 m0, %0" ::"i"(J < 4 ? 14 : 13));
  else if constexpr (J > 0) asm volatile("s_bitset0_b32 m0, 12");
  if constexpr (L >= 0 && (L & 3) == 0) asm volatile("s_mov_b32 m0, s%c0" ::"i"(L < 16 ? 76 + (L >> 2) : 87));
  constexpr int QR = LAST ? e_qrd(eg) : -1;
  if constexpr (QR == 0) asm volatile("s_waitcnt vmcnt(%0)" ::"i"(8 + e_nld(0, eg) + ES[EM].st[eg]) : "memory");
  if constexpr (QR == 8) asm volatile("s_waitcnt vmcnt(%0)" ::"i"(e_nld(64, eg) + ES[EM].st[eg] - ES[EM].st[60]) : "memory");
#define GMF(D, X, Y, C) asm volatile("v_mfma_f32_16x16x32_bf16 " D "[%c0:%c1], " X ", " Y ", " C ::"i"(d), "i"(d + 3), "i"(a), "i"(a + 3), "i"(b), "i"(b + 3))
#define GMF2(D, C) do { if constexpr (TR) GMF(D, "v[%c2:%c3]", "a[%c4:%c5]", C); else GMF(D, "a[%c4:%c5]", "v[%c2:%c3]", C); } while (0)
  if constexpr (FIRST && !(q & 1)) { if constexpr (m < 4) GMF2("v", "0"); else GMF2("a", "0"); }
  else if constexpr (m < 4) GMF2("v", "v[%c0:%c1]");
  else GMF2("a", "a[%c0:%c1]");
#undef GMF2
#undef GMF
  if constexpr (J >= 0 && J < 4) g_dma<false, 84, (1 - H) * 2 + (gj & 1) + (gj >> 1) * 4>();
  else if constexpr (J >= 4) g_dma<true, 85, (1 - H) * 4 + gj>();
  if constexpr (L >= 0)
    asm volatile("buffer_load_dwordx4 v89, s[%c0:%c1], s%c2 offen offset:%c3 sc1 lds" ::"i"(L >= 4 && L < 8 || L >= 16 ? 68 : 56), "i"(L >= 4 && L < 8 || L >= 16 ? 71 : 59),
                 "i"(L < 16 ? 72 + (L >> 2) : 82), "i"((L & 3) * 1024) : "memory");
  if constexpr (QR >= 0)
    asm volatile("ds_read_b128 a[%c1:%c2], v%c0 offset:%c3" ::"i"(QR < 12 ? 87 : 95), "i"(128 + 4 * QR), "i"(131 + 4 * QR), "i"(QR < 12 ? QR * 4096 : 49152 + (QR - 12) * 1024));
  if constexpr ((q == 1 || q == 2) && !(LAST && m == 7)) g_aread<(m < 7 ? P : P ^ 1), (m + 1) & 7, q - 1>(g);
  if constexpr (!LAST) {
    if constexpr (MD == 1 && H == 1 && q >= 3 && q < 11) g_wread<P ^ 1, (G >> 5), ((G >> 4) & 1) * 8 + q - 3>(g);
    if constexpr (MD != 1 && q >= 3 && q < 11 && (q & 1)) g_wread<P ^ 1, H, (G >> 4) * 4 + (q - 3) / 2>(g);
    if constexpr (MD == 1 && eg >= 62 && eg < 80) g_tag<P, eg - 62>();
    if constexpr (H == 1 && G == 0) asm volatile("s_mov_b32 s90, s91");
    if constexpr (H == 1 && G == 4) asm volatile("v_lshl_add_u32 v84, s90, 7, v86\n\ts_bfe_u32 s91, s88, 0x40000" SCC3);
    if constexpr (H == 1 && G == 6) asm volatile("s_lshr_b64 s[88:89], s[88:89], 4\n\ts_lshl_b32 s92, s91, 19" SCC3);
    if constexpr (H == 1 && G == 10) asm volatile("v_add_u32 v85, s92, v87");
  } else if constexpr (eg < 16) g_last_setup<TR, eg>(wid);
  if constexpr (ST0 && G == 23) asm volatile("v_lshl_add_u32 v85, s91, 19, v87");
  if constexpr (ST0 && G < 36 && G % 3 == 0) g_part<G < 12 ? 2 : G < 24 ? 1 : 3, (G / 3) & 3>();
  if constexpr (ST0 && G >= 36 && G < 48 && G % 3 == 0) g_pro1<16 + G / 3>();
  if constexpr (MD == 3 && eg == 0) sld<63, A_VOQ>();
  if constexpr (MD == 3 && eg == 4) asm volatile("v_add_u32 v95, s63, v89\n\ts_mov_b32 s94, %0" ::"i"(__builtin_bit_cast(unsigned, CS)));
  if constexpr (MD >= 2) e_ops<EM, eg>();
  constexpr int pg = 128 * PT + 64 * H + G;
  if constexpr (PT == 2 && eg == 0) sld<93, A_ZO>();
  if constexpr (PT >= 0) p_ops<pg>(pa);
  if constexpr (G == 61 && !LAST) {
    constexpr int ns = MD >= 2 ? ES[0].st[eg + 1] - ES[0].st[eg >= GD13 ? eg - GD13 : 0] : PT >= 0 ? PS.vm[pg + 1] - PS.vm[pg >= GD13 ? pg - GD13 : 0] : 0;
    asm volatile("s_waitcnt vmcnt(%0) lgkmcnt(0)\n\ts_barrier" ::"i"(12 + ns) : "memory");
  } else if constexpr (G == 61) asm volatile("s_waitcnt lgkmcnt(0)\n\ts_barrier" ::: "memory");
  GAP();
}
template <bool TR, int P, int H, int MD, int PT, int... Gs> DI void g_half_(const GM& g, int wid, const PA& pa, iseq<Gs...>) { (g_gap<TR, P, H, Gs, MD, PT>(g, wid, pa), ...); }
template <bool TR, int P, int H, int MD> DI void g_half(const GM& g, int wid = 0) { g_half_<TR, P, H, MD, -1>(g, wid, PA{}, make_iseq<64>{}); }
template <int T, int H> DI void q_half(const GM& g, const PA& pa) { g_half_<false, (T & 1) ^ 1, H, T == 0, T>(g, 0, pa, make_iseq<64>{}); }
typedef __attribute__((ext_vector_type(16))) unsigned u32x16;
struct RSV { u32x16 a, b, c; };
DI void rsv_begin(RSV& r) { asm volatile("rsv_begin:" : "={v[32:47]}"(r.a), "={v[48:63]}"(r.b), "={v[64:79]}"(r.c)); }
DI void rsv_end(const RSV& r) { asm volatile("rsv_end:" ::"{v[32:47]}"(r.a), "{v[48:63]}"(r.b), "{v[64:79]}"(r.c)); }
template <int P, int... Is> DI void g_read0(const GM& g, iseq<Is...>) {
  (g_wread<P, 0, Is>(g), ...); (g_wread<P, 1, Is>(g), ...);
  g_aread<P, 0, 0>(g); g_aread<P, 0, 1>(g);
}
template <int I> DI void g_pro1() {
  constexpr int grp = I >> 2, i = I & 3, t = grp < 4 ? 0 : grp < 7 ? 1 : 2;
  constexpr bool isA = grp == 0 || grp == 3 || grp == 5;
  constexpr int jj = isA ? (grp == 3 ? 2 : 0) + (i & 1) + (i >> 1) * 4 : (grp == 2 || grp == 6 ? 4 : 0) + i;
  asm volatile("s_add_u32 m0, s%c0, %1\n\ts_nop 0" ::"i"(t == 1 ? 92 : 90), "i"((isA ? 0 : 65536) + jj * 4096) : "scc");
  g_dma<!isA, (isA ? 84 : 85) + (t ? 0 : 2), jj>();
}
template <int B, int... Is> DI void g_pro(iseq<Is...>) { (g_pro1<B + Is>(), ...); }
template <bool TR, bool SCL, class HO, class HE> DI void g_loop(const GM& g, HO&& odd, HE&& even) {
  constexpr int nkt = SCL ? 7 : 16, P0 = SCL ? 1 : 0;
#pragma unroll 1
  for (int t = 1;; t += 2) {
    g_half<TR, P0 ^ 1, 0, 0>(g);
    odd(t);
    GAP();
    g_half<TR, P0 ^ 1, 1, 0>(g);
    if (SCL && t + 2 >= nkt) break;
    g_half<TR, P0, 0, 0>(g);
    even(t + 1);
    GAP();
    g_half<TR, P0, 1, 0>(g);
    if (!SCL && t + 3 >= nkt) break;
  }
}

typedef __attribute__((ext_vector_type(4))) unsigned u32x4;
typedef __attribute__((ext_vector_type(4))) float f32x4;

constexpr int R_S = 128, R_P = 192, R_CI = 224, R_E = 96;
constexpr int R_TS = 112, R_RS = 114, R_M = 116;
constexpr int R_PD = 118, R_THR = 120, R_TMP = 121;
struct AC {
  u64 redo;
  unsigned rare;
};
template <int T, int D, int K> DI void d_op() {
  constexpr int t = T + 8 * (D & 1);
  if constexpr (K < 8) asm volatile("v_accvgpr_read_b32 v%c0, a[%c1]" ::"i"(t + K), "i"(8 * D + K));
  else if constexpr (K < 12) asm volatile("v_cvt_pk_bf16_f32 v%c0, v%c1, v%c2" ::"i"(t + K - 8), "i"(t + 2 * (K - 8)), "i"(t + 2 * (K - 8) + 1));
  else
    asm volatile("buffer_store_dwordx4 v[%c0:%c1], v%c2, " SW ", s%c3 offen offset:%c4 sc1" ::"i"(t), "i"(t + 3), "i"(84 + ((D >> 2) & 1)), "i"(90 + (D >> 3)),
                 "i"((D & 3) * 1024) : "memory");
}
template <int T, int O0, int... Os> DI void d_ops(iseq<Os...>) { (d_op<T, (O0 + Os) / 13, (O0 + Os) % 13>(), ...); }
template <int D> DI void d_store() { d_ops<160, 13 * D>(make_iseq<13>{}); }
template <int X, int F> DI void kread() {
  constexpr int r = 192 + 4 * (F & 15);
  asm volatile("ds_read_b128 a[%c0:%c1], v89 offset:%c2" ::"i"(r), "i"(r + 3), "i"(X * 32768 + F * 1024));
}
template <int X, int F> DI void vread() {
  constexpr int r = 192 + 4 * (F & 15);
  asm volatile("ds_read_b128 a[%c0:%c1], v86 offset:%c2" ::"i"(r), "i"(r + 3), "i"(X * 32768 + (F & 1) * 16384 + (F >> 1) * 1024));
}
template <int X, int... Fs> DI void kreads(iseq<Fs...>) { (kread<X, Fs>(), ...); }
template <int X, int... Fs> DI void vreads(iseq<Fs...>) { (vread<X, Fs>(), ...); }
template <int X, int J> DI void dma_m0() {
  if constexpr (J == 0) asm volatile("s_mov_b32 m0, s%c0" ::"i"(80 + X));
  else if constexpr (J == 4) asm volatile("s_or_b32 m0, s%c0, 0x1000" ::"i"(80 + X) SCC1);
  else if constexpr (J == 8) asm volatile("s_bitset0_b32 m0, 16");
  else if constexpr (J == 12) asm volatile("s_bitset0_b32 m0, 12");
}
template <int J> DI void dma_at() {
  asm volatile("buffer_load_dwordx4 v%c0, " SW ", s%c1 offen offset:%c2 sc1 lds" ::"i"(J >= 4 && J < 12 ? 91 : 90), "i"(J < 8 ? 82 : 84), "i"((J & 3) * 1024) : "memory");
}
template <int V, int S> DI void dma4() {
  asm volatile("buffer_load_dwordx4 v%c0, " SW ", s%c1 offen sc1 lds\n\tbuffer_load_dwordx4 v%c0, " SW ", s%c1 offen offset:1024 sc1 lds\n\t"
               "buffer_load_dwordx4 v%c0, " SW ", s%c1 offen offset:2048 sc1 lds\n\tbuffer_load_dwordx4 v%c0, " SW ", s%c1 offen offset:3072 sc1 lds"
               ::"i"(V), "i"(S) : "memory");
}
template <int Q> DI void k01_dma() {
  asm volatile("s_add_u32 m0, s80, %0\n\ts_nop 0" ::"i"((Q >> 1) * 32768 + (Q & 1) * 4096 - 65536) SCC1);
  dma4<90 + (Q & 1), 83 + (Q >> 1)>();
}
template <int R, int I, int ST = 4, int V = 89> DI void f_load() {
  asm volatile("buffer_load_dwordx4 a[%c0:%c1], v%c3, " SW ", s72 offen offset:%c2 sc1" ::"i"(R + ST * I), "i"(R + ST * I + 3), "i"((I & 3) * 1024), "i"(V) : "memory");
}
DI void f_next() { asm volatile("s_add_u32 s72, s72, 0x1000" SCC3); }
constexpr int o_wreg(int o, int f) { return o == 0 ? 128 + 4 * f : o == 3 ? 192 + 4 * f : 8 * f + 4 * (o - 1); }
template <int F, int LASTF = 3> DI void w_load() {
  f_load<o_wreg(F >> 4, 0), F & 15, o_wreg(F >> 4, 1) - o_wreg(F >> 4, 0), (F & 1) ? 84 : 89>();
  if constexpr ((F & 3) >= LASTF) asm volatile("s_add_u32 s72, s72, %0" ::"i"(F < 4 ? 0x1000 : F < 8 ? 0xf000 : F & 4 ? 0x7000 : 0x1000) SCC1);
}
#define NWF 40
constexpr int CV_KP = 5120 - 32, CV_KS = 10304 - 32;
struct CV { unsigned pa[4], pb, lw, vzo; };
constexpr int cv_J(int q) { int J = 7; while (q > J) { q -= J + 1; --J; } return J; }
constexpr int cv_i(int q) { int J = 7; while (q > J) { q -= J + 1; --J; } return q; }
template <int r_, int N> DI void cv_rd(const CV& c) {
  constexpr int R = (r_ + 1) & 7;
  if constexpr (N < 16) {
    constexpr int i = N >> 1, o = 16 * R + 128 * (i & 1) + 2 * (N & 1), r = 192 + 32 * (r_ & 1) + 2 * N;
    asm volatile("ds_read2_b32 a[%c1:%c2], %0 offset0:%c3 offset1:%c4" ::"v"(c.pa[i >> 1]), "i"(r), "i"(r + 1), "i"(o), "i"(o + 1));
  } else {
    constexpr int m = N - 16, r = 160 + 32 * (r_ & 1) + 4 * m;
    asm volatile("ds_read_b128 v[%c1:%c2], %0 offset:%c3" ::"v"(c.pb), "i"(r), "i"(r + 3), "i"(64 * R + 512 * (7 - m)));
  }
}
template <int O> DI void cv_z(const CV& c) {
  constexpr int pr = O / 7, o = O % 7, w = 128 + 4 * (7 - 2 * pr), y = w + 2 * (o & 1) - 4 * (o >> 1);
  if constexpr (o == 4) asm volatile("s_add_u32 s63, s65, %2\n\tv_permlane16_swap_b32_e32 v%c0, v%c1" ::"i"(w), "i"(w + 2), "i"((6 - 2 * pr) * 8 * 3584) SCC1);
  else if constexpr (o == 5) asm volatile("v_permlane16_swap_b32_e32 v%c0, v%c1" ::"i"(w + 1), "i"(w + 3));
  else if constexpr (o == 6) asm volatile("buffer_store_dwordx4 v[%c0:%c1], %2, " SW ", s63 offen sc1" ::"i"(w), "i"(w + 3), "v"(c.vzo) : "memory");
  else asm volatile("v_cvt_pk_bf16_f32 v%c0, v%c1, v%c2" ::"i"(w + o), "i"(y), "i"(y + 1));
}
#define CVS 6
#define CVF 150
#define CVP 206
#define CVK 250
constexpr int CVQ = 16 * CVS + 2;
constexpr int cv_vm(int T) {
  return T < 16 * CVS && T % CVS == CVS - 1 ? 1 : T >= CVQ && T < CVQ + 96 && (T - CVQ) % 6 == 0 ? 2 : T == CVF ? 4 : T == CVP ? 5 : T > CVK && T <= CVK + 48 && (T - CVK) % 3 == 1 ? 7 : 0;
}
constexpr int cv_cnt(int T0, int T1, int kind = 0) { int n = 0; for (int T = T0; T < T1; ++T) n += kind ? cv_vm(T) == kind : cv_vm(T) != 0; return n; }
static_assert(16 * CVS + 4 <= 108 && cv_vm(CVF) == 4 && cv_vm(CVP) == 5 && CVQ + 96 < CVK);
constexpr int cv_z0(int T) { return T < 271 ? 0 : T < 278 ? T - 271 : T < 281 ? 7 : T - 274; }
template <int I> DI void k01_dma1() {
  if constexpr ((I & 3) == 0) asm volatile("s_add_u32 m0, s80, %0\n\ts_nop 0" ::"i"((I >> 3) * 32768 + ((I >> 2) & 1) * 4096 - 65536) SCC1);
  asm volatile("buffer_load_dwordx4 v%c0, " SW ", s%c1 offen offset:%c2 sc1 lds" ::"i"(90 + ((I >> 2) & 1)), "i"(83 + (I >> 3)), "i"((I & 3) * 1024) : "memory");
}
template <int T> DI void cv_slot(const CV& c) {
  constexpr int r = T / 36, Q = T % 36, i = cv_i(Q), J = cv_J(Q), d = 128 + 4 * J, a = 192 + 32 * (r & 1) + 4 * i, b = 160 + 32 * (r & 1) + 4 * (J - i), vm = cv_vm(T);
  if constexpr (Q == 0) asm volatile("s_waitcnt lgkmcnt(0)");
  if constexpr (r == 0 && i == 0)
    asm volatile("v_mfma_f32_16x16x32_bf16 v[%c0:%c1], a[%c2:%c3], v[%c4:%c5], 0" ::"i"(d), "i"(d + 3), "i"(a), "i"(a + 3), "i"(b), "i"(b + 3));
  else
    asm volatile("v_mfma_f32_16x16x32_bf16 v[%c0:%c1], a[%c2:%c3], v[%c4:%c5], v[%c0:%c1]" ::"i"(d), "i"(d + 3), "i"(a), "i"(a + 3), "i"(b), "i"(b + 3));
  if constexpr (r == 7 && i == 0) asm volatile("v_mfma_f32_16x16x32_bf16 v[%c0:%c1], v[96:99], v[%c2:%c3], v[%c0:%c1]" ::"i"(d), "i"(d + 3), "i"(b), "i"(b + 3));
  if constexpr (Q < 24 && r < 7) cv_rd<r + 1, Q>(c);
  if constexpr (T == 32) asm volatile("ds_read_u16 v121, %0 offset:5120" ::"v"(c.lw));
  if constexpr (T == 33) asm volatile("ds_write_b128 %0, v[114:117] offset:%c1\n\tds_write_b128 %0, v[114:117] offset:%c2" ::"v"(c.lw), "i"(CV_KS), "i"(CV_KS + 16));
  if constexpr (T == 62) asm volatile("ds_write_b16 %0, v121 offset:%c1" ::"v"(c.lw), "i"(CV_KS + 30));
  if constexpr (T == 34) sld<72, A_OQ>();
  if constexpr (T == 35) sld<65, A_ZO>();
  if constexpr (T == CVQ - 2) { asm volatile("s_waitcnt vmcnt(16)" ::: "memory"); fl_check(); }
  if constexpr (T < 16 * CVS) d_ops<224, 13 * (T / CVS) + (T % CVS * 13) / CVS>(make_iseq<((T % CVS + 1) * 13) / CVS - (T % CVS * 13) / CVS>{});
  if constexpr (vm == 2) { f_load<128, (T - CVQ) / 6>(); if constexpr ((T - CVQ) / 6 % 4 == 3) f_next(); }
  if constexpr (vm == 4) {
    asm volatile("s_waitcnt vmcnt(%0)\n\ts_barrier" ::"i"(cv_cnt(16 * CVS, T)) : "memory");
    set_flag<A_FG>();
  }
  if constexpr (T == CVP - 3) fl_mask<A_N01, -1>();
  if constexpr (vm == 5) fl_issue();
  if constexpr (T == CVK) { asm volatile("s_waitcnt vmcnt(%0)" ::"i"(cv_cnt(CVP + 1, T)) : "memory"); fl_check(); }
  if constexpr (vm == 7) k01_dma1<cv_cnt(CVK, T, 7)>();
  if constexpr (cv_z0(T + 1) > cv_z0(T)) cv_z<cv_z0(T)>(c);
  GAP();
}
template <int... T> DI void cv_slots(const CV& c, iseq<T...>) { (cv_slot<T>(c), ...); }
template <int... N> DI void cv_rd0(const CV& c, iseq<N...>) { (cv_rd<0, N>(c), ...); }
template <int O> DI void cv_tail(const CV& c) {
  constexpr int k = cv_cnt(CVK, 288, 7) + O / 6;
  if constexpr (O % 6 == 0 && k < 16) k01_dma1<k>();
  cv_z<cv_z0(288) + O>(c);
}
template <int... O> DI void cv_tails(const CV& c, iseq<O...>) { (cv_tail<O>(c), ...); }
static_assert(cv_cnt(CVK, 288, 7) >= 8 && cv_cnt(CVK, 288, 7) + 3 >= 16);
constexpr int cv_end() {
  int T = CVK; while (cv_cnt(CVK, T + 1, 7) < 8) ++T;
  int n = 8; for (int o = cv_z0(T); o < 28; ++o) n += o % 7 == 6;
  return n;
}

template <int Q> DI void acc_zero() {
  asm volatile("v_mfma_f32_32x32x16_bf16 a[%c0:%c1], v[208:211], v[208:211], 0" ::"i"(16 * Q), "i"(16 * Q + 15));
}
struct SMS { int ex[128], op0[128], nop[128], last; };
constexpr SMS sm_sched() {
  SMS z{};
  int et[32] = {}, e = 0;
  for (int t = 0; t < 128; ++t) {
    const int g = t & 15;
    const bool x = t < 48 ? (g >= 8 && (g & 1)) : (g >= 8 && g != 10 && g != 13 && g != 15);
    z.ex[t] = x && e < 32 ? e : -1;
    if (z.ex[t] >= 0) et[e++] = t;
  }
  int o = 0;
  for (int t = 0; t < 128; ++t) {
    const int g = t & 15;
    int cap = g < 8 ? (g == 0 ? 0 : 1) : (z.ex[t] >= 0 || t < 48 ? 0 : 2);
    z.op0[t] = o; z.nop[t] = 0;
    while (cap > 0 && o < 48 && et[2 * (o / 3) + (o % 3 == 0 ? 0 : 1)] + 2 <= t) {
      const int el = 2 * (o / 3) + o % 3;
      if (!(o % 3 < 2 && (el == 0 || el == 4))) --cap;
      ++o; ++z.nop[t]; z.last = t;
    }
  }
  return z;
}
constexpr SMS SM = sm_sched();
static_assert(SM.last <= 115);
constexpr int p_reg(int Y, int k) { return R_P + 16 * Y + 4 * ((k >> 3) * 2 + ((k >> 1) & 1)) + ((k >> 2) & 1) * 2 + (k & 1); }
template <int Y, int O> DI void sm_op() {
  constexpr int k = O / 3, e = 2 * k + O % 3, ts = R_TS + ((e >> 2) & 1);
  if constexpr (O % 3 == 2) asm volatile("v_cvt_pk_bf16_f32 v%c0, v%c1, v%c2" ::"i"(p_reg(Y, k)), "i"(R_E + ((2 * k) & 15)), "i"(R_E + ((2 * k + 1) & 15)));
  else if constexpr (e == 1 || e == 5) asm volatile("v_add_f32_e32 v%c0, v%c1, v%c2" ::"i"(ts), "i"(R_E + e - 1), "i"(R_E + e));
  else if constexpr (e != 0 && e != 4) asm volatile("v_add_f32_e32 v%c0, v%c0, v%c1" ::"i"(ts), "i"(R_E + (e & 15)));
}
template <int Y, int T> DI void sm_gap(u64& redo) {
  if constexpr (SM.ex[T] >= 0) asm volatile("v_exp_f32 v%c0, v%c1" ::"i"(R_E + (SM.ex[T] & 15)), "i"(R_S + 32 * Y + SM.ex[T]));
  if constexpr (SM.nop[T] > 0) sm_op<Y, SM.op0[T]>();
  if constexpr (SM.nop[T] > 1) sm_op<Y, SM.op0[T] + 1>();
  if constexpr (SM.nop[T] > 2) sm_op<Y, SM.op0[T] + 2>();
  if constexpr (T == SM.last + 1) asm volatile("v_add_f32_e32 v%c0, v%c1, v%c2" ::"i"(R_TMP), "i"(R_TS), "i"(R_TS + 1));
  if constexpr (T == SM.last + 2) asm volatile("v_cmp_nle_f32_e64 %0, v%c1, v%c2" : "=s"(redo) : "i"(R_TMP), "i"(R_THR));
}
#define EVG 35
template <int X, int KB, int QB, int S_, bool FIRST, bool HEAD = S_ == 0> DI void qk_mfma() {
  constexpr int r = 192 + 4 * ((KB & 1) * 8 + S_), q = 128 + 4 * (QB * 8 + S_);
  constexpr int d = R_S + 32 * X + 4 * (KB * 2 + QB), ci = HEAD ? R_CI + 4 * (KB * 2 + QB) : d;
  if constexpr (FIRST && HEAD)
    asm volatile("v_mfma_f32_16x16x32_bf16 v[%c0:%c1], a[%c2:%c3], a[%c4:%c5], 0" ::"i"(d), "i"(d + 3), "i"(r), "i"(r + 3), "i"(q), "i"(q + 3));
  else
    asm volatile("v_mfma_f32_16x16x32_bf16 v[%c0:%c1], a[%c2:%c3], a[%c4:%c5], v[%c6:%c7]" ::"i"(d), "i"(d + 3), "i"(r), "i"(r + 3), "i"(q), "i"(q + 3),
                 "i"(ci), "i"(ci + 3));
}
template <int X, int FIRST, bool NOQK, int G> DI void gapA(AC& c) {
  constexpr int Xp = X ^ 1, kb = G >> 4, g = G & 15;
  if constexpr (NOQK && G < 48 && G % 3 == 0) { f_load<128, G / 3>(); if constexpr (G % 12 == 9) f_next(); }
  if constexpr (G == 48) asm volatile("s_waitcnt vmcnt(%0) lgkmcnt(0)\n\ts_barrier" ::"i"(NOQK ? 16 : FIRST == 2 ? 63 : FIRST == 1 ? 9 : FIRST == 3 ? 8 : 0) : "memory");
  else if constexpr (g == 0 && !NOQK) asm volatile("s_waitcnt lgkmcnt(0)");
  if constexpr (!NOQK) {
    if constexpr (SNK & 1) qk_mfma<X, kb, (g >> 3) ^ (kb & 1), g < 8 ? g : 15 - g, FIRST != 0, !(g & 7)>();
    else qk_mfma<X, kb, (g >> 3), g & 7, FIRST != 0>();
    if constexpr (g < 8 && kb < 3) kread<X, (kb + 1) * 8 + g>();
    if constexpr (G == 54) asm volatile("s_add_u32 s82, s83, 0x1000000" SCC3);
    if constexpr (G == 55) dma_m0<X, 0>();
    if constexpr (G >= 56 && !(G & 1) && FIRST != 2) dma_at<(G - 56) / 2>();
    if constexpr (G == EVG) asm volatile("v_readlane_b32 s87, v93, s86");
    if constexpr (G == EVG + 2) asm volatile("s_add_u32 s86, s86, 1" SCC3);
    if constexpr (G == 57) asm volatile("s_mov_b32 s83, s84\n\ts_andn2_b32 s84, s87, 0x3ff" SCC3);
    if constexpr (G == 59 && FIRST) asm volatile("s_and_b32 %0, s87, 0x3ff" : "=s"(c.rare) SCC2);
    if constexpr (G == 59 && !FIRST) asm volatile("s_and_b32 s63, s87, 0x3ff\n\ts_cmp_lg_u64 %0, 0" ::"s"(c.redo) SCC1);
    if constexpr (G == 61 && !FIRST) asm volatile("s_addc_u32 %0, s63, 0" : "=s"(c.rare) SCC2);
  } else if constexpr (SM.nop[64 + G] > 0) asm volatile("s_nop 0");
  if constexpr (!FIRST && g < 8 && kb == 3) vread<Xp, g>();
  if constexpr (FIRST == 2) d_ops<160, 13 * (G / 8) + (G % 8 * 13) / 8>(make_iseq<((G % 8 + 1) * 13) / 8 - (G % 8 * 13) / 8>{});
  if constexpr (FIRST == 2 && G == 34)
    asm volatile("v_and_b32 v124, 0xf0, v89\n\ts_waitcnt vmcnt(8)\n\t" A_CHECK("v[122:123]", "s[96:97]", "s72")
                 "buffer_load_dwordx2 v[122:123], v124, " SW ", s73 offen offset:1024 sc1" ::: "vcc", "scc", "memory");
  if constexpr (FIRST == 1 && G >= 8 && G < 40 && G % 4 == 1)
    asm volatile("s_mov_b32 m0, s75\n\ts_add_u32 s75, s75, 0x1000\n\tbuffer_load_dwordx4 v84, s[64:67], s94 offen lds\n\ts_add_u32 s94, s94, 0x3800" ::: SCC_ "memory");
  if constexpr (FIRST == 1 && G == 42) asm volatile("s_mov_b32 m0, 0x1f800\n\ts_mov_b64 s[64:65], s[88:89]\n\ts_mov_b32 s66, 0x1c0000\n\tbuffer_load_dword v121, s[64:67], 0 offen lds" ::: "memory");
  if constexpr (FIRST == 3) {
    constexpr int n = (G >> 4) * 4 + (G & 3), f = G < 32 ? (G - 17) / 4 : 4 + (G - 33) / 3;
    if constexpr (G == 2) { asm volatile("v_xor_b32 v84, 32, v85"); sld<94, B_WD, 1>(); }
    if constexpr (G < 32 && (G & 12) == 8)
      asm volatile("ds_read_b64_tr_b16 v[%c0:%c1], v%c4 offset:%c5\n\tds_read_b64_tr_b16 v[%c2:%c3], v%c4 offset:%c6" ::"i"(224 + 4 * n), "i"(225 + 4 * n), "i"(226 + 4 * n),
                   "i"(227 + 4 * n), "i"(n & 1 ? 84 : 85), "i"((n >> 1) * 8192), "i"((n >> 1) * 8192 + 4096));
    if constexpr (G == 32) asm volatile("s_add_u32 s94, s94, 0x1000" SCC3);
    if constexpr (G >= 17 && G < 45 && (G < 32 ? G % 4 == 1 : G % 3 == 0 && G > 32))
      asm volatile("buffer_store_dwordx4 v[%c0:%c1], v89, " SW ", s94 offen offset:%c2 sc1" ::"i"(224 + 4 * f), "i"(227 + 4 * f), "i"((f & 3) * 1024) : "memory");
  }
  if constexpr (FIRST == 3 && G == 42) sld<63, B_FSET, 1>();
  if constexpr (FIRST == 3 && G == 48) asm volatile(A_FLAG ::: "memory");
  if constexpr (FIRST && G == 44) asm volatile("v_mov_b64 v[208:209], 0\n\tv_mov_b64 v[210:211], 0");
  if constexpr (!FIRST) sm_gap<Xp, 64 + G>(c.redo);
  GAP();
}
template <int R> DI void rescale1(float alpha) {
  float t;
  asm volatile("v_accvgpr_read_b32 %0, a[%c2]\n\tv_mul_f32 %0, %0, %1\n\tv_accvgpr_write_b32 a[%c2], %0" : "=&v"(t) : "v"(alpha), "i"(R));
}
template <int Q0, int R> DI void rescale_q(const float (&alpha)[2]) { if constexpr (((R >> 2) & 1) >= Q0) rescale1<R>(alpha[(R >> 2) & 1]); }
template <int Q0, int... Rs> DI void rescale_all(const float (&alpha)[2], iseq<Rs...>) { (rescale_q<Q0, Rs>(alpha), ...); }
template <bool MAX> DI float row_reduce(float v) {
  auto p = __builtin_amdgcn_permlane32_swap(__float_as_uint(v), __float_as_uint(v), false, false);
  v = MAX ? fmaxf(__uint_as_float(p[0]), __uint_as_float(p[1])) : __uint_as_float(p[0]) + __uint_as_float(p[1]);
  p = __builtin_amdgcn_permlane16_swap(__float_as_uint(v), __float_as_uint(v), false, false);
  return MAX ? fmaxf(__uint_as_float(p[0]), __uint_as_float(p[1])) : __uint_as_float(p[0]) + __uint_as_float(p[1]);
}
template <int Y, int QB> DI float row_max() {
  constexpr int r = R_S + 32 * Y + 4 * QB;
  float mx;
  asm volatile("v_max_f32 %0, v%c1, v%c2\n\tv_max3_f32 %0, %0, v%c3, v%c4" : "=&v"(mx) : "i"(r), "i"(r + 1), "i"(r + 2), "i"(r + 3));
  static_for([&](auto K) {
    constexpr int q = r + 8 * (K + 1);
    float t = mx;
    asm volatile("v_max3_f32 %0, %0, v%c1, v%c2\n\tv_max3_f32 %0, %0, v%c3, v%c4" : "+v"(t) : "i"(q), "i"(q + 1), "i"(q + 2), "i"(q + 3));
    mx = t;
  }, make_iseq<3>{});
  return row_reduce<true>(mx);
}
template <int R> DI void v_sub(float d) { asm volatile("v_sub_f32 v%c0, v%c0, %1" ::"i"(R), "v"(d)); }
template <int B> DI void s_shift(const float (&d)[2]) { static_for([&](auto E) { v_sub<B + E>(d[(E >> 2) & 1]); }, make_iseq<32>{}); }
DI void m_shift(const float (&d)[2]) {
  s_shift<R_CI>(d);
  v_sub<R_M>(-d[0]); v_sub<R_M + 1>(-d[1]);
}
#define A_MASK(R0, R1, R2, R3, K0, K1, K2, K3) \
    "v_cmp_gt_i32_e64 s[64:65], " K0 ", v95\n\tv_cmp_gt_i32_e64 s[66:67], " K1 ", v95\n\tv_cmp_gt_i32_e64 s[68:69], " K2 ", v95\n\tv_cmp_gt_i32_e64 s[70:71], " K3 ", v95\n\t" \
    "v_cndmask_b32_e64 " R0 ", -v116, v127, s[64:65]\n\tv_cndmask_b32_e64 " R1 ", -v116, v127, s[66:67]\n\t" \
    "v_cndmask_b32_e64 " R2 ", -v116, v127, s[68:69]\n\tv_cndmask_b32_e64 " R3 ", -v116, v127, s[70:71]\n\t"
template <int K, int T> DI void mask_cmp() {
  asm volatile("v_cmp_gt_i32_e64 s[64:65], %c0, v%c4\n\tv_cmp_gt_i32_e64 s[66:67], %c1, v%c4\n\tv_cmp_gt_i32_e64 s[68:69], %c2, v%c4\n\tv_cmp_gt_i32_e64 s[70:71], %c3, v%c4"
               ::"i"(K), "i"(K + 1), "i"(K + 2), "i"(K + 3), "i"(T));
}
template <int R, int S> DI void mask_sel() {
  if constexpr (S < 0)
    asm volatile("v_cndmask_b32_e64 v%c0, v%c0, v127, s[64:65]\n\tv_cndmask_b32_e64 v%c1, v%c1, v127, s[66:67]\n\t"
                 "v_cndmask_b32_e64 v%c2, v%c2, v127, s[68:69]\n\tv_cndmask_b32_e64 v%c3, v%c3, v127, s[70:71]" ::"i"(R), "i"(R + 1), "i"(R + 2), "i"(R + 3));
  else
    asm volatile("v_cndmask_b32_e64 v%c0, -v%c4, v127, s[64:65]\n\tv_cndmask_b32_e64 v%c1, -v%c4, v127, s[66:67]\n\t"
                 "v_cndmask_b32_e64 v%c2, -v%c4, v127, s[68:69]\n\tv_cndmask_b32_e64 v%c3, -v%c4, v127, s[70:71]" ::"i"(R), "i"(R + 1), "i"(R + 2), "i"(R + 3), "i"(S));
}
template <int X, int Q0 = 0> DI void sm_redo() {
  constexpr int Xp = X ^ 1;
  float d[2] = {0.f, 0.f}, dt[2] = {0.f, 0.f}, alpha[2] = {1.f, 1.f}, ts[2] = {0.f, 0.f};
  asm volatile("s_nop 7");
  static_for([&](auto Q) {
    constexpr int qb = Q + Q0;
    const float pd = vget<R_PD + qb>();
    d[qb] = fmaxf(row_max<Xp, qb>() - pd, 0.f);
    dt[qb] = d[qb] + pd;
    vset<R_PD + qb>(d[qb]);
    alpha[qb] = __builtin_amdgcn_exp2f(-d[qb]);
    vset<R_RS + qb>(vget<R_RS + qb>() * alpha[qb]);
  }, make_iseq<2 - Q0>{});
  vset<R_THR>(d[0] > 0.f || d[1] > 0.f ? -1.f : 4096.f);
  m_shift(d);
  static_for([&](auto K) {
    constexpr int k = K, qb = (k >> 1) & 1;
    if constexpr (qb >= Q0) {
      const float e0 = __builtin_amdgcn_exp2f(vget<R_S + 32 * Xp + 2 * k>() - dt[qb]), e1 = __builtin_amdgcn_exp2f(vget<R_S + 32 * Xp + 2 * k + 1>() - dt[qb]);
      ts[qb] += e0; ts[qb] += e1;
      vset<p_reg(Xp, k)>(__uint_as_float(pk2(e0, e1)));
    }
  }, make_iseq<16>{});
  vset<R_TS>(ts[0]); vset<R_TS + 1>(ts[1]);
  rescale_all<Q0>(alpha, make_iseq<128>{});
  asm volatile("s_nop 1" ::: "memory");
  GAP();
}
template <int X, int FIRST, bool NOQK, int... Gs> DI void phaseA_(AC& c, iseq<Gs...>) {
  (gapA<X, FIRST, NOQK, Gs>(c), ...);
}
template <int X, int FIRST, bool NOQK> DI void phaseA(AC& c) { phaseA_<X, FIRST, NOQK>(c, make_iseq<64>{}); }

template <int Y, int F, int QB> DI void pv_mfma() {
  constexpr int r = 192 + 4 * (F & 15), o = 4 * ((F >> 1) * 2 + QB), p = R_P + 16 * Y + 4 * ((F & 1) * 2 + QB);
  asm volatile("v_mfma_f32_16x16x32_bf16 a[%c0:%c1], a[%c2:%c3], v[%c4:%c5], a[%c0:%c1]" ::"i"(o), "i"(o + 3), "i"(r), "i"(r + 3), "i"(p), "i"(p + 3));
}
template <int X, bool SMX, int G> DI void gapB(AC& c) {
  constexpr int Xp = X ^ 1, blk = G >> 4, g = G & 15;
  if constexpr (g == 0) asm volatile("s_waitcnt lgkmcnt(0)");
  if constexpr (SMX && g == 7 && blk < 3) dma_m0<X, blk * 4 + 4>();
  if constexpr (SNK & 2) pv_mfma<Xp, (G >> 2) * 2 + ((g ^ (g >> 1)) & 1), ((g >> 1) ^ (g >> 2)) & 1>();
  else pv_mfma<Xp, (G >> 2) * 2 + (g & 1), (g >> 1) & 1>();
  if constexpr (SMX && (G == 1 || G == 2)) asm volatile("v_add_f32_e32 v%c0, v%c0, v%c1" ::"i"(R_RS + G - 1), "i"(R_TS + G - 1));
  if constexpr (g < 8) {
    if constexpr (blk < 3) vread<Xp, (blk + 1) * 8 + g>();
    else if constexpr (SMX) kread<Xp, g>();
  }
  if constexpr (SMX) {
    if constexpr (g >= 8 && !(g & 1) && blk < 3) dma_at<blk * 4 + 4 + (g - 8) / 2>();
    sm_gap<X, G>(c.redo);
    GAP();
  }
}
template <int X, int... Gs> DI void phaseB_(AC& c, iseq<Gs...>) { (gapB<X, true, Gs>(c), ...); }
template <int X> DI void phaseB(AC& c) { phaseB_<X>(c, make_iseq<64>{}); }
constexpr int sm_nex() { int n = 0; for (int t = 0; t < 64; ++t) n += SM.ex[t] >= 0; return n; }
template <bool ST> DI void phaseB0(bool diag0, unsigned vml) {
  float d[2];
  dma_m0<0, 4>();
  if constexpr (ST) {
    asm volatile("s_waitcnt vmcnt(4)" ::: "memory");
    dma_at<4>(); d_store<8>();
    d[0] = vget<122>(); d[1] = vget<123>();
    dma_at<5>();
  } else {
    kreads<1>(make_iseq<8>{});
    dma_at<4>();
    asm volatile("s_nop 3");
    if (__builtin_expect(diag0, 0))
      static_for([&](auto I) { constexpr int i = I; mask_cmp<16 * i, 95>(); mask_sel<R_S + 8 * i, -1>(); }, make_iseq<4>{});
    d[0] = row_max<0, 0>(); GAP(); dma_at<5>();
    d[1] = row_max<0, 1>(); GAP(); dma_at<6>();
#pragma unroll
    for (int qb = 0; qb < 2; ++qb) if (!(d[qb] > -3.0e38f)) d[qb] = 0.f;
    asm volatile("buffer_store_dwordx2 %0, %1, " SW ", s73 offen offset:1024 sc1" ::"v"((f32x2c){d[0], d[1]}), "v"(vml) : "memory");
  }
  vset<R_M>(d[0]); vset<R_M + 1>(d[1]);
  const f32x2c nd[2] = {{-d[0], -d[0]}, {-d[1], -d[1]}};
  static_for([&](auto E) {
    constexpr int e = E;
    const f32x2c v = nd[(e >> 1) & 1];
    asm volatile("v_pk_add_f32 v[%c0:%c1], v[%c0:%c1], %4\n\tv_mov_b64 v[%c2:%c3], %4" ::"i"(R_S + 2 * e), "i"(R_S + 2 * e + 1), "i"(R_CI + 2 * e), "i"(R_CI + 2 * e + 1), "v"(v));
    if constexpr (ST && e == 0) d_store<9>();
    if constexpr (ST && e == 2) dma_at<6>();
    if constexpr (e == (ST ? 5 : 3)) dma_at<7>();
    if constexpr (e == 6) dma_m0<0, 8>();
    if constexpr (e == 7 || e == 11 || e == 15) dma_at<8 + (e - 7) / 4>();
    if constexpr (ST && (e == 4 || e == 9)) d_store<10 + e / 8>();
  }, make_iseq<16>{});
  static_assert(sm_nex() >= 16 && SM.op0[64] >= 3 * ((sm_nex() - 15) / 2) && SM.op0[64] <= 24);
  static_for([&](auto E) {
    constexpr int e = E;
    asm volatile("v_exp_f32 v%c0, v%c1" ::"i"(R_E + e), "i"(R_S + e));
    if constexpr (e % 3 == 0 && e < 15) acc_zero<e / 3>();
    if constexpr (e == 3) dma_at<11>();
    if constexpr (e == 6) dma_m0<0, 12>();
    if constexpr (e == 7 || e == 11 || e == 15) dma_at<12 + (e - 7) / 4>();
    if constexpr (ST && (e == 1 || e == 9)) d_store<12 + e / 8>();
  }, make_iseq<16>{});
  if constexpr (ST) {
    d_store<14>();
    asm volatile("s_barrier" ::: "memory");
    kreads<1>(make_iseq<8>{});
  }
  static_for([&](auto O) { sm_op<0, O>(); if constexpr (O % 7 == 1 && O < (ST ? 14 : 21)) acc_zero<5 + O / 7>(); }, make_iseq<SM.op0[64]>{});
  dma_at<15>();
  if constexpr (ST) { d_store<15>(); acc_zero<7>(); }
  static_for([&](auto E) { asm volatile("v_exp_f32 v%c0, v%c1" ::"i"(R_E + (int)E), "i"(R_S + 16 + (int)E)); }, make_iseq<sm_nex() - 16>{});
  GAP();
}
struct EP { float fa[2], fb[2]; };
template <int I, int E, int MERGE, int TB = 96> DI void epi_pair(const EP& p) {
  constexpr int qb = I & 1, r = 16 * (I >> 1) + 4 * qb + (E >> 1) * 8 + (E & 1) * 2, t = TB + 2 * E, w = 128 + 4 * I + E;
  if constexpr (MERGE == 2) asm volatile("v_accvgpr_read_b32 v%c0, a[%c2]\n\tv_accvgpr_read_b32 v%c1, a[%c3]" ::"i"(t), "i"(t + 1), "i"(r), "i"(r + 1));
  else
    asm volatile("v_accvgpr_read_b32 v%c0, a[%c2]\n\tv_accvgpr_read_b32 v%c1, a[%c3]\n\tv_mul_f32 v%c0, v%c0, %4\n\tv_mul_f32 v%c1, v%c1, %4"
                 ::"i"(t), "i"(t + 1), "i"(r), "i"(r + 1), "v"(p.fa[qb]));
  if constexpr (MERGE == 1)
    asm volatile("v_lshlrev_b32 v104, 16, v%c2\n\tv_and_b32 v105, 0xffff0000, v%c2\n\tv_fmac_f32 v%c0, v104, %3\n\tv_fmac_f32 v%c1, v105, %3"
                 ::"i"(t), "i"(t + 1), "i"(w), "v"(p.fb[qb]));
  asm volatile("v_cvt_pk_bf16_f32 v%c0, v%c1, v%c2" ::"i"(w), "i"(t), "i"(t + 1));
}
template <int I> DI void ctx_store() {
  asm volatile("buffer_store_dwordx4 v[%c0:%c1], v85, " SW ", s76 offen offset:%c2 sc1\n\tds_write_b128 v85, v[%c0:%c1] offset:%c3"
               ::"i"(128 + 4 * I), "i"(131 + 4 * I), "i"((I & 2) * 1024), "i"((I & 1) * 32768 + (I >> 1) * 2048) : "memory");
  if constexpr (I & 2) asm volatile("s_add_u32 s76, s76, %0" ::"i"(I == 14 ? 0x5000 : 0x1000) SCC1);
}
constexpr int mg_reg(int L, int I) { return I >= 8 ? 8 + 4 * I : I >= 4 ? R_CI + 8 * (I - 4) : R_S + 32 * L + 8 * I; }
#define STF(k)
template <int L, bool MG, int G> DI void gapAD(AC& c) {
  constexpr int kb = G >> 3, s = G & 7, db = ((G >> 1) + 8) & 15, o = 4 * (db * 2 + (G & 1)), a = 72 + 2 * (db & 1), b = mg_reg(L, (db >> 1) * 2 + (G & 1));
  if constexpr (G == 0) STF(0);
  if constexpr (G == 16) STF(1);
  if constexpr (G == 28) STF(2);
  if constexpr (G == 29) STF(3);
  if constexpr (G == 0) asm volatile("s_waitcnt lgkmcnt(0)");
  else if constexpr (G >= 8 && G < 28 && !(G & 1)) asm volatile("s_waitcnt lgkmcnt(%0)" ::"i"(G <= 24 ? 6 : 4));
  else if constexpr (G == 28) asm volatile("s_waitcnt vmcnt(16) lgkmcnt(0)\n\ts_barrier" ::: "memory");
  qk_mfma<L, kb, 1, s, false>();
  if constexpr (MG && G < 8) asm volatile("ds_read_b128 v[%c0:%c1], v85 offset:%c2" ::"i"(mg_reg(L, G)), "i"(mg_reg(L, G) + 3), "i"(G * 1024));
  if constexpr (G < 24) kread<L, 8 + G>();
  if constexpr (G < 24 && s == 1) mask_cmp<16 * (kb + 1), 126>();
  if constexpr (G < 24 && s == 3) mask_sel<R_CI + 8 * (kb + 1) + 4, R_M + 1>();
  if constexpr (G >= 4 && G < 28 && G % 3 == 2) w_load<(G - 4) / 3>();
  if constexpr (G == 25) asm volatile("s_add_u32 s82, s83, 0x1000000" SCC3);
  if constexpr (G == 27) dma_m0<L, 0>();
  if constexpr (G >= 28) { vread<L ^ 1, 2 * (G - 28)>(); vread<L ^ 1, 2 * (G - 28) + 1>(); dma_at<G - 28>(); }
  sm_gap<L ^ 1, 64 + 2 * G>(c.redo);
  if constexpr (MG) asm volatile("v_mfma_f32_16x16x32_bf16 a[%c0:%c1], v[%c2:%c3], v[%c4:%c5], a[%c0:%c1]" ::"i"(o), "i"(o + 3), "i"(a), "i"(a + 3), "i"(b), "i"(b + 3));
  sm_gap<L ^ 1, 65 + 2 * G>(c.redo);
  GAP();
}
template <int L, bool MG, int... Gs> DI void phaseAD(AC& c, iseq<Gs...>) {
  sld<72, B_NXT, 1>();
  mask_cmp<0, 126>(); mask_sel<R_CI + 4, R_M + 1>();
  (gapAD<L, MG, Gs>(c), ...);
}
#define BDX 8
constexpr int bd_w(bool EPI, int G) { return EPI && G >= 19 && G % 4 == 3 && (G - 19) / 4 < BDX ? 16 + (G - 19) / 4 : -1; }
template <int L, bool EPI, int G> DI void gapBD(AC& c, const EP& p) {
  constexpr int g = G & 15, e = (G >> 4) * 8 + (g - 8), o = e - 3;
  if constexpr (G == 0) STF(5);
  if constexpr (G == 8) STF(6);
  if constexpr (G == 9) STF(7);
  if constexpr (G == 16) STF(8);
  if constexpr (G == 17) STF(9);
  if constexpr (G == 32) STF(10);
  if constexpr (G == 48) STF(11);
  if constexpr (G == 1) dma_m0<L, 4>();
  if constexpr (G == 8) asm volatile("s_waitcnt vmcnt(14)" ::: "memory");
  if constexpr (G == 16) asm volatile("s_waitcnt lgkmcnt(0)\n\ts_barrier" ::: "memory");
  gapB<L, false, G>(c);
  if constexpr (G >= 8 && G < 12)
    asm volatile("ds_read_b128 a[%c0:%c1], v90 offset:%c4\n\tds_read_b128 a[%c2:%c3], v90 offset:%c5" ::"i"(96 + 8 * G), "i"(99 + 8 * G), "i"(100 + 8 * G), "i"(103 + 8 * G),
                 "i"((L ^ 1) * 32768 + (G - 8) * 2048), "i"((L ^ 1) * 32768 + (G - 8) * 2048 + 1024));
  if constexpr (G == 1 || G == 2) asm volatile("v_add_f32_e32 v%c0, v%c0, v%c1" ::"i"(R_RS + G - 1), "i"(R_TS + G - 1));
  if constexpr (G >= 3 && G < 19 && G % 4 == 3) dma_at<4 + (G - 3) / 4>();
  if constexpr (!EPI && G == 17)
    asm volatile("s_cmp_eq_u32 s97, 0\n\ts_cbranch_scc1 1f\n\ts_add_u32 s64, s100, 4 * 800\n\ts_addc_u32 s65, s101, 0\n\ts_swappc_b64 s[98:99], s[64:65]\n1:" ::: SCC_ "memory");
  if constexpr (bd_w(EPI, G) >= 0) w_load<bd_w(EPI, G)>();
  if constexpr (g >= 8 && e < 16) asm volatile("v_exp_f32 v%c0, v%c1" ::"i"(R_E + e), "i"(R_S + 32 * L + 4 + 8 * (e >> 2) + (e & 3)));
  if constexpr (g >= 8 && o >= 1 && o < 16) {
    if constexpr (o == 1) asm volatile("v_add_f32_e32 v%c0, v%c1, v%c2" ::"i"(R_TS + 1), "i"(R_E), "i"(R_E + 1));
    else asm volatile("v_add_f32_e32 v%c0, v%c0, v%c1" ::"i"(R_TS + 1), "i"(R_E + o));
    if constexpr (o & 1) asm volatile("v_cvt_pk_bf16_f32 v%c0, v%c1, v%c2" ::"i"(p_reg(L, (o >> 2) * 4 + 2 + ((o >> 1) & 1))), "i"(R_E + o - 1), "i"(R_E + o));
  }
  if constexpr (G == 45) asm volatile("v_cmp_nle_f32_e64 %0, v%c1, v%c2" : "=s"(c.redo) : "i"(R_TS + 1), "i"(R_THR));
  if constexpr (EPI && G >= 10 && G < 58 && !(G & 1)) epi_pair<((G - 10) >> 3) * 2, ((G - 10) >> 1) & 3, 0, 224>(p);
  if constexpr (EPI && G >= 17 && G < 58 && (G & 7) == 1) ctx_store<((G - 17) >> 3) * 2>();
  GAP();
}
template <int L, bool EPI, int... Gs> DI void phaseBD(AC& c, const EP& p, iseq<Gs...>) {
  if constexpr (!EPI) { sld<96, B_LM, 1>(); sld<97, B_LM + 1, 1>(); asm volatile("s_nop 0"); fl_check(); }
  (gapBD<L, EPI, Gs>(c, p), ...);
}

#define ACLOB_ALL "a0","a1","a2","a3","a4","a5","a6","a7","a8","a9","a10","a11","a12","a13","a14","a15","a16","a17","a18","a19","a20","a21","a22","a23","a24","a25","a26","a27","a28","a29","a30","a31","a32","a33","a34","a35","a36","a37","a38","a39","a40","a41","a42","a43","a44","a45","a46","a47","a48","a49","a50","a51","a52","a53","a54","a55","a56","a57","a58","a59","a60","a61","a62","a63","a64","a65","a66","a67","a68","a69","a70","a71","a72","a73","a74","a75","a76","a77","a78","a79","a80","a81","a82","a83","a84","a85","a86","a87","a88","a89","a90","a91","a92","a93","a94","a95","a96","a97","a98","a99","a100","a101","a102","a103","a104","a105","a106","a107","a108","a109","a110","a111","a112","a113","a114","a115","a116","a117","a118","a119","a120","a121","a122","a123","a124","a125","a126","a127","a128","a129","a130","a131","a132","a133","a134","a135","a136","a137","a138","a139","a140","a141","a142","a143","a144","a145","a146","a147","a148","a149","a150","a151","a152","a153","a154","a155","a156","a157","a158","a159","a160","a161","a162","a163","a164","a165","a166","a167","a168","a169","a170","a171","a172","a173","a174","a175","a176","a177","a178","a179","a180","a181","a182","a183","a184","a185","a186","a187","a188","a189","a190","a191","a192","a193","a194","a195","a196","a197","a198","a199","a200","a201","a202","a203","a204","a205","a206","a207","a208","a209","a210","a211","a212","a213","a214","a215","a216","a217","a218","a219","a220","a221","a222","a223","a224","a225","a226","a227","a228","a229","a230","a231","a232","a233","a234","a235","a236","a237","a238","a239","a240","a241","a242","a243","a244","a245","a246","a247","a248","a249","a250","a251","a252","a253","a254","a255"

template <int I> DI void epi_store() {
  asm volatile("buffer_store_dwordx4 v[%c0:%c1], v90, " SW ", s%c2 offen offset:%c3 sc1" ::"i"(128 + 4 * I), "i"(131 + 4 * I), "i"(76 + (I >> 2)), "i"((I & 3) * 1024) : "memory");
}
template <int HI> DI void epi_init() {
  asm volatile("v_readlane_b32 s76, v93, %c0\n\ts_nop 0\n\ts_add_u32 s77, s76, 0x1000\n\ts_add_u32 s78, s76, %1\n\ts_add_u32 s79, s78, 0x1000" ::"i"(B_SO), "i"(HI) SCC1);
}
template <int I> DI void epi_pread() {
  asm volatile("ds_read_b128 v[%c0:%c1], v107 offset:%c2" ::"i"(128 + 4 * I), "i"(131 + 4 * I), "i"(I * 1024));
}
template <int... Is> DI void epi_preads(iseq<Is...>) { (epi_pread<Is>(), ...); }
template <int G> DI void gapF(AC& c, const EP& p) {
  if constexpr (G < 64) gapB<ODD, false, G>(c);
  if constexpr (G == 1) asm volatile("v_mov_b32 v93, v94\n\ts_brev_b32 s73, 1");
  if constexpr (G >= 3 && G < 10) att_item<G - 3>();
  if constexpr (G >= 10) epi_pair<(G - 10) / 4, (G - 10) & 3, 2>(p);
  if constexpr (G >= 14 && ((G - 14) & 3) == 0) epi_store<(G - 14) / 4>();
  if constexpr (G == 73) epi_store<15>();
  GAP();
}
template <int... Gs> DI void phaseF(AC& c, const EP& p, iseq<Gs...>) { (gapF<Gs>(c, p), ...); }
constexpr int fd_w(bool EPI, int G) {
  const int f0 = EPI ? 16 + BDX : 16, n = NWF - f0;
  for (int i = 0; i < n; ++i) if (G == (EPI ? 2 + (i * 37) / n : i < 16 ? 2 + 2 * i : 4 + 2 * i)) return f0 + i;
  return -1;
}
#define FDA 50
#define CXT 0
template <int H> DI void ctx_touch() {
  asm volatile("v_readlane_b32 s63, v93, %c0\n\tv_mad_u32_u24 v110, v89, 7, v90\n\ts_nop 3\n\tbuffer_load_dword v111, v110, " SW ", s63 offen offset:0 sc1"
               ::"i"(B_SO) : "memory");
}
template <> DI void ctx_touch<1>() {
  asm volatile("v_readlane_b32 s63, v93, %c0\n\tv_mad_u32_u24 v110, v89, 7, v90\n\tv_add_u32 v110, 0x8000, v110\n\ts_nop 2\n\tbuffer_load_dword v111, v110, " SW ", s63 offen sc1"
               ::"i"(B_SO) : "memory");
}
#define FDE 20
constexpr int fd_cnt(bool EPI, int G0, int G1) { int n = 0; for (int G = G0; G < G1; ++G) n += (fd_w(EPI, G) >= 0) + ((G & 3) == 3); return n; }
constexpr bool fd_ok() {
  for (int G = 0; G < 64; ++G) {
    const int a = fd_w(0, G), b = fd_w(1, G);
    if ((a >= 16 && G < 2 * (a & 15) + (a >= 32 ? 34 : 2)) || (b >= 28 && G < (b >= 32 ? 2 * (b - 32) + 10 : 2 * (b - 28) + 2))) return false;
  }
  return true;
}
static_assert(fd_ok() && NWF == 40 && BDX <= 12);
template <int L, bool EPI, int G> DI void gapFD(const EP& p) {
  constexpr int I = EPI ? (G < 8 ? 12 + (G >> 2) * 2 : ((G - 8) >> 2) * 2 + 1) : G < 32 ? (G >> 2) * 2 : ((G - 32) >> 2) * 2 + 1;
  constexpr int u = EPI ? G : G - 2, uf = SNK & 2 ? u ^ ((u >> 1) & 1) : u;
  if constexpr (G == 0) STF(14);
  if constexpr (G == 8) STF(15);
  if constexpr (G == 16) STF(16);
  if constexpr (G == 21) STF(18);
  if constexpr (G == 32) STF(19);
  if constexpr (u >= 16 && u < 32 && (u & 3) == 0) asm volatile("s_waitcnt lgkmcnt(%0)" ::"i"(28 - u));
  if constexpr (u >= 0 && u < 32) pv_mfma<L, uf, 1>();
  if constexpr (u >= 0 && u < 16) vread<L, uf + 16>();
  constexpr int F = fd_w(EPI, G);
  if constexpr (F >= 0) w_load<F>();
  epi_pair<I, G & 3, !EPI>(p);
  if constexpr ((G & 3) == 3) ctx_store<I>();
  if constexpr (G == (EPI ? FDE : FDA)) {
    STF(17);
    asm volatile("s_waitcnt vmcnt(%0)\n\ts_barrier" ::"i"(fd_cnt(EPI, EPI ? 8 : 32, G + 1)) : "memory");
    set_flag<A_FC>();
    if constexpr (CXT & 1) ctx_touch<0>();
  }
  GAP();
}
template <int L, bool EPI, int... Gs> DI void phaseFD(const EP& p, iseq<Gs...>) { (gapFD<L, EPI, Gs>(p), ...); }
#define A_CT2 "v_readlane_b32 s63, v93, 3\n\ts_sub_u32 s64, s100, at_event - at_tail\n\ts_subb_u32 s65, s101, 0\n\ts_mov_b32 s66, at_event - at_tail\n\ts_mov_b32 s67, s59\n\t" \
    "v_lshl_add_u32 v78, v89, 9, s63\n\ts_mov_b64 exec, 0x1f\n\tbuffer_load_dword v84, v78, s[64:67], 0 offen\n\ts_mov_b64 exec, -1\n\t"
#define A_CT4 "s_mov_b64 s[64:65], s[100:101]\n\ts_movk_i32 s66, 0xf00\n\ts_mov_b32 s67, s59\n\tv_lshlrev_b32 v78, 3, v89\n\tbuffer_load_dword v84, v78, s[64:67], 0 offen\n\t"
#define A_RET "s_setpc_b64 s[98:99]\n\t"
#define A_EFLAG(L) "v_readlane_b32 s63, v93, " #L "\n\ts_nop 3\n\t" A_FLAG
#define A_EPOLL "v_readlane_b32 s96, v93, s85\n\ts_add_u32 s85, s85, 1\n\tv_readlane_b32 s97, v93, s85\n\ts_add_u32 s85, s85, 1\n\t" A_POLL("v[122:123]", "s[96:97]", "s62")
#define A_Q1 "buffer_load_dwordx4 v87, s[64:67], s95 offen lds\n\ts_add_u32 m0, m0, 0x400\n\ts_add_u32 s95, s95, 0x1c00\n\t"
#define A_EPOLL1 "s_mov_b32 exec_lo, 0xffff0000\n\ts_mov_b32 exec_hi, 0xffff\n\tbuffer_load_dwordx2 v[124:125], v88, " SW ", s92 offen sc1\n\ts_mov_b64 exec, -1\n\t" A_CT2
#define A_Z1(K, O) "v_xor_b32 v49, " K ", v48\n\tbuffer_load_dwordx4 v49, " SW ", s63 offen offset:" O " sc1 lds\n\t"
#define A_EQ "v_readlane_b32 s68, v92, 30\n\tv_readlane_b32 s95, v92, 28\n\ts_mov_b64 s[64:65], s[88:89]\n\ts_mov_b32 s66, 0x1c0000\n\ts_mov_b32 s67, s59\n\t" \
    "s_add_u32 m0, s68, 0x1000\n\ts_nop 1\n\t" A_Q1 A_Q1 A_Q1 A_Q1
#define A_ELOAD "v_readlane_b32 s68, v92, 30\n\tv_readlane_b32 s63, v92, 29\n\tv_lshrrev_b32 v48, 5, v89\n\ts_mov_b32 s96, 0\n\ts_mov_b32 s97, 0x3fff0000\n\t" \
    "v_and_b32 v48, 16, v48\n\t" A_CHECK("v[124:125]", "s[96:97]", "s92") "v_xor_b32 v48, v48, v89\n\ts_mov_b32 m0, s68\n\t" \
    A_Z1("0", "0") A_Z1("32", "1024") A_Z1("64", "2048") "s_mov_b32 exec_hi, 0\n\t" A_Z1("0x60", "3072") "s_mov_b32 exec_hi, -1\n\t"
#define A_PZ2(Z0, Z1, O) "ds_read_u16_d16_hi " Z0 ", v46 offset:" O "\n\tds_read_u16_d16_hi " Z1 ", v47 offset:" O "\n\t"
#define A_PRD(X, Q, O, Z0, Z1, Z2, Z3, Z4, Z5, Z6, Z7) "v_xor_b32 v46, " X ", v44\n\tds_read_b128 " Q ", v45 offset:" O "\n\tv_xor_b32 v47, 64, v46\n\t" \
    A_PZ2(Z0, Z1, "0") A_PZ2(Z2, Z3, "128") A_PZ2(Z4, Z5, "256") A_PZ2(Z6, Z7, "384")
#define A_PM2(Q, Z0, Z1) "v_lshlrev_b32 v40, 16, " Q "\n\tv_and_b32 v41, 0xffff0000, " Q "\n\tv_mul_f32 v40, v40, " Z0 "\n\tv_mul_f32 v41, v41, " Z1 "\n\tv_cvt_pk_bf16_f32 " Q ", v40, v41\n\t"
#define A_PST(Q) "buffer_store_dwordx4 " Q ", v43, s[64:67], s63 offen\n\ts_add_u32 s63, s63, 0x3800\n\t"
#define A_PA "v64", "v65", "v66", "v67", "v68", "v69", "v70", "v71"
#define A_PB "v72", "v73", "v74", "v75", "v76", "v77", "v78", "v79"
#define A_PRD_(...) A_PRD(__VA_ARGS__)
#define A_EPOST "v_readlane_b32 s68, v92, 30\n\tv_lshrrev_b32 v40, 4, v89\n\tv_readlane_b32 s64, v92, 0\n\tv_mul_u32_u24 v41, 37, v40\n\tv_readlane_b32 s65, v92, 1\n\t" \
    "v_lshrrev_b32 v41, 8, v41\n\tv_readlane_b32 s63, v94, 23\n\tv_mad_i32_i24 v42, v41, -7, v40\n\ts_movk_i32 s66, 0x380\n\ts_movk_i32 s67, 0x210\n\t" \
    "v_mad_u32_u24 v43, v41, s66, v87\n\tv_lshl_add_u32 v44, v41, 1, s68\n\tv_add_u32 v45, s68, v89\n\tv_mad_u32_u24 v44, v42, s67, v44\n\t" \
    "v_mov_b64 v[64:65], 0\n\tv_mov_b64 v[66:67], 0\n\tv_mov_b64 v[68:69], 0\n\tv_mov_b64 v[70:71], 0\n\t" \
    "v_mov_b64 v[72:73], 0\n\tv_mov_b64 v[74:75], 0\n\tv_mov_b64 v[76:77], 0\n\tv_mov_b64 v[78:79], 0\n\ts_mov_b32 s66, 0x380000\n\ts_mov_b32 s67, s59\n\t" \
    A_PRD_("0", "v[48:51]", "4096", A_PA) A_PRD_("16", "v[52:55]", "5120", A_PB) "s_waitcnt lgkmcnt(9)\n\t" \
    A_PM2("v48", "v64", "v65") A_PM2("v49", "v66", "v67") A_PM2("v50", "v68", "v69") A_PM2("v51", "v70", "v71") A_PST("v[48:51]") \
    A_PRD_("32", "v[56:59]", "6144", A_PA) "s_waitcnt lgkmcnt(9)\n\t" \
    A_PM2("v52", "v72", "v73") A_PM2("v53", "v74", "v75") A_PM2("v54", "v76", "v77") A_PM2("v55", "v78", "v79") A_PST("v[52:55]") \
    A_PRD_("48", "v[60:63]", "7168", A_PB) "s_waitcnt lgkmcnt(9)\n\t" \
    A_PM2("v56", "v64", "v65") A_PM2("v57", "v66", "v67") A_PM2("v58", "v68", "v69") A_PM2("v59", "v70", "v71") A_PST("v[56:59]") "s_waitcnt lgkmcnt(0)\n\t" \
    A_PM2("v60", "v72", "v73") A_PM2("v61", "v74", "v75") A_PM2("v62", "v76", "v77") A_PM2("v63", "v78", "v79") A_PST("v[60:63]")
#define A_TR(R0, R1, R2, R3, O0, O1) "ds_read_b64_tr_b16 " R0 ", v85 offset:" O0 "\n\tds_read_b64_tr_b16 " R1 ", v85 offset:" O1 "\n\t" \
    "ds_read_b64_tr_b16 " R2 ", v84 offset:" O0 "\n\tds_read_b64_tr_b16 " R3 ", v84 offset:" O1 "\n\t"
#define A_WS(R, S, O) "buffer_store_dwordx4 " R ", v89, " SW ", " S " offen offset:" O " sc1\n\t"
#define A_PL1(O) "buffer_load_dwordx4 v89, " SW ", s63 offen offset:" O " sc1 lds\n\t"
#define A_PL2(R, O) "buffer_load_dwordx4 " R ", v89, " SW ", s95 offen offset:" O " sc1\n\t"
#define A_EPLOAD "v_readlane_b32 s68, v92, 30\n\tv_readlane_b32 s63, v93, 21\n\ts_mov_b32 m0, s68\n\ts_add_u32 s95, s63, 0x8000\n\ts_nop 2\n\t" \
    A_PL1("0") A_PL1("1024") A_PL1("2048") A_PL1("3072") "s_bitset1_b32 m0, 12\n\ts_add_u32 s63, s63, 0x1000\n\t" A_PL1("0") A_PL1("1024") A_PL1("2048") A_PL1("3072") \
    A_PL2("v[40:43]", "0") A_PL2("v[44:47]", "1024") A_PL2("v[48:51]", "2048") A_PL2("v[52:55]", "3072") "s_add_u32 s95, s95, 0x1000\n\t" \
    A_PL2("v[56:59]", "0") A_PL2("v[60:63]", "1024") A_PL2("v[64:67]", "2048") A_PL2("v[68:71]", "3072") \
    "v_and_b32 v78, 0xf0, v89\n\tbuffer_load_dwordx4 v[72:75], v78, " SW ", s73 offen sc1\n\t"
#define A_EPOLLZ "s_mov_b32 exec_lo, 0\n\ts_mov_b32 exec_hi, 0x3fff0000\n\tbuffer_load_dwordx2 v[124:125], v88, " SW ", s92 offen sc1\n\ts_mov_b64 exec, -1\n\t"
#define WTOUCH 0
#define A_WT
#define A_ECHK1 "s_mov_b32 s70, 0xffff0000\n\ts_mov_b32 s71, 0xffff\n\t" A_CHECK("v[124:125]", "s[70:71]", "s92") A_WT
#define A_AT(N) ".fill (at_event + 4 * " #N " - .) / 4, 4, 0xbf800000\n\t"
#define A_EVENT \
    ".p2align 6\nat_event:\n\t" A_RET \
    A_AT(16) "v_cmp_eq_u64 vcc, s[60:61], v[122:123]\n\ts_and_b64 vcc, vcc, s[96:97]\n\ts_cmp_eq_u64 vcc, s[96:97]\n\ts_cbranch_scc0 7f\n\t" A_RET \
    A_AT(32) A_EFLAG(5) A_CT4 A_RET \
    A_AT(48) A_EFLAG(5) A_AT(56) A_EPOLL A_RET \
    A_AT(80) A_EFLAG(4) A_RET \
    A_AT(96) A_EFLAG(5) A_CT4 A_EPOLL A_RET \
    A_AT(128) "v_xor_b32 v84, 32, v85\n\tv_readlane_b32 s63, v92, 27\n\t" \
    A_TR("v[48:49]", "v[50:51]", "v[52:53]", "v[54:55]", "0", "4096") A_TR("v[56:57]", "v[58:59]", "v[60:61]", "v[62:63]", "8192", "12288") \
    A_TR("v[64:65]", "v[66:67]", "v[68:69]", "v[70:71]", "16384", "20480") A_TR("v[72:73]", "v[74:75]", "v[76:77]", "v[78:79]", "24576", "28672") \
    "s_add_u32 s95, s63, 0x1000\n\ts_waitcnt lgkmcnt(8)\n\t" \
    A_WS("v[48:51]", "s63", "0") A_WS("v[52:55]", "s63", "1024") A_WS("v[56:59]", "s63", "2048") A_WS("v[60:63]", "s63", "3072") "s_waitcnt lgkmcnt(0)\n\t" \
    A_WS("v[64:67]", "s95", "0") A_WS("v[68:71]", "s95", "1024") A_WS("v[72:75]", "s95", "2048") A_WS("v[76:79]", "s95", "3072") A_RET \
    A_AT(192) A_EPOLL1 A_RET \
    A_AT(228) A_EPOLL A_AT(240) A_EPOLL1 \
    A_MASK("v224", "v225", "v226", "v227", "0", "1", "2", "3") \
    A_MASK("v232", "v233", "v234", "v235", "16", "17", "18", "19") \
    A_MASK("v240", "v241", "v242", "v243", "32", "33", "34", "35") \
    A_MASK("v248", "v249", "v250", "v251", "48", "49", "50", "51") A_RET \
    "7:\n\t" A_SPIN("v[122:123]", "s[96:97]", "s62") A_RET \
    A_AT(344) A_EFLAG(4) A_EPOLLZ A_EQ A_RET \
    A_AT(400) A_ELOAD A_RET \
    A_AT(448) A_EFLAG(5) A_CT4 A_EPOLLZ A_EQ A_RET \
    A_AT(512) A_EPOST A_RET \
    A_AT(768) "5:\n\t" A_CHECK("v[122:123]", "s[96:97]", "s62") "s_branch 6f\n\t" A_AT(800) "6:\n\t" A_EPLOAD A_RET \
    A_AT(872) A_ECHK1 A_RET \
    A_AT(912) A_ECHK1 "s_branch 5b\n\t"
static_assert(B_WT == 24 && B_CT == 3 && B_FSET == 5 && B_FW == 4 && A_M0F == 30 && A_PQ == 28 && A_PZ == 29 && A_WDST == 27 && C_PO == 23 && B_LP == 21);
DI void event() {
  asm volatile("s_and_b32 s63, s87, 0x3ff\n\ts_lshl2_add_u32 s64, s63, s100\n\ts_addc_u32 s65, s101, 0\n\ts_swappc_b64 s[98:99], s[64:65]" ::: "vcc", SCC_ "memory",
               "v78", "v79");
  GAP();
}

#define OFU 20
static_assert(OFU >= 4 && OFU < 28 && OFU % 2 == 0);
#define OGB 14
constexpr int o_qo(int P) { return O_SEQ[P] & 3; }
constexpr int o_o(int P) { return O_SEQ[P] >> 4; }
constexpr int o_ks(int k) { return k % 7 < 4 ? 4 + k % 7 : k % 7 - 4; }
constexpr int o_sq(int P) {
  for (int q = P; q < 16; ++q) if (P < 1 || o_qo(q) == o_qo(P - 1)) return -1;
  return o_qo(P - 1);
}
constexpr int o_last() { int l = 0; for (int P = 0; P < 16; ++P) if (o_sq(P) >= 0) l = P; return l; }
constexpr bool o_first(int P) { for (int q = 0; q < P; ++q) if (o_qo(q) == o_qo(P)) return false; return true; }
template <int P, int MB, int KK> DI void o_read() {
  constexpr int r = 128 + 4 * (8 * MB + KK), sl = o_k(P) < 0 ? o_qo(P) : o_ks(o_k(P));
  asm volatile("ds_read_b128 v[%c0:%c1], v%c2 offset:%c3" ::"i"(r), "i"(r + 3), "i"(sl < 4 ? 96 + sl : 86), "i"((sl < 4 ? 0 : (sl & 3) * 16384) + (2 * KK + MB) * 1024));
}
template <int... Is> DI void o_read0(iseq<Is...>) { (o_read<0, (Is >> 3), Is & 7>(), ...); }
constexpr int o_gb(int NF) { return NF == 2 ? OGB : 0; }
#define OTAB {40, 44, 48, -1}, {52, -1, 0, 1}, {56, -1, 2, 60}, {-1, -1, 3, 4}, {-1, -1, 5, 6}, {-1, -1, 7, -1}, {-1, -1, 8, -1}, {-1, -1, 9, -1}, {-1, -1, 10, -1}, {-1, -1, 11, -1}
constexpr int O_TAB[][4] = {OTAB};
constexpr int o_op(int P, int U, int NF) {
  const int c = U / 8 == 1 && o_sq(P) >= 0 ? 99 : P < (int)(sizeof(O_TAB) / 16) ? O_TAB[P][U / 8] : -1, i = (U & 7) / 2;
  if (c < 0 || (U & 1)) return 0;
  if (c < 32) return 200 + 4 * c + i;
  if (c == 99) return i == 3 || (i == 1 && NF == 2) ? 300 + i : 0;
  return NF == 1 && (i & 1) ? 0 : 100 + c + i;
}
constexpr int o_vm(int P, int NF) {
  int n = 0;
  for (int i = 0; i <= P; ++i) {
    for (int U = 0; U < (i < P ? 32 : o_gb(NF)); ++U) {
      const int op = o_op(i, U, NF);
      if (op) ++n;
      if (op >= 200 && op < 300 && (op - 200) / 4 == o_k(P + 1)) n = 0;
      if (op >= 100 && op < 200 && (op - 100) / 16 == o_o(P + 1)) n = 0;
    }
  }
  return n;
}
constexpr bool o_ok() {
  for (int P = 1; P < 16; ++P) {
    for (int i = P - 1; i < 16; ++i) for (int U = 0; U < 32; ++U) {
      const int op = o_op(i, U, 2);
      if (i > P - 1 || U >= 0) {
        if (op >= 200 && op < 300 && (op - 200) / 4 == o_k(P)) return false;
        if (op >= 100 && op < 200 && (op - 100) / 16 == o_o(P)) return false;
      }
    }
  }
  for (int i = 0; i < 16; ++i) for (int U = 0; U < 32; ++U) {
    const int op = o_op(i, U, 2);
    if (op >= 200 && op < 300) {
      const int k = (op - 200) / 4, sl = o_ks(k);
      int prev = -1;
      for (int P = 0; P < 16; ++P) if ((o_k(P) < 0 ? o_qo(P) : o_ks(o_k(P))) == sl && (o_k(P) < k)) prev = P;
      if (prev >= 0 && !(i > prev + 1 || (i == prev + 1 && U >= 16))) return false;
    }
  }
  return true;
}
static_assert(NWF == 40 && o_ok());
template <int K> DI void o_m0() {
  constexpr int sl = o_ks(K);
  if constexpr (sl < 4) asm volatile("s_add_u32 m0, s80, s%c0" ::"i"(64 + sl) SCC1);
  else asm volatile("s_add_u32 m0, s80, %0" ::"i"(sl * 16384) SCC1);
}
template <int NF, int SR, int C> DI void o_pack() {
  if constexpr (NF == 2 || !(C & 2))
    asm volatile("v_cvt_pk_bf16_f32 v%c0, v%c1, v%c2" ::"i"(NF == 2 ? SR + C : SR + (C >> 2) * 2 + (C & 1)), "i"(SR + 2 * C), "i"(SR + 2 * C + 1));
}
template <int SR, int F> DI void o_sst() {
  asm volatile("buffer_store_dwordx4 v[%c0:%c1], v89, " SW ", s77 offen offset:%c2 sc1" ::"i"(SR + 4 * F), "i"(SR + 4 * F + 3), "i"(F * 1024) : "memory");
}
template <int I, int V> DI void o_slab() {
  asm volatile("buffer_load_dwordx4 v[%c0:%c1], v%c4, " SW ", s%c2 offen offset:%c3 sc1" ::"i"(192 + 4 * I), "i"(195 + 4 * I), "i"(64 + I / 2), "i"((I & 1) * 1024), "i"(V) : "memory");
}
template <int NF, int P, int G> DI void o_gap() {
  constexpr int NG = 16 * NF, ch = G >> 3, mb = ch / NF, nfi = (SNK & 16) && NF == 2 ? (ch ^ (ch >> 1)) & 1 : ch % NF, kk = (SNK & 16) && NF == 2 && (ch & 1) ? 7 - (G & 7) : G & 7, sq = o_sq(P), sr = 192 + 16 * sq;
  constexpr int w = o_wreg(o_o(P), 2 * kk + nfi), b = 128 + 4 * (8 * mb + kk), d = 192 + 16 * o_qo(P) + 4 * (2 * mb + nfi);
  constexpr bool nxt = P != 15;
  constexpr int U = NF == 2 ? G : 2 * G, op = o_op(P, U, NF), opn = U < 24 ? o_op(P, U + 8, NF) : nxt ? o_op(P + 1, U - 24, NF) : 0;
  if constexpr (G == 0) asm volatile("s_waitcnt lgkmcnt(8)" ::: "memory");
  if constexpr (G == NG / 2) asm volatile("s_waitcnt lgkmcnt(%0)" ::"i"(nxt && NF == 1 ? 8 : 0) : "memory");
  if constexpr (nxt && G == o_gb(NF) && P >= 1 && (o_k(nxt ? P + 1 : P) >= 0 || P < 3)) asm volatile("s_waitcnt vmcnt(%0)\n\ts_barrier" ::"i"(o_vm(P, NF)) : "memory");
  if constexpr (P == 1 && G == o_gb(NF))
  {
    asm volatile("v_readlane_b32 s63, v92, %c0\n\ts_nop 0\n\ts_add_u32 s63, s63, 32\n\t" A_FLAG ::"i"(A_FC) : SCC_ "memory");
    if constexpr (CXT & 2) ctx_touch<1>();
  }
  if constexpr (P == o_last() + 1 && U == OFU) {
    asm volatile("s_waitcnt vmcnt(0)" ::: "memory");
    set_flag<A_FS>();
  }
  if constexpr (op >= 200 && op < 300 && (op & 3) == 0) o_m0<(op - 200) / 4>();
  if constexpr (o_first(P) && !(G & 7))
    asm volatile("v_mfma_f32_16x16x32_bf16 v[%c0:%c1], a[%c2:%c3], v[%c4:%c5], 0" ::"i"(d), "i"(d + 3), "i"(w), "i"(w + 3), "i"(b), "i"(b + 3));
  else
    asm volatile("v_mfma_f32_16x16x32_bf16 v[%c0:%c1], a[%c2:%c3], v[%c4:%c5], v[%c0:%c1]" ::"i"(d), "i"(d + 3), "i"(w), "i"(w + 3), "i"(b), "i"(b + 3));
  if constexpr (nxt && G >= NG - 16) o_read<P + 1, (G >> 3) & 1, kk>();
  if constexpr (op >= 100 && op < 200) w_load<op - 100, 1 + NF>();
  if constexpr (op >= 200 && op < 300)
    asm volatile("buffer_load_dwordx4 v89, " SW ", s%c0 offen offset:%c1 sc1 lds" ::"i"(73 + U / 8), "i"((op & 3) * 1024) : "memory");
  if constexpr (sq >= 0) {
    if constexpr (G == 0) sld<77, A_SS + sq>();
    if constexpr (NF == 2 && G >= 6 && G < 14) o_pack<2, sr, G - 6>();
    if constexpr (NF == 1 && (G == 4 || G == 5)) { o_pack<1, sr, 4 * (G - 4)>(); o_pack<1, sr, 4 * (G - 4) + 1>(); }
    if constexpr (op >= 300) o_sst<sr, NF == 2 ? (op - 300) / 2 : 0>();
  }
  if constexpr (opn >= 200 && opn < 300 && (opn & 3) == 0) sld<73 + ((U + 8) & 31) / 8, A_PC + (opn - 200) / 4>();
  if constexpr (P == 12 && G == NG - 4) { sld<62, A_FSW>(); fl_mask<A_FSM, -1>(); }
  if constexpr (P == 13 && G == 1) fl_issue();
  if constexpr (P == 13 && G >= 2 && G < 5) sld<62 + G, A_SL + G - 2>();
  if constexpr (P == 13 && G == NG - 2)
    asm volatile("v_cmp_eq_u64 vcc, s[60:61], v[122:123]\n\ts_and_b64 vcc, vcc, s[96:97]\n\ts_cmp_eq_u64 vcc, s[96:97]\n\ts_cselect_b32 s67, 0, 0x80000000\n\t"
                 "v_or_b32 v100, s67, v89" ::: "vcc", SCC_ "memory");
  if constexpr (P == 14 && G % NF == 0 && G >= 2 * NF && G < 14 * NF && !((G / NF) & 1) && (NF == 2 || !((G / NF) & 2) == false)) o_slab<G / NF / 2 - 1, 100>();
  GAP();
}
template <int NF, int P, int... Gs> DI void o_interval_(iseq<Gs...>) {
  (o_gap<NF, P, Gs>(), ...);
}
template <int NF, int P> DI void o_interval() { o_interval_<NF, P>(make_iseq<16 * NF>{}); }
template <int NF> DI void o_slabs() {
  unsigned late;
  asm volatile("s_mov_b32 %0, s67" : "=s"(late));
  if (late) {
    fl_wait();
    static_for([&](auto I) { if constexpr (NF == 2 || !(I & 1)) o_slab<I, 89>(); }, make_iseq<6>{});
  }
  GAP();
}
template <int NF> DI void o_sum() {
  static_for([&](auto I) {
    constexpr int i = I, q = i >> 2, f = i & 3, b = 192 + 8 * q + (NF == 2 ? 4 * (f >> 1) : 0), a = 72 + 2 * (NF == 2 ? f & 1 : f >> 1);
    if constexpr (f == 0) asm volatile("s_waitcnt vmcnt(%0)" ::"i"((2 - q) * NF) : "memory");
    if constexpr (NF == 2 || !(f & 1))
      asm volatile("v_mfma_f32_16x16x32_bf16 v[%c0:%c1], v[%c2:%c3], v[%c4:%c5], v[%c0:%c1]" ::"i"(240 + 4 * f), "i"(243 + 4 * f), "i"(a), "i"(a + 3), "i"(b), "i"(b + 3));
  }, make_iseq<12>{});
  asm volatile("s_nop 7");
}

#define NSG 64
#define KWAIT "s_waitcnt lgkmcnt(0)\n\t"
#define EARLY2 0
#define TOUCH0 0x20
#define TOUCH1 0x20
__global__ __launch_bounds__(256, 1) __attribute__((amdgpu_num_vgpr(82), amdgpu_num_sgpr(NSG)))
void fused_kernel(const bf16* a0, const bf16* a1, const bf16* a2, const bf16* a3, const bf16* a4, char* ws, const bf16* __restrict__ ak, const bf16* __restrict__ av,
                  const bf16* __restrict__ ash, const bf16* __restrict__ assmk, const bf16* __restrict__ assmD, const bf16* __restrict__ wo,
                  const bf16* __restrict__ aq, bf16* __restrict__ out) {
  int b = blockIdx.x;
  asm volatile("s_load_dwordx8 s[92:99], %1, 0\n\ts_load_dwordx2 s[60:61], %1, 32" : "+s"(b) : "s"(__builtin_amdgcn_kernarg_segment_ptr()));
  const bf16* ckv;
  const int bp = b >> 3;
  extern __shared__ __attribute__((aligned(16))) char lds[];
  asm volatile("at_code_begin:" ::: ACLOB_ALL, "v255", "s101");
  KST(0, 0);
  const int tid = threadIdx.x;
  int lane = tid & 63;
  const int wid = __builtin_amdgcn_readfirstlane(tid >> 6);
  const int j = b & 7, r = b >> 3, rr = r & 15, pj = rr >> 1, kv = pj & 1;
  const bool isY = r >= 16, doA = !isY && pj != NCV;
  const int cr = rr - (pj > NCV ? 2 : 0);
  const bf16* wkv;
  const int dq = Q44 ? (j & 1) - ((rr >> 1) & 1) : 0;
  unsigned vq, vw, tb0, vbA2, vD;
  const char* tp;
  {
    GM g;
    {
      static_assert(Q44 && K2(1) == 3);
#define CTP 0
#define CTM "0xff"
#define P_TM "s_memrealtime s[52:53]\n\ts_waitcnt lgkmcnt(0)\n\t"
#define P_T40
#define P_T41
#define P_T42
#define P_CT "s_getpc_b64 s[76:77]\n\tv_mbcnt_lo_u32_b32 v99, -1, 0\n\ts_lshr_b32 s88, %4, 3\n\ts_and_b32 s76, s76, 0xffffff80\n\ts_lshl_b32 s88, s88, 9\n\ts_and_b32 s77, s77, 0xffff\n\t" \
             "s_lshl1_add_u32 s88, s90, s88\n\ts_mov_b32 s78, pf_code_end - at_code_begin\n\ts_mov_b32 s79, 0x20000\n\tv_lshl_add_u32 v99, v99, 14, s88\n\ts_mov_b32 m0, %5\n\t" \
             "s_mov_b64 exec, " CTM "\n\tbuffer_load_dword v99, s[76:79], 0 offen lds\n\ts_mov_b64 exec, -1\n\t"
#define P_CTL(R0, R1, R2, R3, RR) "s_getpc_b64 s[" R0 ":" R1 "]\n\tv_mbcnt_lo_u32_b32 v100, -1, 0\n\tv_readfirstlane_b32 s89, %3\n\ts_lshr_b32 s88, %4, 3\n\ts_and_b32 s" R0 ", s" R0 ", 0xffffff80\n\t" \
             "s_lshl_b32 s88, s88, 9\n\ts_and_b32 s" R1 ", s" R1 ", 0xffff\n\ts_lshl1_add_u32 s88, s89, s88\n\ts_mov_b32 s" R2 ", pf_code_end - at_code_begin\n\ts_mov_b32 s" R3 ", 0x20000\n\t" \
             "v_lshl_add_u32 v100, v100, 14, s88\n\ts_mov_b32 m0, %5\n\ts_mov_b64 exec, 0xff\n\tbuffer_load_dword v100, " RR ", 0 offen lds\n\ts_mov_b64 exec, -1\n\t"
#define P_CT0 P_CT
#define P_CTX1
#define P_CTX2
#define P_LA(SO) "buffer_load_dwordx4 v86, s[64:67], " SO " offen" GSC " lds\n\t"
#define P_LW(SO) "buffer_load_dwordx4 v87, s[68:71], " SO " offen" GSC " lds\n\t"
      asm volatile("v_readfirstlane_b32 s90, %3\n\tv_lshrrev_b32 v96, 4, %3\n\t" P_CT0 "v_lshrrev_b32 v97, 3, %3\n\ts_and_b32 s63, %2, 1\n\tv_xor_b32 v98, v96, %3\n\ts_lshl_b32 s88, s63, 2\n\t"
                   "s_bfe_u32 s89, %2, 0x20005\n\tv_and_b32 v98, 7, v98\n\ts_movk_i32 s62, 0x800\n\ts_bitcmp1_b32 %2, 7\n\t"
                   "s_cselect_b32 s88, s88, 0\n\ts_cmovk_i32 s62, 0x380\n\ts_add_u32 s89, s89, s88\n\ts_lshl_b32 s89, s89, 8\n\tv_lshlrev_b32 v98, 4, v98\n\t"
                   "v_add_u32 v97, s89, v97\n\ts_lshl_b32 s90, s90, 4\n\ts_and_b32 s88, %2, 0x80\n\ts_lshl_b32 s88, s88, 8\n\tv_mad_u32_u24 v86, v97, s62, v98\n\t"
                   "s_or_b32 s90, s90, s88\n\ts_lshl_b32 s66, s62, 11\n\ts_mov_b32 s67, 0x20000\n\ts_mov_b32 m0, s90\n\ts_lshl_b32 s72, s62, 5\n\ts_lshl_b32 s75, s62, 7\n\t"
                   "s_add_u32 s76, s75, s72\n\t" P_T40 "s_bitcmp1_b32 %2, 7\n\t" KWAIT "s_cselect_b64 s[64:65], s[94:95], s[92:93]\n\t"
                   P_LA("0") P_T41
                   "s_add_u32 m0, s90, 0x1000\n\tv_and_b32 v99, 3, v96\n\t" P_LA("s72")
                   "s_add_u32 m0, s90, 0x4000\n\tv_lshrrev_b32 v97, 6, %3\n\t" P_LA("s75")
                   "s_add_u32 m0, s90, 0x5000\n\tv_lshl_or_b32 v99, v99, 2, v97\n\t" P_LA("s76")
                   P_T42 P_CTX1 "s_bfe_u32 s88, %2, 0x10004\n\tv_xor_b32 v99, v99, %3\n\ts_sub_u32 s63, s63, s88\n\tv_and_b32 v99, 15, v99\n\ts_and_b32 s88, %2, 15\n\t"
                   "v_lshlrev_b32 v99, 4, v99\n\ts_bitcmp1_b32 %2, 7\n\ts_cselect_b32 s63, s63, 0\n\tv_lshl_add_u32 v99, v96, 13, v99\n\ts_sub_u32 s88, s88, s63\n\t"
                   "s_lshl_b32 s88, s88, 9\n\ts_bitcmp1_b32 %2, 4\n\ts_cselect_b64 %1, s[98:99], s[96:97]\n\tv_add_u32 v87, s88, v99\n\ts_bitcmp1_b32 %2, 7\n\t"
                   "s_cselect_b64 s[68:69], s[60:61], %1\n\ts_lshl_b32 s70, s62, 12\n\ts_add_u32 m0, s90, 0x10000\n\ts_mov_b32 s71, 0x20000\n\t"
                   P_LW("0")
                   "s_add_u32 m0, s90, 0x11000\n\ts_mov_b32 s80, 0x20000\n\t" P_LW("s80")
                   "s_add_u32 m0, s90, 0x12000\n\ts_movk_i32 s81, 0x100\n\t" P_LW("s81")
                   "s_add_u32 m0, s90, 0x13000\n\ts_mov_b32 s82, 0x20100\n\t" P_LW("s82")
                   "s_add_u32 m0, s90, 0x14000\n\ts_mov_b32 s83, 0x40000\n\t" P_LW("s83")
                   "s_add_u32 m0, s90, 0x15000\n\ts_mov_b32 s84, 0x60000\n\t" P_LW("s84")
                   "s_add_u32 m0, s90, 0x16000\n\ts_mov_b32 s85, 0x40100\n\t" P_LW("s85")
                   "s_add_u32 m0, s90, 0x17000\n\ts_mov_b32 s86, 0x60100\n\t" P_LW("s86")
                   P_CTX2 "s_mov_b64 %0, s[92:93]\n\ts_lshl_b32 s73, s72, 1\n\ts_add_u32 s74, s73, s72\n\ts_add_u32 s77, s75, s73\n\ts_add_u32 s78, s75, s74\n\t"
                   "s_and_b32 s79, s90, 0xfff\n\ts_add_u32 s87, s79, 0x10000\n\tv_add_u32 v84, 0x80, v86\n\tv_add_u32 v85, 0x80000, v87\n\t"
                   "s_mov_b32 s58, 0x6400000\n\ts_mov_b32 s59, 0x20000\n\ts_xor_b32 s92, s90, 0x8000\n\t"
                   "s_bfe_u32 s88, %2, 0x40003\n\ts_bfe_u32 s89, %2, 0x20005\n\ts_add_u32 s88, s88, s89\n\ts_and_b32 s88, s88, 3\n\ts_bitcmp1_b32 %2, 7\n\t"
                   "s_cselect_b32 s88, 0, s88\n\ts_add_u32 s91, s88, 2"
                   : "=&s"(ckv), "=&s"(wkv) : "s"(b), "v"(tid), "s"(blockIdx.x), "i"(L_SP + 16384) : "scc", "memory");
      GAP();
      asm volatile("" : "+s"(ws), "+s"(ak), "+s"(av), "+s"(ash), "+s"(assmk), "+s"(assmD), "+s"(wo), "+s"(aq), "+s"(out));
    }
    auto addrs = [&]() __attribute__((always_inline)) {
      asm volatile("" : "+v"(lane));
      const int fr = lane & 15, fq = lane >> 4, q = (lane & 15) >> 2, p = lane & 3;
#pragma unroll
      for (int k2 = 0; k2 < 2; ++k2)
        g.aA[k2] = (wid >> 1) * 16384 + (fr >> 3) * 1024 + (fr & 7) * 128 + (((4 * k2 + fq) ^ (fr >> 1)) * 16);
#pragma unroll
      for (int hf = 0; hf < 2; ++hf) {
        const unsigned a0 = 65536 + (wid & 1) * 8192 + off_b(8 * fq + 4 * hf + q, p >> 1) + 8 * (p & 1);
#pragma unroll
        for (int nf = 0; nf < 8; ++nf) { g.aB[hf][nf] = a0 ^ (nf * 32); asm volatile("" : "+v"(g.aB[hf][nf])); }
      }
      GAP();
    };
    auto consts2 = [&]() __attribute__((always_inline)) {
      const int tl = (lane * 37) >> 8, ck = lane - tl * 7;
      vq = lane < 56 ? (tl * DA + ck * 8) * 2 : OOB;
      asm volatile("" : "+v"(vq));
    };
    auto consts = [&](auto Yc) __attribute__((always_inline)) {
      const int fr = lane & 15, fq = lane >> 4;
      if constexpr (Yc) {
        vbA2 = ((((rr >> 2) + 4) * 256 + wid * 8 + (lane >> 3)) * DC + ((lane & 7) ^ (((wid & 1) * 4 + fq) & 7)) * 8) * 2;
        asm volatile("" : "+v"(vbA2));
      } else {
        consts2();
        vw = (4 * wid + fq) * DA * 2 + 16 * (fr ^ ((fq << 2) | wid));
        tb0 = L_SP + off_b(4 * fq + (fr >> 2), wid * 4 + ((lane & 3) >> 1)) + 8 * (lane & 1);
        tp = (lane < 32 && doA ? (const char*)assmk + (j * 56 + cr * 4 + wid) * 4096 : (const char*)ckv + 0x200000 + ((b & 127) * 4 + wid) * 4096) + (lane & 31) * 128;
        asm volatile("" : "+v"(vw), "+v"(tb0), "+v"(tp));
      }
      GAP();
    };
    addrs();
    PA pa;
    const int pu = p_unit(j, rr);
    if (isY) {
      const int rp = tid >> 3, c = tid & 7, ch = tid >> 4, q = tid & 15;
      pa.vo = pu < 0 ? OOB : (((pu & 15) * 128 + rp * 4) * DA + (pu >> 4) * 64 + c * 8) * 2;
      pa.lw = L_SP + 20480 + wid * 1024 + c * 64;
      pa.lx = L_SP + c * 2048 + (((31 - rp) * 8) ^ (c << 4));
      pa.lr = L_SP + ch * 256 + 16 * (q ^ (ch >> 3));
      pa.vs = ch * (SEQ * 2) + q * 16;
      asm volatile("" : "+v"(pa.vo), "+v"(pa.lw), "+v"(pa.lx), "+v"(pa.lr), "+v"(pa.vs));
      consts(std::true_type{});
      asm volatile("" : "=v"(vq), "=v"(vw), "=v"(tb0), "=v"(tp));
    } else {
      consts(std::false_type{});
      asm volatile("" : "=v"(pa.vo), "=v"(pa.lw), "=v"(pa.lx), "=v"(pa.lr), "=v"(pa.vs), "=v"(vbA2));
    }
    {
      const int fr = lane & 15, fq = lane >> 4;
      vseti<88>(fq * 4352 + fr * 8); vseti<89>(lane * 16); vseti<90>(wid * 8192 + lane * 16); vseti<91>(wid * 8192 + lane * 16 + 4096);
      asm volatile("s_getpc_b64 s[100:101]\n\ts_add_u32 s100, s100, at_event@rel32@lo+4\n\ts_addc_u32 s101, s101, at_event@rel32@hi+12" ::: "scc");
      asm volatile("s_lshl_b32 s63, %0, 18\n\tv_add_u32 v115, s63, v85\n\ts_xor_b32 s63, s63, 0x40000\n\tv_add_u32 v116, s63, v85\n\t"
                   "s_mul_i32 s63, %0, s73\n\tv_add_u32 v117, s63, v84\n\ts_sub_u32 s63, s73, s63\n\tv_add_u32 v118, s63, v84\n\t"
                   "s_lshl_b32 s63, %0, 13\n\ts_add_u32 s98, s92, s63\n\ts_xor_b32 s99, s98, 0x2000\n\t"
                   "s_add_u32 s96, s98, s63\n\ts_bitset1_b32 s96, 16\n\ts_xor_b32 s97, s96, 0x4000" ::"s"((rr + (rr >> 2)) & 1) : "scc");
      g_pro<12>(make_iseq<4>{});
      g_part<0, 0>(); g_part<0, 1>(); g_part<0, 2>(); g_part<0, 3>();
      {
        const char* xp;
        asm volatile("s_mov_b64 %0, s[94:95]" : "=s"(xp));
        asm volatile("s_mov_b32 m0, %1\n\ts_mov_b64 exec, 0x3fff\n\tglobal_load_lds_dword %0, off\n\ts_mov_b64 exec, -1"
                     ::"v"(xp + ((b * 4 + wid) * 14 + lane) * 128), "i"(L_SP + 16384) : "memory");
      }
      asm volatile("s_mov_b64 s[56:57], %0\n\ts_mov_b32 s63, %2\n\t"
                   "global_load_dwordx3 v[92:94], %1, off\n\tglobal_load_ushort v95, %3, off\n\tbuffer_load_dwordx2 v[120:121], off, " SW ", s63 sc1"
                   ::"s"(ws), "v"(&PTAB[j].v[r][0][tid][0]), "i"(fwo(6144)), "v"(assmD + j * 56 + (doA ? cr * 4 + wid : 0)) : "memory");
      pa.sk = __builtin_amdgcn_make_buffer_rsrc((void*)ak, (short)0, SEQ * DA * 2, 0x00020000);
      pa.sv = __builtin_amdgcn_make_buffer_rsrc((void*)av, (short)0, SEQ * DA * 2, 0x00020000);
      pa.sw = __builtin_amdgcn_make_buffer_rsrc((void*)(ash + (pu >> 4) * 256), (short)0, pu < 0 ? 0 : 512, 0x00020000);
      asm volatile("" : "+s"(pa.sk), "+s"(pa.sv), "+s"(pa.sw));
    }
    auto start = [&](auto Pc) __attribute__((always_inline)) {
      constexpr int P = Pc;
#define XW0 0
      asm volatile("s_waitcnt vmcnt(%0) lgkmcnt(0)\n\ts_barrier" ::"i"((CTP == 2 ? 13 : 12) + XW0) : "memory");
      g_read0<P>(g, make_iseq<16>{});
      asm volatile("s_waitcnt lgkmcnt(0)\n\ts_barrier" ::: "memory");
      GAP();
    };
    if (isY) {
      start(std::integral_constant<int, 1>{});
      RSV rsv;
      rsv_begin(rsv);
      static_for([&](auto Tc) __attribute__((always_inline)) {
        constexpr int T = Tc;
        q_half<T, 0>(g, pa);
        if constexpr (T == 4) {
          const bf16* w_ = wkv; const int d_ = dq * 512;
          sld<88, A_TAB>(); sld<89, A_TAB + 1>();
          asm volatile("s_mov_b64 s[68:69], %0\n\ts_mov_b32 s70, 0x800000\n\tv_add_u32 v87, %1, v87" ::"s"(w_), "s"(d_));
          code_touch<0>();
        }
        if constexpr (T == 5) {
          const bf16* c_ = ckv; const unsigned v_ = vbA2;
          asm volatile("s_mov_b64 s[64:65], %0\n\ts_mov_b32 s66, 0x400000\n\tv_mov_b32 v86, %1\n\ts_mov_b32 s72, 0x10000\n\ts_mov_b32 s73, 0x20000\n\t"
                       "s_mov_b32 s74, 0x30000\n\ts_mov_b32 s75, 0x40000\n\ts_mov_b32 s76, 0x50000\n\ts_mov_b32 s77, 0x60000\n\ts_mov_b32 s78, 0x70000"
                       ::"s"(c_), "v"(v_));
        }
        GAP();
        q_half<T, 1>(g, pa);
      }, make_iseq<6>{});
      rsv_end(rsv);
      asm volatile("v_lshrrev_b32 %0, 4, v89" : "=v"(lane));
      consts2();
      asm volatile("" : "=v"(vw), "=v"(tb0), "=v"(tp), "=v"(vD));
      GAP();
      STT(0);
      g_half<false, 1, 0, 3>(g);
      g_half<false, 1, 1, 3>(g);
      set_flag<A_FPREP>();
      KST(0, 3);
    }
    unsigned om = 0x7000u | (isY ? 4u : (wid & 1) ? TOUCH1 : TOUCH0);
    asm volatile("" : "+s"(om));
    auto pollG = [&]() __attribute__((always_inline)) {
      sld<62, A_NPB>();
      fl_mask<A_NP, A_NP + 1>();
      asm volatile("s_nop 1");
      fl_issue();
    };
    auto oddG = [&](int t) __attribute__((always_inline)) {
      if (__builtin_expect((om >> t) & 1, 0)) {
        if (t != 13) {
          asm volatile("s_mov_b32 m0, %1\n\ts_nop 0\n\tglobal_load_lds_dword %0, off" ::"v"(tp), "i"(L_SP) : "memory");
        } else {
          fl_check();
          sld<63, A_VBB>(); sld<87, A_MB2>();
          asm volatile("s_mov_b64 s[68:69], %0\n\ts_mov_b32 s70, %1\n\ts_mov_b64 s[88:89], 0\n\tv_add_u32 v87, s63, v89\n\t"
                       "s_movk_i32 s80, 0x400\n\ts_movk_i32 s81, 0x800\n\ts_movk_i32 s82, 0xc00\n\ts_movk_i32 s83, 0x1000\n\t"
                       "s_movk_i32 s84, 0x1400\n\ts_movk_i32 s85, 0x1800\n\ts_movk_i32 s86, 0x1c00"
                       ::"s"(isY ? (const void*)ws : (const void*)assmk), "s"(isY ? 0x6400000 : DA * SEQ * 2));
        }
      }
    };
    auto evenG = [&](int t) __attribute__((always_inline)) {
      if (__builtin_expect((om >> t) & 1, 0)) {
        if (t == 2) set_flag<A_FT2>();
        else if (t == 12) pollG();
        else {
          sld<63, A_VBA>(); sld<79, A_MA2>();
          asm volatile("s_mov_b64 s[64:65], s[56:57]\n\ts_mov_b32 s66, s58\n\tv_add_u32 v87, 0x2000, v87\n\tv_add_u32 v86, s63, v89\n\t"
                       "s_movk_i32 s72, 0x1000\n\ts_movk_i32 s75, 0x4000\n\ts_movk_i32 s76, 0x5000");
          code_touch<1>();
        }
      }
    };
    auto tileG = [&](auto TRc) __attribute__((always_inline)) {
      constexpr bool TR = TRc;
      if (isY) {
        STT(5);
        g_half<TR, 0, 0, 2>(g);
        g_half<TR, 0, 1, 2>(g);
      } else {
        start(std::integral_constant<int, 0>{});
        g_half<TR, 0, 0, 1>(g);
        g_half<TR, 0, 1, 1>(g);
        vD = __float_as_uint(vget<95>());
      }
      g_loop<TR, false>(g, oddG, evenG);
      STT(10); STT(11);
      g_half<TR, 1, 0, 4>(g, wid);
      g_half<TR, 1, 1, 4>(g, wid);
      GAP();
    };
    if (kv) tileG(std::true_type{});
    else tileG(std::false_type{});
    STT(16);
  }
  att_item<3>(); att_item<4>(); att_item<5>(); att_item<6>();
  vseti<95>(wid * 16 + (lane & 15) - 4 * (lane >> 4)); vseti<126>(48 - wid * 16 + (lane & 15) - 4 * (lane >> 4)); vseti<127>(0xff800000u);
  asm volatile("s_mov_b64 s[88:89], %0\n\tv_mov_b32 v87, %1\n\ts_mov_b64 s[96:97], %2\n\tv_writelane_b32 v92, s96, 0\n\tv_writelane_b32 v92, s97, 1" ::"s"(aq), "v"(vq), "s"(out));
  KST(0, 4);
  if (isY) {
    sld<72, A_FCW>(); sld<96, B_MM, 1>(); sld<73, B_OP, 1>();
    asm volatile("s_mov_b32 s97, 0\n\ts_add_u32 m0, s78, 0x2000\n\ts_add_u32 s74, s74, 0x2000\n\t" A_POLL("v[122:123]", "s[96:97]", "s72") ::: "scc", "memory");
    dma4<89, 74>();
  } else if (doA) {
    const int l16 = lane & 15, fq = lane >> 4;
    CV c;
    c.lw = 65536 + wid * 15488;
    asm volatile("ds_write_b128 %0, v[114:117] offset:4096" ::"v"(c.lw + lane * 16));
    fl_mask<-1, A_NQ>();
#pragma unroll
    for (int i = 0; i < 4; ++i) c.pa[i] = c.lw + (((l16 + 1) & 1) ? CV_KS : CV_KP) + 4 * ((8 * fq + l16 + 1) >> 1) + 1024 * i;
    c.pb = c.lw + (2032 - 16 * l16 + 8 * fq) * 2 - 512 * 7;
    KST(0, 5);
    cv_rd0(c, make_iseq<24>{});
    static_for([&](auto I) { vseti<96 + I>(15 - l16 >> 1 == 4 * fq + I ? vD << (l16 & 1 ? 0 : 16) : 0u); }, make_iseq<4>{});
    fl_issue();
    c.vzo = (l16 >> 1) * 3584 + (l16 & 1) * 32 + (fq >> 1) * 16 + (1 - (fq & 1)) * 8 * 3584;
    GAP();
    cv_slots(c, make_iseq<144>{});
    static_for([&](auto T) { cv_slot<144 + T>(c); }, make_iseq<144>{});
    cv_tails(c, make_iseq<28 - cv_z0(288)>{});
    KST(0, 6);
    asm volatile("s_waitcnt vmcnt(%0) lgkmcnt(0)\n\ts_barrier" ::"i"(cv_end()) : "memory");
  } else {
    d_ops<160, 0>(make_iseq<208>{});
    fl_mask<-1, A_NQ>();
    sld<72, A_OQ>();
    fl_wait();
    asm volatile("s_waitcnt lgkmcnt(0)\n\ts_barrier" ::: "memory");
    set_flag<A_FG>();
    static_for([&](auto I) { f_load<128, I>(); if constexpr ((I & 3) == 3) f_next(); }, make_iseq<16>{});
    KST(0, 5);
    fl_mask<A_N01, -1>();
    fl_wait();
    k01_dma<0>(); k01_dma<1>(); k01_dma<2>(); k01_dma<3>();
    KST(0, 6);
    asm volatile("s_waitcnt vmcnt(8)\n\ts_barrier" ::: "memory");
  }
  if (!isY) {
    sld<94, A_WSRC>(); sld<73, B_OP, 1>();
    asm volatile("s_mov_b64 s[64:65], %0\n\ts_mov_b32 s66, 0x380000\n\ts_mov_b32 s67, s59\n\ts_mov_b32 s75, %1\n\t"
                 "v_mov_b32 v84, %2\n\tv_mov_b32 v85, %3\n\tv_mov_b32 v121, %4"
                 ::"s"(wo), "s"(L_SP + wid * 1024), "v"(vw), "v"(tb0), "v"(lane < 28 ? ((b * 4 + wid) * 28 + lane) * 128 : OOB));
  }
  u32x16 rs0, rs1; u32x4 rs2; u32x2 rs3;
#define STA(k)
  {
    AC c;
    asm volatile("rsv2_begin:" : "={v[40:55]}"(rs0), "={v[56:71]}"(rs1), "={v[72:75]}"(rs2), "={v[76:77]}"(rs3));
    const unsigned vml = (lane & 15) * 16;
#pragma unroll 1
    for (int it = r >> 4;; ++it) {
      if (it == r >> 4) KST(0, 8); else KST(0, 11);
      kreads<0>(make_iseq<8>{});
      const int n = prm<B_N, 1>();
      GAP();
      STA(0);
      if (isY) {
        phaseA<0, 2, false>(c);
        STA(1); STA(2);
        phaseB0<true>(false, vml);
      } else {
        if (it) phaseA<0, 3, false>(c);
        else phaseA<0, 1, false>(c);
        STA(1);
        if (c.rare) event();
        STA(2);
        phaseB0<false>(it && n == 2, vml);
      }
      STA(3);
      const int nl = n - it;
#pragma unroll 1
      for (int i = 1; i != nl; i += 2) {
        phaseA<1, 0, false>(c);
        STA(9 + i);
        if (__builtin_expect(c.rare != 0, 0)) { if (c.redo) sm_redo<1>(); event(); }
        phaseB<1>(c);
        if (i + 1 == nl) break;
        phaseA<0, 0, false>(c);
        STA(10 + i);
        if (__builtin_expect(c.rare != 0, 0)) { if (c.redo) sm_redo<0>(); event(); }
        phaseB<0>(c);
      }
      STA(4);
      if (it) break;
      sld<72, B_NXT, 1>(); sld<73, B_OP, 1>();
      epi_init<32768>();
      phaseA<ODD, 0, true>(c);
      if (__builtin_expect(c.redo != 0, 0)) sm_redo<ODD>();
      KST(0, 9);
      const float l[2] = {row_reduce<false>(vget<R_RS>() + vget<R_TS>()), row_reduce<false>(vget<R_RS + 1>() + vget<R_TS + 1>())};
      const f32x4 ml = {vget<R_M>(), l[0], vget<R_M + 1>(), l[1]};
      asm volatile("buffer_store_dwordx4 %0, %1, " SW ", s73 offen sc1" ::"v"(ml), "v"(vml) : "memory");
      phaseF(c, EP{}, make_iseq<74>{});
      asm volatile("s_waitcnt vmcnt(16)" ::: "memory");
      KST(0, 10);
    }
    asm volatile("at_tail:");
    vseti<84>(wid == 3 ? OOB : lane * 16);
    auto sel = [&]() __attribute__((always_inline)) {
      const unsigned one = (lane >> 4) == ((lane & 15) >> 2) ? 0x3f80u << ((lane & 1) * 16) : 0u, s0 = lane & 2 ? 0u : one, s1 = lane & 2 ? one : 0u;
      asm volatile("v_mov_b32 v72, %0\n\tv_mov_b32 v73, %1\n\tv_mov_b64 v[74:75], 0\n\tv_mov_b32 v76, %0\n\tv_mov_b32 v77, %1" ::"v"(s0), "v"(s1));
    };
    auto fin = [&](auto Lc, auto Mc) __attribute__((always_inline)) {
      constexpr int L = Lc, M = Mc;
      EP p;
      phaseAD<L, M == 1>(c, make_iseq<32>{});
      if (__builtin_expect(c.redo != 0, 0)) sm_redo<L>();
      STA(5); STF(4);
      sld<76, B_SO, 1>();
      vseti<85>((wid >> 1) * 16384 + (wid & 1) * 1024 + lane * 16);
      if constexpr (M < 2) p.fa[0] = __builtin_amdgcn_rcpf(row_reduce<false>(vget<R_RS>() + vget<R_TS>()));
      GAP();
      phaseBD<L, M < 2>(c, p, make_iseq<64>{});
      if (__builtin_expect(c.redo != 0, 0)) sm_redo<L ^ 1, 1>();
      STA(6);
      KST(0, 12);
      STF(12);
      asm volatile("s_waitcnt vmcnt(%0)\n\ts_barrier" ::"i"(M < 2 ? 6 + BDX : 0) : "memory");
      STF(13);
      if constexpr (M == 2) { asm volatile("v_add_u32 v107, %0, v90" ::"i"(L_SP)); epi_preads(make_iseq<8>{}); }
      vreads<L>(make_iseq<16>{});
      const float l1 = row_reduce<false>(vget<R_RS + 1>() + vget<R_TS + 1>());
      if constexpr (M < 2) {
        p.fa[1] = __builtin_amdgcn_rcpf(l1);
        asm volatile("s_waitcnt lgkmcnt(0)" ::: "memory");
        phaseFD<L, true>(p, make_iseq<40>{});
      } else {
        const float l[2] = {row_reduce<false>(vget<R_RS>()), l1}, cm[2] = {vget<R_M>(), vget<R_M + 1>()};
        const f32x4 ml = {vget<72>(), vget<73>(), vget<74>(), vget<75>()};
#pragma unroll
        for (int e = 0; e < 2; ++e) {
          const float mm = fmaxf(cm[e], ml[2 * e]);
          const float wa = __builtin_amdgcn_exp2f(cm[e] - mm), wb = __builtin_amdgcn_exp2f(ml[2 * e] - mm);
          const float inv = __builtin_amdgcn_rcpf(l[e] * wa + ml[2 * e + 1] * wb);
          p.fa[e] = wa * inv; p.fb[e] = wb * inv;
        }
        sel();
        static_for([&](auto I) { asm volatile("v_mov_b32 v%c0, v%c1" ::"i"(160 + (int)I), "i"(40 + (int)I)); }, make_iseq<32>{});
        asm volatile("s_waitcnt lgkmcnt(0)" ::: "memory");
        phaseFD<L, false>(p, make_iseq<64>{});
      }
    };
    using I0 = std::integral_constant<int, 0>; using I1 = std::integral_constant<int, 1>; using I2 = std::integral_constant<int, 2>;
    using LY = std::integral_constant<int, ODD ? 0 : 1>;
    if (isY) {
      const unsigned late = prm<B_LM + 1, 1>();
      asm volatile("s_waitcnt vmcnt(12)" ::: "memory");
      const float cm[2] = {vget<R_M>(), vget<R_M + 1>()};
      const f32x4 ml = {vget<72>(), vget<73>(), vget<74>(), vget<75>()};
      if (__builtin_expect(!late && __builtin_amdgcn_ballot_w64(cm[0] != ml[0] || cm[1] != ml[2]) == 0, 1)) {
        vset<R_RS>(vget<R_RS>() + 0.25f * ml[1]); vset<R_RS + 1>(vget<R_RS + 1>() + 0.25f * ml[3]);
        sel();
        asm volatile("v_add_u32 v85, %0, v90" ::"i"(L_SP));
        fin(LY{}, I1{});
      } else fin(LY{}, I2{});
    } else { sel(); fin(I1{}, I0{}); }
    KST(0, 13);
    STF(20);
  }
  {
    sld<64, A_OWN>(); sld<65, A_OWN + 1>(); sld<66, A_OWN + 2>(); sld<67, A_OWN + 3>();
    asm volatile("s_lshl_b32 s80, %0, 12\n\tv_add_u32 v96, s64, v89\n\tv_add_u32 v97, s65, v89\n\tv_add_u32 v98, s66, v89\n\tv_add_u32 v99, s67, v89\n\t"
                 "s_waitcnt lgkmcnt(0)\n\ts_barrier" ::"s"(wid) : "scc", "memory");
    STF(21);
    o_read0(make_iseq<16>{});
    {
      const int fr = lane & 15, fq = lane >> 4;
      const unsigned vo = wid < 3 || !(fq & 1) ? prm<A_OUT>() + (fr * OUTW + (fq & 1) * 16 + (fq >> 1) * 8) * 2 : OOB;
      asm volatile("v_mov_b32 v101, %0\n\tv_add_u32 v102, %1, %0\n\ts_mov_b64 s[88:89], %2\n\ts_mov_b32 s90, 0x380000\n\ts_mov_b32 s91, s59"
                   ::"v"(vo), "s"((j < 4 ? 16 : -16) * OUTW * 2), "s"(out));
      GAP();
    }
    if (wid < 3) {
      static_for([&](auto P) {
        o_interval<2, P>();
        if constexpr (P == 0) STF(22);
        if constexpr (P == 1) STF(23);
        if constexpr (P == 2) STF(24);
        if constexpr (P == 3) STF(25);
        if constexpr (P == 1) KST(0, 27);
        if constexpr (P == 3) KST(0, 28);
      }, make_iseq<15>{});
      o_interval<2, 15>();
      o_slabs<2>();
      o_sum<2>();
    } else {
      sld<62, A_FCW>();
      fl_mask<A_FCM, -1>();
      GAP();
      static_for([&](auto Pc) {
        constexpr int P = Pc;
        o_interval<1, P>();
        if constexpr (P == 0 || P == 2) { fl_wait(); asm volatile("s_lshl_b32 s96, s96, 4" ::: "scc"); }
      }, make_iseq<15>{});
      o_interval<1, 15>();
      o_slabs<1>();
      o_sum<1>();
    }
    asm volatile("rsv2_end:" ::"{v[40:55]}"(rs0), "{v[56:71]}"(rs1), "{v[72:75]}"(rs2), "{v[76:77]}"(rs3));
    KST(0, 29);
#define A_CV(D, S0, S1) "v_cvt_pk_bf16_f32 v" #D ", v" #S0 ", v" #S1 "\n\t"
    asm volatile(A_CV(224, 240, 241) A_CV(226, 244, 245) A_CV(225, 242, 243) A_CV(227, 246, 247) A_CV(228, 248, 249) A_CV(230, 252, 253) A_CV(229, 250, 251) A_CV(231, 254, 255)
                 "v_permlane16_swap_b32_e32 v224, v226\n\tv_permlane16_swap_b32_e32 v225, v227\n\tv_permlane16_swap_b32_e32 v228, v230\n\tv_permlane16_swap_b32_e32 v229, v231\n\t"
                 "buffer_store_dwordx4 v[224:227], v101, s[88:91], 0 offen\n\tbuffer_store_dwordx4 v[228:231], v102, s[88:91], 0 offen" ::: "memory");
    set_flag<A_FE>();
  }
  KST(0, 1);
  asm volatile("s_endpgm\n" A_EVENT "\npf_code_end:" ::: "memory");
}

extern "C" void kernel_launch(void* const* d_in, const int* in_sizes, int n_in, void* d_out, int out_size, void* d_ws,
                              size_t ws_size, hipStream_t stream) {
  const bf16* Aq = (const bf16*)d_in[0];
  const bf16* Ak = (const bf16*)d_in[1];
  const bf16* Av = (const bf16*)d_in[2];
  const bf16* Ash = (const bf16*)d_in[3];
  const bf16* Assmk = (const bf16*)d_in[4];
  const bf16* AssmD = (const bf16*)d_in[5];
  const bf16* Bx = (const bf16*)d_in[6];
  const bf16* Bckv = (const bf16*)d_in[7];
  const bf16* Bwq = (const bf16*)d_in[8];
  const bf16* Bwuk = (const bf16*)d_in[9];
  const bf16* Bwuv = (const bf16*)d_in[10];
  const bf16* Bwo = (const bf16*)d_in[11];
  bf16* out = (bf16*)d_out;
  (void)hipFuncSetAttribute((const void*)fused_kernel, hipFuncAttributeMaxDynamicSharedMemorySize, 163840);
  fused_kernel<<<256, 256, 163840, stream>>>(Bckv, Bx, Bwuk, Bwuv, Bwq, (char*)d_ws + ((ws_size - 0x8000000) & ~(size_t)0x1fffff), Ak, Av, Ash, Assmk, AssmD, Bwo, Aq, out);
}
```

```cpp
#include <hip/hip_runtime.h>
#include <hip/hip_bf16.h>
#include <stdint.h>
#include <utility>
#include <type_traits>

typedef __hip_bfloat16 bf16;
typedef __attribute__((ext_vector_type(4))) short bf16x4;
typedef __attribute__((ext_vector_type(2))) unsigned u32x2;

constexpr int SEQ = 2048;
constexpr int DA = 448;
constexpr int NH = 16;
constexpr int HD = 256;
constexpr int DC = 1024;
constexpr int OUTW = 896;

#define KST(kid, which)

__device__ __forceinline__ float bf2f(unsigned short u) { return __uint_as_float(((unsigned)u) << 16); }
__device__ __forceinline__ unsigned short f2bf(float f) { return __bfloat16_as_ushort(__float2bfloat16(f)); }
__device__ __forceinline__ float rbf(float f) { return bf2f(f2bf(f)); }
typedef __attribute__((ext_vector_type(2))) __bf16 bf16x2n;
typedef __attribute__((ext_vector_type(2))) float f32x2n;
__device__ __forceinline__ unsigned pk2(float a, float b) {
  return __builtin_bit_cast(unsigned, __builtin_convertvector((f32x2n){a, b}, bf16x2n));
}

__device__ __forceinline__ int off_b(int row, int ch) { return 256 * row + 16 * (ch ^ (((row & 3) << 2) | ((row >> 2) & 3))); }

#define DI __device__ __forceinline__
#define SCC_
#define SCC1
#define SCC2
#define SCC3
#define GAP() __builtin_amdgcn_sched_barrier(0)
template <int R> DI float vget() { float t; asm volatile("v_mov_b32 %0, v%c1" : "=v"(t) : "i"(R)); return t; }
template <int R> DI void vset(float x) { asm volatile("v_mov_b32 v%c0, %1" ::"i"(R), "v"(x)); }
template <int R> DI void vseti(unsigned x) { asm volatile("v_mov_b32 v%c0, %1" ::"i"(R), "v"(x)); }
template <int R> DI float acc_read1() { float t; asm volatile("v_accvgpr_read_b32 %0, a[%c1]" : "=v"(t) : "i"(R)); return t; }
template <class F, int... I> DI void static_for(F&& f, std::integer_sequence<int, I...>) {
  (f(std::integral_constant<int, I>{}), ...);
}
template <int... Is> using iseq = std::integer_sequence<int, Is...>;
template <int N> using make_iseq = std::make_integer_sequence<int, N>;
typedef unsigned long long u64;

constexpr unsigned O_K = 1u << 24, O_V = 2u << 24, O_C = 3u << 24, O_P = 4u << 24, O_F = O_P + 128 * 67584, O_S = O_F + 400 * 4352,
                   O_X = O_S + (1u << 24), O_Z = O_X + DA * SEQ * 2, O_W = O_Z + DA * SEQ * 2;
static_assert(O_W + 16 * 262144 <= 0x6400000);
constexpr unsigned fwo(int w) { return O_F + (w >> 4) * 4352 + (w & 15) * 8; }
constexpr unsigned OOB = 0x80000000u, M0X = 0x28000;
constexpr unsigned L_SP = 131072;
#define CMAX 10
#define NYA 15
#define NYB 15
#define MAP4 2
#define TSEQ 1
constexpr int O_SEQ[16] = {0x00, 0x01, 0x02, 0x10, 0x20, 0x30, 0x11, 0x21, 0x31, 0x12, 0x22, 0x32, 0x13, 0x23, 0x33, 0x03};
constexpr int o_k(int P) { int k = 0; for (int q = 0; q < P; ++q) k += O_SEQ[q] >= 16; return O_SEQ[P] >= 16 ? k : -1; }
#define Q44 1
#define KTYV 0x76543210fedcba98ull
#define LATEP 0
#define PKS 3
#define PPOS 99, 10, 9, 9, 9, 8
#define TABX 0xfedcb9875a463210ull, 0xefadc6982b574310ull, 0xfebad7693c285410ull, 0xefcba8764d392510ull
#define K2(c) (2 + (c))
#define POSTM 7
#define NCV 0
#define WP0 8
constexpr int ODD = NYA & 1;
static_assert((NYB & 1) == ODD);
constexpr int NYS[8] = {NYA, NYA, NYA, NYA, NYB, NYB, NYB, NYB};
enum { A_TAB = 0, A_FPREP = 2, A_FT2, A_NP, A_NPB = 6, A_VBB, A_MB2, A_VBA, A_MA2, A_VOO, A_SX, A_MX = 16, A_VOQ = 20, A_FG, A_NQ, A_N01, A_ZO, A_OQ,
       A_WSRC, A_WDST, A_PQ, A_PZ, A_M0F, A_GH, A_PC = 32, A_WLD = 44, A_FC, A_FCW, A_FCM, A_SS, A_FS = 51, A_FSW, A_FSM, A_SL, A_OUT = 57, A_OWN, A_FE = 62, A_GF };
enum { B_N = 0, B_ST, B_MS, B_CT, B_FW, B_FSET, B_NXT, B_SO, B_OP, B_MM, B_PM, C_FCM = 16, C_QT, B_LM, B_LP = 21, B_WD, C_PO, B_WT, B_ACT = 32 };
enum { E_CHK = 1, E_PACK = 4, E_POLL = 8, E_POLL1 = 16, E_MASK = 32, E_FLAG = 64, E_WFLAG = 128, E_POLLZ = 256, E_LOAD = 512, E_POST = 1024, E_PLOAD = 2048, E_CHK1 = 4096 };
constexpr unsigned ev_code(unsigned a) {
  return a == 0 ? 0 : a == E_CHK ? 16 : a == E_FLAG ? 32 : a == (E_FLAG | E_POLL) ? 96 : a == E_POLL ? 56 : a == (E_WFLAG | E_POLLZ) ? 344 : a == E_WFLAG ? 80 : a == (E_FLAG | E_POLLZ) ? 448 : a == E_LOAD ? 400 : a == E_POST ? 512 : a == E_PLOAD ? 768 : a == (E_PLOAD | E_CHK1) ? 912 : a == E_CHK1 ? 872 :
         a == E_PACK ? 128 : a == E_POLL1 ? 192 : a == (E_POLL | E_POLL1 | E_MASK) ? 228 : a == (E_POLL1 | E_MASK) ? 240 : 1u << 31;
}
struct PTab { unsigned v[32][4][64][3]; bool bad; };
constexpr int p_unit(int j, int rr) { return (j & 1) && rr < 4 ? -1 : (j >> 1) * 28 + (j & 1) * 12 + rr; }
constexpr int o_head(int ks, int p) { return 2 * ks + (p >> 1) + 8 * (p & 1); }
constexpr PTab make_tab(int j) {
  PTab t{};
  for (int r = 0; r < 32; ++r) for (int wid = 0; wid < 4; ++wid) {
    auto& e = t.v[r][wid];
    const int rr = r & 15, hsg = rr & 1, pjg = rr >> 1, kv = pjg & 1, gw = j * 128;
    const bool isY = r >= 16;
    const int ks = j >> 1, p = MAP4 ? rr & 3 : (j & 1) * 2 + hsg, h = o_head(ks, p), hsel = h >> 3, ga = (h & 7) * 128;
    const int pj = MAP4 == 1 ? (rr >> 2) * 2 + (j & 1) : MAP4 == 2 ? (rr >> 2) + 4 * (j & 1) : pjg;
    const int mt = (rr >> 2) + (isY ? 4 : 0), cr = rr - (pjg > NCV ? 2 : 0), cch = j * 56 + cr * 4 + wid, NY = NYS[pj];
    const bool doA = !isY && pjg != NCV;
    const unsigned oG = (unsigned)(j + 8 * hsg) << 20;
    const int nx0 = 32 - 2 * pj - NY, cx = nx0 < CMAX ? nx0 : CMAX, ex = nx0 - cx, dg = 30 - 2 * pj;
    int seq[2][32] = {}, ns = 0;
    auto run = [&](int it, int t0, int t1) { for (int q = t0; q < t1; ++q) seq[it][ns++] = q; };
    const int x1 = TSEQ ? dg - ex : 16, c4 = 32 - 8 - NY < CMAX ? 32 - 8 - NY : CMAX;
    if (!isY) {
      run(0, 0, cx); run(0, x1, x1 + ex);
      ns = 0;
      const int s1 = TSEQ >= 2 && nx0 + TSEQ - 2 + 2 <= 2 * pj ? (nx0 < 4 ? 2 : nx0 + TSEQ - 2) : 0;
      run(1, s1, 2 * pj); run(1, 0, s1); run(1, 2 * pj, 2 * pj + 2);
    } else if (ex || !TSEQ) { run(0, cx, x1); run(0, x1 + ex, dg + 2); }
    else { run(0, c4 > cx ? c4 : cx, 16); run(0, cx, c4); run(0, 16, dg + 2); }
    const u64 own = h == j + 8 * hsg && isY ? 1ull << (hsel * 16 + kv * 8 + mt) : 0;
    u64 needB = 0;
    int c1 = 99;
    for (int i = (isY ? NY : nx0) - 1; i >= 0; --i) if (seq[0][i] >= 16) { needB |= 0x101ull << (hsel * 16 + (seq[0][i] >> 2)); c1 = i - 2; }
    needB &= ~own;
    const u64 needQ = (1ull << (32 + hsel * 8 + (pj >> 1))) | (1ull << (32 + hsel * 8 + ((15 - pj) >> 1)));
    const u64 needA = 0x0f0full << (hsel * 16);
    const unsigned oQ = (unsigned)h << 20, oK = oQ + O_K, oP = O_P + (h * 8 + pj) * 67584;
    const unsigned oQ0 = oQ + ((15 - pj) * 128 + wid * 32) * 512, oK0 = oK + seq[0][0] * 32768, lowb = ((wid & 1) + (wid >> 1) * 4) * 4096;
    auto w0 = [&](unsigned x) { return wid == 0 ? x : OOB; };
    const int cls = (rr + (rr >> 2)) & 3;
    const u64 tabX[4] = {TABX};
    const u64 tab = isY ? KTYV : tabX[cls];
    if ((tabX[cls] & 0xfff) != 0x10u + K2(cls) * 0x100) t.bad = true;
    e[C_QT][2] = 0x6543;
    e[A_TAB][0] = (unsigned)tab; e[A_TAB + 1][0] = (unsigned)(tab >> 32);
    const int pu = p_unit(j, rr);
    e[A_FPREP][0] = pu < 0 ? OOB : w0(fwo(6160 + pu));
    const int hq = Q44 ? o_head(ks, rr & 3) : j + 8 * hsg, tq = Q44 ? (rr >> 2) + 4 * (j & 1) : pjg;
    e[A_FT2][0] = isY ? w0(fwo((hq & 7) * 128 + 32 + (hq >> 3) * 8 + tq)) : OOB;
    const u64 np = isY ? needA | needQ : 0xffffull;
    e[A_NP][0] = (unsigned)np; e[A_NP + 1][0] = (unsigned)(np >> 32); e[A_NPB][0] = fwo(isY ? ga : 6160 + (cch >> 6) * 16);
    e[A_VBB][0] = oQ0; e[A_MB2][0] = isY ? 65536 + wid * 1024 : M0X;
    e[A_VBA][0] = oK0 + wid * 1024; e[A_MA2][0] = isY ? wid * 1024 : M0X;
    e[A_VOO][0] = oG + (kv ? O_V : O_K) + mt * 131072 + (wid >> 1) * 65536 + (wid & 1) * (kv ? 8192 : 4096);
    e[A_SX][0] = isY ? oK0 + lowb + 8192 : O_X + cch * 4096; e[A_SX + 1][0] = isY ? oQ0 + 12288 : cch * 4096;
    e[A_SX + 2][0] = oK0 + 32768 + lowb; e[A_SX + 3][0] = oK0 + O_K + wid * 8192;
    e[A_MX][0] = isY ? lowb + 8192 : doA ? 65536 + wid * 15488 : M0X; e[A_MX + 1][0] = isY ? 114688 + wid * 4096 : e[A_MX][0] + 5120;
    e[A_MX + 2][0] = isY ? 32768 + lowb : M0X; e[A_MX + 3][0] = isY ? 65536 + wid * 8192 : M0X;
    e[A_VOQ][0] = ((unsigned)hq << 20) + tq * 131072 + (wid >> 1) * 65536 + (wid & 1) * 4096;
    e[A_FG][0] = w0(fwo(gw + hsg * 16 + kv * 8 + mt));
    e[A_NQ][0] = (unsigned)(needQ >> 32); e[A_N01][0] = 0x0101u << (hsel * 16);
    e[A_ZO][0] = isY ? (pu < 0 ? OOB : O_X + (pu >> 4) * (64 * SEQ * 2) + (15 - (pu & 15)) * 256) : O_Z + j * (56 * SEQ * 2) + (cr * 4 + wid) * 64; e[A_OQ][0] = oQ0;
    const int qbl = isY ? 15 - pj : pj, u = 2 * pj, tile = qbl * 4 + p;
    e[A_WSRC][0] = ((o_head(ks, p ^ (u >> 2)) * 256 + (u & 3) * 64) * DA + p * 112) * 2;
    e[A_WLD][0] = O_W + h * 262144 + wid * 8192;
    e[A_WDST][0] = e[A_WLD][0] + u * 16384;
    e[A_PQ][0] = ((rr * 4 + wid) * 32 * DA + j * 56) * 2; e[A_PZ][0] = O_Z + (j * 64 + rr * 4 + wid) * 3584;
    e[A_M0F][0] = L_SP + wid * 8192; e[C_PO][2] = ((rr * 4 + wid) * 32 * OUTW + j * 56) * 2;
    e[A_GH][0] = fwo(gw + 64);
    auto qo = [&](int i) { return i == 3 ? ks : i + (i >= ks); };
    auto ctxo = [&](int o, int qb, int q) { return O_C + ((unsigned)o_head(ks, p ^ o) << 20) + qb * 65536 + q * 16384 + wid * 4096; };
    auto slab = [&](int tl, int q, int src) { return O_S + tl * 262144 + (q * 8 + src) * 8192 + wid * 2048; };
    for (int q = 0; q < 16; ++q) if (o_k(q) >= 0) e[A_PC + o_k(q)][0] = ctxo(O_SEQ[q] >> 4, qbl, qo(O_SEQ[q] & 3));
    e[A_FC][0] = w0(fwo(1024 + (qbl * 4 + ks) * 16 + p)); e[A_FCW][0] = fwo(1024 + (qbl * 4 + ks) * 16);
    e[A_FCM][0] = 15 & ~(1 << p);
    for (int q = 0; q < 3; ++q) { e[A_SS + q][0] = slab(tile, qo(q), 2 * ks); e[A_SL + q][0] = slab(tile, ks, 2 * qo(q)); }
    e[A_FS][0] = fwo(2048 + (tile * 4 + wid) * 16 + 2 * ks); e[A_FSW][0] = fwo(2048 + (tile * 4 + wid) * 16); e[A_FSM][0] = 0x55 & ~(1 << (2 * ks));
    e[A_OUT][0] = ((qbl * 128 + (ks < 2 ? 2 * ks : 11 - 2 * ks) * 16) * OUTW + DA + p * 112 + wid * 32) * 2;
    for (int q = 0; q < 4; ++q) e[A_OWN + q][0] = qo(q) * 16384;
    e[A_FE][0] = r == 28 && j == 4 ? w0(fwo(6144)) : OOB;
    e[A_GF][0] = fwo(ga);
    int wstep = isY || pj >= PKS ? 99 : 4, wpos = 0;
    for (int it = 0; it < 2; ++it) {
      const int R = 1 + it, n = isY ? NY : it ? 2 * pj + 2 : nx0, qb = isY || !it ? 15 - pj : pj;
      const bool last = isY || it;
      unsigned act[32] = {};
      int np_ = 0;
      auto poll = [&](int i, u64 m) {
        const int l = B_PM + 2 * np_++;
        e[l][R] = (unsigned)m; e[l + 1][R] = (unsigned)(m >> 32);
        if (act[i] & E_POLL) t.bad = true;
        act[i] |= E_POLL; act[i + 1] |= E_CHK;
      };
      if (!it || isY) act[1] |= E_FLAG;
      const bool late = isY && pj < LATEP;
      if (isY) {
        const int pa[8] = {99, 99, PPOS};
        int s_ = late ? n - 2 : pa[pj] + 15 - NY < n - 3 ? pa[pj] + 15 - NY : n - 3;
        int q = 1;
        if ((POSTM >> pj) & 1) {
          act[1] |= E_POLLZ; act[2] = E_LOAD;
          q = c1 == 4 ? 5 : 3;
          act[q] = E_POST;
        }
        while (s_ <= q || (s_ + 1 >= c1 - 1 && s_ <= c1)) ++s_;
        if (c1 < 4 || s_ > n - 3 + late) t.bad = true;
        if (c1 < s_) poll(c1 - 1, needB);
        poll(s_, 1ull << (48 + hsel * 8 + pj));
        act[s_ + 1] = late ? 0 : E_PLOAD;
        if (c1 > s_) poll(c1 - 1, needB);
      }
      else if (!it) { if (n > 2) poll(1, needA); if (c1 < 99) poll(c1 - 1, needB); }
      else if (nx0 <= 2) poll(1, needA);
      if (late) e[B_LM + 1][R] = 1u << (16 + hsel * 8 + pj);
      e[B_LP][R] = oP + wid * 8192;
      const int* sq = seq[isY ? 0 : it];
      if (sq[1] != sq[0] + 1 || (!isY && seq[1][0] + 1 >= 4 && nx0 < 4)) t.bad = true;
      if (last && n > 2) act[n - 3] |= E_POLL1 | E_MASK;
      if (!last && pj == 0) act[n - 2] |= E_POLL1;
      if (last) act[n - 2] |= E_CHK1;
      if (it && !isY && pj >= PKS) wstep = 5, wpos = WP0;
      const int wend = (POSTM >> pj) & 1 ? 6 : 8;
      const int ws_[4] = {E_PACK, wend == 6 ? E_WFLAG : E_WFLAG | E_POLLZ, E_LOAD, E_POST};
      for (int i = 0; i < (last ? n - 3 : n) && !isY; ++i, ++wpos)
        if (wstep < wend && wpos >= WP0 && !act[i] && i) act[i] = ws_[wstep++ - 4];
      if (it && wstep < wend) t.bad = true;
      e[B_WD][R] = pj >= PKS ? e[A_WDST][0] : OOB;
      e[B_WT][R] = O_W + h * 262144 + ((isY ? 4 : 0) + (pj & 3)) * 32768 + wid * 8192;
      e[B_FW][R] = wid < 2 ? fwo((j ^ wid) * 128 + 80 + wid * 16 + rr) : OOB;
      e[B_MS][R] = doA ? e[A_MX][0] + 10304 : M0X; e[B_CT][R] = (rr * 4 + wid) * 128;
      for (int i = 0; i < n; ++i) {
        const unsigned sk = i + 2 < n ? oK + sq[i + 2] * 32768 : last ? O_W + h * 262144 + 32768 : oK + (seq[1][0] + (i & 1)) * 32768;
        e[B_ACT + i][R] = sk | ev_code(act[i]);
        if (ev_code(act[i]) > 1023 || (sk & 1023)) t.bad = true;
      }
      e[B_N][R] = n; e[B_ST][R] = oK + sq[0] * 32768;
      e[B_FSET][R] = isY ? e[A_FG][0] : it ? w0(fwo(ga + 48 + hsel * 8 + pj)) : wid == 1 ? fwo(1024 + (qb * 4 + ks) * 16 + 8 + p) : doA ? w0(fwo(gw + 112 + cr)) : OOB;
      e[B_MM][R] = 256u << p;
      e[B_NXT][R] = last ? e[A_WLD][0] : oQ + (pj * 128 + wid * 32) * 512;
      e[B_SO][R] = last ? oQ + O_C + qb * 65536 : oP;
      e[B_OP][R] = oP + 65536 + wid * 256;
    }
  }
  return t;
}
constexpr PTab PT0 = make_tab(0), PT1 = make_tab(1), PT2 = make_tab(2), PT3 = make_tab(3), PT4 = make_tab(4), PT5 = make_tab(5), PT6 = make_tab(6), PT7 = make_tab(7);
static_assert(!PT0.bad && !PT7.bad);
static __device__ const PTab PTAB[8] = {PT0, PT1, PT2, PT3, PT4, PT5, PT6, PT7};

#define SPINB "0x100000"
#define SW "s[56:59]"
template <int S, int L, int ROW = 0> DI void sld() { asm volatile("v_readlane_b32 s%c0, v%c1, %c2" ::"i"(S), "i"(92 + ROW), "i"(L)); }
template <int L, int ROW = 0> DI unsigned prm() { unsigned x; asm volatile("v_readlane_b32 %0, v%c1, %c2\n\ts_nop 4" : "=s"(x) : "i"(92 + ROW), "i"(L)); return x; }
#define A_FLAG "s_mov_b64 exec, 1\n\tbuffer_store_dwordx2 v[82:83], off, " SW ", s63 sc1\n\ts_mov_b64 exec, -1\n\t"
#define A_POLL(V, M, B) "s_mov_b64 exec, " M "\n\tbuffer_load_dwordx2 " V ", v88, " SW ", " B " offen sc1\n\ts_mov_b64 exec, -1\n\t"
#define A_SPIN(V, M, B) "s_mov_b32 s95, " SPINB "\n\ts_mov_b64 exec, " M "\n" \
    "8:\n\tbuffer_load_dwordx2 " V ", v88, " SW ", " B " offen sc1\n\ts_waitcnt vmcnt(0)\n\tv_cmp_eq_u64 vcc, s[60:61], " V "\n\t" \
    "s_cmp_eq_u64 vcc, " M "\n\ts_cbranch_scc1 9f\n\ts_sleep 4\n\ts_sub_u32 s95, s95, 1\n\ts_cbranch_scc0 8b\n9:\n\ts_mov_b64 exec, -1\n\t"
#define A_CHECK(V, M, B) "v_cmp_eq_u64 vcc, s[60:61], " V "\n\ts_and_b64 vcc, vcc, " M "\n\ts_cmp_eq_u64 vcc, " M "\n\ts_cbranch_scc1 9f\n\t" A_SPIN(V, M, B)
#define A_CTOUCH(B, E, M) "v_readlane_b32 s63, v93, 3\n\ts_sub_u32 s96, s100, at_event - " B "\n\ts_subb_u32 s97, s101, 0\n\t" \
    "s_mov_b32 s98, " E " - " B "\n\ts_mov_b32 s99, s59\n\tv_lshl_add_u32 %0, v89, 9, s63\n\t" \
    "s_mov_b32 m0, 0x27f00\n\ts_mov_b64 exec, " M "\n\tbuffer_load_dword %0, s[96:99], 0 offen lds\n\ts_mov_b64 exec, -1\n\t"
template <int K> DI void code_touch() {
  unsigned t;
  if constexpr (K) asm volatile(A_CTOUCH("ct_begin", "at_tail", "0xff") : "=&v"(t) :: "scc", "memory");
  else asm volatile(A_CTOUCH("ct_begin", "rsv2_begin", "0x1f") : "=&v"(t) :: "scc", "memory");
}
template <int L, int ROW = 0> DI void set_flag() {
  asm volatile("v_readlane_b32 s63, v%c0, %c1\n\ts_nop 3\n\t" A_FLAG ::"i"(92 + ROW), "i"(L) : "memory");
}
template <int LO, int HI> DI void fl_mask() {
  if constexpr (LO >= 0) sld<96, LO>(); else asm volatile("s_mov_b32 s96, 0");
  if constexpr (HI >= 0) sld<97, HI>(); else asm volatile("s_mov_b32 s97, 0");
}
DI void fl_issue() { asm volatile(A_POLL("v[122:123]", "s[96:97]", "s62") ::: "memory"); }
DI void fl_check() { asm volatile(A_CHECK("v[122:123]", "s[96:97]", "s62") ::: "vcc", "scc", "memory"); }
DI void fl_wait() { asm volatile("s_nop 4\n\t" A_SPIN("v[122:123]", "s[96:97]", "s62") ::: "vcc", "scc", "memory"); }

typedef __attribute__((ext_vector_type(4))) unsigned u32x4c;
typedef __attribute__((ext_vector_type(4))) float f32x4c;
typedef __attribute__((ext_vector_type(2))) float f32x2c;
__device__ __forceinline__ float bfl(unsigned u) { return __uint_as_float(u << 16); }
__device__ __forceinline__ float bfh(unsigned u) { return __uint_as_float(u & 0xffff0000u); }

#define CODE_TOUCH(LB, LE, wx, NL, m0v) do { unsigned blo_, bhi_, elo_; \
    asm volatile("s_getpc_b64 s[100:101]\n\ts_add_u32 s100, s100, " LB "@rel32@lo+4\n\ts_addc_u32 s101, s101, " LB "@rel32@hi+12\n\t" \
                 "s_mov_b32 %0, s100\n\ts_mov_b32 %1, s101\n\t" \
                 "s_getpc_b64 s[100:101]\n\ts_add_u32 s100, s100, " LE "@rel32@lo+4\n\ts_mov_b32 %2, s100" \
                 : "=s"(blo_), "=s"(bhi_), "=s"(elo_) :: "scc"); \
    const unsigned n_ = elo_ - (blo_ & ~127u); \
    const __amdgpu_buffer_rsrc_t srd_ = __builtin_amdgcn_make_buffer_rsrc((void*)(((unsigned long long)bhi_ << 32) | (blo_ & ~127u)), (short)0, n_ < 262144u ? n_ : 0u, 0x00020000); \
    const unsigned l_ = threadIdx.x & 63, vo_ = l_ < (NL) ? ((wx) + l_ * 128u) * 128u : 0x80000000u; \
    asm volatile("s_mov_b32 m0, %0\n\ts_nop 4\n\tbuffer_load_dword %1, %2, 0 offen lds" ::"s"(m0v), "v"(vo_), "s"(srd_) : "memory"); } while (0)

#define STT(k)
#define SNK 31
#define GSC " sc1"
#define PL0 66
#define PLS 7
#define PCAP 0x55555555
#define PST 272
#define ECAP 3
#define EPACE_K 7
#define EPACE_V 6
typedef __attribute__((ext_vector_type(4))) float f32x4g;
typedef __attribute__((ext_vector_type(4))) unsigned u32x4g;
DI u32x4g pair16(f32x4g x, f32x4g y) {
  const unsigned a0 = pk2(x[0], x[1]), a1 = pk2(x[2], x[3]), b0 = pk2(y[0], y[1]), b1 = pk2(y[2], y[3]);
  auto r0 = __builtin_amdgcn_permlane16_swap(a0, b0, false, false), r1 = __builtin_amdgcn_permlane16_swap(a1, b1, false, false);
  return (u32x4g){r0[0], r1[0], r0[1], r1[1]};
}
constexpr int LDW = NH * HD;
struct GM { unsigned aA[2], aB[2][8]; };
#define GDP 8, 12, 24, 28, 40, 44, 56, 60
constexpr int GDPOS[8] = {GDP};
constexpr int g_j(int G) { for (int i = 0; i < 8; ++i) if (GDPOS[i] == (G & 63)) return i; return -1; }
constexpr int GD13 = 125 - GDPOS[3];
struct PA { unsigned vo, lw, lx, lr, vs; __amdgpu_buffer_rsrc_t sk, sv, sw; };
struct POp { int k, d, a, b; };
struct PSch { POp op[760]; int first[770], vm[770], n; };
constexpr PSch p_sched() {
  PSch z{};
  POp l[760] = {};
  int n = 0, early[760] = {};
  auto put = [&](int k, int d, int a, int b, int e = 0) { l[n] = POp{k, d, a, b}; early[n] = e; ++n; };
  for (int q = 0; q < 10; ++q) put(10, q < 6 ? q : q + 2, 0, 0, PL0 + PLS * q);
  for (int c = 0; c < 8; ++c) {
    const int I = c >> 1, u = c & 1;
    for (int t = 0; t < 4; ++t) put(t & 1, 112 + t, 76 + 2 * u + (t >> 1), 0, c ? 0 : PST);
    if (u && I < 3) put(11, I + 1, 0, 0);
    for (int q = 0; q < 7; ++q) put(u, 116 + q, 32 + 4 * q + I, 0);
    for (int e = 2; e >= 0; e -= 2) {
      put(2, 125, 117 + e, 115); put(2, 123, 116 + e, 115);
      for (int t = 1; t < 4; ++t) { put(4, 125, 117 + e + t, 115 - t); put(4, 123, 116 + e + t, 115 - t); }
      const int T = e ? 126 : 127;
      put(u, 124, 64 + 4 * e + I, 0); put(u, T, 60 + 4 * e + I, 0);
      put(2, 125, 125, 124); put(2, 123, 123, T);
      put(5, T, 125, 123);
    }
    put(12, c, 0, 0, (PST + 2) / 64 * 64 + 62);
  }
  const int nm = n;
  for (int q = 0; q < 4; ++q) put(13, q, 0, 0);
  for (int q = 0; q < 4; ++q) put(14, q, 0, 0);
  int g = 0, i = 0, hold = 0;
  for (; g < 768; ++g) {
    const int q = g & 15;
    int cap = g_j(g) >= 0 ? 0 : (PCAP >> (2 * q)) & 3;
    z.first[g] = z.n; z.vm[g + 1] = z.vm[g];
    if (g == PST - 16) z.op[z.n++] = POp{11, 0, 0, 0};
    if (g == PST - 20) z.op[z.n++] = POp{15, 0, 0, 0};
    if (g >= PST - 16 && g < PST - 13) for (int r = (g - PST + 16) * 7 / 3; r < (g - PST + 17) * 7 / 3; ++r) z.op[z.n++] = POp{16, r, 0, 0};
    while (cap > 0 && i < n && early[i] <= g && hold <= g) {
      const int k = l[i].k;
      if ((k == 11 || k == 13) && q > 2) { if (k == 13) break; }
      z.op[z.n++] = l[i];
      if (k == 10 || k == 14) { ++z.vm[g + 1]; cap = 0; hold = g + 3; }
      if (i == nm - 1) hold = (g + 3) / 64 * 64 + 64;
      if (i == nm + 3) hold = g / 16 * 16 + 16;
      --cap; ++i;
    }
  }
  z.first[768] = z.n;
  if (i < n) z.n = -1;
  return z;
}
constexpr PSch PS = p_sched();
constexpr int p_last() { int g = 0; while (PS.first[g] < PS.n) ++g; return g; }
static_assert(PS.n > 0 && PS.vm[(PST - 16) / 64 * 64 - 3 - GD13] == 10 && PST % 16 == 0);
template <int I> DI void p_op(const PA& p) {
  constexpr POp o = PS.op[I];
  if constexpr (o.k == 0) asm volatile("v_lshlrev_b32_e32 v%c0, 16, v%c1" ::"i"(o.d), "i"(o.a));
  else if constexpr (o.k == 1) asm volatile("v_and_b32_e32 v%c0, s94, v%c1" ::"i"(o.d), "i"(o.a));
  else if constexpr (o.k == 2) asm volatile("v_mul_f32_e32 v%c0, v%c1, v%c2" ::"i"(o.d), "i"(o.a), "i"(o.b));
  else if constexpr (o.k == 4) asm volatile("v_fmac_f32_e32 v%c0, v%c1, v%c2" ::"i"(o.d), "i"(o.a), "i"(o.b));
  else if constexpr (o.k == 5) asm volatile("v_cvt_pk_bf16_f32 v%c0, v%c1, v%c2" ::"i"(o.d), "i"(o.a), "i"(o.b));
  else if constexpr (o.k == 10 && o.d == 0)
    asm volatile("s_mov_b32 s93, m0\n\ts_add_u32 m0, s79, %1\n\ts_mov_b32 s94, 0xffff0000\n\tbuffer_load_dwordx4 v89, %0, 0 offen lds\n\ts_mov_b32 m0, s93"
                 ::"s"(p.sw), "i"(L_SP + 20480) : SCC_ "memory");
  else if constexpr (o.k == 10 && o.d < 6) {
    if constexpr (o.d == 1) asm volatile("v_and_b32 v113, 0x380, v89\n\tv_mul_u32_u24 v114, 21, v113\n\tv_sub_u32 v114, %0, v114\n\ts_mul_i32 s95, s79, 5" ::"v"(p.vo));
    asm volatile("v_add_u32 v112, %1, v114\n\ts_mov_b32 s93, m0\n\ts_add_u32 m0, s95, %2\n\ts_mov_b32 exec_lo, %3\n\ts_mov_b32 exec_hi, %4\n\t"
                 "buffer_load_dwordx4 v112, %0, 0 offen lds\n\ts_mov_b64 exec, -1\n\ts_mov_b32 m0, s93"
                 ::"s"(p.sk), "i"((o.d - 2) * 8 * DA * 2), "i"(L_SP + (o.d - 1) * 1024), "i"(o.d == 1 ? 0 : -1), "i"(o.d == 1 ? -256 : -1) : SCC_ "memory");
  } else if constexpr (o.k == 10)
    asm volatile("buffer_load_dwordx4 v[%c0:%c1], %2, %3, 0 offen offset:%c4" ::"i"(28 + 4 * o.d), "i"(31 + 4 * o.d), "v"(p.vo), "s"(p.sv), "i"((o.d & 3) * DA * 2) : "memory");
  else if constexpr (o.k == 15) asm volatile("v_mad_u32_u24 v113, v113, 3, v89\n\tv_add_u32 v113, s95, v113\n\tv_add_u32 v113, %0, v113" ::"i"(L_SP + 640));
  else if constexpr (o.k == 16) asm volatile("ds_read_b128 v[%c0:%c1], v113 offset:%c2" ::"i"(32 + 4 * o.d), "i"(35 + 4 * o.d), "i"(o.d * 128));
  else if constexpr (o.k == 11) asm volatile("ds_read_b128 v[76:79], %0 offset:%c1" ::"v"(p.lw), "i"(o.d * 16));
  else if constexpr (o.k == 12) asm volatile("ds_write_b64 %0, v[126:127] offset:%c1" ::"v"(p.lx), "i"(o.d * 256));
  else if constexpr (o.k == 13)
    asm volatile("v_xor_b32 v112, %3, %2\n\tds_read_b128 v[%c0:%c1], v112 offset:%c4" ::"i"(32 + 4 * o.d), "i"(35 + 4 * o.d), "v"(p.lr), "i"(o.d * 32), "i"(o.d * 4096));
  else
    asm volatile("buffer_store_dwordx4 v[%c0:%c1], %2, " SW ", s93 offen sc1\n\ts_add_u32 s93, s93, 0x10000" ::"i"(32 + 4 * o.d), "i"(35 + 4 * o.d), "v"(p.vs) : SCC_ "memory");
}
template <int B, int... Is> DI void p_ops_(const PA& p, iseq<Is...>) { (p_op<B + Is>(p), ...); }
template <int Gg> DI void p_ops(const PA& p) { p_ops_<PS.first[Gg]>(p, make_iseq<PS.first[Gg + 1] - PS.first[Gg]>{}); }
constexpr float CS = 0.0625f * 1.4426950408889634f;
struct EOp { int k, d, s, g; };
struct ESch { EOp op[640]; int first[260], st[260], n; bool ok; };
constexpr int e_load(int g) { return g >= 64 ? ((g & 7) == 4 ? g >> 3 : -1) : g % 5 != 4 || g > 59 ? -1 : g < 40 ? g / 5 : g / 5 + 8; }
constexpr int e_nld(int g0, int g1) { int n = 0; for (int g = g0; g < g1; ++g) n += e_load(g) >= 0; return n; }
constexpr int e_qrd(int g) { return g >= 32 && g < 48 && (g & 1) ? (g - 32) / 2 : g >= 104 && g < 120 && (g & 1) ? 8 + (g - 104) / 2 : -1; }
#define PARK 0
#define EPACE_Q 0
constexpr ESch e_sched(int MODE) {
  ESch t{}, z{};
  int cap[260] = {}, used[260] = {}, dead[640] = {};
  const int NG = MODE == 0 ? 256 : 128;
  for (int g = 0; g < NG; ++g) {
    const int q = g & 15;
    if (MODE == 0) cap[g] = g_j(g) >= 0 ? 1 : (q == 1 || q == 2 || q == 3 || q == 5 || q == 7 || q == 9) ? 2 : q == 14 ? 4 : 3;
    else cap[g] = g < 16 ? 1 : (q == 1 || q == 2 || e_load(g) >= 0 || e_qrd(g) >= 0) ? 1 : 2;
  }
  cap[NG] = 1000;
  int n = 0, g = 0;
  auto put = [&](int k, int d, int s_, int early, int dl) {
    if (g < early) g = early < NG ? early : NG;
    while (used[g] >= cap[g]) ++g;
    ++used[g]; if (k == 3) ++used[g];
    t.op[n] = EOp{k, d, s_, g}; dead[n] = dl; ++n;
  };
  auto av = [](int m, int nf) { return 16 * m + 2 * nf + 10; };
  for (int j = 0; j < (MODE == 0 ? 32 : 16); ++j) {
    const int m0 = MODE == 2 ? (j >> 3) * 2 : j >> 2, n0 = MODE == 2 ? j & 7 : (j & 3) * 2;
    const int m1 = MODE == 2 ? m0 + 1 : m0, n1 = MODE == 2 ? n0 : n0 + 1;
    const int T = j < 16 ? 128 + 8 * j : 112 + 8 * (j & 1), dl0 = 128 + 32 * (j >> 3) + 2 * (j & 7) - 1, dl1 = j < 16 ? dl0 : 999;
    const int pace = MODE == 0 ? j * EPACE_Q : (MODE == 1 ? 14 : 26) + j * (MODE == 1 ? EPACE_K : EPACE_V);
    const bool park = MODE == 0 && j >= 32 - PARK;
    if (j >= 16) for (int k = 0; k < 8; ++k) put(0, T + k, 8 * (j - 16) + k, k < 4 ? av(m0, n0) : av(m1, n1), dl0);
    for (int h = 0; h < 2; ++h) {
      const int e = h ? av(m1, n1) : av(m0, n0);
      if (MODE == 0) for (int k = 0; k < 4; ++k) put(1, T + 4 * h + k, 0, e, dl1);
      for (int k = 0; k < 2; ++k) put(2, (park ? 32 + 4 * (j - (32 - PARK)) : T) + 2 * h + k, T + 4 * h + 2 * k, e, dl1);
    }
    if (!park) put(3, T, j, j < 16 || MODE || EPACE_Q ? pace : 0, dl1);
  }
  z.ok = true;
  for (int q = 0; q <= NG; ++q) {
    z.first[q] = z.n; z.st[q + 1] = z.st[q];
    for (int i = 0; i < n; ++i) if (t.op[i].g == q) {
      z.op[z.n++] = t.op[i];
      if (t.op[i].k == 3) ++z.st[q + 1];
      if (MODE == 0 && q > dead[i]) z.ok = false;
    }
  }
  z.first[NG + 1] = z.n;
  return z;
}
constexpr ESch ES[3] = {e_sched(0), e_sched(1), e_sched(2)};
static_assert(ES[0].ok && ES[0].first[256] == ES[0].n && ES[1].st[126] == 16 && ES[2].st[126] == 16);
template <int MODE, int I> DI void e_op() {
  constexpr EOp o = ES[MODE].op[I];
  if constexpr (o.k == 0) asm volatile("v_accvgpr_read_b32 v%c0, a[%c1]" ::"i"(o.d), "i"(o.s));
  else if constexpr (o.k == 1) asm volatile("v_mul_f32_e32 v%c0, s94, v%c0" ::"i"(o.d));
  else if constexpr (o.k == 2) asm volatile("v_cvt_pk_bf16_f32 v%c0, v%c1, v%c2" ::"i"(o.d), "i"(o.s), "i"(o.s + 1));
  else {
    constexpr int m = o.s >> 2;
    if constexpr ((o.s & 3) == 0)
      asm volatile("s_mov_b32 s93, %0" ::"i"(MODE == 2 ? (m >> 1) * 16384 + (m & 1) * 4096 : MODE == 1 ? m * 8192 : (m < 4 ? 2 * m : 15 - 2 * m) * 8192));
    asm volatile("buffer_store_dwordx4 v[%c0:%c1], v%c2, " SW ", s93 offen offset:%c3 sc1" ::"i"(o.d), "i"(o.d + 3), "i"(MODE ? 84 : 95), "i"((o.s & 3) * 1024) : "memory");
  }
}
template <int MODE, int B, int... Is> DI void e_ops_(iseq<Is...>) { (e_op<MODE, B + Is>(), ...); }
template <int MODE, int Gg> DI void e_ops() { e_ops_<MODE, ES[MODE].first[Gg]>(make_iseq<ES[MODE].first[Gg + 1] - ES[MODE].first[Gg]>{}); }
template <int P, int KS, int I> DI void g_wread(const GM& g) {
  constexpr int nf = I >> 1, hf = I & 1, r = 128 + 64 * P + 8 * nf + 4 * KS + 2 * hf;
  asm volatile("ds_read_b64_tr_b16 a[%c1:%c2], %0 offset:%c3" ::"v"(g.aB[hf][nf]), "i"(r), "i"(r + 1), "i"(P * 32768 + KS * 16384));
}
template <int P, int M, int KS> DI void g_aread(const GM& g) {
  constexpr int r = 96 + 8 * (M & 1) + 4 * KS;
  asm volatile("ds_read_b128 v[%c1:%c2], %0 offset:%c3" ::"v"(g.aA[KS]), "i"(r), "i"(r + 3), "i"(P * 32768 + M * 2048));
}
template <bool W, int V, int J> DI void g_dma() {
  if constexpr (W) {
    if constexpr (J == 0) asm volatile("buffer_load_dwordx4 v%c0, s[68:71], 0 offen" GSC " lds" ::"i"(V) : "memory");
    else asm volatile("buffer_load_dwordx4 v%c0, s[68:71], s%c1 offen" GSC " lds" ::"i"(V), "i"(79 + J) : "memory");
  } else {
    if constexpr (J == 0) asm volatile("buffer_load_dwordx4 v%c0, s[64:67], 0 offen" GSC " lds" ::"i"(V) : "memory");
    else asm volatile("buffer_load_dwordx4 v%c0, s[64:67], s%c1 offen" GSC " lds" ::"i"(V), "i"(71 + J) : "memory");
  }
}
template <int Q> DI void att_item() {
  if constexpr (Q == 0) sld<83, B_ST, 1>();
  if constexpr (Q == 1) asm volatile("s_mov_b32 s85, %0" ::"i"(B_PM));
  if constexpr (Q == 2) asm volatile("s_add_u32 s84, s83, 0x8000\n\ts_mov_b32 s86, %0" ::"i"(B_ACT) SCC1);
  if constexpr (Q >= 3 && Q < 6) asm volatile("v_mov_b64 v[%c0:%c1], 0" ::"i"(108 + 2 * Q), "i"(109 + 2 * Q));
  if constexpr (Q == 6) asm volatile("v_mov_b32 v120, 0x45800000");
}
template <int I> DI void g_pro1();
template <int K, int I> DI void g_part() {
  constexpr bool W = K < 2;
  asm volatile("s_add_u32 m0, s%c0, %1\n\ts_nop 0" ::"i"(96 + K), "i"(W ? I * 4096 : (I & 1) * 4096 + (I >> 1) * 16384) : "scc");
  g_dma<W, 115 + K, W ? I : (I & 1) + (I >> 1) * 4>();
}
template <int Y, int I> DI void g_tag() {
  if constexpr (I == 0) { if constexpr (Y) sld<88, C_QT, 2>(); else { sld<88, A_TAB>(); sld<89, A_TAB + 1>(); } }
  if constexpr (I == 1 && !Y) asm volatile("s_lshr_b64 s[88:89], s[88:89], 12" SCC3);
  if constexpr (I == 3) asm volatile("v_readfirstlane_b32 s60, v120");
  if constexpr (I == 5) asm volatile("v_readfirstlane_b32 s61, v121");
  if constexpr (I == 9) asm volatile("s_mul_i32 s61, s61, 0x4c957f2d\n\ts_mul_i32 s63, s60, 0x5851f42d");
  if constexpr (I == 11) asm volatile("s_add_u32 s61, s61, s63\n\ts_mul_hi_u32 s63, s60, 0x4c957f2d" SCC3);
  if constexpr (I == 13) asm volatile("s_add_u32 s61, s61, s63\n\ts_mul_i32 s60, s60, 0x4c957f2d" SCC3);
  if constexpr (I == 15) asm volatile("s_add_u32 s60, s60, 0xf767814f\n\ts_addc_u32 s61, s61, 0x14057b7e" SCC3);
  if constexpr (I == 17) asm volatile("v_mov_b64 v[82:83], s[60:61]");
}
template <bool TR, int G> DI void g_last_setup(int wid) {
  if constexpr (G == 0) { sld<72, A_SX>(); sld<76, A_MX>(); }
  if constexpr (G >= 1 && G < 4) sld<72 + G, A_SX + G>();
  if constexpr (G == 3) sld<63, A_VOO>();
  if constexpr (G >= 5 && G < 8) sld<72 + G, A_MX + G - 4>();
  if constexpr (G == 6) asm volatile("v_add_u32 v84, s63, v89");
  if constexpr (G == 7) asm volatile("s_lshl_b32 s63, %0, 10\n\ts_lshl_b32 s95, %0, 12" ::"s"(wid) SCC1);
  if constexpr (G == 8) asm volatile("v_add_u32 v85, %0, v84\n\tv_add_u32 v86, 0x10000, v89" ::"i"(TR ? 4096 : 8192));
  if constexpr (G == 9) asm volatile("v_add_u32 v87, s63, v86\n\tv_add_u32 v95, s95, v86");
  if constexpr (G == 10) asm volatile("s_lshl_b32 s80, %0, 13\n\ts_bitset1_b32 s80, 16" ::"s"(wid) SCC1);
  if constexpr (G == 11) asm volatile("s_add_u32 s81, s80, 0x8000\n\ts_mov_b32 s90, 0x8000" SCC3);
  if constexpr (G == 13) { sld<62, A_GF>(); asm volatile("s_mov_b32 s91, 0xc000"); }
  if constexpr (G == 14) { att_item<0>(); att_item<1>(); }
  if constexpr (G == 15) { att_item<2>(); sld<92, A_GH>(); }
  if constexpr (G == 12) { sld<87, B_MS, 1>(); asm volatile("s_add_u32 s82, s73, 2" SCC3); }
}
template <bool TR, int P, int H, int G, int MD, int PT> DI void g_gap(const GM& g, int wid, const PA& pa) {
  constexpr bool FIRST = MD == 1 || MD == 2, LAST = MD == 4;
  constexpr int m = 4 * H + (G >> 4), q = G & 15, n = (SNK & 8) && MD < 2 && (m & 1) ? 7 - (q >> 1) : q >> 1, ks = (SNK & 4 ? q ^ (q >> 1) : q) & 1;
  constexpr int a = 96 + 8 * (m & 1) + 4 * ks, b = 128 + 64 * P + 8 * n + 4 * ks, d0 = TR ? 64 * (m >> 1) + 8 * n + 4 * (m & 1) : 32 * m + 4 * n, d = m < 4 ? 128 + d0 : d0 - 128;
  constexpr bool ST0 = MD == 1 && H == 0;
  constexpr int J = LAST ? -1 : ST0 ? (G >= 48 && G % 4 == 0 ? G / 4 - 8 : -1) : g_j(G);
  constexpr int gj = (J & 3) ^ ((J & 3) >> 1);
  constexpr int EM = LAST ? 1 + TR : 0, eg = (MD == 2 ? 128 : 0) + 64 * H + G, L = LAST ? e_load(eg) : -1;
  if constexpr (MD == 1 && G == 32) asm volatile("s_waitcnt vmcnt(%0) lgkmcnt(%1)\n\ts_barrier" ::"i"(H ? 12 + (PT >= 0 ? PS.vm[eg] : 0) : 19), "i"(8 * H) : "memory");
  else if constexpr (q == 0 && G > 0) asm volatile("s_waitcnt lgkmcnt(%0)" ::"i"(LAST ? 0 : MD == 1 ? 8 * H : 4));
  if constexpr (J == 0) asm volatile("s_add_u32 m0, s79, %0" ::"i"((P ^ H ^ 1) * 32768 + (1 - H) * 8192) SCC1);
  else if constexpr (J == 4) asm volatile("s_add_u32 m0, s87, %0" ::"i"((P ^ H) * 32768 + (1 - H) * 16384) SCC1);
  else if constexpr (J > 0 && (J & 3) == 1) asm volatile("s_bitset1_b32 m0, 12");
  else if constexpr (J > 0 && (J & 3) == 2) asm volatile("s_bitset1_b32 m0, %0" ::"i"(J < 4 ? 14 : 13));
  else if constexpr (J > 0) asm volatile("s_bitset0_b32 m0, 12");
  if constexpr (L >= 0 && (L & 3) == 0) asm volatile("s_mov_b32 m0, s%c0" ::"i"(L < 16 ? 76 + (L >> 2) : 87));
  constexpr int QR = LAST ? e_qrd(eg) : -1;
  if constexpr (QR == 0) asm volatile("s_waitcnt vmcnt(%0)" ::"i"(8 + e_nld(0, eg) + ES[EM].st[eg]) : "memory");
  if constexpr (QR == 8) asm volatile("s_waitcnt vmcnt(%0)" ::"i"(e_nld(64, eg) + ES[EM].st[eg] - ES[EM].st[60]) : "memory");
#define GMF(D, X, Y, C) asm volatile("v_mfma_f32_16x16x32_bf16 " D "[%c0:%c1], " X ", " Y ", " C ::"i"(d), "i"(d + 3), "i"(a), "i"(a + 3), "i"(b), "i"(b + 3))
#define GMF2(D, C) do { if constexpr (TR) GMF(D, "v[%c2:%c3]", "a[%c4:%c5]", C); else GMF(D, "a[%c4:%c5]", "v[%c2:%c3]", C); } while (0)
  if constexpr (FIRST && !(q & 1)) { if constexpr (m < 4) GMF2("v", "0"); else GMF2("a", "0"); }
  else if constexpr (m < 4) GMF2("v", "v[%c0:%c1]");
  else GMF2("a", "a[%c0:%c1]");
#undef GMF2
#undef GMF
  if constexpr (J >= 0 && J < 4) g_dma<false, 84, (1 - H) * 2 + (gj & 1) + (gj >> 1) * 4>();
  else if constexpr (J >= 4) g_dma<true, 85, (1 - H) * 4 + gj>();
  if constexpr (L >= 0)
    asm volatile("buffer_load_dwordx4 v89, s[%c0:%c1], s%c2 offen offset:%c3 sc1 lds" ::"i"(L >= 4 && L < 8 || L >= 16 ? 68 : 56), "i"(L >= 4 && L < 8 || L >= 16 ? 71 : 59),
                 "i"(L < 16 ? 72 + (L >> 2) : 82), "i"((L & 3) * 1024) : "memory");
  if constexpr (QR >= 0)
    asm volatile("ds_read_b128 a[%c1:%c2], v%c0 offset:%c3" ::"i"(QR < 12 ? 87 : 95), "i"(128 + 4 * QR), "i"(131 + 4 * QR), "i"(QR < 12 ? QR * 4096 : 49152 + (QR - 12) * 1024));
  if constexpr ((q == 1 || q == 2) && !(LAST && m == 7)) g_aread<(m < 7 ? P : P ^ 1), (m + 1) & 7, q - 1>(g);
  if constexpr (!LAST) {
    if constexpr (MD == 1 && H == 1 && q >= 3 && q < 11) g_wread<P ^ 1, (G >> 5), ((G >> 4) & 1) * 8 + q - 3>(g);
    if constexpr (MD != 1 && q >= 3 && q < 11 && (q & 1)) g_wread<P ^ 1, H, (G >> 4) * 4 + (q - 3) / 2>(g);
    if constexpr (MD == 1 && eg >= 62 && eg < 80) g_tag<P, eg - 62>();
    if constexpr (H == 1 && G == 0) asm volatile("s_mov_b32 s90, s91");
    if constexpr (H == 1 && G == 4) asm volatile("v_lshl_add_u32 v84, s90, 7, v86\n\ts_bfe_u32 s91, s88, 0x40000" SCC3);
    if constexpr (H == 1 && G == 6) asm volatile("s_lshr_b64 s[88:89], s[88:89], 4\n\ts_lshl_b32 s92, s91, 19" SCC3);
    if constexpr (H == 1 && G == 10) asm volatile("v_add_u32 v85, s92, v87");
  } else if constexpr (eg < 16) g_last_setup<TR, eg>(wid);
  if constexpr (ST0 && G == 23) asm volatile("v_lshl_add_u32 v85, s91, 19, v87");
  if constexpr (ST0 && G < 36 && G % 3 == 0) g_part<G < 12 ? 2 : G < 24 ? 1 : 3, (G / 3) & 3>();
  if constexpr (ST0 && G >= 36 && G < 48 && G % 3 == 0) g_pro1<16 + G / 3>();
  if constexpr (MD == 3 && eg == 0) sld<63, A_VOQ>();
  if constexpr (MD == 3 && eg == 4) asm volatile("v_add_u32 v95, s63, v89\n\ts_mov_b32 s94, %0" ::"i"(__builtin_bit_cast(unsigned, CS)));
  if constexpr (MD >= 2) e_ops<EM, eg>();
  constexpr int pg = 128 * PT + 64 * H + G;
  if constexpr (PT == 2 && eg == 0) sld<93, A_ZO>();
  if constexpr (PT >= 0) p_ops<pg>(pa);
  if constexpr (G == 61 && !LAST) {
    constexpr int ns = MD >= 2 ? ES[0].st[eg + 1] - ES[0].st[eg >= GD13 ? eg - GD13 : 0] : PT >= 0 ? PS.vm[pg + 1] - PS.vm[pg >= GD13 ? pg - GD13 : 0] : 0;
    asm volatile("s_waitcnt vmcnt(%0) lgkmcnt(0)\n\ts_barrier" ::"i"(12 + ns) : "memory");
  } else if constexpr (G == 61) asm volatile("s_waitcnt lgkmcnt(0)\n\ts_barrier" ::: "memory");
  GAP();
}
template <bool TR, int P, int H, int MD, int PT, int... Gs> DI void g_half_(const GM& g, int wid, const PA& pa, iseq<Gs...>) { (g_gap<TR, P, H, Gs, MD, PT>(g, wid, pa), ...); }
template <bool TR, int P, int H, int MD> DI void g_half(const GM& g, int wid = 0) { g_half_<TR, P, H, MD, -1>(g, wid, PA{}, make_iseq<64>{}); }
template <int T, int H> DI void q_half(const GM& g, const PA& pa) { g_half_<false, (T & 1) ^ 1, H, T == 0, T>(g, 0, pa, make_iseq<64>{}); }
template <int Q> DI void park_store() {
  constexpr int r = 32 + 16 * Q;
  asm volatile("s_mov_b32 s93, %0\n\tbuffer_store_dwordx4 v[%c1:%c2], v95, " SW ", s93 offen sc1\n\tbuffer_store_dwordx4 v[%c3:%c4], v95, " SW ", s93 offen offset:1024 sc1\n\t"
               "buffer_store_dwordx4 v[%c5:%c6], v95, " SW ", s93 offen offset:2048 sc1\n\tbuffer_store_dwordx4 v[%c7:%c8], v95, " SW ", s93 offen offset:3072 sc1"
               ::"i"((15 - 2 * (5 + Q)) * 8192), "i"(r), "i"(r + 3), "i"(r + 4), "i"(r + 7), "i"(r + 8), "i"(r + 11), "i"(r + 12), "i"(r + 15) : "memory");
}
static_assert(PARK == 0 || PARK == 12);
typedef __attribute__((ext_vector_type(16))) unsigned u32x16;
struct RSV { u32x16 a, b, c; };
DI void rsv_begin(RSV& r) { asm volatile("rsv_begin:" : "={v[32:47]}"(r.a), "={v[48:63]}"(r.b), "={v[64:79]}"(r.c)); }
DI void rsv_end(const RSV& r) { asm volatile("rsv_end:" ::"{v[32:47]}"(r.a), "{v[48:63]}"(r.b), "{v[64:79]}"(r.c)); }
template <int P, int... Is> DI void g_read0(const GM& g, iseq<Is...>) {
  (g_wread<P, 0, Is>(g), ...); (g_wread<P, 1, Is>(g), ...);
  g_aread<P, 0, 0>(g); g_aread<P, 0, 1>(g);
}
template <int I> DI void g_pro1() {
  constexpr int grp = I >> 2, i = I & 3, t = grp < 4 ? 0 : grp < 7 ? 1 : 2;
  constexpr bool isA = grp == 0 || grp == 3 || grp == 5;
  constexpr int jj = isA ? (grp == 3 ? 2 : 0) + (i & 1) + (i >> 1) * 4 : (grp == 2 || grp == 6 ? 4 : 0) + i;
  asm volatile("s_add_u32 m0, s%c0, %1\n\ts_nop 0" ::"i"(t == 1 ? 92 : 90), "i"((isA ? 0 : 65536) + jj * 4096) : "scc");
  g_dma<!isA, (isA ? 84 : 85) + (t ? 0 : 2), jj>();
}
template <int B, int... Is> DI void g_pro(iseq<Is...>) { (g_pro1<B + Is>(), ...); }
template <bool TR, bool SCL, class HO, class HE> DI void g_loop(const GM& g, HO&& odd, HE&& even) {
  constexpr int nkt = SCL ? 7 : 16, P0 = SCL ? 1 : 0;
#pragma unroll 1
  for (int t = 1;; t += 2) {
    g_half<TR, P0 ^ 1, 0, 0>(g);
    odd(t);
    GAP();
    g_half<TR, P0 ^ 1, 1, 0>(g);
    if (SCL && t + 2 >= nkt) break;
    g_half<TR, P0, 0, 0>(g);
    even(t + 1);
    GAP();
    g_half<TR, P0, 1, 0>(g);
    if (!SCL && t + 3 >= nkt) break;
  }
}

typedef __attribute__((ext_vector_type(4))) unsigned u32x4;
typedef __attribute__((ext_vector_type(4))) float f32x4;

constexpr int R_S = 128, R_P = 192, R_CI = 224, R_E = 96;
constexpr int R_TS = 112, R_RS = 114, R_M = 116;
constexpr int R_PD = 118, R_THR = 120, R_TMP = 121;
struct AC {
  u64 redo;
  unsigned rare;
};
template <int T, int D, int K> DI void d_op() {
  constexpr int t = T + 8 * (D & 1);
  if constexpr (K < 8) asm volatile("v_accvgpr_read_b32 v%c0, a[%c1]" ::"i"(t + K), "i"(8 * D + K));
  else if constexpr (K < 12) asm volatile("v_cvt_pk_bf16_f32 v%c0, v%c1, v%c2" ::"i"(t + K - 8), "i"(t + 2 * (K - 8)), "i"(t + 2 * (K - 8) + 1));
  else
    asm volatile("buffer_store_dwordx4 v[%c0:%c1], v%c2, " SW ", s%c3 offen offset:%c4 sc1" ::"i"(t), "i"(t + 3), "i"(84 + ((D >> 2) & 1)), "i"(90 + (D >> 3)),
                 "i"((D & 3) * 1024) : "memory");
}
template <int T, int O0, int... Os> DI void d_ops(iseq<Os...>) { (d_op<T, (O0 + Os) / 13, (O0 + Os) % 13>(), ...); }
template <int D> DI void d_store() { d_ops<160, 13 * D>(make_iseq<13>{}); }
template <int X, int F> DI void kread() {
  constexpr int r = 192 + 4 * (F & 15);
  asm volatile("ds_read_b128 a[%c0:%c1], v89 offset:%c2" ::"i"(r), "i"(r + 3), "i"(X * 32768 + F * 1024));
}
template <int X, int F> DI void vread() {
  constexpr int r = 192 + 4 * (F & 15);
  asm volatile("ds_read_b128 a[%c0:%c1], v86 offset:%c2" ::"i"(r), "i"(r + 3), "i"(X * 32768 + (F & 1) * 16384 + (F >> 1) * 1024));
}
template <int X, int... Fs> DI void kreads(iseq<Fs...>) { (kread<X, Fs>(), ...); }
template <int X, int... Fs> DI void vreads(iseq<Fs...>) { (vread<X, Fs>(), ...); }
template <int X, int J> DI void dma_m0() {
  if constexpr (J == 0) asm volatile("s_mov_b32 m0, s%c0" ::"i"(80 + X));
  else if constexpr (J == 4) asm volatile("s_or_b32 m0, s%c0, 0x1000" ::"i"(80 + X) SCC1);
  else if constexpr (J == 8) asm volatile("s_bitset0_b32 m0, 16");
  else if constexpr (J == 12) asm volatile("s_bitset0_b32 m0, 12");
}
template <int J> DI void dma_at() {
  asm volatile("buffer_load_dwordx4 v%c0, " SW ", s%c1 offen offset:%c2 sc1 lds" ::"i"(J >= 4 && J < 12 ? 91 : 90), "i"(J < 8 ? 82 : 84), "i"((J & 3) * 1024) : "memory");
}
template <int V, int S> DI void dma4() {
  asm volatile("buffer_load_dwordx4 v%c0, " SW ", s%c1 offen sc1 lds\n\tbuffer_load_dwordx4 v%c0, " SW ", s%c1 offen offset:1024 sc1 lds\n\t"
               "buffer_load_dwordx4 v%c0, " SW ", s%c1 offen offset:2048 sc1 lds\n\tbuffer_load_dwordx4 v%c0, " SW ", s%c1 offen offset:3072 sc1 lds"
               ::"i"(V), "i"(S) : "memory");
}
template <int Q> DI void k01_dma() {
  asm volatile("s_add_u32 m0, s80, %0\n\ts_nop 0" ::"i"((Q >> 1) * 32768 + (Q & 1) * 4096 - 65536) SCC1);
  dma4<90 + (Q & 1), 83 + (Q >> 1)>();
}
template <int R, int I, int ST = 4, int V = 89> DI void f_load() {
  asm volatile("buffer_load_dwordx4 a[%c0:%c1], v%c3, " SW ", s72 offen offset:%c2 sc1" ::"i"(R + ST * I), "i"(R + ST * I + 3), "i"((I & 3) * 1024), "i"(V) : "memory");
}
DI void f_next() { asm volatile("s_add_u32 s72, s72, 0x1000" SCC3); }
constexpr int o_wreg(int o, int f) { return o == 0 ? 128 + 4 * f : o == 3 ? 192 + 4 * f : 8 * f + 4 * (o - 1); }
template <int F, int LASTF = 3> DI void w_load() {
  f_load<o_wreg(F >> 4, 0), F & 15, o_wreg(F >> 4, 1) - o_wreg(F >> 4, 0), (F & 1) ? 84 : 89>();
  if constexpr ((F & 3) >= LASTF) asm volatile("s_add_u32 s72, s72, %0" ::"i"(F < 4 ? 0x1000 : F < 8 ? 0xf000 : F & 4 ? 0x7000 : 0x1000) SCC1);
}
#define NWF 40
constexpr int CV_KP = 5120 - 32, CV_KS = 10304 - 32;
struct CV { unsigned pa[4], pb, lw, vzo; };
constexpr int cv_J(int q) { int J = 7; while (q > J) { q -= J + 1; --J; } return J; }
constexpr int cv_i(int q) { int J = 7; while (q > J) { q -= J + 1; --J; } return q; }
template <int r_, int N> DI void cv_rd(const CV& c) {
  constexpr int R = (r_ + 1) & 7;
  if constexpr (N < 16) {
    constexpr int i = N >> 1, o = 16 * R + 128 * (i & 1) + 2 * (N & 1), r = 192 + 32 * (r_ & 1) + 2 * N;
    asm volatile("ds_read2_b32 a[%c1:%c2], %0 offset0:%c3 offset1:%c4" ::"v"(c.pa[i >> 1]), "i"(r), "i"(r + 1), "i"(o), "i"(o + 1));
  } else {
    constexpr int m = N - 16, r = 160 + 32 * (r_ & 1) + 4 * m;
    asm volatile("ds_read_b128 v[%c1:%c2], %0 offset:%c3" ::"v"(c.pb), "i"(r), "i"(r + 3), "i"(64 * R + 512 * (7 - m)));
  }
}
template <int O> DI void cv_z(const CV& c) {
  constexpr int pr = O / 7, o = O % 7, w = 128 + 4 * (7 - 2 * pr), y = w + 2 * (o & 1) - 4 * (o >> 1);
  if constexpr (o == 4) asm volatile("s_add_u32 s63, s65, %2\n\tv_permlane16_swap_b32_e32 v%c0, v%c1" ::"i"(w), "i"(w + 2), "i"((6 - 2 * pr) * 8 * 3584) SCC1);
  else if constexpr (o == 5) asm volatile("v_permlane16_swap_b32_e32 v%c0, v%c1" ::"i"(w + 1), "i"(w + 3));
  else if constexpr (o == 6) asm volatile("buffer_store_dwordx4 v[%c0:%c1], %2, " SW ", s63 offen sc1" ::"i"(w), "i"(w + 3), "v"(c.vzo) : "memory");
  else asm volatile("v_cvt_pk_bf16_f32 v%c0, v%c1, v%c2" ::"i"(w + o), "i"(y), "i"(y + 1));
}
#define CVS 6
#define CVF 150
#define CVP 206
#define CVK 250
constexpr int CVQ = 16 * CVS + 2;
constexpr int cv_vm(int T) {
  return T < 16 * CVS && T % CVS == CVS - 1 ? 1 : T >= CVQ && T < CVQ + 96 && (T - CVQ) % 6 == 0 ? 2 : T == CVF ? 4 : T == CVP ? 5 : T > CVK && T <= CVK + 48 && (T - CVK) % 3 == 1 ? 7 : 0;
}
constexpr int cv_cnt(int T0, int T1, int kind = 0) { int n = 0; for (int T = T0; T < T1; ++T) n += kind ? cv_vm(T) == kind : cv_vm(T) != 0; return n; }
static_assert(16 * CVS + 4 <= 108 && cv_vm(CVF) == 4 && cv_vm(CVP) == 5 && CVQ + 96 < CVK);
constexpr int cv_z0(int T) { return T < 271 ? 0 : T < 278 ? T - 271 : T < 281 ? 7 : T - 274; }
template <int I> DI void k01_dma1() {
  if constexpr ((I & 3) == 0) asm volatile("s_add_u32 m0, s80, %0\n\ts_nop 0" ::"i"((I >> 3) * 32768 + ((I >> 2) & 1) * 4096 - 65536) SCC1);
  asm volatile("buffer_load_dwordx4 v%c0, " SW ", s%c1 offen offset:%c2 sc1 lds" ::"i"(90 + ((I >> 2) & 1)), "i"(83 + (I >> 3)), "i"((I & 3) * 1024) : "memory");
}
template <int T> DI void cv_slot(const CV& c) {
  constexpr int r = T / 36, Q = T % 36, i = cv_i(Q), J = cv_J(Q), d = 128 + 4 * J, a = 192 + 32 * (r & 1) + 4 * i, b = 160 + 32 * (r & 1) + 4 * (J - i), vm = cv_vm(T);
  if constexpr (Q == 0) asm volatile("s_waitcnt lgkmcnt(0)");
  if constexpr (r == 0 && i == 0)
    asm volatile("v_mfma_f32_16x16x32_bf16 v[%c0:%c1], a[%c2:%c3], v[%c4:%c5], 0" ::"i"(d), "i"(d + 3), "i"(a), "i"(a + 3), "i"(b), "i"(b + 3));
  else
    asm volatile("v_mfma_f32_16x16x32_bf16 v[%c0:%c1], a[%c2:%c3], v[%c4:%c5], v[%c0:%c1]" ::"i"(d), "i"(d + 3), "i"(a), "i"(a + 3), "i"(b), "i"(b + 3));
  if constexpr (r == 7 && i == 0) asm volatile("v_mfma_f32_16x16x32_bf16 v[%c0:%c1], v[96:99], v[%c2:%c3], v[%c0:%c1]" ::"i"(d), "i"(d + 3), "i"(b), "i"(b + 3));
  if constexpr (Q < 24 && r < 7) cv_rd<r + 1, Q>(c);
  if constexpr (T == 32) asm volatile("ds_read_u16 v121, %0 offset:5120" ::"v"(c.lw));
  if constexpr (T == 33) asm volatile("ds_write_b128 %0, v[114:117] offset:%c1\n\tds_write_b128 %0, v[114:117] offset:%c2" ::"v"(c.lw), "i"(CV_KS), "i"(CV_KS + 16));
  if constexpr (T == 62) asm volatile("ds_write_b16 %0, v121 offset:%c1" ::"v"(c.lw), "i"(CV_KS + 30));
  if constexpr (T == 34) sld<72, A_OQ>();
  if constexpr (T == 35) sld<65, A_ZO>();
  if constexpr (T == CVQ - 2) { asm volatile("s_waitcnt vmcnt(16)" ::: "memory"); fl_check(); }
  if constexpr (T < 16 * CVS) d_ops<224, 13 * (T / CVS) + (T % CVS * 13) / CVS>(make_iseq<((T % CVS + 1) * 13) / CVS - (T % CVS * 13) / CVS>{});
  if constexpr (vm == 2) { f_load<128, (T - CVQ) / 6>(); if constexpr ((T - CVQ) / 6 % 4 == 3) f_next(); }
  if constexpr (vm == 4) {
    asm volatile("s_waitcnt vmcnt(%0)\n\ts_barrier" ::"i"(cv_cnt(16 * CVS, T)) : "memory");
    set_flag<A_FG>();
  }
  if constexpr (T == CVP - 3) fl_mask<A_N01, -1>();
  if constexpr (vm == 5) fl_issue();
  if constexpr (T == CVK) { asm volatile("s_waitcnt vmcnt(%0)" ::"i"(cv_cnt(CVP + 1, T)) : "memory"); fl_check(); }
  if constexpr (vm == 7) k01_dma1<cv_cnt(CVK, T, 7)>();
  if constexpr (cv_z0(T + 1) > cv_z0(T)) cv_z<cv_z0(T)>(c);
  GAP();
}
template <int... T> DI void cv_slots(const CV& c, iseq<T...>) { (cv_slot<T>(c), ...); }
template <int... N> DI void cv_rd0(const CV& c, iseq<N...>) { (cv_rd<0, N>(c), ...); }
template <int O> DI void cv_tail(const CV& c) {
  constexpr int k = cv_cnt(CVK, 288, 7) + O / 6;
  if constexpr (O % 6 == 0 && k < 16) k01_dma1<k>();
  cv_z<cv_z0(288) + O>(c);
}
template <int... O> DI void cv_tails(const CV& c, iseq<O...>) { (cv_tail<O>(c), ...); }
static_assert(cv_cnt(CVK, 288, 7) >= 8 && cv_cnt(CVK, 288, 7) + 3 >= 16);
constexpr int cv_end() {
  int T = CVK; while (cv_cnt(CVK, T + 1, 7) < 8) ++T;
  int n = 8; for (int o = cv_z0(T); o < 28; ++o) n += o % 7 == 6;
  return n;
}

template <int Q> DI void acc_zero() {
  asm volatile("v_mfma_f32_32x32x16_bf16 a[%c0:%c1], v[208:211], v[208:211], 0" ::"i"(16 * Q), "i"(16 * Q + 15));
}
struct SMS { int ex[128], op0[128], nop[128], last; };
constexpr SMS sm_sched() {
  SMS z{};
  int et[32] = {}, e = 0;
  for (int t = 0; t < 128; ++t) {
    const int g = t & 15;
    const bool x = t < 48 ? (g >= 8 && (g & 1)) : (g >= 8 && g != 10 && g != 13 && g != 15);
    z.ex[t] = x && e < 32 ? e : -1;
    if (z.ex[t] >= 0) et[e++] = t;
  }
  int o = 0;
  for (int t = 0; t < 128; ++t) {
    const int g = t & 15;
    int cap = g < 8 ? (g == 0 ? 0 : 1) : (z.ex[t] >= 0 || t < 48 ? 0 : 2);
    z.op0[t] = o; z.nop[t] = 0;
    while (cap > 0 && o < 48 && et[2 * (o / 3) + (o % 3 == 0 ? 0 : 1)] + 2 <= t) {
      const int el = 2 * (o / 3) + o % 3;
      if (!(o % 3 < 2 && (el == 0 || el == 4))) --cap;
      ++o; ++z.nop[t]; z.last = t;
    }
  }
  return z;
}
constexpr SMS SM = sm_sched();
static_assert(SM.last <= 115);
constexpr int p_reg(int Y, int k) { return R_P + 16 * Y + 4 * ((k >> 3) * 2 + ((k >> 1) & 1)) + ((k >> 2) & 1) * 2 + (k & 1); }
template <int Y, int O> DI void sm_op() {
  constexpr int k = O / 3, e = 2 * k + O % 3, ts = R_TS + ((e >> 2) & 1);
  if constexpr (O % 3 == 2) asm volatile("v_cvt_pk_bf16_f32 v%c0, v%c1, v%c2" ::"i"(p_reg(Y, k)), "i"(R_E + ((2 * k) & 15)), "i"(R_E + ((2 * k + 1) & 15)));
  else if constexpr (e == 1 || e == 5) asm volatile("v_add_f32_e32 v%c0, v%c1, v%c2" ::"i"(ts), "i"(R_E + e - 1), "i"(R_E + e));
  else if constexpr (e != 0 && e != 4) asm volatile("v_add_f32_e32 v%c0, v%c0, v%c1" ::"i"(ts), "i"(R_E + (e & 15)));
}
template <int Y, int T> DI void sm_gap(u64& redo) {
  if constexpr (SM.ex[T] >= 0) asm volatile("v_exp_f32 v%c0, v%c1" ::"i"(R_E + (SM.ex[T] & 15)), "i"(R_S + 32 * Y + SM.ex[T]));
  if constexpr (SM.nop[T] > 0) sm_op<Y, SM.op0[T]>();
  if constexpr (SM.nop[T] > 1) sm_op<Y, SM.op0[T] + 1>();
  if constexpr (SM.nop[T] > 2) sm_op<Y, SM.op0[T] + 2>();
  if constexpr (T == SM.last + 1) asm volatile("v_add_f32_e32 v%c0, v%c1, v%c2" ::"i"(R_TMP), "i"(R_TS), "i"(R_TS + 1));
  if constexpr (T == SM.last + 2) asm volatile("v_cmp_nle_f32_e64 %0, v%c1, v%c2" : "=s"(redo) : "i"(R_TMP), "i"(R_THR));
}
#define EVG 35
template <int X, int KB, int QB, int S_, bool FIRST, bool HEAD = S_ == 0> DI void qk_mfma() {
  constexpr int r = 192 + 4 * ((KB & 1) * 8 + S_), q = 128 + 4 * (QB * 8 + S_);
  constexpr int d = R_S + 32 * X + 4 * (KB * 2 + QB), ci = HEAD ? R_CI + 4 * (KB * 2 + QB) : d;
  if constexpr (FIRST && HEAD)
    asm volatile("v_mfma_f32_16x16x32_bf16 v[%c0:%c1], a[%c2:%c3], a[%c4:%c5], 0" ::"i"(d), "i"(d + 3), "i"(r), "i"(r + 3), "i"(q), "i"(q + 3));
  else
    asm volatile("v_mfma_f32_16x16x32_bf16 v[%c0:%c1], a[%c2:%c3], a[%c4:%c5], v[%c6:%c7]" ::"i"(d), "i"(d + 3), "i"(r), "i"(r + 3), "i"(q), "i"(q + 3),
                 "i"(ci), "i"(ci + 3));
}
template <int X, int FIRST, bool NOQK, int G> DI void gapA(AC& c) {
  constexpr int Xp = X ^ 1, kb = G >> 4, g = G & 15;
  if constexpr (NOQK && G < 48 && G % 3 == 0) { f_load<128, G / 3>(); if constexpr (G % 12 == 9) f_next(); }
  if constexpr (G == 48) asm volatile("s_waitcnt vmcnt(%0) lgkmcnt(0)\n\ts_barrier" ::"i"(NOQK ? 16 : FIRST == 2 ? 63 : FIRST == 1 ? 9 : FIRST == 3 ? 8 : 0) : "memory");
  else if constexpr (g == 0 && !NOQK) asm volatile("s_waitcnt lgkmcnt(0)");
  if constexpr (!NOQK) {
    if constexpr (SNK & 1) qk_mfma<X, kb, (g >> 3) ^ (kb & 1), g < 8 ? g : 15 - g, FIRST != 0, !(g & 7)>();
    else qk_mfma<X, kb, (g >> 3), g & 7, FIRST != 0>();
    if constexpr (g < 8 && kb < 3) kread<X, (kb + 1) * 8 + g>();
    if constexpr (G == 54) asm volatile("s_add_u32 s82, s83, 0x1000000" SCC3);
    if constexpr (G == 55) dma_m0<X, 0>();
    if constexpr (G >= 56 && !(G & 1) && FIRST != 2) dma_at<(G - 56) / 2>();
    if constexpr (G == EVG) asm volatile("v_readlane_b32 s87, v93, s86");
    if constexpr (G == EVG + 2) asm volatile("s_add_u32 s86, s86, 1" SCC3);
    if constexpr (G == 57) asm volatile("s_mov_b32 s83, s84\n\ts_andn2_b32 s84, s87, 0x3ff" SCC3);
    if constexpr (G == 59 && FIRST) asm volatile("s_and_b32 %0, s87, 0x3ff" : "=s"(c.rare) SCC2);
    if constexpr (G == 59 && !FIRST) asm volatile("s_and_b32 s63, s87, 0x3ff\n\ts_cmp_lg_u64 %0, 0" ::"s"(c.redo) SCC1);
    if constexpr (G == 61 && !FIRST) asm volatile("s_addc_u32 %0, s63, 0" : "=s"(c.rare) SCC2);
  } else if constexpr (SM.nop[64 + G] > 0) asm volatile("s_nop 0");
  if constexpr (!FIRST && g < 8 && kb == 3) vread<Xp, g>();
  if constexpr (FIRST == 2) d_ops<160, 13 * (G / 8) + (G % 8 * 13) / 8>(make_iseq<((G % 8 + 1) * 13) / 8 - (G % 8 * 13) / 8>{});
  if constexpr (FIRST == 2 && G == 34)
    asm volatile("v_and_b32 v124, 0xf0, v89\n\ts_waitcnt vmcnt(8)\n\t" A_CHECK("v[122:123]", "s[96:97]", "s72")
                 "buffer_load_dwordx2 v[122:123], v124, " SW ", s73 offen offset:1024 sc1" ::: "vcc", "scc", "memory");
  if constexpr (FIRST == 1 && G >= 8 && G < 40 && G % 4 == 1)
    asm volatile("s_mov_b32 m0, s75\n\ts_add_u32 s75, s75, 0x1000\n\tbuffer_load_dwordx4 v84, s[64:67], s94 offen lds\n\ts_add_u32 s94, s94, 0x3800" ::: SCC_ "memory");
  if constexpr (FIRST == 1 && G == 42) asm volatile("s_mov_b32 m0, 0x1f800\n\ts_mov_b64 s[64:65], s[88:89]\n\ts_mov_b32 s66, 0x1c0000\n\tbuffer_load_dword v121, s[64:67], 0 offen lds" ::: "memory");
  if constexpr (FIRST == 3) {
    constexpr int n = (G >> 4) * 4 + (G & 3), f = G < 32 ? (G - 17) / 4 : 4 + (G - 33) / 3;
    if constexpr (G == 2) { asm volatile("v_xor_b32 v84, 32, v85"); sld<94, B_WD, 1>(); }
    if constexpr (G < 32 && (G & 12) == 8)
      asm volatile("ds_read_b64_tr_b16 v[%c0:%c1], v%c4 offset:%c5\n\tds_read_b64_tr_b16 v[%c2:%c3], v%c4 offset:%c6" ::"i"(224 + 4 * n), "i"(225 + 4 * n), "i"(226 + 4 * n),
                   "i"(227 + 4 * n), "i"(n & 1 ? 84 : 85), "i"((n >> 1) * 8192), "i"((n >> 1) * 8192 + 4096));
    if constexpr (G == 32) asm volatile("s_add_u32 s94, s94, 0x1000" SCC3);
    if constexpr (G >= 17 && G < 45 && (G < 32 ? G % 4 == 1 : G % 3 == 0 && G > 32))
      asm volatile("buffer_store_dwordx4 v[%c0:%c1], v89, " SW ", s94 offen offset:%c2 sc1" ::"i"(224 + 4 * f), "i"(227 + 4 * f), "i"((f & 3) * 1024) : "memory");
  }
  if constexpr (FIRST == 3 && G == 42) sld<63, B_FSET, 1>();
  if constexpr (FIRST == 3 && G == 48) asm volatile(A_FLAG ::: "memory");
  if constexpr (FIRST && G == 44) asm volatile("v_mov_b64 v[208:209], 0\n\tv_mov_b64 v[210:211], 0");
  if constexpr (!FIRST) sm_gap<Xp, 64 + G>(c.redo);
  GAP();
}
template <int R> DI void rescale1(float alpha) {
  float t;
  asm volatile("v_accvgpr_read_b32 %0, a[%c2]\n\tv_mul_f32 %0, %0, %1\n\tv_accvgpr_write_b32 a[%c2], %0" : "=&v"(t) : "v"(alpha), "i"(R));
}
template <int Q0, int R> DI void rescale_q(const float (&alpha)[2]) { if constexpr (((R >> 2) & 1) >= Q0) rescale1<R>(alpha[(R >> 2) & 1]); }
template <int Q0, int... Rs> DI void rescale_all(const float (&alpha)[2], iseq<Rs...>) { (rescale_q<Q0, Rs>(alpha), ...); }
template <bool MAX> DI float row_reduce(float v) {
  auto p = __builtin_amdgcn_permlane32_swap(__float_as_uint(v), __float_as_uint(v), false, false);
  v = MAX ? fmaxf(__uint_as_float(p[0]), __uint_as_float(p[1])) : __uint_as_float(p[0]) + __uint_as_float(p[1]);
  p = __builtin_amdgcn_permlane16_swap(__float_as_uint(v), __float_as_uint(v), false, false);
  return MAX ? fmaxf(__uint_as_float(p[0]), __uint_as_float(p[1])) : __uint_as_float(p[0]) + __uint_as_float(p[1]);
}
template <int Y, int QB> DI float row_max() {
  constexpr int r = R_S + 32 * Y + 4 * QB;
  float mx;
  asm volatile("v_max_f32 %0, v%c1, v%c2\n\tv_max3_f32 %0, %0, v%c3, v%c4" : "=&v"(mx) : "i"(r), "i"(r + 1), "i"(r + 2), "i"(r + 3));
  static_for([&](auto K) {
    constexpr int q = r + 8 * (K + 1);
    float t = mx;
    asm volatile("v_max3_f32 %0, %0, v%c1, v%c2\n\tv_max3_f32 %0, %0, v%c3, v%c4" : "+v"(t) : "i"(q), "i"(q + 1), "i"(q + 2), "i"(q + 3));
    mx = t;
  }, make_iseq<3>{});
  return row_reduce<true>(mx);
}
template <int R> DI void v_sub(float d) { asm volatile("v_sub_f32 v%c0, v%c0, %1" ::"i"(R), "v"(d)); }
template <int B> DI void s_shift(const float (&d)[2]) { static_for([&](auto E) { v_sub<B + E>(d[(E >> 2) & 1]); }, make_iseq<32>{}); }
DI void m_shift(const float (&d)[2]) {
  s_shift<R_CI>(d);
  v_sub<R_M>(-d[0]); v_sub<R_M + 1>(-d[1]);
}
#define A_MASK(R0, R1, R2, R3, K0, K1, K2, K3) \
    "v_cmp_gt_i32_e64 s[64:65], " K0 ", v95\n\tv_cmp_gt_i32_e64 s[66:67], " K1 ", v95\n\tv_cmp_gt_i32_e64 s[68:69], " K2 ", v95\n\tv_cmp_gt_i32_e64 s[70:71], " K3 ", v95\n\t" \
    "v_cndmask_b32_e64 " R0 ", -v116, v127, s[64:65]\n\tv_cndmask_b32_e64 " R1 ", -v116, v127, s[66:67]\n\t" \
    "v_cndmask_b32_e64 " R2 ", -v116, v127, s[68:69]\n\tv_cndmask_b32_e64 " R3 ", -v116, v127, s[70:71]\n\t"
template <int K, int T> DI void mask_cmp() {
  asm volatile("v_cmp_gt_i32_e64 s[64:65], %c0, v%c4\n\tv_cmp_gt_i32_e64 s[66:67], %c1, v%c4\n\tv_cmp_gt_i32_e64 s[68:69], %c2, v%c4\n\tv_cmp_gt_i32_e64 s[70:71], %c3, v%c4"
               ::"i"(K), "i"(K + 1), "i"(K + 2), "i"(K + 3), "i"(T));
}
template <int R, int S> DI void mask_sel() {
  if constexpr (S < 0)
    asm volatile("v_cndmask_b32_e64 v%c0, v%c0, v127, s[64:65]\n\tv_cndmask_b32_e64 v%c1, v%c1, v127, s[66:67]\n\t"
                 "v_cndmask_b32_e64 v%c2, v%c2, v127, s[68:69]\n\tv_cndmask_b32_e64 v%c3, v%c3, v127, s[70:71]" ::"i"(R), "i"(R + 1), "i"(R + 2), "i"(R + 3));
  else
    asm volatile("v_cndmask_b32_e64 v%c0, -v%c4, v127, s[64:65]\n\tv_cndmask_b32_e64 v%c1, -v%c4, v127, s[66:67]\n\t"
                 "v_cndmask_b32_e64 v%c2, -v%c4, v127, s[68:69]\n\tv_cndmask_b32_e64 v%c3, -v%c4, v127, s[70:71]" ::"i"(R), "i"(R + 1), "i"(R + 2), "i"(R + 3), "i"(S));
}
template <int X, int Q0 = 0> DI void sm_redo() {
  constexpr int Xp = X ^ 1;
  float d[2] = {0.f, 0.f}, dt[2] = {0.f, 0.f}, alpha[2] = {1.f, 1.f}, ts[2] = {0.f, 0.f};
  asm volatile("s_nop 7");
  static_for([&](auto Q) {
    constexpr int qb = Q + Q0;
    const float pd = vget<R_PD + qb>();
    d[qb] = fmaxf(row_max<Xp, qb>() - pd, 0.f);
    dt[qb] = d[qb] + pd;
    vset<R_PD + qb>(d[qb]);
    alpha[qb] = __builtin_amdgcn_exp2f(-d[qb]);
    vset<R_RS + qb>(vget<R_RS + qb>() * alpha[qb]);
  }, make_iseq<2 - Q0>{});
  vset<R_THR>(d[0] > 0.f || d[1] > 0.f ? -1.f : 4096.f);
  m_shift(d);
  static_for([&](auto K) {
    constexpr int k = K, qb = (k >> 1) & 1;
    if constexpr (qb >= Q0) {
      const float e0 = __builtin_amdgcn_exp2f(vget<R_S + 32 * Xp + 2 * k>() - dt[qb]), e1 = __builtin_amdgcn_exp2f(vget<R_S + 32 * Xp + 2 * k + 1>() - dt[qb]);
      ts[qb] += e0; ts[qb] += e1;
      vset<p_reg(Xp, k)>(__uint_as_float(pk2(e0, e1)));
    }
  }, make_iseq<16>{});
  vset<R_TS>(ts[0]); vset<R_TS + 1>(ts[1]);
  rescale_all<Q0>(alpha, make_iseq<128>{});
  asm volatile("s_nop 1" ::: "memory");
  GAP();
}
template <int X, int FIRST, bool NOQK, int... Gs> DI void phaseA_(AC& c, iseq<Gs...>) {
  (gapA<X, FIRST, NOQK, Gs>(c), ...);
}
template <int X, int FIRST, bool NOQK> DI void phaseA(AC& c) { phaseA_<X, FIRST, NOQK>(c, make_iseq<64>{}); }

template <int Y, int F, int QB> DI void pv_mfma() {
  constexpr int r = 192 + 4 * (F & 15), o = 4 * ((F >> 1) * 2 + QB), p = R_P + 16 * Y + 4 * ((F & 1) * 2 + QB);
  asm volatile("v_mfma_f32_16x16x32_bf16 a[%c0:%c1], a[%c2:%c3], v[%c4:%c5], a[%c0:%c1]" ::"i"(o), "i"(o + 3), "i"(r), "i"(r + 3), "i"(p), "i"(p + 3));
}
template <int X, bool SMX, int G> DI void gapB(AC& c) {
  constexpr int Xp = X ^ 1, blk = G >> 4, g = G & 15;
  if constexpr (g == 0) asm volatile("s_waitcnt lgkmcnt(0)");
  if constexpr (SMX && g == 7 && blk < 3) dma_m0<X, blk * 4 + 4>();
  if constexpr (SNK & 2) pv_mfma<Xp, (G >> 2) * 2 + ((g ^ (g >> 1)) & 1), ((g >> 1) ^ (g >> 2)) & 1>();
  else pv_mfma<Xp, (G >> 2) * 2 + (g & 1), (g >> 1) & 1>();
  if constexpr (SMX && (G == 1 || G == 2)) asm volatile("v_add_f32_e32 v%c0, v%c0, v%c1" ::"i"(R_RS + G - 1), "i"(R_TS + G - 1));
  if constexpr (g < 8) {
    if constexpr (blk < 3) vread<Xp, (blk + 1) * 8 + g>();
    else if constexpr (SMX) kread<Xp, g>();
  }
  if constexpr (SMX) {
    if constexpr (g >= 8 && !(g & 1) && blk < 3) dma_at<blk * 4 + 4 + (g - 8) / 2>();
    sm_gap<X, G>(c.redo);
    GAP();
  }
}
template <int X, int... Gs> DI void phaseB_(AC& c, iseq<Gs...>) { (gapB<X, true, Gs>(c), ...); }
template <int X> DI void phaseB(AC& c) { phaseB_<X>(c, make_iseq<64>{}); }
constexpr int sm_nex() { int n = 0; for (int t = 0; t < 64; ++t) n += SM.ex[t] >= 0; return n; }
template <bool ST> DI void phaseB0(bool diag0, unsigned vml) {
  float d[2];
  dma_m0<0, 4>();
  if constexpr (ST) {
    asm volatile("s_waitcnt vmcnt(4)" ::: "memory");
    dma_at<4>(); d_store<8>();
    d[0] = vget<122>(); d[1] = vget<123>();
    dma_at<5>();
  } else {
    kreads<1>(make_iseq<8>{});
    dma_at<4>();
    asm volatile("s_nop 3");
    if (__builtin_expect(diag0, 0))
      static_for([&](auto I) { constexpr int i = I; mask_cmp<16 * i, 95>(); mask_sel<R_S + 8 * i, -1>(); }, make_iseq<4>{});
    d[0] = row_max<0, 0>(); GAP(); dma_at<5>();
    d[1] = row_max<0, 1>(); GAP(); dma_at<6>();
#pragma unroll
    for (int qb = 0; qb < 2; ++qb) if (!(d[qb] > -3.0e38f)) d[qb] = 0.f;
    asm volatile("buffer_store_dwordx2 %0, %1, " SW ", s73 offen offset:1024 sc1" ::"v"((f32x2c){d[0], d[1]}), "v"(vml) : "memory");
  }
  vset<R_M>(d[0]); vset<R_M + 1>(d[1]);
  const f32x2c nd[2] = {{-d[0], -d[0]}, {-d[1], -d[1]}};
  static_for([&](auto E) {
    constexpr int e = E;
    const f32x2c v = nd[(e >> 1) & 1];
    asm volatile("v_pk_add_f32 v[%c0:%c1], v[%c0:%c1], %4\n\tv_mov_b64 v[%c2:%c3], %4" ::"i"(R_S + 2 * e), "i"(R_S + 2 * e + 1), "i"(R_CI + 2 * e), "i"(R_CI + 2 * e + 1), "v"(v));
    if constexpr (ST && e == 0) d_store<9>();
    if constexpr (ST && e == 2) dma_at<6>();
    if constexpr (e == (ST ? 5 : 3)) dma_at<7>();
    if constexpr (e == 6) dma_m0<0, 8>();
    if constexpr (e == 7 || e == 11 || e == 15) dma_at<8 + (e - 7) / 4>();
    if constexpr (ST && (e == 4 || e == 9)) d_store<10 + e / 8>();
  }, make_iseq<16>{});
  static_assert(sm_nex() >= 16 && SM.op0[64] >= 3 * ((sm_nex() - 15) / 2) && SM.op0[64] <= 24);
  static_for([&](auto E) {
    constexpr int e = E;
    asm volatile("v_exp_f32 v%c0, v%c1" ::"i"(R_E + e), "i"(R_S + e));
    if constexpr (e % 3 == 0 && e < 15) acc_zero<e / 3>();
    if constexpr (e == 3) dma_at<11>();
    if constexpr (e == 6) dma_m0<0, 12>();
    if constexpr (e == 7 || e == 11 || e == 15) dma_at<12 + (e - 7) / 4>();
    if constexpr (ST && (e == 1 || e == 9)) d_store<12 + e / 8>();
  }, make_iseq<16>{});
  if constexpr (ST) {
    d_store<14>();
    asm volatile("s_barrier" ::: "memory");
    kreads<1>(make_iseq<8>{});
  }
  static_for([&](auto O) { sm_op<0, O>(); if constexpr (O % 7 == 1 && O < (ST ? 14 : 21)) acc_zero<5 + O / 7>(); }, make_iseq<SM.op0[64]>{});
  dma_at<15>();
  if constexpr (ST) { d_store<15>(); acc_zero<7>(); }
  static_for([&](auto E) { asm volatile("v_exp_f32 v%c0, v%c1" ::"i"(R_E + (int)E), "i"(R_S + 16 + (int)E)); }, make_iseq<sm_nex() - 16>{});
  GAP();
}
struct EP { float fa[2], fb[2]; };
template <int I, int E, int MERGE, int TB = 96> DI void epi_pair(const EP& p) {
  constexpr int qb = I & 1, r = 16 * (I >> 1) + 4 * qb + (E >> 1) * 8 + (E & 1) * 2, t = TB + 2 * E, w = 128 + 4 * I + E;
  if constexpr (MERGE == 2) asm volatile("v_accvgpr_read_b32 v%c0, a[%c2]\n\tv_accvgpr_read_b32 v%c1, a[%c3]" ::"i"(t), "i"(t + 1), "i"(r), "i"(r + 1));
  else
    asm volatile("v_accvgpr_read_b32 v%c0, a[%c2]\n\tv_accvgpr_read_b32 v%c1, a[%c3]\n\tv_mul_f32 v%c0, v%c0, %4\n\tv_mul_f32 v%c1, v%c1, %4"
                 ::"i"(t), "i"(t + 1), "i"(r), "i"(r + 1), "v"(p.fa[qb]));
  if constexpr (MERGE == 1)
    asm volatile("v_lshlrev_b32 v104, 16, v%c2\n\tv_and_b32 v105, 0xffff0000, v%c2\n\tv_fmac_f32 v%c0, v104, %3\n\tv_fmac_f32 v%c1, v105, %3"
                 ::"i"(t), "i"(t + 1), "i"(w), "v"(p.fb[qb]));
  asm volatile("v_cvt_pk_bf16_f32 v%c0, v%c1, v%c2" ::"i"(w), "i"(t), "i"(t + 1));
}
template <int I> DI void ctx_store() {
  asm volatile("buffer_store_dwordx4 v[%c0:%c1], v85, " SW ", s76 offen offset:%c2 sc1\n\tds_write_b128 v85, v[%c0:%c1] offset:%c3"
               ::"i"(128 + 4 * I), "i"(131 + 4 * I), "i"((I & 2) * 1024), "i"((I & 1) * 32768 + (I >> 1) * 2048) : "memory");
  if constexpr (I & 2) asm volatile("s_add_u32 s76, s76, %0" ::"i"(I == 14 ? 0x5000 : 0x1000) SCC1);
}
constexpr int mg_reg(int L, int I) { return I >= 8 ? 8 + 4 * I : I >= 4 ? R_CI + 8 * (I - 4) : R_S + 32 * L + 8 * I; }
#define STF(k)
template <int L, bool MG, int G> DI void gapAD(AC& c) {
  constexpr int kb = G >> 3, s = G & 7, db = ((G >> 1) + 8) & 15, o = 4 * (db * 2 + (G & 1)), a = 72 + 2 * (db & 1), b = mg_reg(L, (db >> 1) * 2 + (G & 1));
  if constexpr (G == 0) STF(0);
  if constexpr (G == 16) STF(1);
  if constexpr (G == 28) STF(2);
  if constexpr (G == 29) STF(3);
  if constexpr (G == 0) asm volatile("s_waitcnt lgkmcnt(0)");
  else if constexpr (G >= 8 && G < 28 && !(G & 1)) asm volatile("s_waitcnt lgkmcnt(%0)" ::"i"(G <= 24 ? 6 : 4));
  else if constexpr (G == 28) asm volatile("s_waitcnt vmcnt(16) lgkmcnt(0)\n\ts_barrier" ::: "memory");
  qk_mfma<L, kb, 1, s, false>();
  if constexpr (MG && G < 8) asm volatile("ds_read_b128 v[%c0:%c1], v85 offset:%c2" ::"i"(mg_reg(L, G)), "i"(mg_reg(L, G) + 3), "i"(G * 1024));
  if constexpr (G < 24) kread<L, 8 + G>();
  if constexpr (G < 24 && s == 1) mask_cmp<16 * (kb + 1), 126>();
  if constexpr (G < 24 && s == 3) mask_sel<R_CI + 8 * (kb + 1) + 4, R_M + 1>();
  if constexpr (G >= 4 && G < 28 && G % 3 == 2) w_load<(G - 4) / 3>();
  if constexpr (G == 25) asm volatile("s_add_u32 s82, s83, 0x1000000" SCC3);
  if constexpr (G == 27) dma_m0<L, 0>();
  if constexpr (G >= 28) { vread<L ^ 1, 2 * (G - 28)>(); vread<L ^ 1, 2 * (G - 28) + 1>(); dma_at<G - 28>(); }
  sm_gap<L ^ 1, 64 + 2 * G>(c.redo);
  if constexpr (MG) asm volatile("v_mfma_f32_16x16x32_bf16 a[%c0:%c1], v[%c2:%c3], v[%c4:%c5], a[%c0:%c1]" ::"i"(o), "i"(o + 3), "i"(a), "i"(a + 3), "i"(b), "i"(b + 3));
  sm_gap<L ^ 1, 65 + 2 * G>(c.redo);
  GAP();
}
template <int L, bool MG, int... Gs> DI void phaseAD(AC& c, iseq<Gs...>) {
  sld<72, B_NXT, 1>();
  mask_cmp<0, 126>(); mask_sel<R_CI + 4, R_M + 1>();
  (gapAD<L, MG, Gs>(c), ...);
}
#define BDX 8
constexpr int bd_w(bool EPI, int G) { return EPI && G >= 19 && G % 4 == 3 && (G - 19) / 4 < BDX ? 16 + (G - 19) / 4 : -1; }
template <int L, bool EPI, int G> DI void gapBD(AC& c, const EP& p) {
  constexpr int g = G & 15, e = (G >> 4) * 8 + (g - 8), o = e - 3;
  if constexpr (G == 0) STF(5);
  if constexpr (G == 8) STF(6);
  if constexpr (G == 9) STF(7);
  if constexpr (G == 16) STF(8);
  if constexpr (G == 17) STF(9);
  if constexpr (G == 32) STF(10);
  if constexpr (G == 48) STF(11);
  if constexpr (G == 1) dma_m0<L, 4>();
  if constexpr (G == 8) asm volatile("s_waitcnt vmcnt(14)" ::: "memory");
  if constexpr (G == 16) asm volatile("s_waitcnt lgkmcnt(0)\n\ts_barrier" ::: "memory");
  gapB<L, false, G>(c);
  if constexpr (G >= 8 && G < 12)
    asm volatile("ds_read_b128 a[%c0:%c1], v90 offset:%c4\n\tds_read_b128 a[%c2:%c3], v90 offset:%c5" ::"i"(96 + 8 * G), "i"(99 + 8 * G), "i"(100 + 8 * G), "i"(103 + 8 * G),
                 "i"((L ^ 1) * 32768 + (G - 8) * 2048), "i"((L ^ 1) * 32768 + (G - 8) * 2048 + 1024));
  if constexpr (G == 1 || G == 2) asm volatile("v_add_f32_e32 v%c0, v%c0, v%c1" ::"i"(R_RS + G - 1), "i"(R_TS + G - 1));
  if constexpr (G >= 3 && G < 19 && G % 4 == 3) dma_at<4 + (G - 3) / 4>();
  if constexpr (!EPI && G == 17)
    asm volatile("s_cmp_eq_u32 s97, 0\n\ts_cbranch_scc1 1f\n\ts_add_u32 s64, s100, 4 * 800\n\ts_addc_u32 s65, s101, 0\n\ts_swappc_b64 s[98:99], s[64:65]\n1:" ::: SCC_ "memory");
  if constexpr (bd_w(EPI, G) >= 0) w_load<bd_w(EPI, G)>();
  if constexpr (g >= 8 && e < 16) asm volatile("v_exp_f32 v%c0, v%c1" ::"i"(R_E + e), "i"(R_S + 32 * L + 4 + 8 * (e >> 2) + (e & 3)));
  if constexpr (g >= 8 && o >= 1 && o < 16) {
    if constexpr (o == 1) asm volatile("v_add_f32_e32 v%c0, v%c1, v%c2" ::"i"(R_TS + 1), "i"(R_E), "i"(R_E + 1));
    else asm volatile("v_add_f32_e32 v%c0, v%c0, v%c1" ::"i"(R_TS + 1), "i"(R_E + o));
    if constexpr (o & 1) asm volatile("v_cvt_pk_bf16_f32 v%c0, v%c1, v%c2" ::"i"(p_reg(L, (o >> 2) * 4 + 2 + ((o >> 1) & 1))), "i"(R_E + o - 1), "i"(R_E + o));
  }
  if constexpr (G == 45) asm volatile("v_cmp_nle_f32_e64 %0, v%c1, v%c2" : "=s"(c.redo) : "i"(R_TS + 1), "i"(R_THR));
  if constexpr (EPI && G >= 10 && G < 58 && !(G & 1)) epi_pair<((G - 10) >> 3) * 2, ((G - 10) >> 1) & 3, 0, 224>(p);
  if constexpr (EPI && G >= 17 && G < 58 && (G & 7) == 1) ctx_store<((G - 17) >> 3) * 2>();
  GAP();
}
template <int L, bool EPI, int... Gs> DI void phaseBD(AC& c, const EP& p, iseq<Gs...>) {
  if constexpr (!EPI) { sld<96, B_LM, 1>(); sld<97, B_LM + 1, 1>(); asm volatile("s_nop 0"); fl_check(); }
  (gapBD<L, EPI, Gs>(c, p), ...);
}

#define ACLOB_ALL "a0","a1","a2","a3","a4","a5","a6","a7","a8","a9","a10","a11","a12","a13","a14","a15","a16","a17","a18","a19","a20","a21","a22","a23","a24","a25","a26","a27","a28","a29","a30","a31","a32","a33","a34","a35","a36","a37","a38","a39","a40","a41","a42","a43","a44","a45","a46","a47","a48","a49","a50","a51","a52","a53","a54","a55","a56","a57","a58","a59","a60","a61","a62","a63","a64","a65","a66","a67","a68","a69","a70","a71","a72","a73","a74","a75","a76","a77","a78","a79","a80","a81","a82","a83","a84","a85","a86","a87","a88","a89","a90","a91","a92","a93","a94","a95","a96","a97","a98","a99","a100","a101","a102","a103","a104","a105","a106","a107","a108","a109","a110","a111","a112","a113","a114","a115","a116","a117","a118","a119","a120","a121","a122","a123","a124","a125","a126","a127","a128","a129","a130","a131","a132","a133","a134","a135","a136","a137","a138","a139","a140","a141","a142","a143","a144","a145","a146","a147","a148","a149","a150","a151","a152","a153","a154","a155","a156","a157","a158","a159","a160","a161","a162","a163","a164","a165","a166","a167","a168","a169","a170","a171","a172","a173","a174","a175","a176","a177","a178","a179","a180","a181","a182","a183","a184","a185","a186","a187","a188","a189","a190","a191","a192","a193","a194","a195","a196","a197","a198","a199","a200","a201","a202","a203","a204","a205","a206","a207","a208","a209","a210","a211","a212","a213","a214","a215","a216","a217","a218","a219","a220","a221","a222","a223","a224","a225","a226","a227","a228","a229","a230","a231","a232","a233","a234","a235","a236","a237","a238","a239","a240","a241","a242","a243","a244","a245","a246","a247","a248","a249","a250","a251","a252","a253","a254","a255"

template <int I> DI void epi_store() {
  asm volatile("buffer_store_dwordx4 v[%c0:%c1], v90, " SW ", s%c2 offen offset:%c3 sc1" ::"i"(128 + 4 * I), "i"(131 + 4 * I), "i"(76 + (I >> 2)), "i"((I & 3) * 1024) : "memory");
}
template <int HI> DI void epi_init() {
  asm volatile("v_readlane_b32 s76, v93, %c0\n\ts_nop 0\n\ts_add_u32 s77, s76, 0x1000\n\ts_add_u32 s78, s76, %1\n\ts_add_u32 s79, s78, 0x1000" ::"i"(B_SO), "i"(HI) SCC1);
}
template <int I> DI void epi_pread() {
  asm volatile("ds_read_b128 v[%c0:%c1], v107 offset:%c2" ::"i"(128 + 4 * I), "i"(131 + 4 * I), "i"(I * 1024));
}
template <int... Is> DI void epi_preads(iseq<Is...>) { (epi_pread<Is>(), ...); }
template <int G> DI void gapF(AC& c, const EP& p) {
  if constexpr (G < 64) gapB<ODD, false, G>(c);
  if constexpr (G == 1) asm volatile("v_mov_b32 v93, v94\n\ts_brev_b32 s73, 1");
  if constexpr (G >= 3 && G < 10) att_item<G - 3>();
  if constexpr (G >= 10) epi_pair<(G - 10) / 4, (G - 10) & 3, 2>(p);
  if constexpr (G >= 14 && ((G - 14) & 3) == 0) epi_store<(G - 14) / 4>();
  if constexpr (G == 73) epi_store<15>();
  GAP();
}
template <int... Gs> DI void phaseF(AC& c, const EP& p, iseq<Gs...>) { (gapF<Gs>(c, p), ...); }
constexpr int fd_w(bool EPI, int G) {
  const int f0 = EPI ? 16 + BDX : 16, n = NWF - f0;
  for (int i = 0; i < n; ++i) if (G == (EPI ? 2 + (i * 37) / n : i < 16 ? 2 + 2 * i : 4 + 2 * i)) return f0 + i;
  return -1;
}
#define FDA 50
#define CXT 0
template <int H> DI void ctx_touch() {
  asm volatile("v_readlane_b32 s63, v93, %c0\n\tv_mad_u32_u24 v110, v89, 7, v90\n\ts_nop 3\n\tbuffer_load_dword v111, v110, " SW ", s63 offen offset:0 sc1"
               ::"i"(B_SO) : "memory");
}
template <> DI void ctx_touch<1>() {
  asm volatile("v_readlane_b32 s63, v93, %c0\n\tv_mad_u32_u24 v110, v89, 7, v90\n\tv_add_u32 v110, 0x8000, v110\n\ts_nop 2\n\tbuffer_load_dword v111, v110, " SW ", s63 offen sc1"
               ::"i"(B_SO) : "memory");
}
#define FDE 20
constexpr int fd_cnt(bool EPI, int G0, int G1) { int n = 0; for (int G = G0; G < G1; ++G) n += (fd_w(EPI, G) >= 0) + ((G & 3) == 3); return n; }
constexpr bool fd_ok() {
  for (int G = 0; G < 64; ++G) {
    const int a = fd_w(0, G), b = fd_w(1, G);
    if ((a >= 16 && G < 2 * (a & 15) + (a >= 32 ? 34 : 2)) || (b >= 28 && G < (b >= 32 ? 2 * (b - 32) + 10 : 2 * (b - 28) + 2))) return false;
  }
  return true;
}
static_assert(fd_ok() && NWF == 40 && BDX <= 12);
template <int L, bool EPI, int G> DI void gapFD(const EP& p) {
  constexpr int I = EPI ? (G < 8 ? 12 + (G >> 2) * 2 : ((G - 8) >> 2) * 2 + 1) : G < 32 ? (G >> 2) * 2 : ((G - 32) >> 2) * 2 + 1;
  constexpr int u = EPI ? G : G - 2, uf = SNK & 2 ? u ^ ((u >> 1) & 1) : u;
  if constexpr (G == 0) STF(14);
  if constexpr (G == 8) STF(15);
  if constexpr (G == 16) STF(16);
  if constexpr (G == 21) STF(18);
  if constexpr (G == 32) STF(19);
  if constexpr (u >= 16 && u < 32 && (u & 3) == 0) asm volatile("s_waitcnt lgkmcnt(%0)" ::"i"(28 - u));
  if constexpr (u >= 0 && u < 32) pv_mfma<L, uf, 1>();
  if constexpr (u >= 0 && u < 16) vread<L, uf + 16>();
  constexpr int F = fd_w(EPI, G);
  if constexpr (F >= 0) w_load<F>();
  epi_pair<I, G & 3, !EPI>(p);
  if constexpr ((G & 3) == 3) ctx_store<I>();
  if constexpr (G == (EPI ? FDE : FDA)) {
    STF(17);
    asm volatile("s_waitcnt vmcnt(%0)\n\ts_barrier" ::"i"(fd_cnt(EPI, EPI ? 8 : 32, G + 1)) : "memory");
    set_flag<A_FC>();
    if constexpr (CXT & 1) ctx_touch<0>();
  }
  GAP();
}
template <int L, bool EPI, int... Gs> DI void phaseFD(const EP& p, iseq<Gs...>) { (gapFD<L, EPI, Gs>(p), ...); }
#define A_CT2 "v_readlane_b32 s63, v93, 3\n\ts_sub_u32 s64, s100, at_event - at_tail\n\ts_subb_u32 s65, s101, 0\n\ts_mov_b32 s66, at_event - at_tail\n\ts_mov_b32 s67, s59\n\t" \
    "v_lshl_add_u32 v78, v89, 9, s63\n\ts_mov_b64 exec, 0x1f\n\tbuffer_load_dword v84, v78, s[64:67], 0 offen\n\ts_mov_b64 exec, -1\n\t"
#define A_CT4 "s_mov_b64 s[64:65], s[100:101]\n\ts_movk_i32 s66, 0xf00\n\ts_mov_b32 s67, s59\n\tv_lshlrev_b32 v78, 3, v89\n\tbuffer_load_dword v84, v78, s[64:67], 0 offen\n\t"
#define A_RET "s_setpc_b64 s[98:99]\n\t"
#define A_EFLAG(L) "v_readlane_b32 s63, v93, " #L "\n\ts_nop 3\n\t" A_FLAG
#define A_EPOLL "v_readlane_b32 s96, v93, s85\n\ts_add_u32 s85, s85, 1\n\tv_readlane_b32 s97, v93, s85\n\ts_add_u32 s85, s85, 1\n\t" A_POLL("v[122:123]", "s[96:97]", "s62")
#define A_Q1 "buffer_load_dwordx4 v87, s[64:67], s95 offen lds\n\ts_add_u32 m0, m0, 0x400\n\ts_add_u32 s95, s95, 0x1c00\n\t"
#define A_EPOLL1 "s_mov_b32 exec_lo, 0xffff0000\n\ts_mov_b32 exec_hi, 0xffff\n\tbuffer_load_dwordx2 v[124:125], v88, " SW ", s92 offen sc1\n\ts_mov_b64 exec, -1\n\t" A_CT2
#define A_Z1(K, O) "v_xor_b32 v49, " K ", v48\n\tbuffer_load_dwordx4 v49, " SW ", s63 offen offset:" O " sc1 lds\n\t"
#define A_EQ "v_readlane_b32 s68, v92, 30\n\tv_readlane_b32 s95, v92, 28\n\ts_mov_b64 s[64:65], s[88:89]\n\ts_mov_b32 s66, 0x1c0000\n\ts_mov_b32 s67, s59\n\t" \
    "s_add_u32 m0, s68, 0x1000\n\ts_nop 1\n\t" A_Q1 A_Q1 A_Q1 A_Q1
#define A_ELOAD "v_readlane_b32 s68, v92, 30\n\tv_readlane_b32 s63, v92, 29\n\tv_lshrrev_b32 v48, 5, v89\n\ts_mov_b32 s96, 0\n\ts_mov_b32 s97, 0x3fff0000\n\t" \
    "v_and_b32 v48, 16, v48\n\t" A_CHECK("v[124:125]", "s[96:97]", "s92") "v_xor_b32 v48, v48, v89\n\ts_mov_b32 m0, s68\n\t" \
    A_Z1("0", "0") A_Z1("32", "1024") A_Z1("64", "2048") "s_mov_b32 exec_hi, 0\n\t" A_Z1("0x60", "3072") "s_mov_b32 exec_hi, -1\n\t"
#define A_PZ2(Z0, Z1, O) "ds_read_u16_d16_hi " Z0 ", v46 offset:" O "\n\tds_read_u16_d16_hi " Z1 ", v47 offset:" O "\n\t"
#define A_PRD(X, Q, O, Z0, Z1, Z2, Z3, Z4, Z5, Z6, Z7) "v_xor_b32 v46, " X ", v44\n\tds_read_b128 " Q ", v45 offset:" O "\n\tv_xor_b32 v47, 64, v46\n\t" \
    A_PZ2(Z0, Z1, "0") A_PZ2(Z2, Z3, "128") A_PZ2(Z4, Z5, "256") A_PZ2(Z6, Z7, "384")
#define A_PM2(Q, Z0, Z1) "v_lshlrev_b32 v40, 16, " Q "\n\tv_and_b32 v41, 0xffff0000, " Q "\n\tv_mul_f32 v40, v40, " Z0 "\n\tv_mul_f32 v41, v41, " Z1 "\n\tv_cvt_pk_bf16_f32 " Q ", v40, v41\n\t"
#define A_PST(Q) "buffer_store_dwordx4 " Q ", v43, s[64:67], s63 offen\n\ts_add_u32 s63, s63, 0x3800\n\t"
#define A_PA "v64", "v65", "v66", "v67", "v68", "v69", "v70", "v71"
#define A_PB "v72", "v73", "v74", "v75", "v76", "v77", "v78", "v79"
#define A_PRD_(...) A_PRD(__VA_ARGS__)
#define A_EPOST "v_readlane_b32 s68, v92, 30\n\tv_lshrrev_b32 v40, 4, v89\n\tv_readlane_b32 s64, v92, 0\n\tv_mul_u32_u24 v41, 37, v40\n\tv_readlane_b32 s65, v92, 1\n\t" \
    "v_lshrrev_b32 v41, 8, v41\n\tv_readlane_b32 s63, v94, 23\n\tv_mad_i32_i24 v42, v41, -7, v40\n\ts_movk_i32 s66, 0x380\n\ts_movk_i32 s67, 0x210\n\t" \
    "v_mad_u32_u24 v43, v41, s66, v87\n\tv_lshl_add_u32 v44, v41, 1, s68\n\tv_add_u32 v45, s68, v89\n\tv_mad_u32_u24 v44, v42, s67, v44\n\t" \
    "v_mov_b64 v[64:65], 0\n\tv_mov_b64 v[66:67], 0\n\tv_mov_b64 v[68:69], 0\n\tv_mov_b64 v[70:71], 0\n\t" \
    "v_mov_b64 v[72:73], 0\n\tv_mov_b64 v[74:75], 0\n\tv_mov_b64 v[76:77], 0\n\tv_mov_b64 v[78:79], 0\n\ts_mov_b32 s66, 0x380000\n\ts_mov_b32 s67, s59\n\t" \
    A_PRD_("0", "v[48:51]", "4096", A_PA) A_PRD_("16", "v[52:55]", "5120", A_PB) "s_waitcnt lgkmcnt(9)\n\t" \
    A_PM2("v48", "v64", "v65") A_PM2("v49", "v66", "v67") A_PM2("v50", "v68", "v69") A_PM2("v51", "v70", "v71") A_PST("v[48:51]") \
    A_PRD_("32", "v[56:59]", "6144", A_PA) "s_waitcnt lgkmcnt(9)\n\t" \
    A_PM2("v52", "v72", "v73") A_PM2("v53", "v74", "v75") A_PM2("v54", "v76", "v77") A_PM2("v55", "v78", "v79") A_PST("v[52:55]") \
    A_PRD_("48", "v[60:63]", "7168", A_PB) "s_waitcnt lgkmcnt(9)\n\t" \
    A_PM2("v56", "v64", "v65") A_PM2("v57", "v66", "v67") A_PM2("v58", "v68", "v69") A_PM2("v59", "v70", "v71") A_PST("v[56:59]") "s_waitcnt lgkmcnt(0)\n\t" \
    A_PM2("v60", "v72", "v73") A_PM2("v61", "v74", "v75") A_PM2("v62", "v76", "v77") A_PM2("v63", "v78", "v79") A_PST("v[60:63]")
#define A_TR(R0, R1, R2, R3, O0, O1) "ds_read_b64_tr_b16 " R0 ", v85 offset:" O0 "\n\tds_read_b64_tr_b16 " R1 ", v85 offset:" O1 "\n\t" \
    "ds_read_b64_tr_b16 " R2 ", v84 offset:" O0 "\n\tds_read_b64_tr_b16 " R3 ", v84 offset:" O1 "\n\t"
#define A_WS(R, S, O) "buffer_store_dwordx4 " R ", v89, " SW ", " S " offen offset:" O " sc1\n\t"
#define A_PL1(O) "buffer_load_dwordx4 v89, " SW ", s63 offen offset:" O " sc1 lds\n\t"
#define A_PL2(R, O) "buffer_load_dwordx4 " R ", v89, " SW ", s95 offen offset:" O " sc1\n\t"
#define A_EPLOAD "v_readlane_b32 s68, v92, 30\n\tv_readlane_b32 s63, v93, 21\n\ts_mov_b32 m0, s68\n\ts_add_u32 s95, s63, 0x8000\n\ts_nop 2\n\t" \
    A_PL1("0") A_PL1("1024") A_PL1("2048") A_PL1("3072") "s_bitset1_b32 m0, 12\n\ts_add_u32 s63, s63, 0x1000\n\t" A_PL1("0") A_PL1("1024") A_PL1("2048") A_PL1("3072") \
    A_PL2("v[40:43]", "0") A_PL2("v[44:47]", "1024") A_PL2("v[48:51]", "2048") A_PL2("v[52:55]", "3072") "s_add_u32 s95, s95, 0x1000\n\t" \
    A_PL2("v[56:59]", "0") A_PL2("v[60:63]", "1024") A_PL2("v[64:67]", "2048") A_PL2("v[68:71]", "3072") \
    "v_and_b32 v78, 0xf0, v89\n\tbuffer_load_dwordx4 v[72:75], v78, " SW ", s73 offen sc1\n\t"
#define A_EPOLLZ "s_mov_b32 exec_lo, 0\n\ts_mov_b32 exec_hi, 0x3fff0000\n\tbuffer_load_dwordx2 v[124:125], v88, " SW ", s92 offen sc1\n\ts_mov_b64 exec, -1\n\t"
#define WTOUCH 0
#define A_WT
#define A_ECHK1 "s_mov_b32 s70, 0xffff0000\n\ts_mov_b32 s71, 0xffff\n\t" A_CHECK("v[124:125]", "s[70:71]", "s92") A_WT
#define A_AT(N) ".fill (at_event + 4 * " #N " - .) / 4, 4, 0xbf800000\n\t"
#define A_EVENT \
    ".p2align 6\nat_event:\n\t" A_RET \
    A_AT(16) "v_cmp_eq_u64 vcc, s[60:61], v[122:123]\n\ts_and_b64 vcc, vcc, s[96:97]\n\ts_cmp_eq_u64 vcc, s[96:97]\n\ts_cbranch_scc0 7f\n\t" A_RET \
    A_AT(32) A_EFLAG(5) A_CT4 A_RET \
    A_AT(48) A_EFLAG(5) A_AT(56) A_EPOLL A_RET \
    A_AT(80) A_EFLAG(4) A_RET \
    A_AT(96) A_EFLAG(5) A_CT4 A_EPOLL A_RET \
    A_AT(128) "v_xor_b32 v84, 32, v85\n\tv_readlane_b32 s63, v92, 27\n\t" \
    A_TR("v[48:49]", "v[50:51]", "v[52:53]", "v[54:55]", "0", "4096") A_TR("v[56:57]", "v[58:59]", "v[60:61]", "v[62:63]", "8192", "12288") \
    A_TR("v[64:65]", "v[66:67]", "v[68:69]", "v[70:71]", "16384", "20480") A_TR("v[72:73]", "v[74:75]", "v[76:77]", "v[78:79]", "24576", "28672") \
    "s_add_u32 s95, s63, 0x1000\n\ts_waitcnt lgkmcnt(8)\n\t" \
    A_WS("v[48:51]", "s63", "0") A_WS("v[52:55]", "s63", "1024") A_WS("v[56:59]", "s63", "2048") A_WS("v[60:63]", "s63", "3072") "s_waitcnt lgkmcnt(0)\n\t" \
    A_WS("v[64:67]", "s95", "0") A_WS("v[68:71]", "s95", "1024") A_WS("v[72:75]", "s95", "2048") A_WS("v[76:79]", "s95", "3072") A_RET \
    A_AT(192) A_EPOLL1 A_RET \
    A_AT(228) A_EPOLL A_AT(240) A_EPOLL1 \
    A_MASK("v224", "v225", "v226", "v227", "0", "1", "2", "3") \
    A_MASK("v232", "v233", "v234", "v235", "16", "17", "18", "19") \
    A_MASK("v240", "v241", "v242", "v243", "32", "33", "34", "35") \
    A_MASK("v248", "v249", "v250", "v251", "48", "49", "50", "51") A_RET \
    "7:\n\t" A_SPIN("v[122:123]", "s[96:97]", "s62") A_RET \
    A_AT(344) A_EFLAG(4) A_EPOLLZ A_EQ A_RET \
    A_AT(400) A_ELOAD A_RET \
    A_AT(448) A_EFLAG(5) A_CT4 A_EPOLLZ A_EQ A_RET \
    A_AT(512) A_EPOST A_RET \
    A_AT(768) "5:\n\t" A_CHECK("v[122:123]", "s[96:97]", "s62") "s_branch 6f\n\t" A_AT(800) "6:\n\t" A_EPLOAD A_RET \
    A_AT(872) A_ECHK1 A_RET \
    A_AT(912) A_ECHK1 "s_branch 5b\n\t"
static_assert(B_WT == 24 && B_CT == 3 && B_FSET == 5 && B_FW == 4 && A_M0F == 30 && A_PQ == 28 && A_PZ == 29 && A_WDST == 27 && C_PO == 23 && B_LP == 21);
DI void event() {
  asm volatile("s_and_b32 s63, s87, 0x3ff\n\ts_lshl2_add_u32 s64, s63, s100\n\ts_addc_u32 s65, s101, 0\n\ts_swappc_b64 s[98:99], s[64:65]" ::: "vcc", SCC_ "memory",
               "v78", "v79");
  GAP();
}

#define OFU 20
static_assert(OFU >= 4 && OFU < 28 && OFU % 2 == 0);
#define OGB 14
constexpr int o_qo(int P) { return O_SEQ[P] & 3; }
constexpr int o_o(int P) { return O_SEQ[P] >> 4; }
constexpr int o_ks(int k) { return k % 7 < 4 ? 4 + k % 7 : k % 7 - 4; }
constexpr int o_sq(int P) {
  for (int q = P; q < 16; ++q) if (P < 1 || o_qo(q) == o_qo(P - 1)) return -1;
  return o_qo(P - 1);
}
constexpr int o_last() { int l = 0; for (int P = 0; P < 16; ++P) if (o_sq(P) >= 0) l = P; return l; }
constexpr bool o_first(int P) { for (int q = 0; q < P; ++q) if (o_qo(q) == o_qo(P)) return false; return true; }
template <int P, int MB, int KK> DI void o_read() {
  constexpr int r = 128 + 4 * (8 * MB + KK), sl = o_k(P) < 0 ? o_qo(P) : o_ks(o_k(P));
  asm volatile("ds_read_b128 v[%c0:%c1], v%c2 offset:%c3" ::"i"(r), "i"(r + 3), "i"(sl < 4 ? 96 + sl : 86), "i"((sl < 4 ? 0 : (sl & 3) * 16384) + (2 * KK + MB) * 1024));
}
template <int... Is> DI void o_read0(iseq<Is...>) { (o_read<0, (Is >> 3), Is & 7>(), ...); }
constexpr int o_gb(int NF) { return NF == 2 ? OGB : 0; }
#define OTAB {40, 44, 48, -1}, {52, -1, 0, 1}, {56, -1, 2, 60}, {-1, -1, 3, 4}, {-1, -1, 5, 6}, {-1, -1, 7, -1}, {-1, -1, 8, -1}, {-1, -1, 9, -1}, {-1, -1, 10, -1}, {-1, -1, 11, -1}
constexpr int O_TAB[][4] = {OTAB};
constexpr int o_op(int P, int U, int NF) {
  const int c = U / 8 == 1 && o_sq(P) >= 0 ? 99 : P < (int)(sizeof(O_TAB) / 16) ? O_TAB[P][U / 8] : -1, i = (U & 7) / 2;
  if (c < 0 || (U & 1)) return 0;
  if (c < 32) return 200 + 4 * c + i;
  if (c == 99) return i == 3 || (i == 1 && NF == 2) ? 300 + i : 0;
  return NF == 1 && (i & 1) ? 0 : 100 + c + i;
}
constexpr int o_vm(int P, int NF) {
  int n = 0;
  for (int i = 0; i <= P; ++i) {
    for (int U = 0; U < (i < P ? 32 : o_gb(NF)); ++U) {
      const int op = o_op(i, U, NF);
      if (op) ++n;
      if (op >= 200 && op < 300 && (op - 200) / 4 == o_k(P + 1)) n = 0;
      if (op >= 100 && op < 200 && (op - 100) / 16 == o_o(P + 1)) n = 0;
    }
  }
  return n;
}
constexpr bool o_ok() {
  for (int P = 1; P < 16; ++P) {
    for (int i = P - 1; i < 16; ++i) for (int U = 0; U < 32; ++U) {
      const int op = o_op(i, U, 2);
      if (i > P - 1 || U >= 0) {
        if (op >= 200 && op < 300 && (op - 200) / 4 == o_k(P)) return false;
        if (op >= 100 && op < 200 && (op - 100) / 16 == o_o(P)) return false;
      }
    }
  }
  for (int i = 0; i < 16; ++i) for (int U = 0; U < 32; ++U) {
    const int op = o_op(i, U, 2);
    if (op >= 200 && op < 300) {
      const int k = (op - 200) / 4, sl = o_ks(k);
      int prev = -1;
      for (int P = 0; P < 16; ++P) if ((o_k(P) < 0 ? o_qo(P) : o_ks(o_k(P))) == sl && (o_k(P) < k)) prev = P;
      if (prev >= 0 && !(i > prev + 1 || (i == prev + 1 && U >= 16))) return false;
    }
  }
  return true;
}
static_assert(NWF == 40 && o_ok());
template <int K> DI void o_m0() {
  constexpr int sl = o_ks(K);
  if constexpr (sl < 4) asm volatile("s_add_u32 m0, s80, s%c0" ::"i"(64 + sl) SCC1);
  else asm volatile("s_add_u32 m0, s80, %0" ::"i"(sl * 16384) SCC1);
}
template <int NF, int SR, int C> DI void o_pack() {
  if constexpr (NF == 2 || !(C & 2))
    asm volatile("v_cvt_pk_bf16_f32 v%c0, v%c1, v%c2" ::"i"(NF == 2 ? SR + C : SR + (C >> 2) * 2 + (C & 1)), "i"(SR + 2 * C), "i"(SR + 2 * C + 1));
}
template <int SR, int F> DI void o_sst() {
  asm volatile("buffer_store_dwordx4 v[%c0:%c1], v89, " SW ", s77 offen offset:%c2 sc1" ::"i"(SR + 4 * F), "i"(SR + 4 * F + 3), "i"(F * 1024) : "memory");
}
template <int I, int V> DI void o_slab() {
  asm volatile("buffer_load_dwordx4 v[%c0:%c1], v%c4, " SW ", s%c2 offen offset:%c3 sc1" ::"i"(192 + 4 * I), "i"(195 + 4 * I), "i"(64 + I / 2), "i"((I & 1) * 1024), "i"(V) : "memory");
}
template <int NF, int P, int G> DI void o_gap() {
  constexpr int NG = 16 * NF, ch = G >> 3, mb = ch / NF, nfi = (SNK & 16) && NF == 2 ? (ch ^ (ch >> 1)) & 1 : ch % NF, kk = (SNK & 16) && NF == 2 && (ch & 1) ? 7 - (G & 7) : G & 7, sq = o_sq(P), sr = 192 + 16 * sq;
  constexpr int w = o_wreg(o_o(P), 2 * kk + nfi), b = 128 + 4 * (8 * mb + kk), d = 192 + 16 * o_qo(P) + 4 * (2 * mb + nfi);
  constexpr bool nxt = P != 15;
  constexpr int U = NF == 2 ? G : 2 * G, op = o_op(P, U, NF), opn = U < 24 ? o_op(P, U + 8, NF) : nxt ? o_op(P + 1, U - 24, NF) : 0;
  if constexpr (G == 0) asm volatile("s_waitcnt lgkmcnt(8)" ::: "memory");
  if constexpr (G == NG / 2) asm volatile("s_waitcnt lgkmcnt(%0)" ::"i"(nxt && NF == 1 ? 8 : 0) : "memory");
  if constexpr (nxt && G == o_gb(NF) && P >= 1 && (o_k(nxt ? P + 1 : P) >= 0 || P < 3)) asm volatile("s_waitcnt vmcnt(%0)\n\ts_barrier" ::"i"(o_vm(P, NF)) : "memory");
  if constexpr (P == 1 && G == o_gb(NF))
  {
    asm volatile("v_readlane_b32 s63, v92, %c0\n\ts_nop 0\n\ts_add_u32 s63, s63, 32\n\t" A_FLAG ::"i"(A_FC) : SCC_ "memory");
    if constexpr (CXT & 2) ctx_touch<1>();
  }
  if constexpr (P == o_last() + 1 && U == OFU) {
    asm volatile("s_waitcnt vmcnt(0)" ::: "memory");
    set_flag<A_FS>();
  }
  if constexpr (op >= 200 && op < 300 && (op & 3) == 0) o_m0<(op - 200) / 4>();
  if constexpr (o_first(P) && !(G & 7))
    asm volatile("v_mfma_f32_16x16x32_bf16 v[%c0:%c1], a[%c2:%c3], v[%c4:%c5], 0" ::"i"(d), "i"(d + 3), "i"(w), "i"(w + 3), "i"(b), "i"(b + 3));
  else
    asm volatile("v_mfma_f32_16x16x32_bf16 v[%c0:%c1], a[%c2:%c3], v[%c4:%c5], v[%c0:%c1]" ::"i"(d), "i"(d + 3), "i"(w), "i"(w + 3), "i"(b), "i"(b + 3));
  if constexpr (nxt && G >= NG - 16) o_read<P + 1, (G >> 3) & 1, kk>();
  if constexpr (op >= 100 && op < 200) w_load<op - 100, 1 + NF>();
  if constexpr (op >= 200 && op < 300)
    asm volatile("buffer_load_dwordx4 v89, " SW ", s%c0 offen offset:%c1 sc1 lds" ::"i"(73 + U / 8), "i"((op & 3) * 1024) : "memory");
  if constexpr (sq >= 0) {
    if constexpr (G == 0) sld<77, A_SS + sq>();
    if constexpr (NF == 2 && G >= 6 && G < 14) o_pack<2, sr, G - 6>();
    if constexpr (NF == 1 && (G == 4 || G == 5)) { o_pack<1, sr, 4 * (G - 4)>(); o_pack<1, sr, 4 * (G - 4) + 1>(); }
    if constexpr (op >= 300) o_sst<sr, NF == 2 ? (op - 300) / 2 : 0>();
  }
  if constexpr (opn >= 200 && opn < 300 && (opn & 3) == 0) sld<73 + ((U + 8) & 31) / 8, A_PC + (opn - 200) / 4>();
  if constexpr (P == 12 && G == NG - 4) { sld<62, A_FSW>(); fl_mask<A_FSM, -1>(); }
  if constexpr (P == 13 && G == 1) fl_issue();
  if constexpr (P == 13 && G >= 2 && G < 5) sld<62 + G, A_SL + G - 2>();
  if constexpr (P == 13 && G == NG - 2)
    asm volatile("v_cmp_eq_u64 vcc, s[60:61], v[122:123]\n\ts_and_b64 vcc, vcc, s[96:97]\n\ts_cmp_eq_u64 vcc, s[96:97]\n\ts_cselect_b32 s67, 0, 0x80000000\n\t"
                 "v_or_b32 v100, s67, v89" ::: "vcc", SCC_ "memory");
  if constexpr (P == 14 && G % NF == 0 && G >= 2 * NF && G < 14 * NF && !((G / NF) & 1) && (NF == 2 || !((G / NF) & 2) == false)) o_slab<G / NF / 2 - 1, 100>();
  GAP();
}
template <int NF, int P, int... Gs> DI void o_interval_(iseq<Gs...>) {
  (o_gap<NF, P, Gs>(), ...);
}
template <int NF, int P> DI void o_interval() { o_interval_<NF, P>(make_iseq<16 * NF>{}); }
template <int NF> DI void o_slabs() {
  unsigned late;
  asm volatile("s_mov_b32 %0, s67" : "=s"(late));
  if (late) {
    fl_wait();
    static_for([&](auto I) { if constexpr (NF == 2 || !(I & 1)) o_slab<I, 89>(); }, make_iseq<6>{});
  }
  GAP();
}
template <int NF> DI void o_sum() {
  static_for([&](auto I) {
    constexpr int i = I, q = i >> 2, f = i & 3, b = 192 + 8 * q + (NF == 2 ? 4 * (f >> 1) : 0), a = 72 + 2 * (NF == 2 ? f & 1 : f >> 1);
    if constexpr (f == 0) asm volatile("s_waitcnt vmcnt(%0)" ::"i"((2 - q) * NF) : "memory");
    if constexpr (NF == 2 || !(f & 1))
      asm volatile("v_mfma_f32_16x16x32_bf16 v[%c0:%c1], v[%c2:%c3], v[%c4:%c5], v[%c0:%c1]" ::"i"(240 + 4 * f), "i"(243 + 4 * f), "i"(a), "i"(a + 3), "i"(b), "i"(b + 3));
  }, make_iseq<12>{});
  asm volatile("s_nop 7");
}

#define NSG 64
#define KWAIT "s_waitcnt lgkmcnt(0)\n\t"
#define EARLY2 0
#define TOUCH0 0x20
#define TOUCH1 0x20
__global__ __launch_bounds__(256, 1) __attribute__((amdgpu_num_vgpr(82), amdgpu_num_sgpr(NSG)))
void fused_kernel(const bf16* a0, const bf16* a1, const bf16* a2, const bf16* a3, const bf16* a4, char* ws, const bf16* __restrict__ ak, const bf16* __restrict__ av,
                  const bf16* __restrict__ ash, const bf16* __restrict__ assmk, const bf16* __restrict__ assmD, const bf16* __restrict__ wo,
                  const bf16* __restrict__ aq, bf16* __restrict__ out) {
  int b = blockIdx.x;
  asm volatile("s_load_dwordx8 s[92:99], %1, 0\n\ts_load_dwordx2 s[60:61], %1, 32" : "+s"(b) : "s"(__builtin_amdgcn_kernarg_segment_ptr()));
  const bf16* ckv;
  const int bp = b >> 3;
  extern __shared__ __attribute__((aligned(16))) char lds[];
  asm volatile("at_code_begin:" ::: ACLOB_ALL, "v255", "s101");
  KST(0, 0);
  const int tid = threadIdx.x;
  int lane = tid & 63;
  const int wid = __builtin_amdgcn_readfirstlane(tid >> 6);
  const int j = b & 7, r = b >> 3, rr = r & 15, pj = rr >> 1, kv = pj & 1;
  const bool isY = r >= 16, doA = !isY && pj != NCV;
  const int cr = rr - (pj > NCV ? 2 : 0);
  const bf16* wkv;
  const int dq = Q44 ? (j & 1) - ((rr >> 1) & 1) : 0;
  unsigned vq, vw, tb0, vbA2, vD;
  const char* tp;
  {
    GM g;
    {
      static_assert(Q44 && K2(1) == 3);
#define CTP 0
#define CTM "0xff"
#define P_TM "s_memrealtime s[52:53]\n\ts_waitcnt lgkmcnt(0)\n\t"
#define P_T40
#define P_T41
#define P_T42
#define P_CT "s_getpc_b64 s[76:77]\n\tv_mbcnt_lo_u32_b32 v99, -1, 0\n\ts_lshr_b32 s88, %4, 3\n\ts_and_b32 s76, s76, 0xffffff80\n\ts_lshl_b32 s88, s88, 9\n\ts_and_b32 s77, s77, 0xffff\n\t" \
             "s_lshl1_add_u32 s88, s90, s88\n\ts_mov_b32 s78, pf_code_end - at_code_begin\n\ts_mov_b32 s79, 0x20000\n\tv_lshl_add_u32 v99, v99, 14, s88\n\ts_mov_b32 m0, %5\n\t" \
             "s_mov_b64 exec, " CTM "\n\tbuffer_load_dword v99, s[76:79], 0 offen lds\n\ts_mov_b64 exec, -1\n\t"
#define P_CTL(R0, R1, R2, R3, RR) "s_getpc_b64 s[" R0 ":" R1 "]\n\tv_mbcnt_lo_u32_b32 v100, -1, 0\n\tv_readfirstlane_b32 s89, %3\n\ts_lshr_b32 s88, %4, 3\n\ts_and_b32 s" R0 ", s" R0 ", 0xffffff80\n\t" \
             "s_lshl_b32 s88, s88, 9\n\ts_and_b32 s" R1 ", s" R1 ", 0xffff\n\ts_lshl1_add_u32 s88, s89, s88\n\ts_mov_b32 s" R2 ", pf_code_end - at_code_begin\n\ts_mov_b32 s" R3 ", 0x20000\n\t" \
             "v_lshl_add_u32 v100, v100, 14, s88\n\ts_mov_b32 m0, %5\n\ts_mov_b64 exec, 0xff\n\tbuffer_load_dword v100, " RR ", 0 offen lds\n\ts_mov_b64 exec, -1\n\t"
#define P_CT0 P_CT
#define P_CTX1
#define P_CTX2
#define P_LA(SO) "buffer_load_dwordx4 v86, s[64:67], " SO " offen" GSC " lds\n\t"
#define P_LW(SO) "buffer_load_dwordx4 v87, s[68:71], " SO " offen" GSC " lds\n\t"
      asm volatile("v_readfirstlane_b32 s90, %3\n\tv_lshrrev_b32 v96, 4, %3\n\t" P_CT0 "v_lshrrev_b32 v97, 3, %3\n\ts_and_b32 s63, %2, 1\n\tv_xor_b32 v98, v96, %3\n\ts_lshl_b32 s88, s63, 2\n\t"
                   "s_bfe_u32 s89, %2, 0x20005\n\tv_and_b32 v98, 7, v98\n\ts_movk_i32 s62, 0x800\n\ts_bitcmp1_b32 %2, 7\n\t"
                   "s_cselect_b32 s88, s88, 0\n\ts_cmovk_i32 s62, 0x380\n\ts_add_u32 s89, s89, s88\n\ts_lshl_b32 s89, s89, 8\n\tv_lshlrev_b32 v98, 4, v98\n\t"
                   "v_add_u32 v97, s89, v97\n\ts_lshl_b32 s90, s90, 4\n\ts_and_b32 s88, %2, 0x80\n\ts_lshl_b32 s88, s88, 8\n\tv_mad_u32_u24 v86, v97, s62, v98\n\t"
                   "s_or_b32 s90, s90, s88\n\ts_lshl_b32 s66, s62, 11\n\ts_mov_b32 s67, 0x20000\n\ts_mov_b32 m0, s90\n\ts_lshl_b32 s72, s62, 5\n\ts_lshl_b32 s75, s62, 7\n\t"
                   "s_add_u32 s76, s75, s72\n\t" P_T40 "s_bitcmp1_b32 %2, 7\n\t" KWAIT "s_cselect_b64 s[64:65], s[94:95], s[92:93]\n\t"
                   P_LA("0") P_T41
                   "s_add_u32 m0, s90, 0x1000\n\tv_and_b32 v99, 3, v96\n\t" P_LA("s72")
                   "s_add_u32 m0, s90, 0x4000\n\tv_lshrrev_b32 v97, 6, %3\n\t" P_LA("s75")
                   "s_add_u32 m0, s90, 0x5000\n\tv_lshl_or_b32 v99, v99, 2, v97\n\t" P_LA("s76")
                   P_T42 P_CTX1 "s_bfe_u32 s88, %2, 0x10004\n\tv_xor_b32 v99, v99, %3\n\ts_sub_u32 s63, s63, s88\n\tv_and_b32 v99, 15, v99\n\ts_and_b32 s88, %2, 15\n\t"
                   "v_lshlrev_b32 v99, 4, v99\n\ts_bitcmp1_b32 %2, 7\n\ts_cselect_b32 s63, s63, 0\n\tv_lshl_add_u32 v99, v96, 13, v99\n\ts_sub_u32 s88, s88, s63\n\t"
                   "s_lshl_b32 s88, s88, 9\n\ts_bitcmp1_b32 %2, 4\n\ts_cselect_b64 %1, s[98:99], s[96:97]\n\tv_add_u32 v87, s88, v99\n\ts_bitcmp1_b32 %2, 7\n\t"
                   "s_cselect_b64 s[68:69], s[60:61], %1\n\ts_lshl_b32 s70, s62, 12\n\ts_add_u32 m0, s90, 0x10000\n\ts_mov_b32 s71, 0x20000\n\t"
                   P_LW("0")
                   "s_add_u32 m0, s90, 0x11000\n\ts_mov_b32 s80, 0x20000\n\t" P_LW("s80")
                   "s_add_u32 m0, s90, 0x12000\n\ts_movk_i32 s81, 0x100\n\t" P_LW("s81")
                   "s_add_u32 m0, s90, 0x13000\n\ts_mov_b32 s82, 0x20100\n\t" P_LW("s82")
                   "s_add_u32 m0, s90, 0x14000\n\ts_mov_b32 s83, 0x40000\n\t" P_LW("s83")
                   "s_add_u32 m0, s90, 0x15000\n\ts_mov_b32 s84, 0x60000\n\t" P_LW("s84")
                   "s_add_u32 m0, s90, 0x16000\n\ts_mov_b32 s85, 0x40100\n\t" P_LW("s85")
                   "s_add_u32 m0, s90, 0x17000\n\ts_mov_b32 s86, 0x60100\n\t" P_LW("s86")
                   P_CTX2 "s_mov_b64 %0, s[92:93]\n\ts_lshl_b32 s73, s72, 1\n\ts_add_u32 s74, s73, s72\n\ts_add_u32 s77, s75, s73\n\ts_add_u32 s78, s75, s74\n\t"
                   "s_and_b32 s79, s90, 0xfff\n\ts_add_u32 s87, s79, 0x10000\n\tv_add_u32 v84, 0x80, v86\n\tv_add_u32 v85, 0x80000, v87\n\t"
                   "s_mov_b32 s58, 0x6400000\n\ts_mov_b32 s59, 0x20000\n\ts_xor_b32 s92, s90, 0x8000\n\t"
                   "s_bfe_u32 s88, %2, 0x40003\n\ts_bfe_u32 s89, %2, 0x20005\n\ts_add_u32 s88, s88, s89\n\ts_and_b32 s88, s88, 3\n\ts_bitcmp1_b32 %2, 7\n\t"
                   "s_cselect_b32 s88, 0, s88\n\ts_add_u32 s91, s88, 2"
                   : "=&s"(ckv), "=&s"(wkv) : "s"(b), "v"(tid), "s"(blockIdx.x), "i"(L_SP + 16384) : "scc", "memory");
      GAP();
      asm volatile("" : "+s"(ws), "+s"(ak), "+s"(av), "+s"(ash), "+s"(assmk), "+s"(assmD), "+s"(wo), "+s"(aq), "+s"(out));
    }
    auto addrs = [&]() __attribute__((always_inline)) {
      asm volatile("" : "+v"(lane));
      const int fr = lane & 15, fq = lane >> 4, q = (lane & 15) >> 2, p = lane & 3;
#pragma unroll
      for (int k2 = 0; k2 < 2; ++k2)
        g.aA[k2] = (wid >> 1) * 16384 + (fr >> 3) * 1024 + (fr & 7) * 128 + (((4 * k2 + fq) ^ (fr >> 1)) * 16);
#pragma unroll
      for (int hf = 0; hf < 2; ++hf) {
        const unsigned a0 = 65536 + (wid & 1) * 8192 + off_b(8 * fq + 4 * hf + q, p >> 1) + 8 * (p & 1);
#pragma unroll
        for (int nf = 0; nf < 8; ++nf) { g.aB[hf][nf] = a0 ^ (nf * 32); asm volatile("" : "+v"(g.aB[hf][nf])); }
      }
      GAP();
    };
    auto consts2 = [&]() __attribute__((always_inline)) {
      const int tl = (lane * 37) >> 8, ck = lane - tl * 7;
      vq = lane < 56 ? (tl * DA + ck * 8) * 2 : OOB;
      asm volatile("" : "+v"(vq));
    };
    auto consts = [&](auto Yc) __attribute__((always_inline)) {
      const int fr = lane & 15, fq = lane >> 4;
      if constexpr (Yc) {
        vbA2 = ((((rr >> 2) + 4) * 256 + wid * 8 + (lane >> 3)) * DC + ((lane & 7) ^ (((wid & 1) * 4 + fq) & 7)) * 8) * 2;
        asm volatile("" : "+v"(vbA2));
      } else {
        consts2();
        vw = (4 * wid + fq) * DA * 2 + 16 * (fr ^ ((fq << 2) | wid));
        tb0 = L_SP + off_b(4 * fq + (fr >> 2), wid * 4 + ((lane & 3) >> 1)) + 8 * (lane & 1);
        tp = (lane < 32 && doA ? (const char*)assmk + (j * 56 + cr * 4 + wid) * 4096 : (const char*)ckv + 0x200000 + ((b & 127) * 4 + wid) * 4096) + (lane & 31) * 128;
        asm volatile("" : "+v"(vw), "+v"(tb0), "+v"(tp));
      }
      GAP();
    };
    addrs();
    PA pa;
    const int pu = p_unit(j, rr);
    if (isY) {
      const int rp = tid >> 3, c = tid & 7, ch = tid >> 4, q = tid & 15;
      pa.vo = pu < 0 ? OOB : (((pu & 15) * 128 + rp * 4) * DA + (pu >> 4) * 64 + c * 8) * 2;
      pa.lw = L_SP + 20480 + wid * 1024 + c * 64;
      pa.lx = L_SP + c * 2048 + (((31 - rp) * 8) ^ (c << 4));
      pa.lr = L_SP + ch * 256 + 16 * (q ^ (ch >> 3));
      pa.vs = ch * (SEQ * 2) + q * 16;
      asm volatile("" : "+v"(pa.vo), "+v"(pa.lw), "+v"(pa.lx), "+v"(pa.lr), "+v"(pa.vs));
      consts(std::true_type{});
      asm volatile("" : "=v"(vq), "=v"(vw), "=v"(tb0), "=v"(tp));
    } else {
      consts(std::false_type{});
      asm volatile("" : "=v"(pa.vo), "=v"(pa.lw), "=v"(pa.lx), "=v"(pa.lr), "=v"(pa.vs), "=v"(vbA2));
    }
    {
      const int fr = lane & 15, fq = lane >> 4;
      vseti<88>(fq * 4352 + fr * 8); vseti<89>(lane * 16); vseti<90>(wid * 8192 + lane * 16); vseti<91>(wid * 8192 + lane * 16 + 4096);
      asm volatile("s_getpc_b64 s[100:101]\n\ts_add_u32 s100, s100, at_event@rel32@lo+4\n\ts_addc_u32 s101, s101, at_event@rel32@hi+12" ::: "scc");
      asm volatile("s_lshl_b32 s63, %0, 18\n\tv_add_u32 v115, s63, v85\n\ts_xor_b32 s63, s63, 0x40000\n\tv_add_u32 v116, s63, v85\n\t"
                   "s_mul_i32 s63, %0, s73\n\tv_add_u32 v117, s63, v84\n\ts_sub_u32 s63, s73, s63\n\tv_add_u32 v118, s63, v84\n\t"
                   "s_lshl_b32 s63, %0, 13\n\ts_add_u32 s98, s92, s63\n\ts_xor_b32 s99, s98, 0x2000\n\t"
                   "s_add_u32 s96, s98, s63\n\ts_bitset1_b32 s96, 16\n\ts_xor_b32 s97, s96, 0x4000" ::"s"((rr + (rr >> 2)) & 1) : "scc");
      g_pro<12>(make_iseq<4>{});
      g_part<0, 0>(); g_part<0, 1>(); g_part<0, 2>(); g_part<0, 3>();
      {
        const char* xp;
        asm volatile("s_mov_b64 %0, s[94:95]" : "=s"(xp));
        asm volatile("s_mov_b32 m0, %1\n\ts_mov_b64 exec, 0x3fff\n\tglobal_load_lds_dword %0, off\n\ts_mov_b64 exec, -1"
                     ::"v"(xp + ((b * 4 + wid) * 14 + lane) * 128), "i"(L_SP + 16384) : "memory");
      }
      asm volatile("s_mov_b64 s[56:57], %0\n\ts_mov_b32 s63, %2\n\t"
                   "global_load_dwordx3 v[92:94], %1, off\n\tglobal_load_ushort v95, %3, off\n\tbuffer_load_dwordx2 v[120:121], off, " SW ", s63 sc1"
                   ::"s"(ws), "v"(&PTAB[j].v[r][0][tid][0]), "i"(fwo(6144)), "v"(assmD + j * 56 + (doA ? cr * 4 + wid : 0)) : "memory");
      pa.sk = __builtin_amdgcn_make_buffer_rsrc((void*)ak, (short)0, SEQ * DA * 2, 0x00020000);
      pa.sv = __builtin_amdgcn_make_buffer_rsrc((void*)av, (short)0, SEQ * DA * 2, 0x00020000);
      pa.sw = __builtin_amdgcn_make_buffer_rsrc((void*)(ash + (pu >> 4) * 256), (short)0, pu < 0 ? 0 : 512, 0x00020000);
      asm volatile("" : "+s"(pa.sk), "+s"(pa.sv), "+s"(pa.sw));
    }
    auto start = [&](auto Pc) __attribute__((always_inline)) {
      constexpr int P = Pc;
#define XW0 0
      asm volatile("s_waitcnt vmcnt(%0) lgkmcnt(0)\n\ts_barrier" ::"i"((CTP == 2 ? 13 : 12) + XW0) : "memory");
      g_read0<P>(g, make_iseq<16>{});
      asm volatile("s_waitcnt lgkmcnt(0)\n\ts_barrier" ::: "memory");
      GAP();
    };
    RSV rsv;
    if (isY) {
      start(std::integral_constant<int, 1>{});
      rsv_begin(rsv);
      static_for([&](auto Tc) __attribute__((always_inline)) {
        constexpr int T = Tc;
        q_half<T, 0>(g, pa);
        if constexpr (T == 4) {
          const bf16* w_ = wkv; const int d_ = dq * 512;
          sld<88, A_TAB>(); sld<89, A_TAB + 1>();
          asm volatile("s_mov_b64 s[68:69], %0\n\ts_mov_b32 s70, 0x800000\n\tv_add_u32 v87, %1, v87" ::"s"(w_), "s"(d_));
          code_touch<0>();
        }
        if constexpr (T == 5) {
          const bf16* c_ = ckv; const unsigned v_ = vbA2;
          asm volatile("s_mov_b64 s[64:65], %0\n\ts_mov_b32 s66, 0x400000\n\tv_mov_b32 v86, %1\n\ts_mov_b32 s72, 0x10000\n\ts_mov_b32 s73, 0x20000\n\t"
                       "s_mov_b32 s74, 0x30000\n\ts_mov_b32 s75, 0x40000\n\ts_mov_b32 s76, 0x50000\n\ts_mov_b32 s77, 0x60000\n\ts_mov_b32 s78, 0x70000"
                       ::"s"(c_), "v"(v_));
        }
        GAP();
        q_half<T, 1>(g, pa);
      }, make_iseq<6>{});
      rsv_end(rsv);
      asm volatile("ct_begin:");
      asm volatile("v_lshrrev_b32 %0, 4, v89" : "=v"(lane));
      consts2();
      asm volatile("" : "=v"(vw), "=v"(tb0), "=v"(tp), "=v"(vD));
      GAP();
      STT(0);
      g_half<false, 1, 0, 3>(g);
      g_half<false, 1, 1, 3>(g);
      set_flag<A_FPREP>();
      KST(0, 3);
    }
    unsigned om = 0x7000u | (isY ? (PARK ? 0x1eu : 4u) : (wid & 1) ? TOUCH1 : TOUCH0);
    asm volatile("" : "+s"(om));
    auto pollG = [&]() __attribute__((always_inline)) {
      sld<62, A_NPB>();
      fl_mask<A_NP, A_NP + 1>();
      asm volatile("s_nop 1");
      fl_issue();
    };
    auto oddG = [&](int t) __attribute__((always_inline)) {
      if (__builtin_expect((om >> t) & 1, 0)) {
        if (PARK && t == 1) park_store<0>();
        else if (PARK && t == 3) park_store<2>();
        else if (t != 13) {
          asm volatile("s_mov_b32 m0, %1\n\ts_nop 0\n\tglobal_load_lds_dword %0, off" ::"v"(tp), "i"(L_SP) : "memory");
        } else {
          fl_check();
          sld<63, A_VBB>(); sld<87, A_MB2>();
          asm volatile("s_mov_b64 s[68:69], %0\n\ts_mov_b32 s70, %1\n\ts_mov_b64 s[88:89], 0\n\tv_add_u32 v87, s63, v89\n\t"
                       "s_movk_i32 s80, 0x400\n\ts_movk_i32 s81, 0x800\n\ts_movk_i32 s82, 0xc00\n\ts_movk_i32 s83, 0x1000\n\t"
                       "s_movk_i32 s84, 0x1400\n\ts_movk_i32 s85, 0x1800\n\ts_movk_i32 s86, 0x1c00"
                       ::"s"(isY ? (const void*)ws : (const void*)assmk), "s"(isY ? 0x6400000 : DA * SEQ * 2));
        }
      }
    };
    auto evenG = [&](int t) __attribute__((always_inline)) {
      if (__builtin_expect((om >> t) & 1, 0)) {
        if (PARK && t == 2) park_store<1>();
        else if (t == (PARK ? 4 : 2)) set_flag<A_FT2>();
        else if (t == 12) pollG();
        else {
          sld<63, A_VBA>(); sld<79, A_MA2>();
          asm volatile("s_mov_b64 s[64:65], s[56:57]\n\ts_mov_b32 s66, s58\n\tv_add_u32 v87, 0x2000, v87\n\tv_add_u32 v86, s63, v89\n\t"
                       "s_movk_i32 s72, 0x1000\n\ts_movk_i32 s75, 0x4000\n\ts_movk_i32 s76, 0x5000");
          code_touch<1>();
        }
      }
    };
    auto tileG = [&](auto TRc) __attribute__((always_inline)) {
      constexpr bool TR = TRc;
      if (isY) {
        STT(5);
        g_half<TR, 0, 0, 2>(g);
        g_half<TR, 0, 1, 2>(g);
      } else {
        start(std::integral_constant<int, 0>{});
        g_half<TR, 0, 0, 1>(g);
        g_half<TR, 0, 1, 1>(g);
        vD = __float_as_uint(vget<95>());
      }
      g_loop<TR, false>(g, oddG, evenG);
      STT(10); STT(11);
      g_half<TR, 1, 0, 4>(g, wid);
      g_half<TR, 1, 1, 4>(g, wid);
      GAP();
    };
    if (kv) tileG(std::true_type{});
    else tileG(std::false_type{});
    STT(16);
  }
  att_item<3>(); att_item<4>(); att_item<5>(); att_item<6>();
  vseti<95>(wid * 16 + (lane & 15) - 4 * (lane >> 4)); vseti<126>(48 - wid * 16 + (lane & 15) - 4 * (lane >> 4)); vseti<127>(0xff800000u);
  asm volatile("s_mov_b64 s[88:89], %0\n\tv_mov_b32 v87, %1\n\ts_mov_b64 s[96:97], %2\n\tv_writelane_b32 v92, s96, 0\n\tv_writelane_b32 v92, s97, 1" ::"s"(aq), "v"(vq), "s"(out));
  KST(0, 4);
  if (isY) {
    sld<72, A_FCW>(); sld<96, B_MM, 1>(); sld<73, B_OP, 1>();
    asm volatile("s_mov_b32 s97, 0\n\ts_add_u32 m0, s78, 0x2000\n\ts_add_u32 s74, s74, 0x2000\n\t" A_POLL("v[122:123]", "s[96:97]", "s72") ::: "scc", "memory");
    dma4<89, 74>();
  } else if (doA) {
    const int l16 = lane & 15, fq = lane >> 4;
    CV c;
    c.lw = 65536 + wid * 15488;
    asm volatile("ds_write_b128 %0, v[114:117] offset:4096" ::"v"(c.lw + lane * 16));
    fl_mask<-1, A_NQ>();
#pragma unroll
    for (int i = 0; i < 4; ++i) c.pa[i] = c.lw + (((l16 + 1) & 1) ? CV_KS : CV_KP) + 4 * ((8 * fq + l16 + 1) >> 1) + 1024 * i;
    c.pb = c.lw + (2032 - 16 * l16 + 8 * fq) * 2 - 512 * 7;
    KST(0, 5);
    cv_rd0(c, make_iseq<24>{});
    static_for([&](auto I) { vseti<96 + I>(15 - l16 >> 1 == 4 * fq + I ? vD << (l16 & 1 ? 0 : 16) : 0u); }, make_iseq<4>{});
    fl_issue();
    c.vzo = (l16 >> 1) * 3584 + (l16 & 1) * 32 + (fq >> 1) * 16 + (1 - (fq & 1)) * 8 * 3584;
    GAP();
    cv_slots(c, make_iseq<144>{});
    static_for([&](auto T) { cv_slot<144 + T>(c); }, make_iseq<144>{});
    cv_tails(c, make_iseq<28 - cv_z0(288)>{});
    KST(0, 6);
    asm volatile("s_waitcnt vmcnt(%0) lgkmcnt(0)\n\ts_barrier" ::"i"(cv_end()) : "memory");
  } else {
    d_ops<160, 0>(make_iseq<208>{});
    fl_mask<-1, A_NQ>();
    sld<72, A_OQ>();
    fl_wait();
    asm volatile("s_waitcnt lgkmcnt(0)\n\ts_barrier" ::: "memory");
    set_flag<A_FG>();
    static_for([&](auto I) { f_load<128, I>(); if constexpr ((I & 3) == 3) f_next(); }, make_iseq<16>{});
    KST(0, 5);
    fl_mask<A_N01, -1>();
    fl_wait();
    k01_dma<0>(); k01_dma<1>(); k01_dma<2>(); k01_dma<3>();
    KST(0, 6);
    asm volatile("s_waitcnt vmcnt(8)\n\ts_barrier" ::: "memory");
  }
  if (!isY) {
    sld<94, A_WSRC>(); sld<73, B_OP, 1>();
    asm volatile("s_mov_b64 s[64:65], %0\n\ts_mov_b32 s66, 0x380000\n\ts_mov_b32 s67, s59\n\ts_mov_b32 s75, %1\n\t"
                 "v_mov_b32 v84, %2\n\tv_mov_b32 v85, %3\n\tv_mov_b32 v121, %4"
                 ::"s"(wo), "s"(L_SP + wid * 1024), "v"(vw), "v"(tb0), "v"(lane < 28 ? ((b * 4 + wid) * 28 + lane) * 128 : OOB));
  }
  u32x16 rs0, rs1; u32x4 rs2; u32x2 rs3;
#define STA(k)
  {
    AC c;
    asm volatile("rsv2_begin:" : "={v[40:55]}"(rs0), "={v[56:71]}"(rs1), "={v[72:75]}"(rs2), "={v[76:77]}"(rs3));
    const unsigned vml = (lane & 15) * 16;
#pragma unroll 1
    for (int it = r >> 4;; ++it) {
      if (it == r >> 4) KST(0, 8); else KST(0, 11);
      kreads<0>(make_iseq<8>{});
      const int n = prm<B_N, 1>();
      GAP();
      STA(0);
      if (isY) {
        phaseA<0, 2, false>(c);
        STA(1); STA(2);
        phaseB0<true>(false, vml);
      } else {
        if (it) phaseA<0, 3, false>(c);
        else phaseA<0, 1, false>(c);
        STA(1);
        if (c.rare) event();
        STA(2);
        phaseB0<false>(it && n == 2, vml);
      }
      STA(3);
      const int nl = n - it;
#pragma unroll 1
      for (int i = 1; i != nl; i += 2) {
        phaseA<1, 0, false>(c);
        STA(9 + i);
        if (__builtin_expect(c.rare != 0, 0)) { if (c.redo) sm_redo<1>(); event(); }
        phaseB<1>(c);
        if (i + 1 == nl) break;
        phaseA<0, 0, false>(c);
        STA(10 + i);
        if (__builtin_expect(c.rare != 0, 0)) { if (c.redo) sm_redo<0>(); event(); }
        phaseB<0>(c);
      }
      STA(4);
      if (it) break;
      sld<72, B_NXT, 1>(); sld<73, B_OP, 1>();
      epi_init<32768>();
      phaseA<ODD, 0, true>(c);
      if (__builtin_expect(c.redo != 0, 0)) sm_redo<ODD>();
      KST(0, 9);
      const float l[2] = {row_reduce<false>(vget<R_RS>() + vget<R_TS>()), row_reduce<false>(vget<R_RS + 1>() + vget<R_TS + 1>())};
      const f32x4 ml = {vget<R_M>(), l[0], vget<R_M + 1>(), l[1]};
      asm volatile("buffer_store_dwordx4 %0, %1, " SW ", s73 offen sc1" ::"v"(ml), "v"(vml) : "memory");
      phaseF(c, EP{}, make_iseq<74>{});
      asm volatile("s_waitcnt vmcnt(16)" ::: "memory");
      KST(0, 10);
    }
    asm volatile("at_tail:");
    vseti<84>(wid == 3 ? OOB : lane * 16);
    auto sel = [&]() __attribute__((always_inline)) {
      const unsigned one = (lane >> 4) == ((lane & 15) >> 2) ? 0x3f80u << ((lane & 1) * 16) : 0u, s0 = lane & 2 ? 0u : one, s1 = lane & 2 ? one : 0u;
      asm volatile("v_mov_b32 v72, %0\n\tv_mov_b32 v73, %1\n\tv_mov_b64 v[74:75], 0\n\tv_mov_b32 v76, %0\n\tv_mov_b32 v77, %1" ::"v"(s0), "v"(s1));
    };
    auto fin = [&](auto Lc, auto Mc) __attribute__((always_inline)) {
      constexpr int L = Lc, M = Mc;
      EP p;
      phaseAD<L, M == 1>(c, make_iseq<32>{});
      if (__builtin_expect(c.redo != 0, 0)) sm_redo<L>();
      STA(5); STF(4);
      sld<76, B_SO, 1>();
      vseti<85>((wid >> 1) * 16384 + (wid & 1) * 1024 + lane * 16);
      if constexpr (M < 2) p.fa[0] = __builtin_amdgcn_rcpf(row_reduce<false>(vget<R_RS>() + vget<R_TS>()));
      GAP();
      phaseBD<L, M < 2>(c, p, make_iseq<64>{});
      if (__builtin_expect(c.redo != 0, 0)) sm_redo<L ^ 1, 1>();
      STA(6);
      KST(0, 12);
      STF(12);
      asm volatile("s_waitcnt vmcnt(%0)\n\ts_barrier" ::"i"(M < 2 ? 6 + BDX : 0) : "memory");
      STF(13);
      if constexpr (M == 2) { asm volatile("v_add_u32 v107, %0, v90" ::"i"(L_SP)); epi_preads(make_iseq<8>{}); }
      vreads<L>(make_iseq<16>{});
      const float l1 = row_reduce<false>(vget<R_RS + 1>() + vget<R_TS + 1>());
      if constexpr (M < 2) {
        p.fa[1] = __builtin_amdgcn_rcpf(l1);
        asm volatile("s_waitcnt lgkmcnt(0)" ::: "memory");
        phaseFD<L, true>(p, make_iseq<40>{});
      } else {
        const float l[2] = {row_reduce<false>(vget<R_RS>()), l1}, cm[2] = {vget<R_M>(), vget<R_M + 1>()};
        const f32x4 ml = {vget<72>(), vget<73>(), vget<74>(), vget<75>()};
#pragma unroll
        for (int e = 0; e < 2; ++e) {
          const float mm = fmaxf(cm[e], ml[2 * e]);
          const float wa = __builtin_amdgcn_exp2f(cm[e] - mm), wb = __builtin_amdgcn_exp2f(ml[2 * e] - mm);
          const float inv = __builtin_amdgcn_rcpf(l[e] * wa + ml[2 * e + 1] * wb);
          p.fa[e] = wa * inv; p.fb[e] = wb * inv;
        }
        sel();
        static_for([&](auto I) { asm volatile("v_mov_b32 v%c0, v%c1" ::"i"(160 + (int)I), "i"(40 + (int)I)); }, make_iseq<32>{});
        asm volatile("s_waitcnt lgkmcnt(0)" ::: "memory");
        phaseFD<L, false>(p, make_iseq<64>{});
      }
    };
    using I0 = std::integral_constant<int, 0>; using I1 = std::integral_constant<int, 1>; using I2 = std::integral_constant<int, 2>;
    using LY = std::integral_constant<int, ODD ? 0 : 1>;
    if (isY) {
      const unsigned late = prm<B_LM + 1, 1>();
      asm volatile("s_waitcnt vmcnt(12)" ::: "memory");
      const float cm[2] = {vget<R_M>(), vget<R_M + 1>()};
      const f32x4 ml = {vget<72>(), vget<73>(), vget<74>(), vget<75>()};
      if (__builtin_expect(!late && __builtin_amdgcn_ballot_w64(cm[0] != ml[0] || cm[1] != ml[2]) == 0, 1)) {
        vset<R_RS>(vget<R_RS>() + 0.25f * ml[1]); vset<R_RS + 1>(vget<R_RS + 1>() + 0.25f * ml[3]);
        sel();
        asm volatile("v_add_u32 v85, %0, v90" ::"i"(L_SP));
        fin(LY{}, I1{});
      } else fin(LY{}, I2{});
    } else { sel(); fin(I1{}, I0{}); }
    KST(0, 13);
    STF(20);
  }
  {
    sld<64, A_OWN>(); sld<65, A_OWN + 1>(); sld<66, A_OWN + 2>(); sld<67, A_OWN + 3>();
    asm volatile("s_lshl_b32 s80, %0, 12\n\tv_add_u32 v96, s64, v89\n\tv_add_u32 v97, s65, v89\n\tv_add_u32 v98, s66, v89\n\tv_add_u32 v99, s67, v89\n\t"
                 "s_waitcnt lgkmcnt(0)\n\ts_barrier" ::"s"(wid) : "scc", "memory");
    STF(21);
    o_read0(make_iseq<16>{});
    {
      const int fr = lane & 15, fq = lane >> 4;
      const unsigned vo = wid < 3 || !(fq & 1) ? prm<A_OUT>() + (fr * OUTW + (fq & 1) * 16 + (fq >> 1) * 8) * 2 : OOB;
      asm volatile("v_mov_b32 v101, %0\n\tv_add_u32 v102, %1, %0\n\ts_mov_b64 s[88:89], %2\n\ts_mov_b32 s90, 0x380000\n\ts_mov_b32 s91, s59"
                   ::"v"(vo), "s"((j < 4 ? 16 : -16) * OUTW * 2), "s"(out));
      GAP();
    }
    if (wid < 3) {
      static_for([&](auto P) {
        o_interval<2, P>();
        if constexpr (P == 0) STF(22);
        if constexpr (P == 1) STF(23);
        if constexpr (P == 2) STF(24);
        if constexpr (P == 3) STF(25);
        if constexpr (P == 1) KST(0, 27);
        if constexpr (P == 3) KST(0, 28);
      }, make_iseq<15>{});
      o_interval<2, 15>();
      o_slabs<2>();
      o_sum<2>();
    } else {
      sld<62, A_FCW>();
      fl_mask<A_FCM, -1>();
      GAP();
      static_for([&](auto Pc) {
        constexpr int P = Pc;
        o_interval<1, P>();
        if constexpr (P == 0 || P == 2) { fl_wait(); asm volatile("s_lshl_b32 s96, s96, 4" ::: "scc"); }
      }, make_iseq<15>{});
      o_interval<1, 15>();
      o_slabs<1>();
      o_sum<1>();
    }
    asm volatile("rsv2_end:" ::"{v[40:55]}"(rs0), "{v[56:71]}"(rs1), "{v[72:75]}"(rs2), "{v[76:77]}"(rs3));
    KST(0, 29);
#define A_CV(D, S0, S1) "v_cvt_pk_bf16_f32 v" #D ", v" #S0 ", v" #S1 "\n\t"
    asm volatile(A_CV(224, 240, 241) A_CV(226, 244, 245) A_CV(225, 242, 243) A_CV(227, 246, 247) A_CV(228, 248, 249) A_CV(230, 252, 253) A_CV(229, 250, 251) A_CV(231, 254, 255)
                 "v_permlane16_swap_b32_e32 v224, v226\n\tv_permlane16_swap_b32_e32 v225, v227\n\tv_permlane16_swap_b32_e32 v228, v230\n\tv_permlane16_swap_b32_e32 v229, v231\n\t"
                 "buffer_store_dwordx4 v[224:227], v101, s[88:91], 0 offen\n\tbuffer_store_dwordx4 v[228:231], v102, s[88:91], 0 offen" ::: "memory");
    set_flag<A_FE>();
  }
  KST(0, 1);
  asm volatile("s_endpgm\n" A_EVENT "\npf_code_end:" ::: "memory");
}

extern "C" void kernel_launch(void* const* d_in, const int* in_sizes, int n_in, void* d_out, int out_size, void* d_ws,
                              size_t ws_size, hipStream_t stream) {
  const bf16* Aq = (const bf16*)d_in[0];
  const bf16* Ak = (const bf16*)d_in[1];
  const bf16* Av = (const bf16*)d_in[2];
  const bf16* Ash = (const bf16*)d_in[3];
  const bf16* Assmk = (const bf16*)d_in[4];
  const bf16* AssmD = (const bf16*)d_in[5];
  const bf16* Bx = (const bf16*)d_in[6];
  const bf16* Bckv = (const bf16*)d_in[7];
  const bf16* Bwq = (const bf16*)d_in[8];
  const bf16* Bwuk = (const bf16*)d_in[9];
  const bf16* Bwuv = (const bf16*)d_in[10];
  const bf16* Bwo = (const bf16*)d_in[11];
  bf16* out = (bf16*)d_out;
  (void)hipFuncSetAttribute((const void*)fused_kernel, hipFuncAttributeMaxDynamicSharedMemorySize, 163840);
  fused_kernel<<<256, 256, 163840, stream>>>(Bckv, Bx, Bwuk, Bwuv, Bwq, (char*)d_ws + ((ws_size - 0x8000000) & ~(size_t)0x1fffff), Ak, Av, Ash, Assmk, AssmD, Bwo, Aq, out);
}
```

```cpp
#include <hip/hip_runtime.h>
#include <hip/hip_bf16.h>
#include <stdint.h>
#include <utility>
#include <type_traits>

typedef __hip_bfloat16 bf16;
typedef __attribute__((ext_vector_type(4))) short bf16x4;
typedef __attribute__((ext_vector_type(2))) unsigned u32x2;

constexpr int SEQ = 2048;
constexpr int DA = 448;
constexpr int NH = 16;
constexpr int HD = 256;
constexpr int DC = 1024;
constexpr int OUTW = 896;

#define KST(kid, which)

__device__ __forceinline__ float bf2f(unsigned short u) { return __uint_as_float(((unsigned)u) << 16); }
__device__ __forceinline__ unsigned short f2bf(float f) { return __bfloat16_as_ushort(__float2bfloat16(f)); }
__device__ __forceinline__ float rbf(float f) { return bf2f(f2bf(f)); }
typedef __attribute__((ext_vector_type(2))) __bf16 bf16x2n;
typedef __attribute__((ext_vector_type(2))) float f32x2n;
__device__ __forceinline__ unsigned pk2(float a, float b) {
  return __builtin_bit_cast(unsigned, __builtin_convertvector((f32x2n){a, b}, bf16x2n));
}

__device__ __forceinline__ int off_b(int row, int ch) { return 256 * row + 16 * (ch ^ (((row & 3) << 2) | ((row >> 2) & 3))); }

#define DI __device__ __forceinline__
#define SCC_
#define SCC1
#define SCC2
#define SCC3
#define GAP() __builtin_amdgcn_sched_barrier(0)
template <int R> DI float vget() { float t; asm volatile("v_mov_b32 %0, v%c1" : "=v"(t) : "i"(R)); return t; }
template <int R> DI void vset(float x) { asm volatile("v_mov_b32 v%c0, %1" ::"i"(R), "v"(x)); }
template <int R> DI void vseti(unsigned x) { asm volatile("v_mov_b32 v%c0, %1" ::"i"(R), "v"(x)); }
template <int R> DI float acc_read1() { float t; asm volatile("v_accvgpr_read_b32 %0, a[%c1]" : "=v"(t) : "i"(R)); return t; }
template <class F, int... I> DI void static_for(F&& f, std::integer_sequence<int, I...>) {
  (f(std::integral_constant<int, I>{}), ...);
}
template <int... Is> using iseq = std::integer_sequence<int, Is...>;
template <int N> using make_iseq = std::make_integer_sequence<int, N>;
typedef unsigned long long u64;

constexpr unsigned O_K = 1u << 24, O_V = 2u << 24, O_C = 3u << 24, O_P = 4u << 24, O_F = O_P + 128 * 67584, O_S = O_F + 400 * 4352,
                   O_X = O_S + (1u << 24), O_Z = O_X + DA * SEQ * 2, O_W = O_Z + DA * SEQ * 2;
static_assert(O_W + 16 * 262144 <= 0x6400000);
constexpr unsigned fwo(int w) { return O_F + (w >> 4) * 4352 + (w & 15) * 8; }
constexpr unsigned OOB = 0x80000000u, M0X = 0x28000;
constexpr unsigned L_SP = 131072;
#define CMAX 10
#define NYA 15
#define NYB 15
#define MAP4 2
#define TSEQ 1
constexpr int O_SEQ[16] = {0x00, 0x01, 0x02, 0x10, 0x20, 0x30, 0x11, 0x21, 0x31, 0x12, 0x22, 0x32, 0x13, 0x23, 0x33, 0x03};
constexpr int o_k(int P) { int k = 0; for (int q = 0; q < P; ++q) k += O_SEQ[q] >= 16; return O_SEQ[P] >= 16 ? k : -1; }
#define Q44 1
#define KTYV 0x76543210fedcba98ull
#define LATEP 0
#define PKS 3
#define PPOS 99, 10, 9, 9, 9, 8
#define TABX 0xfedcb9875a463210ull, 0xefadc6982b574310ull, 0xfebad7693c285410ull, 0xefcba8764d392510ull
#define K2(c) (2 + (c))
#define POSTM 7
#define NCV 0
#define WP0 8
constexpr int ODD = NYA & 1;
static_assert((NYB & 1) == ODD);
constexpr int NYS[8] = {NYA, NYA, NYA, NYA, NYB, NYB, NYB, NYB};
enum { A_TAB = 0, A_FPREP = 2, A_FT2, A_NP, A_NPB = 6, A_VBB, A_MB2, A_VBA, A_MA2, A_VOO, A_SX, A_MX = 16, A_VOQ = 20, A_FG, A_NQ, A_N01, A_ZO, A_OQ,
       A_WSRC, A_WDST, A_PQ, A_PZ, A_M0F, A_GH, A_PC = 32, A_WLD = 44, A_FC, A_FCW, A_FCM, A_SS, A_FS = 51, A_FSW, A_FSM, A_SL, A_OUT = 57, A_OWN, A_FE = 62, A_GF };
enum { B_N = 0, B_ST, B_MS, B_CT, B_FW, B_FSET, B_NXT, B_SO, B_OP, B_MM, B_PM, C_FCM = 16, C_QT, B_LM, B_LP = 21, B_WD, C_PO, B_WT, B_ACT = 32 };
enum { E_CHK = 1, E_PACK = 4, E_POLL = 8, E_POLL1 = 16, E_MASK = 32, E_FLAG = 64, E_WFLAG = 128, E_POLLZ = 256, E_LOAD = 512, E_POST = 1024, E_PLOAD = 2048, E_CHK1 = 4096 };
constexpr unsigned ev_code(unsigned a) {
  return a == 0 ? 0 : a == E_CHK ? 16 : a == E_FLAG ? 32 : a == (E_FLAG | E_POLL) ? 96 : a == E_POLL ? 56 : a == (E_WFLAG | E_POLLZ) ? 344 : a == E_WFLAG ? 80 : a == (E_FLAG | E_POLLZ) ? 448 : a == E_LOAD ? 400 : a == E_POST ? 512 : a == E_PLOAD ? 768 : a == (E_PLOAD | E_CHK1) ? 912 : a == E_CHK1 ? 872 :
         a == E_PACK ? 128 : a == E_POLL1 ? 192 : a == (E_POLL | E_POLL1 | E_MASK) ? 228 : a == (E_POLL1 | E_MASK) ? 240 : 1u << 31;
}
struct PTab { unsigned v[32][4][64][3]; bool bad; };
constexpr int p_unit(int j, int rr) { return (j & 1) && rr < 4 ? -1 : (j >> 1) * 28 + (j & 1) * 12 + rr; }
constexpr int o_head(int ks, int p) { return 2 * ks + (p >> 1) + 8 * (p & 1); }
constexpr PTab make_tab(int j) {
  PTab t{};
  for (int r = 0; r < 32; ++r) for (int wid = 0; wid < 4; ++wid) {
    auto& e = t.v[r][wid];
    const int rr = r & 15, hsg = rr & 1, pjg = rr >> 1, kv = pjg & 1, gw = j * 128;
    const bool isY = r >= 16;
    const int ks = j >> 1, p = MAP4 ? rr & 3 : (j & 1) * 2 + hsg, h = o_head(ks, p), hsel = h >> 3, ga = (h & 7) * 128;
    const int pj = MAP4 == 1 ? (rr >> 2) * 2 + (j & 1) : MAP4 == 2 ? (rr >> 2) + 4 * (j & 1) : pjg;
    const int mt = (rr >> 2) + (isY ? 4 : 0), cr = rr - (pjg > NCV ? 2 : 0), cch = j * 56 + cr * 4 + wid, NY = NYS[pj];
    const bool doA = !isY && pjg != NCV;
    const unsigned oG = (unsigned)(j + 8 * hsg) << 20;
    const int nx0 = 32 - 2 * pj - NY, cx = nx0 < CMAX ? nx0 : CMAX, ex = nx0 - cx, dg = 30 - 2 * pj;
    int seq[2][32] = {}, ns = 0;
    auto run = [&](int it, int t0, int t1) { for (int q = t0; q < t1; ++q) seq[it][ns++] = q; };
    const int x1 = TSEQ ? dg - ex : 16, c4 = 32 - 8 - NY < CMAX ? 32 - 8 - NY : CMAX;
    if (!isY) {
      run(0, 0, cx); run(0, x1, x1 + ex);
      ns = 0;
      const int s1 = TSEQ >= 2 && nx0 + TSEQ - 2 + 2 <= 2 * pj ? (nx0 < 4 ? 2 : nx0 + TSEQ - 2) : 0;
      run(1, s1, 2 * pj); run(1, 0, s1); run(1, 2 * pj, 2 * pj + 2);
    } else if (ex || !TSEQ) { run(0, cx, x1); run(0, x1 + ex, dg + 2); }
    else { run(0, c4 > cx ? c4 : cx, 16); run(0, cx, c4); run(0, 16, dg + 2); }
    const u64 own = h == j + 8 * hsg && isY ? 1ull << (hsel * 16 + kv * 8 + mt) : 0;
    u64 needB = 0;
    int c1 = 99;
    for (int i = (isY ? NY : nx0) - 1; i >= 0; --i) if (seq[0][i] >= 16) { needB |= 0x101ull << (hsel * 16 + (seq[0][i] >> 2)); c1 = i - 2; }
    needB &= ~own;
    const u64 needQ = (1ull << (32 + hsel * 8 + (pj >> 1))) | (1ull << (32 + hsel * 8 + ((15 - pj) >> 1)));
    const u64 needA = 0x0f0full << (hsel * 16);
    const unsigned oQ = (unsigned)h << 20, oK = oQ + O_K, oP = O_P + (h * 8 + pj) * 67584;
    const unsigned oQ0 = oQ + ((15 - pj) * 128 + wid * 32) * 512, oK0 = oK + seq[0][0] * 32768, lowb = ((wid & 1) + (wid >> 1) * 4) * 4096;
    auto w0 = [&](unsigned x) { return wid == 0 ? x : OOB; };
    const int cls = (rr + (rr >> 2)) & 3;
    const u64 tabX[4] = {TABX};
    const u64 tab = isY ? KTYV : tabX[cls];
    if ((tabX[cls] & 0xfff) != 0x10u + K2(cls) * 0x100) t.bad = true;
    e[C_QT][2] = 0x6543;
    e[A_TAB][0] = (unsigned)tab; e[A_TAB + 1][0] = (unsigned)(tab >> 32);
    const int pu = p_unit(j, rr);
    e[A_FPREP][0] = pu < 0 ? OOB : w0(fwo(6160 + pu));
    const int hq = Q44 ? o_head(ks, rr & 3) : j + 8 * hsg, tq = Q44 ? (rr >> 2) + 4 * (j & 1) : pjg;
    e[A_FT2][0] = isY ? w0(fwo((hq & 7) * 128 + 32 + (hq >> 3) * 8 + tq)) : OOB;
    const u64 np = isY ? needA | needQ : 0xffffull;
    e[A_NP][0] = (unsigned)np; e[A_NP + 1][0] = (unsigned)(np >> 32); e[A_NPB][0] = fwo(isY ? ga : 6160 + (cch >> 6) * 16);
    e[A_VBB][0] = oQ0; e[A_MB2][0] = isY ? 65536 + wid * 1024 : M0X;
    e[A_VBA][0] = oK0 + wid * 1024; e[A_MA2][0] = isY ? wid * 1024 : M0X;
    e[A_VOO][0] = oG + (kv ? O_V : O_K) + mt * 131072 + (wid >> 1) * 65536 + (wid & 1) * (kv ? 8192 : 4096);
    e[A_SX][0] = isY ? oK0 + lowb + 8192 : O_X + cch * 4096; e[A_SX + 1][0] = isY ? oQ0 + 12288 : cch * 4096;
    e[A_SX + 2][0] = oK0 + 32768 + lowb; e[A_SX + 3][0] = oK0 + O_K + wid * 8192;
    e[A_MX][0] = isY ? lowb + 8192 : doA ? 65536 + wid * 15488 : M0X; e[A_MX + 1][0] = isY ? 114688 + wid * 4096 : e[A_MX][0] + 5120;
    e[A_MX + 2][0] = isY ? 32768 + lowb : M0X; e[A_MX + 3][0] = isY ? 65536 + wid * 8192 : M0X;
    e[A_VOQ][0] = ((unsigned)hq << 20) + tq * 131072 + (wid >> 1) * 65536 + (wid & 1) * 4096;
    e[A_FG][0] = w0(fwo(gw + hsg * 16 + kv * 8 + mt));
    e[A_NQ][0] = (unsigned)(needQ >> 32); e[A_N01][0] = 0x0101u << (hsel * 16);
    e[A_ZO][0] = isY ? (pu < 0 ? OOB : O_X + (pu >> 4) * (64 * SEQ * 2) + (15 - (pu & 15)) * 256) : O_Z + j * (56 * SEQ * 2) + (cr * 4 + wid) * 64; e[A_OQ][0] = oQ0;
    const int qbl = isY ? 15 - pj : pj, u = 2 * pj, tile = qbl * 4 + p;
    e[A_WSRC][0] = ((o_head(ks, p ^ (u >> 2)) * 256 + (u & 3) * 64) * DA + p * 112) * 2;
    e[A_WLD][0] = O_W + h * 262144 + wid * 8192;
    e[A_WDST][0] = e[A_WLD][0] + u * 16384;
    e[A_PQ][0] = ((rr * 4 + wid) * 32 * DA + j * 56) * 2; e[A_PZ][0] = O_Z + (j * 64 + rr * 4 + wid) * 3584;
    e[A_M0F][0] = L_SP + wid * 8192; e[C_PO][2] = ((rr * 4 + wid) * 32 * OUTW + j * 56) * 2;
    e[A_GH][0] = fwo(gw + 64);
    auto qo = [&](int i) { return i == 3 ? ks : i + (i >= ks); };
    auto ctxo = [&](int o, int qb, int q) { return O_C + ((unsigned)o_head(ks, p ^ o) << 20) + qb * 65536 + q * 16384 + wid * 4096; };
    auto slab = [&](int tl, int q, int src) { return O_S + tl * 262144 + (q * 8 + src) * 8192 + wid * 2048; };
    for (int q = 0; q < 16; ++q) if (o_k(q) >= 0) e[A_PC + o_k(q)][0] = ctxo(O_SEQ[q] >> 4, qbl, qo(O_SEQ[q] & 3));
    e[A_FC][0] = w0(fwo(1024 + (qbl * 4 + ks) * 16 + p)); e[A_FCW][0] = fwo(1024 + (qbl * 4 + ks) * 16);
    e[A_FCM][0] = 15 & ~(1 << p);
    for (int q = 0; q < 3; ++q) { e[A_SS + q][0] = slab(tile, qo(q), 2 * ks); e[A_SL + q][0] = slab(tile, ks, 2 * qo(q)); }
    e[A_FS][0] = fwo(2048 + (tile * 4 + wid) * 16 + 2 * ks); e[A_FSW][0] = fwo(2048 + (tile * 4 + wid) * 16); e[A_FSM][0] = 0x55 & ~(1 << (2 * ks));
    e[A_OUT][0] = ((qbl * 128 + (ks < 2 ? 2 * ks : 11 - 2 * ks) * 16) * OUTW + DA + p * 112 + wid * 32) * 2;
    for (int q = 0; q < 4; ++q) e[A_OWN + q][0] = qo(q) * 16384;
    e[A_FE][0] = r == 28 && j == 4 ? w0(fwo(6144)) : OOB;
    e[A_GF][0] = fwo(ga);
    int wstep = isY || pj >= PKS ? 99 : 4, wpos = 0;
    for (int it = 0; it < 2; ++it) {
      const int R = 1 + it, n = isY ? NY : it ? 2 * pj + 2 : nx0, qb = isY || !it ? 15 - pj : pj;
      const bool last = isY || it;
      unsigned act[32] = {};
      int np_ = 0;
      auto poll = [&](int i, u64 m) {
        const int l = B_PM + 2 * np_++;
        e[l][R] = (unsigned)m; e[l + 1][R] = (unsigned)(m >> 32);
        if (act[i] & E_POLL) t.bad = true;
        act[i] |= E_POLL; act[i + 1] |= E_CHK;
      };
      if (!it || isY) act[1] |= E_FLAG;
      const bool late = isY && pj < LATEP;
      if (isY) {
        const int pa[8] = {99, 99, PPOS};
        int s_ = late ? n - 2 : pa[pj] + 15 - NY < n - 3 ? pa[pj] + 15 - NY : n - 3;
        int q = 1;
        if ((POSTM >> pj) & 1) {
          act[1] |= E_POLLZ; act[2] = E_LOAD;
          q = c1 == 4 ? 5 : 3;
          act[q] = E_POST;
        }
        while (s_ <= q || (s_ + 1 >= c1 - 1 && s_ <= c1)) ++s_;
        if (c1 < 4 || s_ > n - 3 + late) t.bad = true;
        if (c1 < s_) poll(c1 - 1, needB);
        poll(s_, 1ull << (48 + hsel * 8 + pj));
        act[s_ + 1] = late ? 0 : E_PLOAD;
        if (c1 > s_) poll(c1 - 1, needB);
      }
      else if (!it) { if (n > 2) poll(1, needA); if (c1 < 99) poll(c1 - 1, needB); }
      else if (nx0 <= 2) poll(1, needA);
      if (late) e[B_LM + 1][R] = 1u << (16 + hsel * 8 + pj);
      e[B_LP][R] = oP + wid * 8192;
      const int* sq = seq[isY ? 0 : it];
      if (sq[1] != sq[0] + 1 || (!isY && seq[1][0] + 1 >= 4 && nx0 < 4)) t.bad = true;
      if (last && n > 2) act[n - 3] |= E_POLL1 | E_MASK;
      if (!last && pj == 0) act[n - 2] |= E_POLL1;
      if (last) act[n - 2] |= E_CHK1;
      if (it && !isY && pj >= PKS) wstep = 5, wpos = WP0;
      const int wend = (POSTM >> pj) & 1 ? 6 : 8;
      const int ws_[4] = {E_PACK, wend == 6 ? E_WFLAG : E_WFLAG | E_POLLZ, E_LOAD, E_POST};
      for (int i = 0; i < (last ? n - 3 : n) && !isY; ++i, ++wpos)
        if (wstep < wend && wpos >= WP0 && !act[i] && i) act[i] = ws_[wstep++ - 4];
      if (it && wstep < wend) t.bad = true;
      e[B_WD][R] = pj >= PKS ? e[A_WDST][0] : OOB;
      e[B_WT][R] = O_W + h * 262144 + ((isY ? 4 : 0) + (pj & 3)) * 32768 + wid * 8192;
      e[B_FW][R] = wid < 2 ? fwo((j ^ wid) * 128 + 80 + wid * 16 + rr) : OOB;
      e[B_MS][R] = doA ? e[A_MX][0] + 10304 : M0X; e[B_CT][R] = (rr * 4 + wid) * 128;
      for (int i = 0; i < n; ++i) {
        const unsigned sk = i + 2 < n ? oK + sq[i + 2] * 32768 : last ? O_W + h * 262144 + 32768 : oK + (seq[1][0] + (i & 1)) * 32768;
        e[B_ACT + i][R] = sk | ev_code(act[i]);
        if (ev_code(act[i]) > 1023 || (sk & 1023)) t.bad = true;
      }
      e[B_N][R] = n; e[B_ST][R] = oK + sq[0] * 32768;
      e[B_FSET][R] = isY ? e[A_FG][0] : it ? w0(fwo(ga + 48 + hsel * 8 + pj)) : wid == 1 ? fwo(1024 + (qb * 4 + ks) * 16 + 8 + p) : doA ? w0(fwo(gw + 112 + cr)) : OOB;
      e[B_MM][R] = 256u << p;
      e[B_NXT][R] = last ? e[A_WLD][0] : oQ + (pj * 128 + wid * 32) * 512;
      e[B_SO][R] = last ? oQ + O_C + qb * 65536 : oP;
      e[B_OP][R] = oP + 65536 + wid * 256;
    }
  }
  return t;
}
constexpr PTab PT0 = make_tab(0), PT1 = make_tab(1), PT2 = make_tab(2), PT3 = make_tab(3), PT4 = make_tab(4), PT5 = make_tab(5), PT6 = make_tab(6), PT7 = make_tab(7);
static_assert(!PT0.bad && !PT7.bad);
static __device__ const PTab PTAB[8] = {PT0, PT1, PT2, PT3, PT4, PT5, PT6, PT7};

#define SPINB "0x100000"
#define SW "s[56:59]"
template <int S, int L, int ROW = 0> DI void sld() { asm volatile("v_readlane_b32 s%c0, v%c1, %c2" ::"i"(S), "i"(92 + ROW), "i"(L)); }
template <int L, int ROW = 0> DI unsigned prm() { unsigned x; asm volatile("v_readlane_b32 %0, v%c1, %c2\n\ts_nop 4" : "=s"(x) : "i"(92 + ROW), "i"(L)); return x; }
#define A_FLAG "s_mov_b64 exec, 1\n\tbuffer_store_dwordx2 v[82:83], off, " SW ", s63 sc1\n\ts_mov_b64 exec, -1\n\t"
#define A_POLL(V, M, B) "s_mov_b64 exec, " M "\n\tbuffer_load_dwordx2 " V ", v88, " SW ", " B " offen sc1\n\ts_mov_b64 exec, -1\n\t"
#define A_SPIN(V, M, B) "s_mov_b32 s95, " SPINB "\n\ts_mov_b64 exec, " M "\n" \
    "8:\n\tbuffer_load_dwordx2 " V ", v88, " SW ", " B " offen sc1\n\ts_waitcnt vmcnt(0)\n\tv_cmp_eq_u64 vcc, s[60:61], " V "\n\t" \
    "s_cmp_eq_u64 vcc, " M "\n\ts_cbranch_scc1 9f\n\ts_sleep 4\n\ts_sub_u32 s95, s95, 1\n\ts_cbranch_scc0 8b\n9:\n\ts_mov_b64 exec, -1\n\t"
#define A_CHECK(V, M, B) "v_cmp_eq_u64 vcc, s[60:61], " V "\n\ts_and_b64 vcc, vcc, " M "\n\ts_cmp_eq_u64 vcc, " M "\n\ts_cbranch_scc1 9f\n\t" A_SPIN(V, M, B)
#define A_CTOUCH(B, E, M) "v_readlane_b32 s63, v93, 3\n\ts_sub_u32 s96, s100, at_event - " B "\n\ts_subb_u32 s97, s101, 0\n\t" \
    "s_mov_b32 s98, " E " - " B "\n\ts_mov_b32 s99, s59\n\tv_lshl_add_u32 %0, v89, 9, s63\n\t" \
    "s_mov_b32 m0, 0x27f00\n\ts_mov_b64 exec, " M "\n\tbuffer_load_dword %0, s[96:99], 0 offen lds\n\ts_mov_b64 exec, -1\n\t"
template <int K> DI void code_touch() {
  unsigned t;
  if constexpr (K) asm volatile(A_CTOUCH("ct_begin", "at_tail", "0xff") : "=&v"(t) :: "scc", "memory");
  else asm volatile(A_CTOUCH("ct_begin", "rsv2_begin", "0x1f") : "=&v"(t) :: "scc", "memory");
}
template <int L, int ROW = 0> DI void set_flag() {
  asm volatile("v_readlane_b32 s63, v%c0, %c1\n\ts_nop 3\n\t" A_FLAG ::"i"(92 + ROW), "i"(L) : "memory");
}
template <int LO, int HI> DI void fl_mask() {
  if constexpr (LO >= 0) sld<96, LO>(); else asm volatile("s_mov_b32 s96, 0");
  if constexpr (HI >= 0) sld<97, HI>(); else asm volatile("s_mov_b32 s97, 0");
}
DI void fl_issue() { asm volatile(A_POLL("v[122:123]", "s[96:97]", "s62") ::: "memory"); }
DI void fl_check() { asm volatile(A_CHECK("v[122:123]", "s[96:97]", "s62") ::: "vcc", "scc", "memory"); }
DI void fl_wait() { asm volatile("s_nop 4\n\t" A_SPIN("v[122:123]", "s[96:97]", "s62") ::: "vcc", "scc", "memory"); }

typedef __attribute__((ext_vector_type(4))) unsigned u32x4c;
typedef __attribute__((ext_vector_type(4))) float f32x4c;
typedef __attribute__((ext_vector_type(2))) float f32x2c;
__device__ __forceinline__ float bfl(unsigned u) { return __uint_as_float(u << 16); }
__device__ __forceinline__ float bfh(unsigned u) { return __uint_as_float(u & 0xffff0000u); }

#define CODE_TOUCH(LB, LE, wx, NL, m0v) do { unsigned blo_, bhi_, elo_; \
    asm volatile("s_getpc_b64 s[100:101]\n\ts_add_u32 s100, s100, " LB "@rel32@lo+4\n\ts_addc_u32 s101, s101, " LB "@rel32@hi+12\n\t" \
                 "s_mov_b32 %0, s100\n\ts_mov_b32 %1, s101\n\t" \
                 "s_getpc_b64 s[100:101]\n\ts_add_u32 s100, s100, " LE "@rel32@lo+4\n\ts_mov_b32 %2, s100" \
                 : "=s"(blo_), "=s"(bhi_), "=s"(elo_) :: "scc"); \
    const unsigned n_ = elo_ - (blo_ & ~127u); \
    const __amdgpu_buffer_rsrc_t srd_ = __builtin_amdgcn_make_buffer_rsrc((void*)(((unsigned long long)bhi_ << 32) | (blo_ & ~127u)), (short)0, n_ < 262144u ? n_ : 0u, 0x00020000); \
    const unsigned l_ = threadIdx.x & 63, vo_ = l_ < (NL) ? ((wx) + l_ * 128u) * 128u : 0x80000000u; \
    asm volatile("s_mov_b32 m0, %0\n\ts_nop 4\n\tbuffer_load_dword %1, %2, 0 offen lds" ::"s"(m0v), "v"(vo_), "s"(srd_) : "memory"); } while (0)

#define STT(k)
#define SNK 31
#define GSC " sc1"
#define PL0 66
#define PLS 7
#define PCAP 0x55555555
#define PST 272
#define ECAP 3
#define EPACE_K 7
#define EPACE_V 6
typedef __attribute__((ext_vector_type(4))) float f32x4g;
typedef __attribute__((ext_vector_type(4))) unsigned u32x4g;
DI u32x4g pair16(f32x4g x, f32x4g y) {
  const unsigned a0 = pk2(x[0], x[1]), a1 = pk2(x[2], x[3]), b0 = pk2(y[0], y[1]), b1 = pk2(y[2], y[3]);
  auto r0 = __builtin_amdgcn_permlane16_swap(a0, b0, false, false), r1 = __builtin_amdgcn_permlane16_swap(a1, b1, false, false);
  return (u32x4g){r0[0], r1[0], r0[1], r1[1]};
}
constexpr int LDW = NH * HD;
struct GM { unsigned aA[2], aB[2][8]; };
#define GDP 8, 12, 24, 28, 40, 44, 56, 60
constexpr int GDPOS[8] = {GDP};
constexpr int g_j(int G) { for (int i = 0; i < 8; ++i) if (GDPOS[i] == (G & 63)) return i; return -1; }
constexpr int GD13 = 125 - GDPOS[3];
struct PA { unsigned vo, lw, lx, lr, vs; __amdgpu_buffer_rsrc_t sk, sv, sw; };
struct POp { int k, d, a, b; };
struct PSch { POp op[760]; int first[770], vm[770], n; };
constexpr PSch p_sched() {
  PSch z{};
  POp l[760] = {};
  int n = 0, early[760] = {};
  auto put = [&](int k, int d, int a, int b, int e = 0) { l[n] = POp{k, d, a, b}; early[n] = e; ++n; };
  for (int q = 0; q < 10; ++q) put(10, q < 6 ? q : q + 2, 0, 0, PL0 + PLS * q);
  for (int c = 0; c < 8; ++c) {
    const int I = c >> 1, u = c & 1;
    for (int t = 0; t < 4; ++t) put(t & 1, 112 + t, 76 + 2 * u + (t >> 1), 0, c ? 0 : PST);
    if (u && I < 3) put(11, I + 1, 0, 0);
    for (int q = 0; q < 7; ++q) put(u, 116 + q, 32 + 4 * q + I, 0);
    for (int e = 2; e >= 0; e -= 2) {
      put(2, 125, 117 + e, 115); put(2, 123, 116 + e, 115);
      for (int t = 1; t < 4; ++t) { put(4, 125, 117 + e + t, 115 - t); put(4, 123, 116 + e + t, 115 - t); }
      const int T = e ? 126 : 127;
      put(u, 124, 64 + 4 * e + I, 0); put(u, T, 60 + 4 * e + I, 0);
      put(2, 125, 125, 124); put(2, 123, 123, T);
      put(5, T, 125, 123);
    }
    put(12, c, 0, 0, (PST + 2) / 64 * 64 + 62);
  }
  const int nm = n;
  for (int q = 0; q < 4; ++q) put(13, q, 0, 0);
  for (int q = 0; q < 4; ++q) put(14, q, 0, 0);
  int g = 0, i = 0, hold = 0;
  for (; g < 768; ++g) {
    const int q = g & 15;
    int cap = g_j(g) >= 0 ? 0 : (PCAP >> (2 * q)) & 3;
    z.first[g] = z.n; z.vm[g + 1] = z.vm[g];
    if (g == PST - 16) z.op[z.n++] = POp{11, 0, 0, 0};
    if (g == PST - 20) z.op[z.n++] = POp{15, 0, 0, 0};
    if (g >= PST - 16 && g < PST - 13) for (int r = (g - PST + 16) * 7 / 3; r < (g - PST + 17) * 7 / 3; ++r) z.op[z.n++] = POp{16, r, 0, 0};
    while (cap > 0 && i < n && early[i] <= g && hold <= g) {
      const int k = l[i].k;
      if ((k == 11 || k == 13) && q > 2) { if (k == 13) break; }
      z.op[z.n++] = l[i];
      if (k == 10 || k == 14) { ++z.vm[g + 1]; cap = 0; hold = g + 3; }
      if (i == nm - 1) hold = (g + 3) / 64 * 64 + 64;
      if (i == nm + 3) hold = g / 16 * 16 + 16;
      --cap; ++i;
    }
  }
  z.first[768] = z.n;
  if (i < n) z.n = -1;
  return z;
}
constexpr PSch PS = p_sched();
constexpr int p_last() { int g = 0; while (PS.first[g] < PS.n) ++g; return g; }
static_assert(PS.n > 0 && PS.vm[(PST - 16) / 64 * 64 - 3 - GD13] == 10 && PST % 16 == 0);
template <int I> DI void p_op(const PA& p) {
  constexpr POp o = PS.op[I];
  if constexpr (o.k == 0) asm volatile("v_lshlrev_b32_e32 v%c0, 16, v%c1" ::"i"(o.d), "i"(o.a));
  else if constexpr (o.k == 1) asm volatile("v_and_b32_e32 v%c0, s94, v%c1" ::"i"(o.d), "i"(o.a));
  else if constexpr (o.k == 2) asm volatile("v_mul_f32_e32 v%c0, v%c1, v%c2" ::"i"(o.d), "i"(o.a), "i"(o.b));
  else if constexpr (o.k == 4) asm volatile("v_fmac_f32_e32 v%c0, v%c1, v%c2" ::"i"(o.d), "i"(o.a), "i"(o.b));
  else if constexpr (o.k == 5) asm volatile("v_cvt_pk_bf16_f32 v%c0, v%c1, v%c2" ::"i"(o.d), "i"(o.a), "i"(o.b));
  else if constexpr (o.k == 10 && o.d == 0)
    asm volatile("s_mov_b32 s93, m0\n\ts_add_u32 m0, s79, %1\n\ts_mov_b32 s94, 0xffff0000\n\tbuffer_load_dwordx4 v89, %0, 0 offen lds\n\ts_mov_b32 m0, s93"
                 ::"s"(p.sw), "i"(L_SP + 20480) : SCC_ "memory");
  else if constexpr (o.k == 10 && o.d < 6) {
    if constexpr (o.d == 1) asm volatile("v_and_b32 v113, 0x380, v89\n\tv_mul_u32_u24 v114, 21, v113\n\tv_sub_u32 v114, %0, v114\n\ts_mul_i32 s95, s79, 5" ::"v"(p.vo));
    asm volatile("v_add_u32 v112, %1, v114\n\ts_mov_b32 s93, m0\n\ts_add_u32 m0, s95, %2\n\ts_mov_b32 exec_lo, %3\n\ts_mov_b32 exec_hi, %4\n\t"
                 "buffer_load_dwordx4 v112, %0, 0 offen lds\n\ts_mov_b64 exec, -1\n\ts_mov_b32 m0, s93"
                 ::"s"(p.sk), "i"((o.d - 2) * 8 * DA * 2), "i"(L_SP + (o.d - 1) * 1024), "i"(o.d == 1 ? 0 : -1), "i"(o.d == 1 ? -256 : -1) : SCC_ "memory");
  } else if constexpr (o.k == 10)
    asm volatile("buffer_load_dwordx4 v[%c0:%c1], %2, %3, 0 offen offset:%c4" ::"i"(28 + 4 * o.d), "i"(31 + 4 * o.d), "v"(p.vo), "s"(p.sv), "i"((o.d & 3) * DA * 2) : "memory");
  else if constexpr (o.k == 15) asm volatile("v_mad_u32_u24 v113, v113, 3, v89\n\tv_add_u32 v113, s95, v113\n\tv_add_u32 v113, %0, v113" ::"i"(L_SP + 640));
  else if constexpr (o.k == 16) asm volatile("ds_read_b128 v[%c0:%c1], v113 offset:%c2" ::"i"(32 + 4 * o.d), "i"(35 + 4 * o.d), "i"(o.d * 128));
  else if constexpr (o.k == 11) asm volatile("ds_read_b128 v[76:79], %0 offset:%c1" ::"v"(p.lw), "i"(o.d * 16));
  else if constexpr (o.k == 12) asm volatile("ds_write_b64 %0, v[126:127] offset:%c1" ::"v"(p.lx), "i"(o.d * 256));
  else if constexpr (o.k == 13)
    asm volatile("v_xor_b32 v112, %3, %2\n\tds_read_b128 v[%c0:%c1], v112 offset:%c4" ::"i"(32 + 4 * o.d), "i"(35 + 4 * o.d), "v"(p.lr), "i"(o.d * 32), "i"(o.d * 4096));
  else
    asm volatile("buffer_store_dwordx4 v[%c0:%c1], %2, " SW ", s93 offen sc1\n\ts_add_u32 s93, s93, 0x10000" ::"i"(32 + 4 * o.d), "i"(35 + 4 * o.d), "v"(p.vs) : SCC_ "memory");
}
template <int B, int... Is> DI void p_ops_(const PA& p, iseq<Is...>) { (p_op<B + Is>(p), ...); }
template <int Gg> DI void p_ops(const PA& p) { p_ops_<PS.first[Gg]>(p, make_iseq<PS.first[Gg + 1] - PS.first[Gg]>{}); }
constexpr float CS = 0.0625f * 1.4426950408889634f;
struct EOp { int k, d, s, g; };
struct ESch { EOp op[640]; int first[260], st[260], n; bool ok; };
constexpr int e_load(int g) { return g >= 64 ? ((g & 7) == 4 ? g >> 3 : -1) : g % 5 != 4 || g > 59 ? -1 : g < 40 ? g / 5 : g / 5 + 8; }
constexpr int e_nld(int g0, int g1) { int n = 0; for (int g = g0; g < g1; ++g) n += e_load(g) >= 0; return n; }
constexpr int e_qrd(int g) { return g >= 32 && g < 48 && (g & 1) ? (g - 32) / 2 : g >= 104 && g < 120 && (g & 1) ? 8 + (g - 104) / 2 : -1; }
#define PARK 0
#define EPACE_Q 0
constexpr ESch e_sched(int MODE) {
  ESch t{}, z{};
  int cap[260] = {}, used[260] = {}, dead[640] = {};
  const int NG = MODE == 0 ? 256 : 128;
  for (int g = 0; g < NG; ++g) {
    const int q = g & 15;
    if (MODE == 0) cap[g] = g_j(g) >= 0 ? 1 : (q == 1 || q == 2 || q == 3 || q == 5 || q == 7 || q == 9) ? 2 : q == 14 ? 4 : 3;
    else cap[g] = g < 16 ? 1 : (q == 1 || q == 2 || e_load(g) >= 0 || e_qrd(g) >= 0) ? 1 : 2;
  }
  cap[NG] = 1000;
  int n = 0, g = 0;
  auto put = [&](int k, int d, int s_, int early, int dl) {
    if (g < early) g = early < NG ? early : NG;
    while (used[g] >= cap[g]) ++g;
    ++used[g]; if (k == 3) ++used[g];
    t.op[n] = EOp{k, d, s_, g}; dead[n] = dl; ++n;
  };
  auto av = [](int m, int nf) { return 16 * m + 2 * nf + 10; };
  for (int j = 0; j < (MODE == 0 ? 32 : 16); ++j) {
    const int m0 = MODE == 2 ? (j >> 3) * 2 : j >> 2, n0 = MODE == 2 ? j & 7 : (j & 3) * 2;
    const int m1 = MODE == 2 ? m0 + 1 : m0, n1 = MODE == 2 ? n0 : n0 + 1;
    const int T = j < 16 ? 128 + 8 * j : 112 + 8 * (j & 1), dl0 = 128 + 32 * (j >> 3) + 2 * (j & 7) - 1, dl1 = j < 16 ? dl0 : 999;
    const int pace = MODE == 0 ? j * EPACE_Q : (MODE == 1 ? 14 : 26) + j * (MODE == 1 ? EPACE_K : EPACE_V);
    const bool park = MODE == 0 && j >= 32 - PARK;
    if (j >= 16) for (int k = 0; k < 8; ++k) put(0, T + k, 8 * (j - 16) + k, k < 4 ? av(m0, n0) : av(m1, n1), dl0);
    for (int h = 0; h < 2; ++h) {
      const int e = h ? av(m1, n1) : av(m0, n0);
      if (MODE == 0) for (int k = 0; k < 4; ++k) put(1, T + 4 * h + k, 0, e, dl1);
      for (int k = 0; k < 2; ++k) put(2, (park ? 32 + 4 * (j - (32 - PARK)) : T) + 2 * h + k, T + 4 * h + 2 * k, e, dl1);
    }
    if (!park) put(3, T, j, j < 16 || MODE || EPACE_Q ? pace : 0, dl1);
  }
  z.ok = true;
  for (int q = 0; q <= NG; ++q) {
    z.first[q] = z.n; z.st[q + 1] = z.st[q];
    for (int i = 0; i < n; ++i) if (t.op[i].g == q) {
      z.op[z.n++] = t.op[i];
      if (t.op[i].k == 3) ++z.st[q + 1];
      if (MODE == 0 && q > dead[i]) z.ok = false;
    }
  }
  z.first[NG + 1] = z.n;
  return z;
}
constexpr ESch ES[3] = {e_sched(0), e_sched(1), e_sched(2)};
static_assert(ES[0].ok && ES[0].first[256] == ES[0].n && ES[1].st[126] == 16 && ES[2].st[126] == 16);
template <int MODE, int I> DI void e_op() {
  constexpr EOp o = ES[MODE].op[I];
  if constexpr (o.k == 0) asm volatile("v_accvgpr_read_b32 v%c0, a[%c1]" ::"i"(o.d), "i"(o.s));
  else if constexpr (o.k == 1) asm volatile("v_mul_f32_e32 v%c0, s94, v%c0" ::"i"(o.d));
  else if constexpr (o.k == 2) asm volatile("v_cvt_pk_bf16_f32 v%c0, v%c1, v%c2" ::"i"(o.d), "i"(o.s), "i"(o.s + 1));
  else {
    constexpr int m = o.s >> 2;
    if constexpr ((o.s & 3) == 0)
      asm volatile("s_mov_b32 s93, %0" ::"i"(MODE == 2 ? (m >> 1) * 16384 + (m & 1) * 4096 : MODE == 1 ? m * 8192 : (m < 4 ? 2 * m : 15 - 2 * m) * 8192));
    asm volatile("buffer_store_dwordx4 v[%c0:%c1], v%c2, " SW ", s93 offen offset:%c3 sc1" ::"i"(o.d), "i"(o.d + 3), "i"(MODE ? 84 : 95), "i"((o.s & 3) * 1024) : "memory");
  }
}
template <int MODE, int B, int... Is> DI void e_ops_(iseq<Is...>) { (e_op<MODE, B + Is>(), ...); }
template <int MODE, int Gg> DI void e_ops() { e_ops_<MODE, ES[MODE].first[Gg]>(make_iseq<ES[MODE].first[Gg + 1] - ES[MODE].first[Gg]>{}); }
template <int P, int KS, int I> DI void g_wread(const GM& g) {
  constexpr int nf = I >> 1, hf = I & 1, r = 128 + 64 * P + 8 * nf + 4 * KS + 2 * hf;
  asm volatile("ds_read_b64_tr_b16 a[%c1:%c2], %0 offset:%c3" ::"v"(g.aB[hf][nf]), "i"(r), "i"(r + 1), "i"(P * 32768 + KS * 16384));
}
template <int P, int M, int KS> DI void g_aread(const GM& g) {
  constexpr int r = 96 + 8 * (M & 1) + 4 * KS;
  asm volatile("ds_read_b128 v[%c1:%c2], %0 offset:%c3" ::"v"(g.aA[KS]), "i"(r), "i"(r + 3), "i"(P * 32768 + M * 2048));
}
template <bool W, int V, int J> DI void g_dma() {
  if constexpr (W) {
    if constexpr (J == 0) asm volatile("buffer_load_dwordx4 v%c0, s[68:71], 0 offen" GSC " lds" ::"i"(V) : "memory");
    else asm volatile("buffer_load_dwordx4 v%c0, s[68:71], s%c1 offen" GSC " lds" ::"i"(V), "i"(79 + J) : "memory");
  } else {
    if constexpr (J == 0) asm volatile("buffer_load_dwordx4 v%c0, s[64:67], 0 offen" GSC " lds" ::"i"(V) : "memory");
    else asm volatile("buffer_load_dwordx4 v%c0, s[64:67], s%c1 offen" GSC " lds" ::"i"(V), "i"(71 + J) : "memory");
  }
}
template <int Q> DI void att_item() {
  if constexpr (Q == 0) sld<83, B_ST, 1>();
  if constexpr (Q == 1) asm volatile("s_mov_b32 s85, %0" ::"i"(B_PM));
  if constexpr (Q == 2) asm volatile("s_add_u32 s84, s83, 0x8000\n\ts_mov_b32 s86, %0" ::"i"(B_ACT) SCC1);
  if constexpr (Q >= 3 && Q < 6) asm volatile("v_mov_b64 v[%c0:%c1], 0" ::"i"(108 + 2 * Q), "i"(109 + 2 * Q));
  if constexpr (Q == 6) asm volatile("v_mov_b32 v120, 0x45800000");
}
template <int I> DI void g_pro1();
template <int K, int I> DI void g_part() {
  constexpr bool W = K < 2;
  asm volatile("s_add_u32 m0, s%c0, %1\n\ts_nop 0" ::"i"(96 + K), "i"(W ? I * 4096 : (I & 1) * 4096 + (I >> 1) * 16384) : "scc");
  g_dma<W, 115 + K, W ? I : (I & 1) + (I >> 1) * 4>();
}
template <int Y, int I> DI void g_tag() {
  if constexpr (I == 0) { if constexpr (Y) sld<88, C_QT, 2>(); else { sld<88, A_TAB>(); sld<89, A_TAB + 1>(); } }
  if constexpr (I == 1 && !Y) asm volatile("s_lshr_b64 s[88:89], s[88:89], 12" SCC3);
  if constexpr (I == 3) asm volatile("v_readfirstlane_b32 s60, v120");
  if constexpr (I == 5) asm volatile("v_readfirstlane_b32 s61, v121");
  if constexpr (I == 9) asm volatile("s_mul_i32 s61, s61, 0x4c957f2d\n\ts_mul_i32 s63, s60, 0x5851f42d");
  if constexpr (I == 11) asm volatile("s_add_u32 s61, s61, s63\n\ts_mul_hi_u32 s63, s60, 0x4c957f2d" SCC3);
  if constexpr (I == 13) asm volatile("s_add_u32 s61, s61, s63\n\ts_mul_i32 s60, s60, 0x4c957f2d" SCC3);
  if constexpr (I == 15) asm volatile("s_add_u32 s60, s60, 0xf767814f\n\ts_addc_u32 s61, s61, 0x14057b7e" SCC3);
  if constexpr (I == 17) asm volatile("v_mov_b64 v[82:83], s[60:61]");
}
template <bool TR, int G> DI void g_last_setup(int wid) {
  if constexpr (G == 0) { sld<72, A_SX>(); sld<76, A_MX>(); }
  if constexpr (G >= 1 && G < 4) sld<72 + G, A_SX + G>();
  if constexpr (G == 3) sld<63, A_VOO>();
  if constexpr (G >= 5 && G < 8) sld<72 + G, A_MX + G - 4>();
  if constexpr (G == 6) asm volatile("v_add_u32 v84, s63, v89");
  if constexpr (G == 7) asm volatile("s_lshl_b32 s63, %0, 10\n\ts_lshl_b32 s95, %0, 12" ::"s"(wid) SCC1);
  if constexpr (G == 8) asm volatile("v_add_u32 v85, %0, v84\n\tv_add_u32 v86, 0x10000, v89" ::"i"(TR ? 4096 : 8192));
  if constexpr (G == 9) asm volatile("v_add_u32 v87, s63, v86\n\tv_add_u32 v95, s95, v86");
  if constexpr (G == 10) asm volatile("s_lshl_b32 s80, %0, 13\n\ts_bitset1_b32 s80, 16" ::"s"(wid) SCC1);
  if constexpr (G == 11) asm volatile("s_add_u32 s81, s80, 0x8000\n\ts_mov_b32 s90, 0x8000" SCC3);
  if constexpr (G == 13) { sld<62, A_GF>(); asm volatile("s_mov_b32 s91, 0xc000"); }
  if constexpr (G == 14) { att_item<0>(); att_item<1>(); }
  if constexpr (G == 15) { att_item<2>(); sld<92, A_GH>(); }
  if constexpr (G == 12) { sld<87, B_MS, 1>(); asm volatile("s_add_u32 s82, s73, 2" SCC3); }
}
template <bool TR, int P, int H, int G, int MD, int PT> DI void g_gap(const GM& g, int wid, const PA& pa) {
  constexpr bool FIRST = MD == 1 || MD == 2, LAST = MD == 4;
  constexpr int m = 4 * H + (G >> 4), q = G & 15, n = (SNK & 8) && MD < 2 && (m & 1) ? 7 - (q >> 1) : q >> 1, ks = (SNK & 4 ? q ^ (q >> 1) : q) & 1;
  constexpr int a = 96 + 8 * (m & 1) + 4 * ks, b = 128 + 64 * P + 8 * n + 4 * ks, d0 = TR ? 64 * (m >> 1) + 8 * n + 4 * (m & 1) : 32 * m + 4 * n, d = m < 4 ? 128 + d0 : d0 - 128;
  constexpr bool ST0 = MD == 1 && H == 0;
  constexpr int J = LAST ? -1 : ST0 ? (G >= 48 && G % 4 == 0 ? G / 4 - 8 : -1) : g_j(G);
  constexpr int gj = (J & 3) ^ ((J & 3) >> 1);
  constexpr int EM = LAST ? 1 + TR : 0, eg = (MD == 2 ? 128 : 0) + 64 * H + G, L = LAST ? e_load(eg) : -1;
  if constexpr (MD == 1 && G == 32) asm volatile("s_waitcnt vmcnt(%0) lgkmcnt(%1)\n\ts_barrier" ::"i"(H ? 12 + (PT >= 0 ? PS.vm[eg] : 0) : 19), "i"(8 * H) : "memory");
  else if constexpr (q == 0 && G > 0) asm volatile("s_waitcnt lgkmcnt(%0)" ::"i"(LAST ? 0 : MD == 1 ? 8 * H : 4));
  if constexpr (J == 0) asm volatile("s_add_u32 m0, s79, %0" ::"i"((P ^ H ^ 1) * 32768 + (1 - H) * 8192) SCC1);
  else if constexpr (J == 4) asm volatile("s_add_u32 m0, s87, %0" ::"i"((P ^ H) * 32768 + (1 - H) * 16384) SCC1);
  else if constexpr (J > 0 && (J & 3) == 1) asm volatile("s_bitset1_b32 m0, 12");
  else if constexpr (J > 0 && (J & 3) == 2) asm volatile("s_bitset1_b32 m0, %0" ::"i"(J < 4 ? 14 : 13));
  else if constexpr (J > 0) asm volatile("s_bitset0_b32 m0, 12");
  if constexpr (L >= 0 && (L & 3) == 0) asm volatile("s_mov_b32 m0, s%c0" ::"i"(L < 16 ? 76 + (L >> 2) : 87));
  constexpr int QR = LAST ? e_qrd(eg) : -1;
  if constexpr (QR == 0) asm volatile("s_waitcnt vmcnt(%0)" ::"i"(8 + e_nld(0, eg) + ES[EM].st[eg]) : "memory");
  if constexpr (QR == 8) asm volatile("s_waitcnt vmcnt(%0)" ::"i"(e_nld(64, eg) + ES[EM].st[eg] - ES[EM].st[60]) : "memory");
#define GMF(D, X, Y, C) asm volatile("v_mfma_f32_16x16x32_bf16 " D "[%c0:%c1], " X ", " Y ", " C ::"i"(d), "i"(d + 3), "i"(a), "i"(a + 3), "i"(b), "i"(b + 3))
#define GMF2(D, C) do { if constexpr (TR) GMF(D, "v[%c2:%c3]", "a[%c4:%c5]", C); else GMF(D, "a[%c4:%c5]", "v[%c2:%c3]", C); } while (0)
  if constexpr (FIRST && !(q & 1)) { if constexpr (m < 4) GMF2("v", "0"); else GMF2("a", "0"); }
  else if constexpr (m < 4) GMF2("v", "v[%c0:%c1]");
  else GMF2("a", "a[%c0:%c1]");
#undef GMF2
#undef GMF
  if constexpr (J >= 0 && J < 4) g_dma<false, 84, (1 - H) * 2 + (gj & 1) + (gj >> 1) * 4>();
  else if constexpr (J >= 4) g_dma<true, 85, (1 - H) * 4 + gj>();
  if constexpr (L >= 0)
    asm volatile("buffer_load_dwordx4 v89, s[%c0:%c1], s%c2 offen offset:%c3 sc1 lds" ::"i"(L >= 4 && L < 8 || L >= 16 ? 68 : 56), "i"(L >= 4 && L < 8 || L >= 16 ? 71 : 59),
                 "i"(L < 16 ? 72 + (L >> 2) : 82), "i"((L & 3) * 1024) : "memory");
  if constexpr (QR >= 0)
    asm volatile("ds_read_b128 a[%c1:%c2], v%c0 offset:%c3" ::"i"(QR < 12 ? 87 : 95), "i"(128 + 4 * QR), "i"(131 + 4 * QR), "i"(QR < 12 ? QR * 4096 : 49152 + (QR - 12) * 1024));
  if constexpr ((q == 1 || q == 2) && !(LAST && m == 7)) g_aread<(m < 7 ? P : P ^ 1), (m + 1) & 7, q - 1>(g);
  if constexpr (!LAST) {
    if constexpr (MD == 1 && H == 1 && q >= 3 && q < 11) g_wread<P ^ 1, (G >> 5), ((G >> 4) & 1) * 8 + q - 3>(g);
    if constexpr (MD != 1 && q >= 3 && q < 11 && (q & 1)) g_wread<P ^ 1, H, (G >> 4) * 4 + (q - 3) / 2>(g);
    if constexpr (MD == 1 && eg >= 62 && eg < 80) g_tag<P, eg - 62>();
    if constexpr (H == 1 && G == 0) asm volatile("s_mov_b32 s90, s91");
    if constexpr (H == 1 && G == 4) asm volatile("v_lshl_add_u32 v84, s90, 7, v86\n\ts_bfe_u32 s91, s88, 0x40000" SCC3);
    if constexpr (H == 1 && G == 6) asm volatile("s_lshr_b64 s[88:89], s[88:89], 4\n\ts_lshl_b32 s92, s91, 19" SCC3);
    if constexpr (H == 1 && G == 10) asm volatile("v_add_u32 v85, s92, v87");
  } else if constexpr (eg < 16) g_last_setup<TR, eg>(wid);
  if constexpr (ST0 && G == 23) asm volatile("v_lshl_add_u32 v85, s91, 19, v87");
  if constexpr (ST0 && G < 36 && G % 3 == 0) g_part<G < 12 ? 2 : G < 24 ? 1 : 3, (G / 3) & 3>();
  if constexpr (ST0 && G >= 36 && G < 48 && G % 3 == 0) g_pro1<16 + G / 3>();
  if constexpr (MD == 3 && eg == 0) sld<63, A_VOQ>();
  if constexpr (MD == 3 && eg == 4) asm volatile("v_add_u32 v95, s63, v89\n\ts_mov_b32 s94, %0" ::"i"(__builtin_bit_cast(unsigned, CS)));
  if constexpr (MD >= 2) e_ops<EM, eg>();
  constexpr int pg = 128 * PT + 64 * H + G;
  if constexpr (PT == 2 && eg == 0) sld<93, A_ZO>();
  if constexpr (PT >= 0) p_ops<pg>(pa);
  if constexpr (G == 61 && !LAST) {
    constexpr int ns = MD >= 2 ? ES[0].st[eg + 1] - ES[0].st[eg >= GD13 ? eg - GD13 : 0] : PT >= 0 ? PS.vm[pg + 1] - PS.vm[pg >= GD13 ? pg - GD13 : 0] : 0;
    asm volatile("s_waitcnt vmcnt(%0) lgkmcnt(0)\n\ts_barrier" ::"i"(12 + ns) : "memory");
  } else if constexpr (G == 61) asm volatile("s_waitcnt lgkmcnt(0)\n\ts_barrier" ::: "memory");
  GAP();
}
template <bool TR, int P, int H, int MD, int PT, int... Gs> DI void g_half_(const GM& g, int wid, const PA& pa, iseq<Gs...>) { (g_gap<TR, P, H, Gs, MD, PT>(g, wid, pa), ...); }
template <bool TR, int P, int H, int MD> DI void g_half(const GM& g, int wid = 0) { g_half_<TR, P, H, MD, -1>(g, wid, PA{}, make_iseq<64>{}); }
template <int T, int H> DI void q_half(const GM& g, const PA& pa) { g_half_<false, (T & 1) ^ 1, H, T == 0, T>(g, 0, pa, make_iseq<64>{}); }
template <int Q> DI void park_store() {
  constexpr int r = 32 + 16 * Q;
  asm volatile("s_mov_b32 s93, %0\n\tbuffer_store_dwordx4 v[%c1:%c2], v95, " SW ", s93 offen sc1\n\tbuffer_store_dwordx4 v[%c3:%c4], v95, " SW ", s93 offen offset:1024 sc1\n\t"
               "buffer_store_dwordx4 v[%c5:%c6], v95, " SW ", s93 offen offset:2048 sc1\n\tbuffer_store_dwordx4 v[%c7:%c8], v95, " SW ", s93 offen offset:3072 sc1"
               ::"i"((15 - 2 * (5 + Q)) * 8192), "i"(r), "i"(r + 3), "i"(r + 4), "i"(r + 7), "i"(r + 8), "i"(r + 11), "i"(r + 12), "i"(r + 15) : "memory");
}
static_assert(PARK == 0 || PARK == 12);
typedef __attribute__((ext_vector_type(16))) unsigned u32x16;
struct RSV { u32x16 a, b, c; };
DI void rsv_begin(RSV& r) { asm volatile("rsv_begin:" : "={v[32:47]}"(r.a), "={v[48:63]}"(r.b), "={v[64:79]}"(r.c)); }
DI void rsv_end(const RSV& r) { asm volatile("rsv_end:" ::"{v[32:47]}"(r.a), "{v[48:63]}"(r.b), "{v[64:79]}"(r.c)); }
template <int P, int... Is> DI void g_read0(const GM& g, iseq<Is...>) {
  (g_wread<P, 0, Is>(g), ...); (g_wread<P, 1, Is>(g), ...);
  g_aread<P, 0, 0>(g); g_aread<P, 0, 1>(g);
}
template <int I> DI void g_pro1() {
  constexpr int grp = I >> 2, i = I & 3, t = grp < 4 ? 0 : grp < 7 ? 1 : 2;
  constexpr bool isA = grp == 0 || grp == 3 || grp == 5;
  constexpr int jj = isA ? (grp == 3 ? 2 : 0) + (i & 1) + (i >> 1) * 4 : (grp == 2 || grp == 6 ? 4 : 0) + i;
  asm volatile("s_add_u32 m0, s%c0, %1\n\ts_nop 0" ::"i"(t == 1 ? 92 : 90), "i"((isA ? 0 : 65536) + jj * 4096) : "scc");
  g_dma<!isA, (isA ? 84 : 85) + (t ? 0 : 2), jj>();
}
template <int B, int... Is> DI void g_pro(iseq<Is...>) { (g_pro1<B + Is>(), ...); }
template <bool TR, bool SCL, class HO, class HE> DI void g_loop(const GM& g, HO&& odd, HE&& even) {
  constexpr int nkt = SCL ? 7 : 16, P0 = SCL ? 1 : 0;
#pragma unroll 1
  for (int t = 1;; t += 2) {
    g_half<TR, P0 ^ 1, 0, 0>(g);
    odd(t);
    GAP();
    g_half<TR, P0 ^ 1, 1, 0>(g);
    if (SCL && t + 2 >= nkt) break;
    g_half<TR, P0, 0, 0>(g);
    even(t + 1);
    GAP();
    g_half<TR, P0, 1, 0>(g);
    if (!SCL && t + 3 >= nkt) break;
  }
}

typedef __attribute__((ext_vector_type(4))) unsigned u32x4;
typedef __attribute__((ext_vector_type(4))) float f32x4;

constexpr int R_S = 128, R_P = 192, R_CI = 224, R_E = 96;
constexpr int R_TS = 112, R_RS = 114, R_M = 116;
constexpr int R_PD = 118, R_THR = 120, R_TMP = 121;
struct AC {
  u64 redo;
  unsigned rare;
};
template <int T, int D, int K> DI void d_op() {
  constexpr int t = T + 8 * (D & 1);
  if constexpr (K < 8) asm volatile("v_accvgpr_read_b32 v%c0, a[%c1]" ::"i"(t + K), "i"(8 * D + K));
  else if constexpr (K < 12) asm volatile("v_cvt_pk_bf16_f32 v%c0, v%c1, v%c2" ::"i"(t + K - 8), "i"(t + 2 * (K - 8)), "i"(t + 2 * (K - 8) + 1));
  else
    asm volatile("buffer_store_dwordx4 v[%c0:%c1], v%c2, " SW ", s%c3 offen offset:%c4 sc1" ::"i"(t), "i"(t + 3), "i"(84 + ((D >> 2) & 1)), "i"(90 + (D >> 3)),
                 "i"((D & 3) * 1024) : "memory");
}
template <int T, int O0, int... Os> DI void d_ops(iseq<Os...>) { (d_op<T, (O0 + Os) / 13, (O0 + Os) % 13>(), ...); }
template <int D> DI void d_store() { d_ops<160, 13 * D>(make_iseq<13>{}); }
template <int X, int F> DI void kread() {
  constexpr int r = 192 + 4 * (F & 15);
  asm volatile("ds_read_b128 a[%c0:%c1], v89 offset:%c2" ::"i"(r), "i"(r + 3), "i"(X * 32768 + F * 1024));
}
template <int X, int F> DI void vread() {
  constexpr int r = 192 + 4 * (F & 15);
  asm volatile("ds_read_b128 a[%c0:%c1], v86 offset:%c2" ::"i"(r), "i"(r + 3), "i"(X * 32768 + (F & 1) * 16384 + (F >> 1) * 1024));
}
template <int X, int... Fs> DI void kreads(iseq<Fs...>) { (kread<X, Fs>(), ...); }
template <int X, int... Fs> DI void vreads(iseq<Fs...>) { (vread<X, Fs>(), ...); }
template <int X, int J> DI void dma_m0() {
  if constexpr (J == 0) asm volatile("s_mov_b32 m0, s%c0" ::"i"(80 + X));
  else if constexpr (J == 4) asm volatile("s_or_b32 m0, s%c0, 0x1000" ::"i"(80 + X) SCC1);
  else if constexpr (J == 8) asm volatile("s_bitset0_b32 m0, 16");
  else if constexpr (J == 12) asm volatile("s_bitset0_b32 m0, 12");
}
template <int J> DI void dma_at() {
  asm volatile("buffer_load_dwordx4 v%c0, " SW ", s%c1 offen offset:%c2 sc1 lds" ::"i"(J >= 4 && J < 12 ? 91 : 90), "i"(J < 8 ? 82 : 84), "i"((J & 3) * 1024) : "memory");
}
template <int V, int S> DI void dma4() {
  asm volatile("buffer_load_dwordx4 v%c0, " SW ", s%c1 offen sc1 lds\n\tbuffer_load_dwordx4 v%c0, " SW ", s%c1 offen offset:1024 sc1 lds\n\t"
               "buffer_load_dwordx4 v%c0, " SW ", s%c1 offen offset:2048 sc1 lds\n\tbuffer_load_dwordx4 v%c0, " SW ", s%c1 offen offset:3072 sc1 lds"
               ::"i"(V), "i"(S) : "memory");
}
template <int Q> DI void k01_dma() {
  asm volatile("s_add_u32 m0, s80, %0\n\ts_nop 0" ::"i"((Q >> 1) * 32768 + (Q & 1) * 4096 - 65536) SCC1);
  dma4<90 + (Q & 1), 83 + (Q >> 1)>();
}
template <int R, int I, int ST = 4, int V = 89> DI void f_load() {
  asm volatile("buffer_load_dwordx4 a[%c0:%c1], v%c3, " SW ", s72 offen offset:%c2 sc1" ::"i"(R + ST * I), "i"(R + ST * I + 3), "i"((I & 3) * 1024), "i"(V) : "memory");
}
DI void f_next() { asm volatile("s_add_u32 s72, s72, 0x1000" SCC3); }
constexpr int o_wreg(int o, int f) { return o == 0 ? 128 + 4 * f : o == 3 ? 192 + 4 * f : 8 * f + 4 * (o - 1); }
template <int F, int LASTF = 3> DI void w_load() {
  f_load<o_wreg(F >> 4, 0), F & 15, o_wreg(F >> 4, 1) - o_wreg(F >> 4, 0), (F & 1) ? 84 : 89>();
  if constexpr ((F & 3) >= LASTF) asm volatile("s_add_u32 s72, s72, %0" ::"i"(F < 4 ? 0x1000 : F < 8 ? 0xf000 : F & 4 ? 0x7000 : 0x1000) SCC1);
}
#define NWF 40
constexpr int CV_KP = 5120 - 32, CV_KS = 10304 - 32;
struct CV { unsigned pa[4], pb, lw, vzo; };
constexpr int cv_J(int q) { int J = 7; while (q > J) { q -= J + 1; --J; } return J; }
constexpr int cv_i(int q) { int J = 7; while (q > J) { q -= J + 1; --J; } return q; }
template <int r_, int N> DI void cv_rd(const CV& c) {
  constexpr int R = (r_ + 1) & 7;
  if constexpr (N < 16) {
    constexpr int i = N >> 1, o = 16 * R + 128 * (i & 1) + 2 * (N & 1), r = 192 + 32 * (r_ & 1) + 2 * N;
    asm volatile("ds_read2_b32 a[%c1:%c2], %0 offset0:%c3 offset1:%c4" ::"v"(c.pa[i >> 1]), "i"(r), "i"(r + 1), "i"(o), "i"(o + 1));
  } else {
    constexpr int m = N - 16, r = 160 + 32 * (r_ & 1) + 4 * m;
    asm volatile("ds_read_b128 v[%c1:%c2], %0 offset:%c3" ::"v"(c.pb), "i"(r), "i"(r + 3), "i"(64 * R + 512 * (7 - m)));
  }
}
template <int O> DI void cv_z(const CV& c) {
  constexpr int pr = O / 7, o = O % 7, w = 128 + 4 * (7 - 2 * pr), y = w + 2 * (o & 1) - 4 * (o >> 1);
  if constexpr (o == 4) asm volatile("s_add_u32 s63, s65, %2\n\tv_permlane16_swap_b32_e32 v%c0, v%c1" ::"i"(w), "i"(w + 2), "i"((6 - 2 * pr) * 8 * 3584) SCC1);
  else if constexpr (o == 5) asm volatile("v_permlane16_swap_b32_e32 v%c0, v%c1" ::"i"(w + 1), "i"(w + 3));
  else if constexpr (o == 6) asm volatile("buffer_store_dwordx4 v[%c0:%c1], %2, " SW ", s63 offen sc1" ::"i"(w), "i"(w + 3), "v"(c.vzo) : "memory");
  else asm volatile("v_cvt_pk_bf16_f32 v%c0, v%c1, v%c2" ::"i"(w + o), "i"(y), "i"(y + 1));
}
#define CVS 6
#define CVF 150
#define CVP 206
#define CVK 250
constexpr int CVQ = 16 * CVS + 2;
constexpr int cv_vm(int T) {
  return T < 16 * CVS && T % CVS == CVS - 1 ? 1 : T >= CVQ && T < CVQ + 96 && (T - CVQ) % 6 == 0 ? 2 : T == CVF ? 4 : T == CVP ? 5 : T > CVK && T <= CVK + 48 && (T - CVK) % 3 == 1 ? 7 : 0;
}
constexpr int cv_cnt(int T0, int T1, int kind = 0) { int n = 0; for (int T = T0; T < T1; ++T) n += kind ? cv_vm(T) == kind : cv_vm(T) != 0; return n; }
static_assert(16 * CVS + 4 <= 108 && cv_vm(CVF) == 4 && cv_vm(CVP) == 5 && CVQ + 96 < CVK);
constexpr int cv_z0(int T) { return T < 271 ? 0 : T < 278 ? T - 271 : T < 281 ? 7 : T - 274; }
template <int I> DI void k01_dma1() {
  if constexpr ((I & 3) == 0) asm volatile("s_add_u32 m0, s80, %0\n\ts_nop 0" ::"i"((I >> 3) * 32768 + ((I >> 2) & 1) * 4096 - 65536) SCC1);
  asm volatile("buffer_load_dwordx4 v%c0, " SW ", s%c1 offen offset:%c2 sc1 lds" ::"i"(90 + ((I >> 2) & 1)), "i"(83 + (I >> 3)), "i"((I & 3) * 1024) : "memory");
}
template <int T> DI void cv_slot(const CV& c) {
  constexpr int r = T / 36, Q = T % 36, i = cv_i(Q), J = cv_J(Q), d = 128 + 4 * J, a = 192 + 32 * (r & 1) + 4 * i, b = 160 + 32 * (r & 1) + 4 * (J - i), vm = cv_vm(T);
  if constexpr (Q == 0) asm volatile("s_waitcnt lgkmcnt(0)");
  if constexpr (r == 0 && i == 0)
    asm volatile("v_mfma_f32_16x16x32_bf16 v[%c0:%c1], a[%c2:%c3], v[%c4:%c5], 0" ::"i"(d), "i"(d + 3), "i"(a), "i"(a + 3), "i"(b), "i"(b + 3));
  else
    asm volatile("v_mfma_f32_16x16x32_bf16 v[%c0:%c1], a[%c2:%c3], v[%c4:%c5], v[%c0:%c1]" ::"i"(d), "i"(d + 3), "i"(a), "i"(a + 3), "i"(b), "i"(b + 3));
  if constexpr (r == 7 && i == 0) asm volatile("v_mfma_f32_16x16x32_bf16 v[%c0:%c1], v[96:99], v[%c2:%c3], v[%c0:%c1]" ::"i"(d), "i"(d + 3), "i"(b), "i"(b + 3));
  if constexpr (Q < 24 && r < 7) cv_rd<r + 1, Q>(c);
  if constexpr (T == 32) asm volatile("ds_read_u16 v121, %0 offset:5120" ::"v"(c.lw));
  if constexpr (T == 33) asm volatile("ds_write_b128 %0, v[114:117] offset:%c1\n\tds_write_b128 %0, v[114:117] offset:%c2" ::"v"(c.lw), "i"(CV_KS), "i"(CV_KS + 16));
  if constexpr (T == 62) asm volatile("ds_write_b16 %0, v121 offset:%c1" ::"v"(c.lw), "i"(CV_KS + 30));
  if constexpr (T == 34) sld<72, A_OQ>();
  if constexpr (T == 35) sld<65, A_ZO>();
  if constexpr (T == CVQ - 2) { asm volatile("s_waitcnt vmcnt(16)" ::: "memory"); fl_check(); }
  if constexpr (T < 16 * CVS) d_ops<224, 13 * (T / CVS) + (T % CVS * 13) / CVS>(make_iseq<((T % CVS + 1) * 13) / CVS - (T % CVS * 13) / CVS>{});
  if constexpr (vm == 2) { f_load<128, (T - CVQ) / 6>(); if constexpr ((T - CVQ) / 6 % 4 == 3) f_next(); }
  if constexpr (vm == 4) {
    asm volatile("s_waitcnt vmcnt(%0)\n\ts_barrier" ::"i"(cv_cnt(16 * CVS, T)) : "memory");
    set_flag<A_FG>();
  }
  if constexpr (T == CVP - 3) fl_mask<A_N01, -1>();
  if constexpr (vm == 5) fl_issue();
  if constexpr (T == CVK) { asm volatile("s_waitcnt vmcnt(%0)" ::"i"(cv_cnt(CVP + 1, T)) : "memory"); fl_check(); }
  if constexpr (vm == 7) k01_dma1<cv_cnt(CVK, T, 7)>();
  if constexpr (cv_z0(T + 1) > cv_z0(T)) cv_z<cv_z0(T)>(c);
  GAP();
}
template <int... T> DI void cv_slots(const CV& c, iseq<T...>) { (cv_slot<T>(c), ...); }
template <int... N> DI void cv_rd0(const CV& c, iseq<N...>) { (cv_rd<0, N>(c), ...); }
template <int O> DI void cv_tail(const CV& c) {
  constexpr int k = cv_cnt(CVK, 288, 7) + O / 6;
  if constexpr (O % 6 == 0 && k < 16) k01_dma1<k>();
  cv_z<cv_z0(288) + O>(c);
}
template <int... O> DI void cv_tails(const CV& c, iseq<O...>) { (cv_tail<O>(c), ...); }
static_assert(cv_cnt(CVK, 288, 7) >= 8 && cv_cnt(CVK, 288, 7) + 3 >= 16);
constexpr int cv_end() {
  int T = CVK; while (cv_cnt(CVK, T + 1, 7) < 8) ++T;
  int n = 8; for (int o = cv_z0(T); o < 28; ++o) n += o % 7 == 6;
  return n;
}

template <int Q> DI void acc_zero() {
  asm volatile("v_mfma_f32_32x32x16_bf16 a[%c0:%c1], v[208:211], v[208:211], 0" ::"i"(16 * Q), "i"(16 * Q + 15));
}
struct SMS { int ex[128], op0[128], nop[128], last; };
constexpr SMS sm_sched() {
  SMS z{};
  int et[32] = {}, e = 0;
  for (int t = 0; t < 128; ++t) {
    const int g = t & 15;
    const bool x = t < 48 ? (g >= 8 && (g & 1)) : (g >= 8 && g != 10 && g != 13 && g != 15);
    z.ex[t] = x && e < 32 ? e : -1;
    if (z.ex[t] >= 0) et[e++] = t;
  }
  int o = 0;
  for (int t = 0; t < 128; ++t) {
    const int g = t & 15;
    int cap = g < 8 ? (g == 0 ? 0 : 1) : (z.ex[t] >= 0 || t < 48 ? 0 : 2);
    z.op0[t] = o; z.nop[t] = 0;
    while (cap > 0 && o < 48 && et[2 * (o / 3) + (o % 3 == 0 ? 0 : 1)] + 2 <= t) {
      const int el = 2 * (o / 3) + o % 3;
      if (!(o % 3 < 2 && (el == 0 || el == 4))) --cap;
      ++o; ++z.nop[t]; z.last = t;
    }
  }
  return z;
}
constexpr SMS SM = sm_sched();
static_assert(SM.last <= 115);
constexpr int p_reg(int Y, int k) { return R_P + 16 * Y + 4 * ((k >> 3) * 2 + ((k >> 1) & 1)) + ((k >> 2) & 1) * 2 + (k & 1); }
template <int Y, int O> DI void sm_op() {
  constexpr int k = O / 3, e = 2 * k + O % 3, ts = R_TS + ((e >> 2) & 1);
  if constexpr (O % 3 == 2) asm volatile("v_cvt_pk_bf16_f32 v%c0, v%c1, v%c2" ::"i"(p_reg(Y, k)), "i"(R_E + ((2 * k) & 15)), "i"(R_E + ((2 * k + 1) & 15)));
  else if constexpr (e == 1 || e == 5) asm volatile("v_add_f32_e32 v%c0, v%c1, v%c2" ::"i"(ts), "i"(R_E + e - 1), "i"(R_E + e));
  else if constexpr (e != 0 && e != 4) asm volatile("v_add_f32_e32 v%c0, v%c0, v%c1" ::"i"(ts), "i"(R_E + (e & 15)));
}
template <int Y, int T> DI void sm_gap(u64& redo) {
  if constexpr (SM.ex[T] >= 0) asm volatile("v_exp_f32 v%c0, v%c1" ::"i"(R_E + (SM.ex[T] & 15)), "i"(R_S + 32 * Y + SM.ex[T]));
  if constexpr (SM.nop[T] > 0) sm_op<Y, SM.op0[T]>();
  if constexpr (SM.nop[T] > 1) sm_op<Y, SM.op0[T] + 1>();
  if constexpr (SM.nop[T] > 2) sm_op<Y, SM.op0[T] + 2>();
  if constexpr (T == SM.last + 1) asm volatile("v_add_f32_e32 v%c0, v%c1, v%c2" ::"i"(R_TMP), "i"(R_TS), "i"(R_TS + 1));
  if constexpr (T == SM.last + 2) asm volatile("v_cmp_nle_f32_e64 %0, v%c1, v%c2" : "=s"(redo) : "i"(R_TMP), "i"(R_THR));
}
#define EVG 35
template <int X, int KB, int QB, int S_, bool FIRST, bool HEAD = S_ == 0> DI void qk_mfma() {
  constexpr int r = 192 + 4 * ((KB & 1) * 8 + S_), q = 128 + 4 * (QB * 8 + S_);
  constexpr int d = R_S + 32 * X + 4 * (KB * 2 + QB), ci = HEAD ? R_CI + 4 * (KB * 2 + QB) : d;
  if constexpr (FIRST && HEAD)
    asm volatile("v_mfma_f32_16x16x32_bf16 v[%c0:%c1], a[%c2:%c3], a[%c4:%c5], 0" ::"i"(d), "i"(d + 3), "i"(r), "i"(r + 3), "i"(q), "i"(q + 3));
  else
    asm volatile("v_mfma_f32_16x16x32_bf16 v[%c0:%c1], a[%c2:%c3], a[%c4:%c5], v[%c6:%c7]" ::"i"(d), "i"(d + 3), "i"(r), "i"(r + 3), "i"(q), "i"(q + 3),
                 "i"(ci), "i"(ci + 3));
}
template <int X, int FIRST, bool NOQK, int G> DI void gapA(AC& c) {
  constexpr int Xp = X ^ 1, kb = G >> 4, g = G & 15;
  if constexpr (NOQK && G < 48 && G % 3 == 0) { f_load<128, G / 3>(); if constexpr (G % 12 == 9) f_next(); }
  if constexpr (G == 48) asm volatile("s_waitcnt vmcnt(%0) lgkmcnt(0)\n\ts_barrier" ::"i"(NOQK ? 16 : FIRST == 2 ? 63 : FIRST == 1 ? 9 : FIRST == 3 ? 8 : 0) : "memory");
  else if constexpr (g == 0 && !NOQK) asm volatile("s_waitcnt lgkmcnt(0)");
  if constexpr (!NOQK) {
    if constexpr (SNK & 1) qk_mfma<X, kb, (g >> 3) ^ (kb & 1), g < 8 ? g : 15 - g, FIRST != 0, !(g & 7)>();
    else qk_mfma<X, kb, (g >> 3), g & 7, FIRST != 0>();
    if constexpr (g < 8 && kb < 3) kread<X, (kb + 1) * 8 + g>();
    if constexpr (G == 54) asm volatile("s_add_u32 s82, s83, 0x1000000" SCC3);
    if constexpr (G == 55) dma_m0<X, 0>();
    if constexpr (G >= 56 && !(G & 1) && FIRST != 2) dma_at<(G - 56) / 2>();
    if constexpr (G == EVG) asm volatile("v_readlane_b32 s87, v93, s86");
    if constexpr (G == EVG + 2) asm volatile("s_add_u32 s86, s86, 1" SCC3);
    if constexpr (G == 57) asm volatile("s_mov_b32 s83, s84\n\ts_andn2_b32 s84, s87, 0x3ff" SCC3);
    if constexpr (G == 59 && FIRST) asm volatile("s_and_b32 %0, s87, 0x3ff" : "=s"(c.rare) SCC2);
    if constexpr (G == 59 && !FIRST) asm volatile("s_and_b32 s63, s87, 0x3ff\n\ts_cmp_lg_u64 %0, 0" ::"s"(c.redo) SCC1);
    if constexpr (G == 61 && !FIRST) asm volatile("s_addc_u32 %0, s63, 0" : "=s"(c.rare) SCC2);
  } else if constexpr (SM.nop[64 + G] > 0) asm volatile("s_nop 0");
  if constexpr (!FIRST && g < 8 && kb == 3) vread<Xp, g>();
  if constexpr (FIRST == 2) d_ops<160, 13 * (G / 8) + (G % 8 * 13) / 8>(make_iseq<((G % 8 + 1) * 13) / 8 - (G % 8 * 13) / 8>{});
  if constexpr (FIRST == 2 && G == 34)
    asm volatile("v_and_b32 v124, 0xf0, v89\n\ts_waitcnt vmcnt(8)\n\t" A_CHECK("v[122:123]", "s[96:97]", "s72")
                 "buffer_load_dwordx2 v[122:123], v124, " SW ", s73 offen offset:1024 sc1" ::: "vcc", "scc", "memory");
  if constexpr (FIRST == 1 && G >= 8 && G < 40 && G % 4 == 1)
    asm volatile("s_mov_b32 m0, s75\n\ts_add_u32 s75, s75, 0x1000\n\tbuffer_load_dwordx4 v84, s[64:67], s94 offen lds\n\ts_add_u32 s94, s94, 0x3800" ::: SCC_ "memory");
  if constexpr (FIRST == 1 && G == 42) asm volatile("s_mov_b32 m0, 0x1f800\n\ts_mov_b64 s[64:65], s[88:89]\n\ts_mov_b32 s66, 0x1c0000\n\tbuffer_load_dword v121, s[64:67], 0 offen lds" ::: "memory");
  if constexpr (FIRST == 3) {
    constexpr int n = (G >> 4) * 4 + (G & 3), f = G < 32 ? (G - 17) / 4 : 4 + (G - 33) / 3;
    if constexpr (G == 2) { asm volatile("v_xor_b32 v84, 32, v85"); sld<94, B_WD, 1>(); }
    if constexpr (G < 32 && (G & 12) == 8)
      asm volatile("ds_read_b64_tr_b16 v[%c0:%c1], v%c4 offset:%c5\n\tds_read_b64_tr_b16 v[%c2:%c3], v%c4 offset:%c6" ::"i"(224 + 4 * n), "i"(225 + 4 * n), "i"(226 + 4 * n),
                   "i"(227 + 4 * n), "i"(n & 1 ? 84 : 85), "i"((n >> 1) * 8192), "i"((n >> 1) * 8192 + 4096));
    if constexpr (G == 32) asm volatile("s_add_u32 s94, s94, 0x1000" SCC3);
    if constexpr (G >= 17 && G < 45 && (G < 32 ? G % 4 == 1 : G % 3 == 0 && G > 32))
      asm volatile("buffer_store_dwordx4 v[%c0:%c1], v89, " SW ", s94 offen offset:%c2 sc1" ::"i"(224 + 4 * f), "i"(227 + 4 * f), "i"((f & 3) * 1024) : "memory");
  }
  if constexpr (FIRST == 3 && G == 42) sld<63, B_FSET, 1>();
  if constexpr (FIRST == 3 && G == 48) asm volatile(A_FLAG ::: "memory");
  if constexpr (FIRST && G == 44) asm volatile("v_mov_b64 v[208:209], 0\n\tv_mov_b64 v[210:211], 0");
  if constexpr (!FIRST) sm_gap<Xp, 64 + G>(c.redo);
  GAP();
}
template <int R> DI void rescale1(float alpha) {
  float t;
  asm volatile("v_accvgpr_read_b32 %0, a[%c2]\n\tv_mul_f32 %0, %0, %1\n\tv_accvgpr_write_b32 a[%c2], %0" : "=&v"(t) : "v"(alpha), "i"(R));
}
template <int Q0, int R> DI void rescale_q(const float (&alpha)[2]) { if constexpr (((R >> 2) & 1) >= Q0) rescale1<R>(alpha[(R >> 2) & 1]); }
template <int Q0, int... Rs> DI void rescale_all(const float (&alpha)[2], iseq<Rs...>) { (rescale_q<Q0, Rs>(alpha), ...); }
template <bool MAX> DI float row_reduce(float v) {
  auto p = __builtin_amdgcn_permlane32_swap(__float_as_uint(v), __float_as_uint(v), false, false);
  v = MAX ? fmaxf(__uint_as_float(p[0]), __uint_as_float(p[1])) : __uint_as_float(p[0]) + __uint_as_float(p[1]);
  p = __builtin_amdgcn_permlane16_swap(__float_as_uint(v), __float_as_uint(v), false, false);
  return MAX ? fmaxf(__uint_as_float(p[0]), __uint_as_float(p[1])) : __uint_as_float(p[0]) + __uint_as_float(p[1]);
}
template <int Y, int QB> DI float row_max() {
  constexpr int r = R_S + 32 * Y + 4 * QB;
  float mx;
  asm volatile("v_max_f32 %0, v%c1, v%c2\n\tv_max3_f32 %0, %0, v%c3, v%c4" : "=&v"(mx) : "i"(r), "i"(r + 1), "i"(r + 2), "i"(r + 3));
  static_for([&](auto K) {
    constexpr int q = r + 8 * (K + 1);
    float t = mx;
    asm volatile("v_max3_f32 %0, %0, v%c1, v%c2\n\tv_max3_f32 %0, %0, v%c3, v%c4" : "+v"(t) : "i"(q), "i"(q + 1), "i"(q + 2), "i"(q + 3));
    mx = t;
  }, make_iseq<3>{});
  return row_reduce<true>(mx);
}
template <int R> DI void v_sub(float d) { asm volatile("v_sub_f32 v%c0, v%c0, %1" ::"i"(R), "v"(d)); }
template <int B> DI void s_shift(const float (&d)[2]) { static_for([&](auto E) { v_sub<B + E>(d[(E >> 2) & 1]); }, make_iseq<32>{}); }
DI void m_shift(const float (&d)[2]) {
  s_shift<R_CI>(d);
  v_sub<R_M>(-d[0]); v_sub<R_M + 1>(-d[1]);
}
#define A_MASK(R0, R1, R2, R3, K0, K1, K2, K3) \
    "v_cmp_gt_i32_e64 s[64:65], " K0 ", v95\n\tv_cmp_gt_i32_e64 s[66:67], " K1 ", v95\n\tv_cmp_gt_i32_e64 s[68:69], " K2 ", v95\n\tv_cmp_gt_i32_e64 s[70:71], " K3 ", v95\n\t" \
    "v_cndmask_b32_e64 " R0 ", -v116, v127, s[64:65]\n\tv_cndmask_b32_e64 " R1 ", -v116, v127, s[66:67]\n\t" \
    "v_cndmask_b32_e64 " R2 ", -v116, v127, s[68:69]\n\tv_cndmask_b32_e64 " R3 ", -v116, v127, s[70:71]\n\t"
template <int K, int T> DI void mask_cmp() {
  asm volatile("v_cmp_gt_i32_e64 s[64:65], %c0, v%c4\n\tv_cmp_gt_i32_e64 s[66:67], %c1, v%c4\n\tv_cmp_gt_i32_e64 s[68:69], %c2, v%c4\n\tv_cmp_gt_i32_e64 s[70:71], %c3, v%c4"
               ::"i"(K), "i"(K + 1), "i"(K + 2), "i"(K + 3), "i"(T));
}
template <int R, int S> DI void mask_sel() {
  if constexpr (S < 0)
    asm volatile("v_cndmask_b32_e64 v%c0, v%c0, v127, s[64:65]\n\tv_cndmask_b32_e64 v%c1, v%c1, v127, s[66:67]\n\t"
                 "v_cndmask_b32_e64 v%c2, v%c2, v127, s[68:69]\n\tv_cndmask_b32_e64 v%c3, v%c3, v127, s[70:71]" ::"i"(R), "i"(R + 1), "i"(R + 2), "i"(R + 3));
  else
    asm volatile("v_cndmask_b32_e64 v%c0, -v%c4, v127, s[64:65]\n\tv_cndmask_b32_e64 v%c1, -v%c4, v127, s[66:67]\n\t"
                 "v_cndmask_b32_e64 v%c2, -v%c4, v127, s[68:69]\n\tv_cndmask_b32_e64 v%c3, -v%c4, v127, s[70:71]" ::"i"(R), "i"(R + 1), "i"(R + 2), "i"(R + 3), "i"(S));
}
template <int X, int Q0 = 0> DI void sm_redo() {
  constexpr int Xp = X ^ 1;
  float d[2] = {0.f, 0.f}, dt[2] = {0.f, 0.f}, alpha[2] = {1.f, 1.f}, ts[2] = {0.f, 0.f};
  asm volatile("s_nop 7");
  static_for([&](auto Q) {
    constexpr int qb = Q + Q0;
    const float pd = vget<R_PD + qb>();
    d[qb] = fmaxf(row_max<Xp, qb>() - pd, 0.f);
    dt[qb] = d[qb] + pd;
    vset<R_PD + qb>(d[qb]);
    alpha[qb] = __builtin_amdgcn_exp2f(-d[qb]);
    vset<R_RS + qb>(vget<R_RS + qb>() * alpha[qb]);
  }, make_iseq<2 - Q0>{});
  vset<R_THR>(d[0] > 0.f || d[1] > 0.f ? -1.f : 4096.f);
  m_shift(d);
  static_for([&](auto K) {
    constexpr int k = K, qb = (k >> 1) & 1;
    if constexpr (qb >= Q0) {
      const float e0 = __builtin_amdgcn_exp2f(vget<R_S + 32 * Xp + 2 * k>() - dt[qb]), e1 = __builtin_amdgcn_exp2f(vget<R_S + 32 * Xp + 2 * k + 1>() - dt[qb]);
      ts[qb] += e0; ts[qb] += e1;
      vset<p_reg(Xp, k)>(__uint_as_float(pk2(e0, e1)));
    }
  }, make_iseq<16>{});
  vset<R_TS>(ts[0]); vset<R_TS + 1>(ts[1]);
  rescale_all<Q0>(alpha, make_iseq<128>{});
  asm volatile("s_nop 1" ::: "memory");
  GAP();
}
template <int X, int FIRST, bool NOQK, int... Gs> DI void phaseA_(AC& c, iseq<Gs...>) {
  (gapA<X, FIRST, NOQK, Gs>(c), ...);
}
template <int X, int FIRST, bool NOQK> DI void phaseA(AC& c) { phaseA_<X, FIRST, NOQK>(c, make_iseq<64>{}); }

template <int Y, int F, int QB> DI void pv_mfma() {
  constexpr int r = 192 + 4 * (F & 15), o = 4 * ((F >> 1) * 2 + QB), p = R_P + 16 * Y + 4 * ((F & 1) * 2 + QB);
  asm volatile("v_mfma_f32_16x16x32_bf16 a[%c0:%c1], a[%c2:%c3], v[%c4:%c5], a[%c0:%c1]" ::"i"(o), "i"(o + 3), "i"(r), "i"(r + 3), "i"(p), "i"(p + 3));
}
template <int X, bool SMX, int G> DI void gapB(AC& c) {
  constexpr int Xp = X ^ 1, blk = G >> 4, g = G & 15;
  if constexpr (g == 0) asm volatile("s_waitcnt lgkmcnt(0)");
  if constexpr (SMX && g == 7 && blk < 3) dma_m0<X, blk * 4 + 4>();
  if constexpr (SNK & 2) pv_mfma<Xp, (G >> 2) * 2 + ((g ^ (g >> 1)) & 1), ((g >> 1) ^ (g >> 2)) & 1>();
  else pv_mfma<Xp, (G >> 2) * 2 + (g & 1), (g >> 1) & 1>();
  if constexpr (SMX && (G == 1 || G == 2)) asm volatile("v_add_f32_e32 v%c0, v%c0, v%c1" ::"i"(R_RS + G - 1), "i"(R_TS + G - 1));
  if constexpr (g < 8) {
    if constexpr (blk < 3) vread<Xp, (blk + 1) * 8 + g>();
    else if constexpr (SMX) kread<Xp, g>();
  }
  if constexpr (SMX) {
    if constexpr (g >= 8 && !(g & 1) && blk < 3) dma_at<blk * 4 + 4 + (g - 8) / 2>();
    sm_gap<X, G>(c.redo);
    GAP();
  }
}
template <int X, int... Gs> DI void phaseB_(AC& c, iseq<Gs...>) { (gapB<X, true, Gs>(c), ...); }
template <int X> DI void phaseB(AC& c) { phaseB_<X>(c, make_iseq<64>{}); }
constexpr int sm_nex() { int n = 0; for (int t = 0; t < 64; ++t) n += SM.ex[t] >= 0; return n; }
template <bool ST> DI void phaseB0(bool diag0, unsigned vml) {
  float d[2];
  dma_m0<0, 4>();
  if constexpr (ST) {
    asm volatile("s_waitcnt vmcnt(4)" ::: "memory");
    dma_at<4>(); d_store<8>();
    d[0] = vget<122>(); d[1] = vget<123>();
    dma_at<5>();
  } else {
    kreads<1>(make_iseq<8>{});
    dma_at<4>();
    asm volatile("s_nop 3");
    if (__builtin_expect(diag0, 0))
      static_for([&](auto I) { constexpr int i = I; mask_cmp<16 * i, 95>(); mask_sel<R_S + 8 * i, -1>(); }, make_iseq<4>{});
    d[0] = row_max<0, 0>(); GAP(); dma_at<5>();
    d[1] = row_max<0, 1>(); GAP(); dma_at<6>();
#pragma unroll
    for (int qb = 0; qb < 2; ++qb) if (!(d[qb] > -3.0e38f)) d[qb] = 0.f;
    asm volatile("buffer_store_dwordx2 %0, %1, " SW ", s73 offen offset:1024 sc1" ::"v"((f32x2c){d[0], d[1]}), "v"(vml) : "memory");
  }
  vset<R_M>(d[0]); vset<R_M + 1>(d[1]);
  const f32x2c nd[2] = {{-d[0], -d[0]}, {-d[1], -d[1]}};
  static_for([&](auto E) {
    constexpr int e = E;
    const f32x2c v = nd[(e >> 1) & 1];
    asm volatile("v_pk_add_f32 v[%c0:%c1], v[%c0:%c1], %4\n\tv_mov_b64 v[%c2:%c3], %4" ::"i"(R_S + 2 * e), "i"(R_S + 2 * e + 1), "i"(R_CI + 2 * e), "i"(R_CI + 2 * e + 1), "v"(v));
    if constexpr (ST && e == 0) d_store<9>();
    if constexpr (ST && e == 2) dma_at<6>();
    if constexpr (e == (ST ? 5 : 3)) dma_at<7>();
    if constexpr (e == 6) dma_m0<0, 8>();
    if constexpr (e == 7 || e == 11 || e == 15) dma_at<8 + (e - 7) / 4>();
    if constexpr (ST && (e == 4 || e == 9)) d_store<10 + e / 8>();
  }, make_iseq<16>{});
  static_assert(sm_nex() >= 16 && SM.op0[64] >= 3 * ((sm_nex() - 15) / 2) && SM.op0[64] <= 24);
  static_for([&](auto E) {
    constexpr int e = E;
    asm volatile("v_exp_f32 v%c0, v%c1" ::"i"(R_E + e), "i"(R_S + e));
    if constexpr (e % 3 == 0 && e < 15) acc_zero<e / 3>();
    if constexpr (e == 3) dma_at<11>();
    if constexpr (e == 6) dma_m0<0, 12>();
    if constexpr (e == 7 || e == 11 || e == 15) dma_at<12 + (e - 7) / 4>();
    if constexpr (ST && (e == 1 || e == 9)) d_store<12 + e / 8>();
  }, make_iseq<16>{});
  if constexpr (ST) {
    d_store<14>();
    asm volatile("s_barrier" ::: "memory");
    kreads<1>(make_iseq<8>{});
  }
  static_for([&](auto O) { sm_op<0, O>(); if constexpr (O % 7 == 1 && O < (ST ? 14 : 21)) acc_zero<5 + O / 7>(); }, make_iseq<SM.op0[64]>{});
  dma_at<15>();
  if constexpr (ST) { d_store<15>(); acc_zero<7>(); }
  static_for([&](auto E) { asm volatile("v_exp_f32 v%c0, v%c1" ::"i"(R_E + (int)E), "i"(R_S + 16 + (int)E)); }, make_iseq<sm_nex() - 16>{});
  GAP();
}
struct EP { float fa[2], fb[2]; };
template <int I, int E, int MERGE, int TB = 96> DI void epi_pair(const EP& p) {
  constexpr int qb = I & 1, r = 16 * (I >> 1) + 4 * qb + (E >> 1) * 8 + (E & 1) * 2, t = TB + 2 * E, w = 128 + 4 * I + E;
  if constexpr (MERGE == 2) asm volatile("v_accvgpr_read_b32 v%c0, a[%c2]\n\tv_accvgpr_read_b32 v%c1, a[%c3]" ::"i"(t), "i"(t + 1), "i"(r), "i"(r + 1));
  else
    asm volatile("v_accvgpr_read_b32 v%c0, a[%c2]\n\tv_accvgpr_read_b32 v%c1, a[%c3]\n\tv_mul_f32 v%c0, v%c0, %4\n\tv_mul_f32 v%c1, v%c1, %4"
                 ::"i"(t), "i"(t + 1), "i"(r), "i"(r + 1), "v"(p.fa[qb]));
  if constexpr (MERGE == 1)
    asm volatile("v_lshlrev_b32 v104, 16, v%c2\n\tv_and_b32 v105, 0xffff0000, v%c2\n\tv_fmac_f32 v%c0, v104, %3\n\tv_fmac_f32 v%c1, v105, %3"
                 ::"i"(t), "i"(t + 1), "i"(w), "v"(p.fb[qb]));
  asm volatile("v_cvt_pk_bf16_f32 v%c0, v%c1, v%c2" ::"i"(w), "i"(t), "i"(t + 1));
}
template <int I> DI void ctx_store() {
  asm volatile("buffer_store_dwordx4 v[%c0:%c1], v85, " SW ", s76 offen offset:%c2 sc1\n\tds_write_b128 v85, v[%c0:%c1] offset:%c3"
               ::"i"(128 + 4 * I), "i"(131 + 4 * I), "i"((I & 2) * 1024), "i"((I & 1) * 32768 + (I >> 1) * 2048) : "memory");
  if constexpr (I & 2) asm volatile("s_add_u32 s76, s76, %0" ::"i"(I == 14 ? 0x5000 : 0x1000) SCC1);
}
constexpr int mg_reg(int L, int I) { return I >= 8 ? 8 + 4 * I : I >= 4 ? R_CI + 8 * (I - 4) : R_S + 32 * L + 8 * I; }
#define STF(k)
template <int L, bool MG, int G> DI void gapAD(AC& c) {
  constexpr int kb = G >> 3, s = G & 7, db = ((G >> 1) + 8) & 15, o = 4 * (db * 2 + (G & 1)), a = 72 + 2 * (db & 1), b = mg_reg(L, (db >> 1) * 2 + (G & 1));
  if constexpr (G == 0) STF(0);
  if constexpr (G == 16) STF(1);
  if constexpr (G == 28) STF(2);
  if constexpr (G == 29) STF(3);
  if constexpr (G == 0) asm volatile("s_waitcnt lgkmcnt(0)");
  else if constexpr (G >= 8 && G < 28 && !(G & 1)) asm volatile("s_waitcnt lgkmcnt(%0)" ::"i"(G <= 24 ? 6 : 4));
  else if constexpr (G == 28) asm volatile("s_waitcnt vmcnt(16) lgkmcnt(0)\n\ts_barrier" ::: "memory");
  qk_mfma<L, kb, 1, s, false>();
  if constexpr (MG && G < 8) asm volatile("ds_read_b128 v[%c0:%c1], v85 offset:%c2" ::"i"(mg_reg(L, G)), "i"(mg_reg(L, G) + 3), "i"(G * 1024));
  if constexpr (G < 24) kread<L, 8 + G>();
  if constexpr (G < 24 && s == 1) mask_cmp<16 * (kb + 1), 126>();
  if constexpr (G < 24 && s == 3) mask_sel<R_CI + 8 * (kb + 1) + 4, R_M + 1>();
  if constexpr (G >= 4 && G < 28 && G % 3 == 2) w_load<(G - 4) / 3>();
  if constexpr (G == 25) asm volatile("s_add_u32 s82, s83, 0x1000000" SCC3);
  if constexpr (G == 27) dma_m0<L, 0>();
  if constexpr (G >= 28) { vread<L ^ 1, 2 * (G - 28)>(); vread<L ^ 1, 2 * (G - 28) + 1>(); dma_at<G - 28>(); }
  sm_gap<L ^ 1, 64 + 2 * G>(c.redo);
  if constexpr (MG) asm volatile("v_mfma_f32_16x16x32_bf16 a[%c0:%c1], v[%c2:%c3], v[%c4:%c5], a[%c0:%c1]" ::"i"(o), "i"(o + 3), "i"(a), "i"(a + 3), "i"(b), "i"(b + 3));
  sm_gap<L ^ 1, 65 + 2 * G>(c.redo);
  GAP();
}
template <int L, bool MG, int... Gs> DI void phaseAD(AC& c, iseq<Gs...>) {
  sld<72, B_NXT, 1>();
  mask_cmp<0, 126>(); mask_sel<R_CI + 4, R_M + 1>();
  (gapAD<L, MG, Gs>(c), ...);
}
#define BDX 8
constexpr int bd_w(bool EPI, int G) { return EPI && G >= 19 && G % 4 == 3 && (G - 19) / 4 < BDX ? 16 + (G - 19) / 4 : -1; }
template <int L, bool EPI, int G> DI void gapBD(AC& c, const EP& p) {
  constexpr int g = G & 15, e = (G >> 4) * 8 + (g - 8), o = e - 3;
  if constexpr (G == 0) STF(5);
  if constexpr (G == 8) STF(6);
  if constexpr (G == 9) STF(7);
  if constexpr (G == 16) STF(8);
  if constexpr (G == 17) STF(9);
  if constexpr (G == 32) STF(10);
  if constexpr (G == 48) STF(11);
  if constexpr (G == 1) dma_m0<L, 4>();
  if constexpr (G == 8) asm volatile("s_waitcnt vmcnt(14)" ::: "memory");
  if constexpr (G == 16) asm volatile("s_waitcnt lgkmcnt(0)\n\ts_barrier" ::: "memory");
  gapB<L, false, G>(c);
  if constexpr (G >= 8 && G < 12)
    asm volatile("ds_read_b128 a[%c0:%c1], v90 offset:%c4\n\tds_read_b128 a[%c2:%c3], v90 offset:%c5" ::"i"(96 + 8 * G), "i"(99 + 8 * G), "i"(100 + 8 * G), "i"(103 + 8 * G),
                 "i"((L ^ 1) * 32768 + (G - 8) * 2048), "i"((L ^ 1) * 32768 + (G - 8) * 2048 + 1024));
  if constexpr (G == 1 || G == 2) asm volatile("v_add_f32_e32 v%c0, v%c0, v%c1" ::"i"(R_RS + G - 1), "i"(R_TS + G - 1));
  if constexpr (G >= 3 && G < 19 && G % 4 == 3) dma_at<4 + (G - 3) / 4>();
  if constexpr (!EPI && G == 17)
    asm volatile("s_cmp_eq_u32 s97, 0\n\ts_cbranch_scc1 1f\n\ts_add_u32 s64, s100, 4 * 800\n\ts_addc_u32 s65, s101, 0\n\ts_swappc_b64 s[98:99], s[64:65]\n1:" ::: SCC_ "memory");
  if constexpr (bd_w(EPI, G) >= 0) w_load<bd_w(EPI, G)>();
  if constexpr (g >= 8 && e < 16) asm volatile("v_exp_f32 v%c0, v%c1" ::"i"(R_E + e), "i"(R_S + 32 * L + 4 + 8 * (e >> 2) + (e & 3)));
  if constexpr (g >= 8 && o >= 1 && o < 16) {
    if constexpr (o == 1) asm volatile("v_add_f32_e32 v%c0, v%c1, v%c2" ::"i"(R_TS + 1), "i"(R_E), "i"(R_E + 1));
    else asm volatile("v_add_f32_e32 v%c0, v%c0, v%c1" ::"i"(R_TS + 1), "i"(R_E + o));
    if constexpr (o & 1) asm volatile("v_cvt_pk_bf16_f32 v%c0, v%c1, v%c2" ::"i"(p_reg(L, (o >> 2) * 4 + 2 + ((o >> 1) & 1))), "i"(R_E + o - 1), "i"(R_E + o));
  }
  if constexpr (G == 45) asm volatile("v_cmp_nle_f32_e64 %0, v%c1, v%c2" : "=s"(c.redo) : "i"(R_TS + 1), "i"(R_THR));
  if constexpr (EPI && G >= 10 && G < 58 && !(G & 1)) epi_pair<((G - 10) >> 3) * 2, ((G - 10) >> 1) & 3, 0, 224>(p);
  if constexpr (EPI && G >= 17 && G < 58 && (G & 7) == 1) ctx_store<((G - 17) >> 3) * 2>();
  GAP();
}
template <int L, bool EPI, int... Gs> DI void phaseBD(AC& c, const EP& p, iseq<Gs...>) {
  if constexpr (!EPI) { sld<96, B_LM, 1>(); sld<97, B_LM + 1, 1>(); asm volatile("s_nop 0"); fl_check(); }
  (gapBD<L, EPI, Gs>(c, p), ...);
}

#define ACLOB_ALL "a0","a1","a2","a3","a4","a5","a6","a7","a8","a9","a10","a11","a12","a13","a14","a15","a16","a17","a18","a19","a20","a21","a22","a23","a24","a25","a26","a27","a28","a29","a30","a31","a32","a33","a34","a35","a36","a37","a38","a39","a40","a41","a42","a43","a44","a45","a46","a47","a48","a49","a50","a51","a52","a53","a54","a55","a56","a57","a58","a59","a60","a61","a62","a63","a64","a65","a66","a67","a68","a69","a70","a71","a72","a73","a74","a75","a76","a77","a78","a79","a80","a81","a82","a83","a84","a85","a86","a87","a88","a89","a90","a91","a92","a93","a94","a95","a96","a97","a98","a99","a100","a101","a102","a103","a104","a105","a106","a107","a108","a109","a110","a111","a112","a113","a114","a115","a116","a117","a118","a119","a120","a121","a122","a123","a124","a125","a126","a127","a128","a129","a130","a131","a132","a133","a134","a135","a136","a137","a138","a139","a140","a141","a142","a143","a144","a145","a146","a147","a148","a149","a150","a151","a152","a153","a154","a155","a156","a157","a158","a159","a160","a161","a162","a163","a164","a165","a166","a167","a168","a169","a170","a171","a172","a173","a174","a175","a176","a177","a178","a179","a180","a181","a182","a183","a184","a185","a186","a187","a188","a189","a190","a191","a192","a193","a194","a195","a196","a197","a198","a199","a200","a201","a202","a203","a204","a205","a206","a207","a208","a209","a210","a211","a212","a213","a214","a215","a216","a217","a218","a219","a220","a221","a222","a223","a224","a225","a226","a227","a228","a229","a230","a231","a232","a233","a234","a235","a236","a237","a238","a239","a240","a241","a242","a243","a244","a245","a246","a247","a248","a249","a250","a251","a252","a253","a254","a255"

template <int I> DI void epi_store() {
  asm volatile("buffer_store_dwordx4 v[%c0:%c1], v90, " SW ", s%c2 offen offset:%c3 sc1" ::"i"(128 + 4 * I), "i"(131 + 4 * I), "i"(76 + (I >> 2)), "i"((I & 3) * 1024) : "memory");
}
template <int HI> DI void epi_init() {
  asm volatile("v_readlane_b32 s76, v93, %c0\n\ts_nop 0\n\ts_add_u32 s77, s76, 0x1000\n\ts_add_u32 s78, s76, %1\n\ts_add_u32 s79, s78, 0x1000" ::"i"(B_SO), "i"(HI) SCC1);
}
template <int I> DI void epi_pread() {
  asm volatile("ds_read_b128 v[%c0:%c1], v107 offset:%c2" ::"i"(128 + 4 * I), "i"(131 + 4 * I), "i"(I * 1024));
}
template <int... Is> DI void epi_preads(iseq<Is...>) { (epi_pread<Is>(), ...); }
template <int G> DI void gapF(AC& c, const EP& p) {
  if constexpr (G < 64) gapB<ODD, false, G>(c);
  if constexpr (G == 1) asm volatile("v_mov_b32 v93, v94\n\ts_brev_b32 s73, 1");
  if constexpr (G >= 3 && G < 10) att_item<G - 3>();
  if constexpr (G >= 10) epi_pair<(G - 10) / 4, (G - 10) & 3, 2>(p);
  if constexpr (G >= 14 && ((G - 14) & 3) == 0) epi_store<(G - 14) / 4>();
  if constexpr (G == 73) epi_store<15>();
  GAP();
}
template <int... Gs> DI void phaseF(AC& c, const EP& p, iseq<Gs...>) { (gapF<Gs>(c, p), ...); }
constexpr int fd_w(bool EPI, int G) {
  const int f0 = EPI ? 16 + BDX : 16, n = NWF - f0;
  for (int i = 0; i < n; ++i) if (G == (EPI ? 2 + (i * 37) / n : i < 16 ? 2 + 2 * i : 4 + 2 * i)) return f0 + i;
  return -1;
}
#define UB 0
#define FDA 50
#define CXT 0
template <int H> DI void ctx_touch() {
  asm volatile("v_readlane_b32 s63, v93, %c0\n\tv_mad_u32_u24 v110, v89, 7, v90\n\ts_nop 3\n\tbuffer_load_dword v111, v110, " SW ", s63 offen offset:0 sc1"
               ::"i"(B_SO) : "memory");
}
template <> DI void ctx_touch<1>() {
  asm volatile("v_readlane_b32 s63, v93, %c0\n\tv_mad_u32_u24 v110, v89, 7, v90\n\tv_add_u32 v110, 0x8000, v110\n\ts_nop 2\n\tbuffer_load_dword v111, v110, " SW ", s63 offen sc1"
               ::"i"(B_SO) : "memory");
}
#define FDE 20
constexpr int fd_cnt(bool EPI, int G0, int G1) { int n = 0; for (int G = G0; G < G1; ++G) n += (fd_w(EPI, G) >= 0) + ((G & 3) == 3); return n; }
constexpr bool fd_ok() {
  for (int G = 0; G < 64; ++G) {
    const int a = fd_w(0, G), b = fd_w(1, G);
    if ((a >= 16 && G < 2 * (a & 15) + (a >= 32 ? 34 : 2)) || (b >= 28 && G < (b >= 32 ? 2 * (b - 32) + 10 : 2 * (b - 28) + 2))) return false;
  }
  return true;
}
static_assert(fd_ok() && NWF == 40 && BDX <= 12);
template <int L, bool EPI, int G> DI void gapFD(const EP& p) {
  constexpr int I = EPI ? (G < 8 ? 12 + (G >> 2) * 2 : ((G - 8) >> 2) * 2 + 1) : G < 32 ? (G >> 2) * 2 : ((G - 32) >> 2) * 2 + 1;
  constexpr int u = EPI ? G : G - 2, uf = SNK & 2 ? u ^ ((u >> 1) & 1) : u;
  if constexpr (G == 0) STF(14);
  if constexpr (G == 8) STF(15);
  if constexpr (G == 16) STF(16);
  if constexpr (G == 21) STF(18);
  if constexpr (G == 32) STF(19);
  if constexpr (u >= 16 && u < 32 && (u & 3) == 0) asm volatile("s_waitcnt lgkmcnt(%0)" ::"i"(28 - u));
  if constexpr (u >= 0 && u < 32) pv_mfma<L, uf, 1>();
  if constexpr (u >= 0 && u < 16) vread<L, uf + 16>();
  constexpr int F = fd_w(EPI, G);
  if constexpr (F >= 0) w_load<F>();
  epi_pair<I, G & 3, !EPI>(p);
  if constexpr ((G & 3) == 3) ctx_store<I>();
  if constexpr (G == (EPI ? FDE : FDA)) {
    STF(17);
    if constexpr (!(UB & 2)) asm volatile("s_waitcnt vmcnt(%0)\n\ts_barrier" ::"i"(fd_cnt(EPI, EPI ? 8 : 32, G + 1)) : "memory");
    set_flag<A_FC>();
    if constexpr (CXT & 1) ctx_touch<0>();
  }
  GAP();
}
template <int L, bool EPI, int... Gs> DI void phaseFD(const EP& p, iseq<Gs...>) { (gapFD<L, EPI, Gs>(p), ...); }
#define A_CT2 "v_readlane_b32 s63, v93, 3\n\ts_sub_u32 s64, s100, at_event - at_tail\n\ts_subb_u32 s65, s101, 0\n\ts_mov_b32 s66, at_event - at_tail\n\ts_mov_b32 s67, s59\n\t" \
    "v_lshl_add_u32 v78, v89, 9, s63\n\ts_mov_b64 exec, 0x1f\n\tbuffer_load_dword v84, v78, s[64:67], 0 offen\n\ts_mov_b64 exec, -1\n\t"
#define A_CT4 "s_mov_b64 s[64:65], s[100:101]\n\ts_movk_i32 s66, 0xf00\n\ts_mov_b32 s67, s59\n\tv_lshlrev_b32 v78, 3, v89\n\tbuffer_load_dword v84, v78, s[64:67], 0 offen\n\t"
#define A_RET "s_setpc_b64 s[98:99]\n\t"
#define A_EFLAG(L) "v_readlane_b32 s63, v93, " #L "\n\ts_nop 3\n\t" A_FLAG
#define A_EPOLL "v_readlane_b32 s96, v93, s85\n\ts_add_u32 s85, s85, 1\n\tv_readlane_b32 s97, v93, s85\n\ts_add_u32 s85, s85, 1\n\t" A_POLL("v[122:123]", "s[96:97]", "s62")
#define A_Q1 "buffer_load_dwordx4 v87, s[64:67], s95 offen lds\n\ts_add_u32 m0, m0, 0x400\n\ts_add_u32 s95, s95, 0x1c00\n\t"
#define A_EPOLL1 "s_mov_b32 exec_lo, 0xffff0000\n\ts_mov_b32 exec_hi, 0xffff\n\tbuffer_load_dwordx2 v[124:125], v88, " SW ", s92 offen sc1\n\ts_mov_b64 exec, -1\n\t" A_CT2
#define A_Z1(K, O) "v_xor_b32 v49, " K ", v48\n\tbuffer_load_dwordx4 v49, " SW ", s63 offen offset:" O " sc1 lds\n\t"
#define A_EQ "v_readlane_b32 s68, v92, 30\n\tv_readlane_b32 s95, v92, 28\n\ts_mov_b64 s[64:65], s[88:89]\n\ts_mov_b32 s66, 0x1c0000\n\ts_mov_b32 s67, s59\n\t" \
    "s_add_u32 m0, s68, 0x1000\n\ts_nop 1\n\t" A_Q1 A_Q1 A_Q1 A_Q1
#define A_ELOAD "v_readlane_b32 s68, v92, 30\n\tv_readlane_b32 s63, v92, 29\n\tv_lshrrev_b32 v48, 5, v89\n\ts_mov_b32 s96, 0\n\ts_mov_b32 s97, 0x3fff0000\n\t" \
    "v_and_b32 v48, 16, v48\n\t" A_CHECK("v[124:125]", "s[96:97]", "s92") "v_xor_b32 v48, v48, v89\n\ts_mov_b32 m0, s68\n\t" \
    A_Z1("0", "0") A_Z1("32", "1024") A_Z1("64", "2048") "s_mov_b32 exec_hi, 0\n\t" A_Z1("0x60", "3072") "s_mov_b32 exec_hi, -1\n\t"
#define A_PZ2(Z0, Z1, O) "ds_read_u16_d16_hi " Z0 ", v46 offset:" O "\n\tds_read_u16_d16_hi " Z1 ", v47 offset:" O "\n\t"
#define A_PRD(X, Q, O, Z0, Z1, Z2, Z3, Z4, Z5, Z6, Z7) "v_xor_b32 v46, " X ", v44\n\tds_read_b128 " Q ", v45 offset:" O "\n\tv_xor_b32 v47, 64, v46\n\t" \
    A_PZ2(Z0, Z1, "0") A_PZ2(Z2, Z3, "128") A_PZ2(Z4, Z5, "256") A_PZ2(Z6, Z7, "384")
#define A_PM2(Q, Z0, Z1) "v_lshlrev_b32 v40, 16, " Q "\n\tv_and_b32 v41, 0xffff0000, " Q "\n\tv_mul_f32 v40, v40, " Z0 "\n\tv_mul_f32 v41, v41, " Z1 "\n\tv_cvt_pk_bf16_f32 " Q ", v40, v41\n\t"
#define A_PST(Q) "buffer_store_dwordx4 " Q ", v43, s[64:67], s63 offen\n\ts_add_u32 s63, s63, 0x3800\n\t"
#define A_PA "v64", "v65", "v66", "v67", "v68", "v69", "v70", "v71"
#define A_PB "v72", "v73", "v74", "v75", "v76", "v77", "v78", "v79"
#define A_PRD_(...) A_PRD(__VA_ARGS__)
#define A_EPOST "v_readlane_b32 s68, v92, 30\n\tv_lshrrev_b32 v40, 4, v89\n\tv_readlane_b32 s64, v92, 0\n\tv_mul_u32_u24 v41, 37, v40\n\tv_readlane_b32 s65, v92, 1\n\t" \
    "v_lshrrev_b32 v41, 8, v41\n\tv_readlane_b32 s63, v94, 23\n\tv_mad_i32_i24 v42, v41, -7, v40\n\ts_movk_i32 s66, 0x380\n\ts_movk_i32 s67, 0x210\n\t" \
    "v_mad_u32_u24 v43, v41, s66, v87\n\tv_lshl_add_u32 v44, v41, 1, s68\n\tv_add_u32 v45, s68, v89\n\tv_mad_u32_u24 v44, v42, s67, v44\n\t" \
    "v_mov_b64 v[64:65], 0\n\tv_mov_b64 v[66:67], 0\n\tv_mov_b64 v[68:69], 0\n\tv_mov_b64 v[70:71], 0\n\t" \
    "v_mov_b64 v[72:73], 0\n\tv_mov_b64 v[74:75], 0\n\tv_mov_b64 v[76:77], 0\n\tv_mov_b64 v[78:79], 0\n\ts_mov_b32 s66, 0x380000\n\ts_mov_b32 s67, s59\n\t" \
    A_PRD_("0", "v[48:51]", "4096", A_PA) A_PRD_("16", "v[52:55]", "5120", A_PB) "s_waitcnt lgkmcnt(9)\n\t" \
    A_PM2("v48", "v64", "v65") A_PM2("v49", "v66", "v67") A_PM2("v50", "v68", "v69") A_PM2("v51", "v70", "v71") A_PST("v[48:51]") \
    A_PRD_("32", "v[56:59]", "6144", A_PA) "s_waitcnt lgkmcnt(9)\n\t" \
    A_PM2("v52", "v72", "v73") A_PM2("v53", "v74", "v75") A_PM2("v54", "v76", "v77") A_PM2("v55", "v78", "v79") A_PST("v[52:55]") \
    A_PRD_("48", "v[60:63]", "7168", A_PB) "s_waitcnt lgkmcnt(9)\n\t" \
    A_PM2("v56", "v64", "v65") A_PM2("v57", "v66", "v67") A_PM2("v58", "v68", "v69") A_PM2("v59", "v70", "v71") A_PST("v[56:59]") "s_waitcnt lgkmcnt(0)\n\t" \
    A_PM2("v60", "v72", "v73") A_PM2("v61", "v74", "v75") A_PM2("v62", "v76", "v77") A_PM2("v63", "v78", "v79") A_PST("v[60:63]")
#define A_TR(R0, R1, R2, R3, O0, O1) "ds_read_b64_tr_b16 " R0 ", v85 offset:" O0 "\n\tds_read_b64_tr_b16 " R1 ", v85 offset:" O1 "\n\t" \
    "ds_read_b64_tr_b16 " R2 ", v84 offset:" O0 "\n\tds_read_b64_tr_b16 " R3 ", v84 offset:" O1 "\n\t"
#define A_WS(R, S, O) "buffer_store_dwordx4 " R ", v89, " SW ", " S " offen offset:" O " sc1\n\t"
#define A_PL1(O) "buffer_load_dwordx4 v89, " SW ", s63 offen offset:" O " sc1 lds\n\t"
#define A_PL2(R, O) "buffer_load_dwordx4 " R ", v89, " SW ", s95 offen offset:" O " sc1\n\t"
#define A_EPLOAD "v_readlane_b32 s68, v92, 30\n\tv_readlane_b32 s63, v93, 21\n\ts_mov_b32 m0, s68\n\ts_add_u32 s95, s63, 0x8000\n\ts_nop 2\n\t" \
    A_PL1("0") A_PL1("1024") A_PL1("2048") A_PL1("3072") "s_bitset1_b32 m0, 12\n\ts_add_u32 s63, s63, 0x1000\n\t" A_PL1("0") A_PL1("1024") A_PL1("2048") A_PL1("3072") \
    A_PL2("v[40:43]", "0") A_PL2("v[44:47]", "1024") A_PL2("v[48:51]", "2048") A_PL2("v[52:55]", "3072") "s_add_u32 s95, s95, 0x1000\n\t" \
    A_PL2("v[56:59]", "0") A_PL2("v[60:63]", "1024") A_PL2("v[64:67]", "2048") A_PL2("v[68:71]", "3072") \
    "v_and_b32 v78, 0xf0, v89\n\tbuffer_load_dwordx4 v[72:75], v78, " SW ", s73 offen sc1\n\t"
#define A_EPOLLZ "s_mov_b32 exec_lo, 0\n\ts_mov_b32 exec_hi, 0x3fff0000\n\tbuffer_load_dwordx2 v[124:125], v88, " SW ", s92 offen sc1\n\ts_mov_b64 exec, -1\n\t"
#define WTOUCH 0
#define A_WT
#define A_ECHK1 "s_mov_b32 s70, 0xffff0000\n\ts_mov_b32 s71, 0xffff\n\t" A_CHECK("v[124:125]", "s[70:71]", "s92") A_WT
#define A_AT(N) ".fill (at_event + 4 * " #N " - .) / 4, 4, 0xbf800000\n\t"
#define A_EVENT \
    ".p2align 6\nat_event:\n\t" A_RET \
    A_AT(16) "v_cmp_eq_u64 vcc, s[60:61], v[122:123]\n\ts_and_b64 vcc, vcc, s[96:97]\n\ts_cmp_eq_u64 vcc, s[96:97]\n\ts_cbranch_scc0 7f\n\t" A_RET \
    A_AT(32) A_EFLAG(5) A_CT4 A_RET \
    A_AT(48) A_EFLAG(5) A_AT(56) A_EPOLL A_RET \
    A_AT(80) A_EFLAG(4) A_RET \
    A_AT(96) A_EFLAG(5) A_CT4 A_EPOLL A_RET \
    A_AT(128) "v_xor_b32 v84, 32, v85\n\tv_readlane_b32 s63, v92, 27\n\t" \
    A_TR("v[48:49]", "v[50:51]", "v[52:53]", "v[54:55]", "0", "4096") A_TR("v[56:57]", "v[58:59]", "v[60:61]", "v[62:63]", "8192", "12288") \
    A_TR("v[64:65]", "v[66:67]", "v[68:69]", "v[70:71]", "16384", "20480") A_TR("v[72:73]", "v[74:75]", "v[76:77]", "v[78:79]", "24576", "28672") \
    "s_add_u32 s95, s63, 0x1000\n\ts_waitcnt lgkmcnt(8)\n\t" \
    A_WS("v[48:51]", "s63", "0") A_WS("v[52:55]", "s63", "1024") A_WS("v[56:59]", "s63", "2048") A_WS("v[60:63]", "s63", "3072") "s_waitcnt lgkmcnt(0)\n\t" \
    A_WS("v[64:67]", "s95", "0") A_WS("v[68:71]", "s95", "1024") A_WS("v[72:75]", "s95", "2048") A_WS("v[76:79]", "s95", "3072") A_RET \
    A_AT(192) A_EPOLL1 A_RET \
    A_AT(228) A_EPOLL A_AT(240) A_EPOLL1 \
    A_MASK("v224", "v225", "v226", "v227", "0", "1", "2", "3") \
    A_MASK("v232", "v233", "v234", "v235", "16", "17", "18", "19") \
    A_MASK("v240", "v241", "v242", "v243", "32", "33", "34", "35") \
    A_MASK("v248", "v249", "v250", "v251", "48", "49", "50", "51") A_RET \
    "7:\n\t" A_SPIN("v[122:123]", "s[96:97]", "s62") A_RET \
    A_AT(344) A_EFLAG(4) A_EPOLLZ A_EQ A_RET \
    A_AT(400) A_ELOAD A_RET \
    A_AT(448) A_EFLAG(5) A_CT4 A_EPOLLZ A_EQ A_RET \
    A_AT(512) A_EPOST A_RET \
    A_AT(768) "5:\n\t" A_CHECK("v[122:123]", "s[96:97]", "s62") "s_branch 6f\n\t" A_AT(800) "6:\n\t" A_EPLOAD A_RET \
    A_AT(872) A_ECHK1 A_RET \
    A_AT(912) A_ECHK1 "s_branch 5b\n\t"
static_assert(B_WT == 24 && B_CT == 3 && B_FSET == 5 && B_FW == 4 && A_M0F == 30 && A_PQ == 28 && A_PZ == 29 && A_WDST == 27 && C_PO == 23 && B_LP == 21);
DI void event() {
  asm volatile("s_and_b32 s63, s87, 0x3ff\n\ts_lshl2_add_u32 s64, s63, s100\n\ts_addc_u32 s65, s101, 0\n\ts_swappc_b64 s[98:99], s[64:65]" ::: "vcc", SCC_ "memory",
               "v78", "v79");
  GAP();
}

#define OFU 20
static_assert(OFU >= 4 && OFU < 28 && OFU % 2 == 0);
#define OGB 14
constexpr int o_qo(int P) { return O_SEQ[P] & 3; }
constexpr int o_o(int P) { return O_SEQ[P] >> 4; }
constexpr int o_ks(int k) { return k % 7 < 4 ? 4 + k % 7 : k % 7 - 4; }
constexpr int o_sq(int P) {
  for (int q = P; q < 16; ++q) if (P < 1 || o_qo(q) == o_qo(P - 1)) return -1;
  return o_qo(P - 1);
}
constexpr int o_last() { int l = 0; for (int P = 0; P < 16; ++P) if (o_sq(P) >= 0) l = P; return l; }
constexpr bool o_first(int P) { for (int q = 0; q < P; ++q) if (o_qo(q) == o_qo(P)) return false; return true; }
template <int P, int MB, int KK> DI void o_read() {
  constexpr int r = 128 + 4 * (8 * MB + KK), sl = o_k(P) < 0 ? o_qo(P) : o_ks(o_k(P));
  asm volatile("ds_read_b128 v[%c0:%c1], v%c2 offset:%c3" ::"i"(r), "i"(r + 3), "i"(sl < 4 ? 96 + sl : 86), "i"((sl < 4 ? 0 : (sl & 3) * 16384) + (2 * KK + MB) * 1024));
}
template <int... Is> DI void o_read0(iseq<Is...>) { (o_read<0, (Is >> 3), Is & 7>(), ...); }
constexpr int o_gb(int NF) { return NF == 2 ? OGB : 0; }
#define OTAB {40, 44, 48, -1}, {52, -1, 0, 1}, {56, -1, 2, 60}, {-1, -1, 3, 4}, {-1, -1, 5, 6}, {-1, -1, 7, -1}, {-1, -1, 8, -1}, {-1, -1, 9, -1}, {-1, -1, 10, -1}, {-1, -1, 11, -1}
constexpr int O_TAB[][4] = {OTAB};
constexpr int o_op(int P, int U, int NF) {
  const int c = U / 8 == 1 && o_sq(P) >= 0 ? 99 : P < (int)(sizeof(O_TAB) / 16) ? O_TAB[P][U / 8] : -1, i = (U & 7) / 2;
  if (c < 0 || (U & 1)) return 0;
  if (c < 32) return 200 + 4 * c + i;
  if (c == 99) return i == 3 || (i == 1 && NF == 2) ? 300 + i : 0;
  return NF == 1 && (i & 1) ? 0 : 100 + c + i;
}
constexpr int o_vm(int P, int NF) {
  int n = 0;
  for (int i = 0; i <= P; ++i) {
    for (int U = 0; U < (i < P ? 32 : o_gb(NF)); ++U) {
      const int op = o_op(i, U, NF);
      if (op) ++n;
      if (op >= 200 && op < 300 && (op - 200) / 4 == o_k(P + 1)) n = 0;
      if (op >= 100 && op < 200 && (op - 100) / 16 == o_o(P + 1)) n = 0;
    }
  }
  return n;
}
constexpr bool o_ok() {
  for (int P = 1; P < 16; ++P) {
    for (int i = P - 1; i < 16; ++i) for (int U = 0; U < 32; ++U) {
      const int op = o_op(i, U, 2);
      if (i > P - 1 || U >= 0) {
        if (op >= 200 && op < 300 && (op - 200) / 4 == o_k(P)) return false;
        if (op >= 100 && op < 200 && (op - 100) / 16 == o_o(P)) return false;
      }
    }
  }
  for (int i = 0; i < 16; ++i) for (int U = 0; U < 32; ++U) {
    const int op = o_op(i, U, 2);
    if (op >= 200 && op < 300) {
      const int k = (op - 200) / 4, sl = o_ks(k);
      int prev = -1;
      for (int P = 0; P < 16; ++P) if ((o_k(P) < 0 ? o_qo(P) : o_ks(o_k(P))) == sl && (o_k(P) < k)) prev = P;
      if (prev >= 0 && !(i > prev + 1 || (i == prev + 1 && U >= 16))) return false;
    }
  }
  return true;
}
static_assert(NWF == 40 && o_ok());
template <int K> DI void o_m0() {
  constexpr int sl = o_ks(K);
  if constexpr (sl < 4) asm volatile("s_add_u32 m0, s80, s%c0" ::"i"(64 + sl) SCC1);
  else asm volatile("s_add_u32 m0, s80, %0" ::"i"(sl * 16384) SCC1);
}
template <int NF, int SR, int C> DI void o_pack() {
  if constexpr (NF == 2 || !(C & 2))
    asm volatile("v_cvt_pk_bf16_f32 v%c0, v%c1, v%c2" ::"i"(NF == 2 ? SR + C : SR + (C >> 2) * 2 + (C & 1)), "i"(SR + 2 * C), "i"(SR + 2 * C + 1));
}
template <int SR, int F> DI void o_sst() {
  asm volatile("buffer_store_dwordx4 v[%c0:%c1], v89, " SW ", s77 offen offset:%c2 sc1" ::"i"(SR + 4 * F), "i"(SR + 4 * F + 3), "i"(F * 1024) : "memory");
}
template <int I, int V> DI void o_slab() {
  asm volatile("buffer_load_dwordx4 v[%c0:%c1], v%c4, " SW ", s%c2 offen offset:%c3 sc1" ::"i"(192 + 4 * I), "i"(195 + 4 * I), "i"(64 + I / 2), "i"((I & 1) * 1024), "i"(V) : "memory");
}
template <int NF, int P, int G> DI void o_gap() {
  constexpr int NG = 16 * NF, ch = G >> 3, mb = ch / NF, nfi = (SNK & 16) && NF == 2 ? (ch ^ (ch >> 1)) & 1 : ch % NF, kk = (SNK & 16) && NF == 2 && (ch & 1) ? 7 - (G & 7) : G & 7, sq = o_sq(P), sr = 192 + 16 * sq;
  constexpr int w = o_wreg(o_o(P), 2 * kk + nfi), b = 128 + 4 * (8 * mb + kk), d = 192 + 16 * o_qo(P) + 4 * (2 * mb + nfi);
  constexpr bool nxt = P != 15;
  constexpr int U = NF == 2 ? G : 2 * G, op = o_op(P, U, NF), opn = U < 24 ? o_op(P, U + 8, NF) : nxt ? o_op(P + 1, U - 24, NF) : 0;
  if constexpr (G == 0) asm volatile("s_waitcnt lgkmcnt(8)" ::: "memory");
  if constexpr (G == NG / 2) asm volatile("s_waitcnt lgkmcnt(%0)" ::"i"(nxt && NF == 1 ? 8 : 0) : "memory");
  if constexpr (nxt && G == o_gb(NF) && P >= 1 && (o_k(nxt ? P + 1 : P) >= 0 || P < 3)) asm volatile("s_waitcnt vmcnt(%0)\n\ts_barrier" ::"i"(UB & 4 ? 63 : o_vm(P, NF)) : "memory");
  if constexpr (P == 1 && G == o_gb(NF))
  {
    asm volatile("v_readlane_b32 s63, v92, %c0\n\ts_nop 0\n\ts_add_u32 s63, s63, 32\n\t" A_FLAG ::"i"(A_FC) : SCC_ "memory");
    if constexpr (CXT & 2) ctx_touch<1>();
  }
  if constexpr (P == o_last() + 1 && U == OFU) {
    if constexpr (!(UB & 8)) asm volatile("s_waitcnt vmcnt(0)" ::: "memory");
    set_flag<A_FS>();
  }
  if constexpr (op >= 200 && op < 300 && (op & 3) == 0) o_m0<(op - 200) / 4>();
  if constexpr (o_first(P) && !(G & 7))
    asm volatile("v_mfma_f32_16x16x32_bf16 v[%c0:%c1], a[%c2:%c3], v[%c4:%c5], 0" ::"i"(d), "i"(d + 3), "i"(w), "i"(w + 3), "i"(b), "i"(b + 3));
  else
    asm volatile("v_mfma_f32_16x16x32_bf16 v[%c0:%c1], a[%c2:%c3], v[%c4:%c5], v[%c0:%c1]" ::"i"(d), "i"(d + 3), "i"(w), "i"(w + 3), "i"(b), "i"(b + 3));
  if constexpr (nxt && G >= NG - 16) o_read<P + 1, (G >> 3) & 1, kk>();
  if constexpr (op >= 100 && op < 200) w_load<op - 100, 1 + NF>();
  if constexpr (op >= 200 && op < 300)
    asm volatile("buffer_load_dwordx4 v89, " SW ", s%c0 offen offset:%c1 sc1 lds" ::"i"(73 + U / 8), "i"((op & 3) * 1024) : "memory");
  if constexpr (sq >= 0) {
    if constexpr (G == 0) sld<77, A_SS + sq>();
    if constexpr (NF == 2 && G >= 6 && G < 14) o_pack<2, sr, G - 6>();
    if constexpr (NF == 1 && (G == 4 || G == 5)) { o_pack<1, sr, 4 * (G - 4)>(); o_pack<1, sr, 4 * (G - 4) + 1>(); }
    if constexpr (op >= 300) o_sst<sr, NF == 2 ? (op - 300) / 2 : 0>();
  }
  if constexpr (opn >= 200 && opn < 300 && (opn & 3) == 0) sld<73 + ((U + 8) & 31) / 8, A_PC + (opn - 200) / 4>();
  if constexpr (P == 12 && G == NG - 4) { sld<62, A_FSW>(); fl_mask<A_FSM, -1>(); }
  if constexpr (P == 13 && G == 1) fl_issue();
  if constexpr (P == 13 && G >= 2 && G < 5) sld<62 + G, A_SL + G - 2>();
  if constexpr (P == 13 && G == NG - 2)
    asm volatile("v_cmp_eq_u64 vcc, s[60:61], v[122:123]\n\ts_and_b64 vcc, vcc, s[96:97]\n\ts_cmp_eq_u64 vcc, s[96:97]\n\ts_cselect_b32 s67, 0, 0x80000000\n\t"
                 "v_or_b32 v100, s67, v89" ::: "vcc", SCC_ "memory");
  if constexpr (P == 14 && G % NF == 0 && G >= 2 * NF && G < 14 * NF && !((G / NF) & 1) && (NF == 2 || !((G / NF) & 2) == false)) o_slab<G / NF / 2 - 1, 100>();
  GAP();
}
template <int NF, int P, int... Gs> DI void o_interval_(iseq<Gs...>) {
  (o_gap<NF, P, Gs>(), ...);
}
template <int NF, int P> DI void o_interval() { o_interval_<NF, P>(make_iseq<16 * NF>{}); }
template <int NF> DI void o_slabs() {
  unsigned late;
  asm volatile("s_mov_b32 %0, s67" : "=s"(late));
  if (late) {
    fl_wait();
    static_for([&](auto I) { if constexpr (NF == 2 || !(I & 1)) o_slab<I, 89>(); }, make_iseq<6>{});
  }
  GAP();
}
template <int NF> DI void o_sum() {
  static_for([&](auto I) {
    constexpr int i = I, q = i >> 2, f = i & 3, b = 192 + 8 * q + (NF == 2 ? 4 * (f >> 1) : 0), a = 72 + 2 * (NF == 2 ? f & 1 : f >> 1);
    if constexpr (f == 0) asm volatile("s_waitcnt vmcnt(%0)" ::"i"((2 - q) * NF) : "memory");
    if constexpr (NF == 2 || !(f & 1))
      asm volatile("v_mfma_f32_16x16x32_bf16 v[%c0:%c1], v[%c2:%c3], v[%c4:%c5], v[%c0:%c1]" ::"i"(240 + 4 * f), "i"(243 + 4 * f), "i"(a), "i"(a + 3), "i"(b), "i"(b + 3));
  }, make_iseq<12>{});
  asm volatile("s_nop 7");
}

#define NSG 64
#define KWAIT "s_waitcnt lgkmcnt(0)\n\t"
#define EARLY2 0
#define TOUCH0 0x20
#define TOUCH1 0x20
__global__ __launch_bounds__(256, 1) __attribute__((amdgpu_num_vgpr(82), amdgpu_num_sgpr(NSG)))
void fused_kernel(const bf16* a0, const bf16* a1, const bf16* a2, const bf16* a3, const bf16* a4, char* ws, const bf16* __restrict__ ak, const bf16* __restrict__ av,
                  const bf16* __restrict__ ash, const bf16* __restrict__ assmk, const bf16* __restrict__ assmD, const bf16* __restrict__ wo,
                  const bf16* __restrict__ aq, bf16* __restrict__ out) {
  int b = blockIdx.x;
  asm volatile("s_load_dwordx8 s[92:99], %1, 0\n\ts_load_dwordx2 s[60:61], %1, 32" : "+s"(b) : "s"(__builtin_amdgcn_kernarg_segment_ptr()));
  const bf16* ckv;
  const int bp = b >> 3;
  extern __shared__ __attribute__((aligned(16))) char lds[];
  asm volatile("at_code_begin:" ::: ACLOB_ALL, "v255", "s101");
  KST(0, 0);
  const int tid = threadIdx.x;
  int lane = tid & 63;
  const int wid = __builtin_amdgcn_readfirstlane(tid >> 6);
  const int j = b & 7, r = b >> 3, rr = r & 15, pj = rr >> 1, kv = pj & 1;
  const bool isY = r >= 16, doA = !isY && pj != NCV;
  const int cr = rr - (pj > NCV ? 2 : 0);
  const bf16* wkv;
  const int dq = Q44 ? (j & 1) - ((rr >> 1) & 1) : 0;
  unsigned vq, vw, tb0, vbA2, vD;
  const char* tp;
  {
    GM g;
    {
      static_assert(Q44 && K2(1) == 3);
#define CTP 0
#define CTM "0xff"
#define P_TM "s_memrealtime s[52:53]\n\ts_waitcnt lgkmcnt(0)\n\t"
#define P_T40
#define P_T41
#define P_T42
#define P_CT "s_getpc_b64 s[76:77]\n\tv_mbcnt_lo_u32_b32 v99, -1, 0\n\ts_lshr_b32 s88, %4, 3\n\ts_and_b32 s76, s76, 0xffffff80\n\ts_lshl_b32 s88, s88, 9\n\ts_and_b32 s77, s77, 0xffff\n\t" \
             "s_lshl1_add_u32 s88, s90, s88\n\ts_mov_b32 s78, pf_code_end - at_code_begin\n\ts_mov_b32 s79, 0x20000\n\tv_lshl_add_u32 v99, v99, 14, s88\n\ts_mov_b32 m0, %5\n\t" \
             "s_mov_b64 exec, " CTM "\n\tbuffer_load_dword v99, s[76:79], 0 offen lds\n\ts_mov_b64 exec, -1\n\t"
#define P_CTL(R0, R1, R2, R3, RR) "s_getpc_b64 s[" R0 ":" R1 "]\n\tv_mbcnt_lo_u32_b32 v100, -1, 0\n\tv_readfirstlane_b32 s89, %3\n\ts_lshr_b32 s88, %4, 3\n\ts_and_b32 s" R0 ", s" R0 ", 0xffffff80\n\t" \
             "s_lshl_b32 s88, s88, 9\n\ts_and_b32 s" R1 ", s" R1 ", 0xffff\n\ts_lshl1_add_u32 s88, s89, s88\n\ts_mov_b32 s" R2 ", pf_code_end - at_code_begin\n\ts_mov_b32 s" R3 ", 0x20000\n\t" \
             "v_lshl_add_u32 v100, v100, 14, s88\n\ts_mov_b32 m0, %5\n\ts_mov_b64 exec, 0xff\n\tbuffer_load_dword v100, " RR ", 0 offen lds\n\ts_mov_b64 exec, -1\n\t"
#define P_CT0 P_CT
#define P_CTX1
#define P_CTX2
#define P_LA(SO) "buffer_load_dwordx4 v86, s[64:67], " SO " offen" GSC " lds\n\t"
#define P_LW(SO) "buffer_load_dwordx4 v87, s[68:71], " SO " offen" GSC " lds\n\t"
      asm volatile("v_readfirstlane_b32 s90, %3\n\tv_lshrrev_b32 v96, 4, %3\n\t" P_CT0 "v_lshrrev_b32 v97, 3, %3\n\ts_and_b32 s63, %2, 1\n\tv_xor_b32 v98, v96, %3\n\ts_lshl_b32 s88, s63, 2\n\t"
                   "s_bfe_u32 s89, %2, 0x20005\n\tv_and_b32 v98, 7, v98\n\ts_movk_i32 s62, 0x800\n\ts_bitcmp1_b32 %2, 7\n\t"
                   "s_cselect_b32 s88, s88, 0\n\ts_cmovk_i32 s62, 0x380\n\ts_add_u32 s89, s89, s88\n\ts_lshl_b32 s89, s89, 8\n\tv_lshlrev_b32 v98, 4, v98\n\t"
                   "v_add_u32 v97, s89, v97\n\ts_lshl_b32 s90, s90, 4\n\ts_and_b32 s88, %2, 0x80\n\ts_lshl_b32 s88, s88, 8\n\tv_mad_u32_u24 v86, v97, s62, v98\n\t"
                   "s_or_b32 s90, s90, s88\n\ts_lshl_b32 s66, s62, 11\n\ts_mov_b32 s67, 0x20000\n\ts_mov_b32 m0, s90\n\ts_lshl_b32 s72, s62, 5\n\ts_lshl_b32 s75, s62, 7\n\t"
                   "s_add_u32 s76, s75, s72\n\t" P_T40 "s_bitcmp1_b32 %2, 7\n\t" KWAIT "s_cselect_b64 s[64:65], s[94:95], s[92:93]\n\t"
                   P_LA("0") P_T41
                   "s_add_u32 m0, s90, 0x1000\n\tv_and_b32 v99, 3, v96\n\t" P_LA("s72")
                   "s_add_u32 m0, s90, 0x4000\n\tv_lshrrev_b32 v97, 6, %3\n\t" P_LA("s75")
                   "s_add_u32 m0, s90, 0x5000\n\tv_lshl_or_b32 v99, v99, 2, v97\n\t" P_LA("s76")
                   P_T42 P_CTX1 "s_bfe_u32 s88, %2, 0x10004\n\tv_xor_b32 v99, v99, %3\n\ts_sub_u32 s63, s63, s88\n\tv_and_b32 v99, 15, v99\n\ts_and_b32 s88, %2, 15\n\t"
                   "v_lshlrev_b32 v99, 4, v99\n\ts_bitcmp1_b32 %2, 7\n\ts_cselect_b32 s63, s63, 0\n\tv_lshl_add_u32 v99, v96, 13, v99\n\ts_sub_u32 s88, s88, s63\n\t"
                   "s_lshl_b32 s88, s88, 9\n\ts_bitcmp1_b32 %2, 4\n\ts_cselect_b64 %1, s[98:99], s[96:97]\n\tv_add_u32 v87, s88, v99\n\ts_bitcmp1_b32 %2, 7\n\t"
                   "s_cselect_b64 s[68:69], s[60:61], %1\n\ts_lshl_b32 s70, s62, 12\n\ts_add_u32 m0, s90, 0x10000\n\ts_mov_b32 s71, 0x20000\n\t"
                   P_LW("0")
                   "s_add_u32 m0, s90, 0x11000\n\ts_mov_b32 s80, 0x20000\n\t" P_LW("s80")
                   "s_add_u32 m0, s90, 0x12000\n\ts_movk_i32 s81, 0x100\n\t" P_LW("s81")
                   "s_add_u32 m0, s90, 0x13000\n\ts_mov_b32 s82, 0x20100\n\t" P_LW("s82")
                   "s_add_u32 m0, s90, 0x14000\n\ts_mov_b32 s83, 0x40000\n\t" P_LW("s83")
                   "s_add_u32 m0, s90, 0x15000\n\ts_mov_b32 s84, 0x60000\n\t" P_LW("s84")
                   "s_add_u32 m0, s90, 0x16000\n\ts_mov_b32 s85, 0x40100\n\t" P_LW("s85")
                   "s_add_u32 m0, s90, 0x17000\n\ts_mov_b32 s86, 0x60100\n\t" P_LW("s86")
                   P_CTX2 "s_mov_b64 %0, s[92:93]\n\ts_lshl_b32 s73, s72, 1\n\ts_add_u32 s74, s73, s72\n\ts_add_u32 s77, s75, s73\n\ts_add_u32 s78, s75, s74\n\t"
                   "s_and_b32 s79, s90, 0xfff\n\ts_add_u32 s87, s79, 0x10000\n\tv_add_u32 v84, 0x80, v86\n\tv_add_u32 v85, 0x80000, v87\n\t"
                   "s_mov_b32 s58, 0x6400000\n\ts_mov_b32 s59, 0x20000\n\ts_xor_b32 s92, s90, 0x8000\n\t"
                   "s_bfe_u32 s88, %2, 0x40003\n\ts_bfe_u32 s89, %2, 0x20005\n\ts_add_u32 s88, s88, s89\n\ts_and_b32 s88, s88, 3\n\ts_bitcmp1_b32 %2, 7\n\t"
                   "s_cselect_b32 s88, 0, s88\n\ts_add_u32 s91, s88, 2"
                   : "=&s"(ckv), "=&s"(wkv) : "s"(b), "v"(tid), "s"(blockIdx.x), "i"(L_SP + 16384) : "scc", "memory");
      GAP();
      asm volatile("" : "+s"(ws), "+s"(ak), "+s"(av), "+s"(ash), "+s"(assmk), "+s"(assmD), "+s"(wo), "+s"(aq), "+s"(out));
    }
    auto addrs = [&]() __attribute__((always_inline)) {
      asm volatile("" : "+v"(lane));
      const int fr = lane & 15, fq = lane >> 4, q = (lane & 15) >> 2, p = lane & 3;
#pragma unroll
      for (int k2 = 0; k2 < 2; ++k2)
        g.aA[k2] = (wid >> 1) * 16384 + (fr >> 3) * 1024 + (fr & 7) * 128 + (((4 * k2 + fq) ^ (fr >> 1)) * 16);
#pragma unroll
      for (int hf = 0; hf < 2; ++hf) {
        const unsigned a0 = 65536 + (wid & 1) * 8192 + off_b(8 * fq + 4 * hf + q, p >> 1) + 8 * (p & 1);
#pragma unroll
        for (int nf = 0; nf < 8; ++nf) { g.aB[hf][nf] = a0 ^ (nf * 32); asm volatile("" : "+v"(g.aB[hf][nf])); }
      }
      GAP();
    };
    auto consts2 = [&]() __attribute__((always_inline)) {
      const int tl = (lane * 37) >> 8, ck = lane - tl * 7;
      vq = lane < 56 ? (tl * DA + ck * 8) * 2 : OOB;
      asm volatile("" : "+v"(vq));
    };
    auto consts = [&](auto Yc) __attribute__((always_inline)) {
      const int fr = lane & 15, fq = lane >> 4;
      if constexpr (Yc) {
        vbA2 = ((((rr >> 2) + 4) * 256 + wid * 8 + (lane >> 3)) * DC + ((lane & 7) ^ (((wid & 1) * 4 + fq) & 7)) * 8) * 2;
        asm volatile("" : "+v"(vbA2));
      } else {
        consts2();
        vw = (4 * wid + fq) * DA * 2 + 16 * (fr ^ ((fq << 2) | wid));
        tb0 = L_SP + off_b(4 * fq + (fr >> 2), wid * 4 + ((lane & 3) >> 1)) + 8 * (lane & 1);
#define SSMT 1
        tp = (SSMT && lane < 32 && doA ? (const char*)assmk + (j * 56 + cr * 4 + wid) * 4096 : (const char*)ckv + 0x200000 + ((b & 127) * 4 + wid) * 4096) + (lane & 31) * 128;
        asm volatile("" : "+v"(vw), "+v"(tb0), "+v"(tp));
      }
      GAP();
    };
    addrs();
    PA pa;
    const int pu = p_unit(j, rr);
    if (isY) {
      const int rp = tid >> 3, c = tid & 7, ch = tid >> 4, q = tid & 15;
      pa.vo = pu < 0 ? OOB : (((pu & 15) * 128 + rp * 4) * DA + (pu >> 4) * 64 + c * 8) * 2;
      pa.lw = L_SP + 20480 + wid * 1024 + c * 64;
      pa.lx = L_SP + c * 2048 + (((31 - rp) * 8) ^ (c << 4));
      pa.lr = L_SP + ch * 256 + 16 * (q ^ (ch >> 3));
      pa.vs = ch * (SEQ * 2) + q * 16;
      asm volatile("" : "+v"(pa.vo), "+v"(pa.lw), "+v"(pa.lx), "+v"(pa.lr), "+v"(pa.vs));
      consts(std::true_type{});
      asm volatile("" : "=v"(vq), "=v"(vw), "=v"(tb0), "=v"(tp));
    } else {
      consts(std::false_type{});
      asm volatile("" : "=v"(pa.vo), "=v"(pa.lw), "=v"(pa.lx), "=v"(pa.lr), "=v"(pa.vs), "=v"(vbA2));
    }
    {
      const int fr = lane & 15, fq = lane >> 4;
      vseti<88>(fq * 4352 + fr * 8); vseti<89>(lane * 16); vseti<90>(wid * 8192 + lane * 16); vseti<91>(wid * 8192 + lane * 16 + 4096);
      asm volatile("s_getpc_b64 s[100:101]\n\ts_add_u32 s100, s100, at_event@rel32@lo+4\n\ts_addc_u32 s101, s101, at_event@rel32@hi+12" ::: "scc");
      asm volatile("s_lshl_b32 s63, %0, 18\n\tv_add_u32 v115, s63, v85\n\ts_xor_b32 s63, s63, 0x40000\n\tv_add_u32 v116, s63, v85\n\t"
                   "s_mul_i32 s63, %0, s73\n\tv_add_u32 v117, s63, v84\n\ts_sub_u32 s63, s73, s63\n\tv_add_u32 v118, s63, v84\n\t"
                   "s_lshl_b32 s63, %0, 13\n\ts_add_u32 s98, s92, s63\n\ts_xor_b32 s99, s98, 0x2000\n\t"
                   "s_add_u32 s96, s98, s63\n\ts_bitset1_b32 s96, 16\n\ts_xor_b32 s97, s96, 0x4000" ::"s"((rr + (rr >> 2)) & 1) : "scc");
      g_pro<12>(make_iseq<4>{});
      g_part<0, 0>(); g_part<0, 1>(); g_part<0, 2>(); g_part<0, 3>();
      {
        const char* xp;
        asm volatile("s_mov_b64 %0, s[94:95]" : "=s"(xp));
        asm volatile("s_mov_b32 m0, %1\n\ts_mov_b64 exec, 0x3fff\n\tglobal_load_lds_dword %0, off\n\ts_mov_b64 exec, -1"
                     ::"v"(xp + ((b * 4 + wid) * 14 + lane) * 128), "i"(L_SP + 16384) : "memory");
      }
      asm volatile("s_mov_b64 s[56:57], %0\n\ts_mov_b32 s63, %2\n\t"
                   "global_load_dwordx3 v[92:94], %1, off\n\tglobal_load_ushort v95, %3, off\n\tbuffer_load_dwordx2 v[120:121], off, " SW ", s63 sc1"
                   ::"s"(ws), "v"(&PTAB[j].v[r][0][tid][0]), "i"(fwo(6144)), "v"(assmD + j * 56 + (doA ? cr * 4 + wid : 0)) : "memory");
      pa.sk = __builtin_amdgcn_make_buffer_rsrc((void*)ak, (short)0, SEQ * DA * 2, 0x00020000);
      pa.sv = __builtin_amdgcn_make_buffer_rsrc((void*)av, (short)0, SEQ * DA * 2, 0x00020000);
      pa.sw = __builtin_amdgcn_make_buffer_rsrc((void*)(ash + (pu >> 4) * 256), (short)0, pu < 0 ? 0 : 512, 0x00020000);
      asm volatile("" : "+s"(pa.sk), "+s"(pa.sv), "+s"(pa.sw));
    }
    auto start = [&](auto Pc) __attribute__((always_inline)) {
      constexpr int P = Pc;
#define XW0 0
      asm volatile("s_waitcnt vmcnt(%0) lgkmcnt(0)\n\ts_barrier" ::"i"((CTP == 2 ? 13 : 12) + XW0) : "memory");
      g_read0<P>(g, make_iseq<16>{});
      asm volatile("s_waitcnt lgkmcnt(0)\n\ts_barrier" ::: "memory");
      GAP();
    };
    RSV rsv;
    if (isY) {
      start(std::integral_constant<int, 1>{});
      rsv_begin(rsv);
      static_for([&](auto Tc) __attribute__((always_inline)) {
        constexpr int T = Tc;
        q_half<T, 0>(g, pa);
        if constexpr (T == 4) {
          const bf16* w_ = wkv; const int d_ = dq * 512;
          sld<88, A_TAB>(); sld<89, A_TAB + 1>();
          asm volatile("s_mov_b64 s[68:69], %0\n\ts_mov_b32 s70, 0x800000\n\tv_add_u32 v87, %1, v87" ::"s"(w_), "s"(d_));
          code_touch<0>();
        }
        if constexpr (T == 5) {
          const bf16* c_ = ckv; const unsigned v_ = vbA2;
          asm volatile("s_mov_b64 s[64:65], %0\n\ts_mov_b32 s66, 0x400000\n\tv_mov_b32 v86, %1\n\ts_mov_b32 s72, 0x10000\n\ts_mov_b32 s73, 0x20000\n\t"
                       "s_mov_b32 s74, 0x30000\n\ts_mov_b32 s75, 0x40000\n\ts_mov_b32 s76, 0x50000\n\ts_mov_b32 s77, 0x60000\n\ts_mov_b32 s78, 0x70000"
                       ::"s"(c_), "v"(v_));
        }
        GAP();
        q_half<T, 1>(g, pa);
      }, make_iseq<6>{});
      rsv_end(rsv);
      asm volatile("ct_begin:");
      asm volatile("v_lshrrev_b32 %0, 4, v89" : "=v"(lane));
      consts2();
      asm volatile("" : "=v"(vw), "=v"(tb0), "=v"(tp), "=v"(vD));
      GAP();
      STT(0);
      g_half<false, 1, 0, 3>(g);
      g_half<false, 1, 1, 3>(g);
      set_flag<A_FPREP>();
      KST(0, 3);
    }
    unsigned om = 0x7000u | (isY ? (PARK ? 0x1eu : 4u) : (wid & 1) ? TOUCH1 : TOUCH0);
    asm volatile("" : "+s"(om));
    auto pollG = [&]() __attribute__((always_inline)) {
      sld<62, A_NPB>();
      fl_mask<A_NP, A_NP + 1>();
      asm volatile("s_nop 1");
      fl_issue();
    };
    auto oddG = [&](int t) __attribute__((always_inline)) {
      if (__builtin_expect((om >> t) & 1, 0)) {
        if (PARK && t == 1) park_store<0>();
        else if (PARK && t == 3) park_store<2>();
        else if (t != 13) {
          asm volatile("s_mov_b32 m0, %1\n\ts_nop 0\n\tglobal_load_lds_dword %0, off" ::"v"(tp), "i"(L_SP) : "memory");
        } else {
          fl_check();
          sld<63, A_VBB>(); sld<87, A_MB2>();
          asm volatile("s_mov_b64 s[68:69], %0\n\ts_mov_b32 s70, %1\n\ts_mov_b64 s[88:89], 0\n\tv_add_u32 v87, s63, v89\n\t"
                       "s_movk_i32 s80, 0x400\n\ts_movk_i32 s81, 0x800\n\ts_movk_i32 s82, 0xc00\n\ts_movk_i32 s83, 0x1000\n\t"
                       "s_movk_i32 s84, 0x1400\n\ts_movk_i32 s85, 0x1800\n\ts_movk_i32 s86, 0x1c00"
                       ::"s"(isY ? (const void*)ws : (const void*)assmk), "s"(isY ? 0x6400000 : DA * SEQ * 2));
        }
      }
    };
    auto evenG = [&](int t) __attribute__((always_inline)) {
      if (__builtin_expect((om >> t) & 1, 0)) {
        if (PARK && t == 2) park_store<1>();
        else if (t == (PARK ? 4 : 2)) set_flag<A_FT2>();
        else if (t == 12) pollG();
        else {
          sld<63, A_VBA>(); sld<79, A_MA2>();
          asm volatile("s_mov_b64 s[64:65], s[56:57]\n\ts_mov_b32 s66, s58\n\tv_add_u32 v87, 0x2000, v87\n\tv_add_u32 v86, s63, v89\n\t"
                       "s_movk_i32 s72, 0x1000\n\ts_movk_i32 s75, 0x4000\n\ts_movk_i32 s76, 0x5000");
          code_touch<1>();
        }
      }
    };
    auto tileG = [&](auto TRc) __attribute__((always_inline)) {
      constexpr bool TR = TRc;
      if (isY) {
        STT(5);
        g_half<TR, 0, 0, 2>(g);
        g_half<TR, 0, 1, 2>(g);
      } else {
        start(std::integral_constant<int, 0>{});
        g_half<TR, 0, 0, 1>(g);
        g_half<TR, 0, 1, 1>(g);
        vD = __float_as_uint(vget<95>());
      }
      g_loop<TR, false>(g, oddG, evenG);
      STT(10); STT(11);
      g_half<TR, 1, 0, 4>(g, wid);
      g_half<TR, 1, 1, 4>(g, wid);
      GAP();
    };
    if (kv) tileG(std::true_type{});
    else tileG(std::false_type{});
    STT(16);
  }
  att_item<3>(); att_item<4>(); att_item<5>(); att_item<6>();
  vseti<95>(wid * 16 + (lane & 15) - 4 * (lane >> 4)); vseti<126>(48 - wid * 16 + (lane & 15) - 4 * (lane >> 4)); vseti<127>(0xff800000u);
  asm volatile("s_mov_b64 s[88:89], %0\n\tv_mov_b32 v87, %1\n\ts_mov_b64 s[96:97], %2\n\tv_writelane_b32 v92, s96, 0\n\tv_writelane_b32 v92, s97, 1" ::"s"(aq), "v"(vq), "s"(out));
  KST(0, 4);
  if (isY) {
    sld<72, A_FCW>(); sld<96, B_MM, 1>(); sld<73, B_OP, 1>();
    asm volatile("s_mov_b32 s97, 0\n\ts_add_u32 m0, s78, 0x2000\n\ts_add_u32 s74, s74, 0x2000\n\t" A_POLL("v[122:123]", "s[96:97]", "s72") ::: "scc", "memory");
    dma4<89, 74>();
  } else if (doA) {
    const int l16 = lane & 15, fq = lane >> 4;
    CV c;
    c.lw = 65536 + wid * 15488;
    asm volatile("ds_write_b128 %0, v[114:117] offset:4096" ::"v"(c.lw + lane * 16));
    fl_mask<-1, A_NQ>();
#pragma unroll
    for (int i = 0; i < 4; ++i) c.pa[i] = c.lw + (((l16 + 1) & 1) ? CV_KS : CV_KP) + 4 * ((8 * fq + l16 + 1) >> 1) + 1024 * i;
    c.pb = c.lw + (2032 - 16 * l16 + 8 * fq) * 2 - 512 * 7;
    KST(0, 5);
    cv_rd0(c, make_iseq<24>{});
    static_for([&](auto I) { vseti<96 + I>(15 - l16 >> 1 == 4 * fq + I ? vD << (l16 & 1 ? 0 : 16) : 0u); }, make_iseq<4>{});
    fl_issue();
    c.vzo = (l16 >> 1) * 3584 + (l16 & 1) * 32 + (fq >> 1) * 16 + (1 - (fq & 1)) * 8 * 3584;
    GAP();
    cv_slots(c, make_iseq<144>{});
    static_for([&](auto T) { cv_slot<144 + T>(c); }, make_iseq<144>{});
    cv_tails(c, make_iseq<28 - cv_z0(288)>{});
    KST(0, 6);
    asm volatile("s_waitcnt vmcnt(%0) lgkmcnt(0)\n\ts_barrier" ::"i"(cv_end()) : "memory");
  } else {
    d_ops<160, 0>(make_iseq<208>{});
    fl_mask<-1, A_NQ>();
    sld<72, A_OQ>();
    fl_wait();
    asm volatile("s_waitcnt lgkmcnt(0)\n\ts_barrier" ::: "memory");
    set_flag<A_FG>();
    static_for([&](auto I) { f_load<128, I>(); if constexpr ((I & 3) == 3) f_next(); }, make_iseq<16>{});
    KST(0, 5);
    fl_mask<A_N01, -1>();
    fl_wait();
    k01_dma<0>(); k01_dma<1>(); k01_dma<2>(); k01_dma<3>();
    KST(0, 6);
    asm volatile("s_waitcnt vmcnt(8)\n\ts_barrier" ::: "memory");
  }
  if (!isY) {
    sld<94, A_WSRC>(); sld<73, B_OP, 1>();
    asm volatile("s_mov_b64 s[64:65], %0\n\ts_mov_b32 s66, 0x380000\n\ts_mov_b32 s67, s59\n\ts_mov_b32 s75, %1\n\t"
                 "v_mov_b32 v84, %2\n\tv_mov_b32 v85, %3\n\tv_mov_b32 v121, %4"
                 ::"s"(wo), "s"(L_SP + wid * 1024), "v"(vw), "v"(tb0), "v"(lane < 28 ? ((b * 4 + wid) * 28 + lane) * 128 : OOB));
  }
  u32x16 rs0, rs1; u32x4 rs2; u32x2 rs3;
#define STA(k)
  {
    AC c;
    asm volatile("rsv2_begin:" : "={v[40:55]}"(rs0), "={v[56:71]}"(rs1), "={v[72:75]}"(rs2), "={v[76:77]}"(rs3));
    const unsigned vml = (lane & 15) * 16;
#pragma unroll 1
    for (int it = r >> 4;; ++it) {
      if (it == r >> 4) KST(0, 8); else KST(0, 11);
      kreads<0>(make_iseq<8>{});
      const int n = prm<B_N, 1>();
      GAP();
      STA(0);
      if (isY) {
        phaseA<0, 2, false>(c);
        STA(1); STA(2);
        phaseB0<true>(false, vml);
      } else {
        if (it) phaseA<0, 3, false>(c);
        else phaseA<0, 1, false>(c);
        STA(1);
        if (c.rare) event();
        STA(2);
        phaseB0<false>(it && n == 2, vml);
      }
      STA(3);
      const int nl = n - it;
#pragma unroll 1
      for (int i = 1; i != nl; i += 2) {
        phaseA<1, 0, false>(c);
        STA(9 + i);
        if (__builtin_expect(c.rare != 0, 0)) { if (c.redo) sm_redo<1>(); event(); }
        phaseB<1>(c);
        if (i + 1 == nl) break;
        phaseA<0, 0, false>(c);
        STA(10 + i);
        if (__builtin_expect(c.rare != 0, 0)) { if (c.redo) sm_redo<0>(); event(); }
        phaseB<0>(c);
      }
      STA(4);
      if (it) break;
      sld<72, B_NXT, 1>(); sld<73, B_OP, 1>();
      epi_init<32768>();
      phaseA<ODD, 0, true>(c);
      if (__builtin_expect(c.redo != 0, 0)) sm_redo<ODD>();
      KST(0, 9);
      const float l[2] = {row_reduce<false>(vget<R_RS>() + vget<R_TS>()), row_reduce<false>(vget<R_RS + 1>() + vget<R_TS + 1>())};
      const f32x4 ml = {vget<R_M>(), l[0], vget<R_M + 1>(), l[1]};
      asm volatile("buffer_store_dwordx4 %0, %1, " SW ", s73 offen sc1" ::"v"(ml), "v"(vml) : "memory");
      phaseF(c, EP{}, make_iseq<74>{});
      asm volatile("s_waitcnt vmcnt(16)" ::: "memory");
      KST(0, 10);
    }
    asm volatile("at_tail:");
    vseti<84>(wid == 3 ? OOB : lane * 16);
    auto sel = [&]() __attribute__((always_inline)) {
      const unsigned one = (lane >> 4) == ((lane & 15) >> 2) ? 0x3f80u << ((lane & 1) * 16) : 0u, s0 = lane & 2 ? 0u : one, s1 = lane & 2 ? one : 0u;
      asm volatile("v_mov_b32 v72, %0\n\tv_mov_b32 v73, %1\n\tv_mov_b64 v[74:75], 0\n\tv_mov_b32 v76, %0\n\tv_mov_b32 v77, %1" ::"v"(s0), "v"(s1));
    };
    auto fin = [&](auto Lc, auto Mc) __attribute__((always_inline)) {
      constexpr int L = Lc, M = Mc;
      EP p;
      phaseAD<L, M == 1>(c, make_iseq<32>{});
      if (__builtin_expect(c.redo != 0, 0)) sm_redo<L>();
      STA(5); STF(4);
      sld<76, B_SO, 1>();
      vseti<85>((wid >> 1) * 16384 + (wid & 1) * 1024 + lane * 16);
      if constexpr (M < 2) p.fa[0] = __builtin_amdgcn_rcpf(row_reduce<false>(vget<R_RS>() + vget<R_TS>()));
      GAP();
      phaseBD<L, M < 2>(c, p, make_iseq<64>{});
      if (__builtin_expect(c.redo != 0, 0)) sm_redo<L ^ 1, 1>();
      STA(6);
      KST(0, 12);
      STF(12);
      asm volatile("s_waitcnt vmcnt(%0)\n\ts_barrier" ::"i"(M < 2 ? 6 + BDX : 0) : "memory");
      STF(13);
      if constexpr (M == 2) { asm volatile("v_add_u32 v107, %0, v90" ::"i"(L_SP)); epi_preads(make_iseq<8>{}); }
      vreads<L>(make_iseq<16>{});
      const float l1 = row_reduce<false>(vget<R_RS + 1>() + vget<R_TS + 1>());
      if constexpr (M < 2) {
        p.fa[1] = __builtin_amdgcn_rcpf(l1);
#if !(UB & 1)
        asm volatile("s_waitcnt lgkmcnt(0)" ::: "memory");
#endif
        phaseFD<L, true>(p, make_iseq<40>{});
      } else {
        const float l[2] = {row_reduce<false>(vget<R_RS>()), l1}, cm[2] = {vget<R_M>(), vget<R_M + 1>()};
        const f32x4 ml = {vget<72>(), vget<73>(), vget<74>(), vget<75>()};
#pragma unroll
        for (int e = 0; e < 2; ++e) {
          const float mm = fmaxf(cm[e], ml[2 * e]);
          const float wa = __builtin_amdgcn_exp2f(cm[e] - mm), wb = __builtin_amdgcn_exp2f(ml[2 * e] - mm);
          const float inv = __builtin_amdgcn_rcpf(l[e] * wa + ml[2 * e + 1] * wb);
          p.fa[e] = wa * inv; p.fb[e] = wb * inv;
        }
        sel();
        static_for([&](auto I) { asm volatile("v_mov_b32 v%c0, v%c1" ::"i"(160 + (int)I), "i"(40 + (int)I)); }, make_iseq<32>{});
        asm volatile("s_waitcnt lgkmcnt(0)" ::: "memory");
        phaseFD<L, false>(p, make_iseq<64>{});
      }
    };
    using I0 = std::integral_constant<int, 0>; using I1 = std::integral_constant<int, 1>; using I2 = std::integral_constant<int, 2>;
    using LY = std::integral_constant<int, ODD ? 0 : 1>;
    if (isY) {
      const unsigned late = prm<B_LM + 1, 1>();
      asm volatile("s_waitcnt vmcnt(12)" ::: "memory");
      const float cm[2] = {vget<R_M>(), vget<R_M + 1>()};
      const f32x4 ml = {vget<72>(), vget<73>(), vget<74>(), vget<75>()};
      if (__builtin_expect(!late && __builtin_amdgcn_ballot_w64(cm[0] != ml[0] || cm[1] != ml[2]) == 0, 1)) {
        vset<R_RS>(vget<R_RS>() + 0.25f * ml[1]); vset<R_RS + 1>(vget<R_RS + 1>() + 0.25f * ml[3]);
        sel();
        asm volatile("v_add_u32 v85, %0, v90" ::"i"(L_SP));
        fin(LY{}, I1{});
      } else fin(LY{}, I2{});
    } else { sel(); fin(I1{}, I0{}); }
    KST(0, 13);
    STF(20);
  }
  {
    sld<64, A_OWN>(); sld<65, A_OWN + 1>(); sld<66, A_OWN + 2>(); sld<67, A_OWN + 3>();
    asm volatile("s_lshl_b32 s80, %0, 12\n\tv_add_u32 v96, s64, v89\n\tv_add_u32 v97, s65, v89\n\tv_add_u32 v98, s66, v89\n\tv_add_u32 v99, s67, v89\n\t"
                 "s_waitcnt lgkmcnt(0)\n\ts_barrier" ::"s"(wid) : "scc", "memory");
    STF(21);
    o_read0(make_iseq<16>{});
    {
      const int fr = lane & 15, fq = lane >> 4;
      const unsigned vo = wid < 3 || !(fq & 1) ? prm<A_OUT>() + (fr * OUTW + (fq & 1) * 16 + (fq >> 1) * 8) * 2 : OOB;
      asm volatile("v_mov_b32 v101, %0\n\tv_add_u32 v102, %1, %0\n\ts_mov_b64 s[88:89], %2\n\ts_mov_b32 s90, 0x380000\n\ts_mov_b32 s91, s59"
                   ::"v"(vo), "s"((j < 4 ? 16 : -16) * OUTW * 2), "s"(out));
      GAP();
    }
    if (wid < 3) {
      static_for([&](auto P) {
        o_interval<2, P>();
        if constexpr (P == 0) STF(22);
        if constexpr (P == 1) STF(23);
        if constexpr (P == 2) STF(24);
        if constexpr (P == 3) STF(25);
        if constexpr (P == 1) KST(0, 27);
        if constexpr (P == 3) KST(0, 28);
      }, make_iseq<15>{});
      o_interval<2, 15>();
      o_slabs<2>();
      o_sum<2>();
    } else {
      sld<62, A_FCW>();
      fl_mask<A_FCM, -1>();
      GAP();
      static_for([&](auto Pc) {
        constexpr int P = Pc;
        o_interval<1, P>();
        if constexpr (P == 0 || P == 2) { fl_wait(); asm volatile("s_lshl_b32 s96, s96, 4" ::: "scc"); }
      }, make_iseq<15>{});
      o_interval<1, 15>();
      o_slabs<1>();
      o_sum<1>();
    }
    asm volatile("rsv2_end:" ::"{v[40:55]}"(rs0), "{v[56:71]}"(rs1), "{v[72:75]}"(rs2), "{v[76:77]}"(rs3));
    KST(0, 29);
#define A_CV(D, S0, S1) "v_cvt_pk_bf16_f32 v" #D ", v" #S0 ", v" #S1 "\n\t"
    asm volatile(A_CV(224, 240, 241) A_CV(226, 244, 245) A_CV(225, 242, 243) A_CV(227, 246, 247) A_CV(228, 248, 249) A_CV(230, 252, 253) A_CV(229, 250, 251) A_CV(231, 254, 255)
                 "v_permlane16_swap_b32_e32 v224, v226\n\tv_permlane16_swap_b32_e32 v225, v227\n\tv_permlane16_swap_b32_e32 v228, v230\n\tv_permlane16_swap_b32_e32 v229, v231\n\t"
                 "buffer_store_dwordx4 v[224:227], v101, s[88:91], 0 offen\n\tbuffer_store_dwordx4 v[228:231], v102, s[88:91], 0 offen" ::: "memory");
    set_flag<A_FE>();
  }
  KST(0, 1);
  asm volatile("s_endpgm\n" A_EVENT "\npf_code_end:" ::: "memory");
}

extern "C" void kernel_launch(void* const* d_in, const int* in_sizes, int n_in, void* d_out, int out_size, void* d_ws,
                              size_t ws_size, hipStream_t stream) {
  const bf16* Aq = (const bf16*)d_in[0];
  const bf16* Ak = (const bf16*)d_in[1];
  const bf16* Av = (const bf16*)d_in[2];
  const bf16* Ash = (const bf16*)d_in[3];
  const bf16* Assmk = (const bf16*)d_in[4];
  const bf16* AssmD = (const bf16*)d_in[5];
  const bf16* Bx = (const bf16*)d_in[6];
  const bf16* Bckv = (const bf16*)d_in[7];
  const bf16* Bwq = (const bf16*)d_in[8];
  const bf16* Bwuk = (const bf16*)d_in[9];
  const bf16* Bwuv = (const bf16*)d_in[10];
  const bf16* Bwo = (const bf16*)d_in[11];
  bf16* out = (bf16*)d_out;
  (void)hipFuncSetAttribute((const void*)fused_kernel, hipFuncAttributeMaxDynamicSharedMemorySize, 163840);
  fused_kernel<<<256, 256, 163840, stream>>>(Bckv, Bx, Bwuk, Bwuv, Bwq, (char*)d_ws + ((ws_size - 0x8000000) & ~(size_t)0x1fffff), Ak, Av, Ash, Assmk, AssmD, Bwo, Aq, out);
}
```
